```python
import torch

def _softmax(x, axis=-1):
    return torch.softmax(x, dim=axis)

def _gelu(x):
    return torch.nn.functional.gelu(x, approximate='tanh')

def _silu(x):
    return torch.nn.functional.silu(x)

def _sigmoid(x):
    return torch.sigmoid(x)

def _layernorm(x, axis=-1, eps=1e-05):
    m = x.mean(dim=axis, keepdim=True)
    v = x.var(dim=axis, keepdim=True, unbiased=False)
    return (x - m) / torch.sqrt(v + eps)

def _scatter_add(tgt, idx, src, axis=0):
    return tgt.clone().index_add_(axis, idx, src)

def _segment_sum(src, seg, n):
    out = torch.zeros((n,) + src.shape[1:], dtype=src.dtype)
    return out.index_add_(0, seg, src)

def _pad(x, pw):
    flat = tuple((v for pair in reversed(pw) for v in pair))
    return torch.nn.functional.pad(x, flat)
kernel_name = 'adaLN_cond__linear_kernel_hedgehog__glu_gated_bilinear__prompt'

def setup_inputs():

    _w = lambda a: (lambda t: t.to(torch.bfloat16) if t.is_floating_point() else t)(torch.as_tensor(a))

    def _A_setup():
        B, S, D, H = (1, 2048, 512, 32)
        Dh = D // H
        return {'x': _w((lambda *s: torch.randn(*s, dtype=torch.bfloat16) * 0.1)(B, S, D)), 'wqkv': _w((lambda *s: torch.randn(*s, dtype=torch.bfloat16) * 0.2)(D, 3 * D)), 'w_phi': _w((lambda *s: torch.randn(*s, dtype=torch.bfloat16) * 0.2)(H, Dh, Dh) * Dh ** (-0.5)), 'wo': _w((lambda *s: torch.randn(*s, dtype=torch.bfloat16) * 0.2)(D, D))}
    _A_in = _A_setup()

    def _B_setup():
        B, S, D, F = (1, 2048, 512, 1376)
        return {'x': _w((lambda *s: torch.randn(*s, dtype=torch.bfloat16) * 0.1)(B, S, D)), 'w_gate': _w((lambda *s: torch.randn(*s, dtype=torch.bfloat16) * 3.2)(D, F) * D ** (-0.5)), 'w_up': _w((lambda *s: torch.randn(*s, dtype=torch.bfloat16) * 3.2)(D, F) * D ** (-0.5)), 'w_down': _w((lambda *s: torch.randn(*s, dtype=torch.bfloat16) * 3.2)(F, D) * F ** (-0.5))}
    _B_in = _B_setup()
    return {'A_x': _A_in['x'], 'A_wqkv': _A_in['wqkv'], 'A_w_phi': _A_in['w_phi'], 'A_wo': _A_in['wo'], 'B_x': _B_in['x'], 'B_w_gate': _B_in['w_gate'], 'B_w_up': _B_in['w_up'], 'B_w_down': _B_in['w_down'], 'c': _w((lambda *s: torch.randn(*s, dtype=torch.bfloat16) * 0.4)(1, 512) * 8.0), 'w_mod': _w((lambda *s: torch.randn(*s, dtype=torch.bfloat16) * 0.4)(512, 6 * 512) * 512 ** (-0.5) * 8.0)}

def reference(A_x, A_wqkv, A_w_phi, A_wo, B_x, B_w_gate, B_w_up, B_w_down, c, w_mod):

    def _A_ref(x, wqkv, w_phi, wo):
        B, S, D = x.shape
        H = 32
        Dh = D // H
        qkv = (lambda x, *d: x.permute(*d))((x @ wqkv).reshape(B, S, 3, H, Dh), 2, 0, 3, 1, 4)
        q, k, v = (qkv[0], qkv[1], qkv[2])
        pq = torch.einsum('bhsd,hdf->bhsf', q, w_phi)
        pk = torch.einsum('bhsd,hdf->bhsf', k, w_phi)
        phi_q = torch.concatenate([_softmax(pq, axis=-1), _softmax(-pq, axis=-1)], axis=-1)
        phi_k = torch.concatenate([_softmax(pk, axis=-1), _softmax(-pk, axis=-1)], axis=-1)
        kv = torch.einsum('bhsf,bhsd->bhfd', phi_k, v)
        z = torch.einsum('bhsf->bhf', phi_k)
        num = torch.einsum('bhqf,bhfd->bhqd', phi_q, kv)
        den = torch.einsum('bhqf,bhf->bhq', phi_q, z)[..., None] + 1e-06
        o = num / den
        return (lambda x, *d: x.permute(*d))(o, 0, 2, 1, 3).reshape(B, S, D) @ wo

    def _B_ref(x, w_gate, w_up, w_down):
        g = torch.einsum('bsd,df->bsf', x, w_gate)
        u = torch.einsum('bsd,df->bsf', x, w_up)
        h = g * u
        return torch.einsum('bsf,fd->bsd', h, w_down)
    x = A_x
    mod = (_silu(c) @ w_mod).reshape(c.shape[0], 6, 1, -1)
    sa, ca, ga, sb, cb, gb = (mod[:, 0].clone(), mod[:, 1].clone(), mod[:, 2].clone(), mod[:, 3].clone(), mod[:, 4].clone(), mod[:, 5].clone())
    h = _layernorm(x, axis=-1) * (1 + ca) + sa
    x = x + ga * _A_ref(h, A_wqkv, A_w_phi, A_wo)
    h = _layernorm(x, axis=-1) * (1 + cb) + sb
    x = x + gb * _B_ref(h, B_w_gate, B_w_up, B_w_down)
    return x
```

```cpp
#include <hip/hip_runtime.h>
#include <hip/hip_bf16.h>
typedef unsigned short u16;
__device__ __forceinline__ float bf2f(u16 b){ return __uint_as_float(((unsigned)b)<<16); }
__device__ __forceinline__ u16 f2bf(float f){ unsigned u=__float_as_uint(f); u += 0x7fffu + ((u>>16)&1u); return (u16)(u>>16);}
__device__ __forceinline__ float rb(float f){ return bf2f(f2bf(f)); }
constexpr int S=2048,D=512,H=32,DH=16,F=1376;

__global__ void k_mod(const u16* c,const u16* w,float* mod){
  int n=blockIdx.x*blockDim.x+threadIdx.x; if(n>=6*D) return;
  float acc=0;
  for(int k=0;k<D;k++){ float cv=bf2f(c[k]); float s=rb(cv/(1.f+__expf(-cv))); acc+=s*bf2f(w[k*6*D+n]); }
  mod[n]=rb(acc);
}
__global__ void k_ln(const u16* x,const float* shift,const float* scale,u16* h){
  int r=blockIdx.x; int t=threadIdx.x;
  float v[8]; float s=0;
  for(int j=0;j<8;j++){ v[j]=bf2f(x[r*D+t*8+j]); s+=v[j]; }
  for(int o=32;o;o>>=1) s+=__shfl_xor(s,o);
  float mean=s/D; float q=0;
  for(int j=0;j<8;j++){ float d=v[j]-mean; q+=d*d; }
  for(int o=32;o;o>>=1) q+=__shfl_xor(q,o);
  float var=rb(q/D); float m=rb(mean);
  float den=rb(sqrtf(rb(var+1e-5f)));
  for(int j=0;j<8;j++){ int k=t*8+j; float xn=rb(rb(v[j]-m)/den); float hh=rb(rb(xn*rb(1.f+scale[k]))+shift[k]); h[r*D+k]=f2bf(hh);}
}
__global__ void k_gemm(const u16* A,const u16* B,u16* C,int M,int N,int K){
  int n=blockIdx.x*blockDim.x+threadIdx.x; int m=blockIdx.y; if(n>=N) return;
  float acc=0; for(int k=0;k<K;k++) acc+=bf2f(A[m*K+k])*bf2f(B[k*N+n]);
  C[m*N+n]=f2bf(acc);
}
__global__ void k_phi(const u16* qkv,const u16* wphi,u16* phi  ){
  int i=blockIdx.x*blockDim.x+threadIdx.x; if(i>=2*S*H) return;
  int h=i%H; int s=(i/H)%S; int w=i/(H*S);
  float p[16]; float mx=-1e30f,mn=1e30f;
  for(int f=0;f<16;f++){ float a=0; for(int d=0;d<16;d++) a+=bf2f(qkv[s*3*D+w*D+h*16+d])*bf2f(wphi[h*256+d*16+f]); p[f]=rb(a); mx=fmaxf(mx,p[f]); mn=fminf(mn,p[f]); }
  float s1=0,s2=0; float e1[16],e2[16];
  for(int f=0;f<16;f++){ e1[f]=__expf(p[f]-mx); e2[f]=__expf(mn-p[f]); s1+=e1[f]; s2+=e2[f]; }
  u16* o=phi+((size_t)(w*S+s)*H+h)*32;
  for(int f=0;f<16;f++){ o[f]=f2bf(e1[f]/s1); o[16+f]=f2bf(e2[f]/s2);}
}
__global__ void k_kv(const u16* qkv,const u16* phi,float* kv  ){
  int i=blockIdx.x*blockDim.x+threadIdx.x; if(i>=H*32*17) return;
  int d=i%17; int f=(i/17)%32; int h=i/(17*32);
  float a=0; for(int s=0;s<S;s++){ float pk=bf2f(phi[((size_t)(S+s)*H+h)*32+f]); float v= d<16? bf2f(qkv[s*3*D+2*D+h*16+d]):1.f; a+=pk*v; }
  kv[i]=rb(a);
}
__global__ void k_o(const u16* phi,const float* kv,u16* o){
  int i=blockIdx.x*blockDim.x+threadIdx.x; if(i>=S*D) return;
  int d=i%16; int h=(i/16)%H; int s=i/D;
  float num=0,den=0; for(int f=0;f<32;f++){ float pq=bf2f(phi[((size_t)s*H+h)*32+f]); num+=pq*kv[(h*32+f)*17+d]; den+=pq*kv[(h*32+f)*17+16]; }
  o[i]=f2bf(rb(num)/rb(rb(den)+1e-6f));
}
__global__ void k_res(const u16* x,const u16* y,const float* g,u16* out){
  int i=blockIdx.x*blockDim.x+threadIdx.x; if(i>=S*D) return;
  out[i]=f2bf(bf2f(x[i])+rb(g[i%D]*bf2f(y[i])));
}
__global__ void k_mul(const u16* a,const u16* b,u16* o,int n){ int i=blockIdx.x*blockDim.x+threadIdx.x; if(i<n) o[i]=f2bf(bf2f(a[i])*bf2f(b[i])); }

extern "C" void kernel_launch(void* const* d_in, const int* in_sizes, int n_in, void* d_out, int out_size, void* d_ws, size_t ws_size, hipStream_t stream) {
  const u16* x=(const u16*)d_in[0]; const u16* wqkv=(const u16*)d_in[1]; const u16* wphi=(const u16*)d_in[2]; const u16* wo=(const u16*)d_in[3];
  const u16* wg=(const u16*)d_in[5]; const u16* wu=(const u16*)d_in[6]; const u16* wd=(const u16*)d_in[7]; const u16* c=(const u16*)d_in[8]; const u16* wmod=(const u16*)d_in[9];
  char* ws=(char*)d_ws; size_t off=0; auto al=[&](size_t b){ void* p=ws+off; off+=(b+255)&~(size_t)255; return p; };
  float* mod=(float*)al(6*D*4); u16* h=(u16*)al(S*D*2); u16* qkv=(u16*)al(S*3*D*2); u16* phi=(u16*)al((size_t)2*S*H*32*2); float* kv=(float*)al(H*32*17*4);
  u16* o=(u16*)al(S*D*2); u16* ao=(u16*)al(S*D*2); u16* x1=(u16*)al(S*D*2); u16* h2=(u16*)al(S*D*2); u16* g=(u16*)al(S*F*2); u16* u=(u16*)al(S*F*2); u16* hh=(u16*)al(S*F*2); u16* dn=(u16*)al(S*D*2);
  k_mod<<<12,256,0,stream>>>(c,wmod,mod);
  k_ln<<<S,64,0,stream>>>(x,mod,mod+D,h);
  k_gemm<<<dim3(6,S),256,0,stream>>>(h,wqkv,qkv,S,3*D,D);
  k_phi<<<2*S*H/256,256,0,stream>>>(qkv,wphi,phi);
  k_kv<<<(H*32*17+255)/256,256,0,stream>>>(qkv,phi,kv);
  k_o<<<S*D/256,256,0,stream>>>(phi,kv,o);
  k_gemm<<<dim3(2,S),256,0,stream>>>(o,wo,ao,S,D,D);
  k_res<<<S*D/256,256,0,stream>>>(x,ao,mod+2*D,x1);
  k_ln<<<S,64,0,stream>>>(x1,mod+3*D,mod+4*D,h2);
  k_gemm<<<dim3(6,S),256,0,stream>>>(h2,wg,g,S,F,D);
  k_gemm<<<dim3(6,S),256,0,stream>>>(h2,wu,u,S,F,D);
  k_mul<<<(S*F+255)/256,256,0,stream>>>(g,u,hh,S*F);
  k_gemm<<<dim3(2,S),256,0,stream>>>(hh,wd,dn,S,D,F);
  k_res<<<S*D/256,256,0,stream>>>(x1,dn,mod+5*D,(u16*)d_out);
}
```

```cpp
#include <hip/hip_runtime.h>

namespace mk {
typedef unsigned short u16;
typedef unsigned int u32;
typedef unsigned long long u64;
typedef u32 v4u __attribute__((ext_vector_type(4)));
typedef u32 v2u __attribute__((ext_vector_type(2)));
typedef float f32x2 __attribute__((ext_vector_type(2)));
typedef float f32x4 __attribute__((ext_vector_type(4)));
typedef float f32x16 __attribute__((ext_vector_type(16)));
typedef __bf16 bf16x2 __attribute__((ext_vector_type(2)));
typedef __bf16 bf16x8 __attribute__((ext_vector_type(8)));
typedef short s16x4 __attribute__((ext_vector_type(4)));
#define LAS __attribute__((address_space(3)))
#define GAS __attribute__((address_space(1)))
#define DI __device__ __forceinline__
#define RLX_AGENT __ATOMIC_RELAXED, __HIP_MEMORY_SCOPE_AGENT
#define SB0() __builtin_amdgcn_sched_barrier(0)
typedef __amdgpu_buffer_rsrc_t rsrc_t;

constexpr int D = 512, F = 1376;
constexpr int OFF_EPOCH = 0;
constexpr int OFF_MODG = 4096;
constexpr int OFF_FLAG = 32768;
constexpr int FL_H = 0, FL_KV = 1, FL_O = 2, FL_X = 3, FL_G = 4;
constexpr int OFF_CG = 768 << 10;
constexpr int CG_PART = 2 * F * 16;
constexpr int OFF_H = 1 << 20;
constexpr int OFF_O = 3 << 20;
constexpr int OFF_X1 = 5 << 20;
constexpr int OFF_ST = 7 << 20;
constexpr int OFF_KVZ = (7 << 20) + (512 << 10);
constexpr int OFF_KVE = 8 << 20;
constexpr int OFF_G = 9 << 20;
constexpr int WS_BYTES = 15 << 20;
constexpr int OFF_TS = 32 << 20;
constexpr int RING = 136;
constexpr int L_T = RING * 1024;
constexpr int L_Z = L_T + 16384;
constexpr int L_KVF = L_Z + 1024;
constexpr int L_ZF = L_KVF + 2048;
constexpr int L_VEC = L_ZF + 256;
constexpr int L_CF = L_VEC + 768;
constexpr int L_HINT = L_CF + 3072;
constexpr int LDS_BYTES = L_HINT + 64;
static_assert(LDS_BYTES <= 163840, "LDS");
constexpr int P_KV = 0, P_Q = 64, P_WO = 96, P_GU = 160, P_WD = 352;
constexpr int slot(int n) { return (n % RING) * 1024; }
constexpr int AUX_SC1 = 16;
constexpr u32 SPIN_CAP = 1u << 16;
constexpr int PRE_MOD = 12, PRE_FL = 14;
constexpr int PD = 5;

struct Params {
  const u16 *x, *wqkv, *wphi, *wo, *wg, *wu, *wd, *c, *wmod;
  u16* out;
  unsigned char* ws;
};

template <int V> struct IC { static constexpr int value = V; };
template <int I, int N, class Fn> DI void static_for(Fn&& f) {
  if constexpr (I < N) { f(IC<I>{}); static_for<I + 1, N>(f); }
}
DI float lo16(u32 v) { return __uint_as_float(v << 16); }
DI float hi16(u32 v) { return __uint_as_float(v & 0xffff0000u); }
DI u32 pk(float a, float b) { f32x2 v = {a, b}; return __builtin_bit_cast(u32, __builtin_convertvector(v, bf16x2)); }
DI float rb(float a) { return lo16(pk(a, 0.f)); }
DI float rcp(float a) { return __builtin_amdgcn_rcpf(a); }
DI f32x16 mfma(v4u a, v4u b, f32x16 c) {
  return __builtin_amdgcn_mfma_f32_32x32x16_bf16(__builtin_bit_cast(bf16x8, a), __builtin_bit_cast(bf16x8, b), c, 0, 0, 0);
}
DI void mfma_pin(v4u a, v4u b, f32x16& c) {
  c = mfma(a, b, c);
  asm volatile("" : "+v"(c));
}
template <int O> DI v4u pack8(const f32x16& x) {
  v4u r; r.x = pk(x[O], x[O + 1]); r.y = pk(x[O + 2], x[O + 3]); r.z = pk(x[O + 4], x[O + 5]); r.w = pk(x[O + 6], x[O + 7]); return r;
}
template <int T> DI v4u ldw(LAS unsigned char* p) {
  s16x4 a = __builtin_amdgcn_ds_read_tr16_b64_v4i16((LAS s16x4*)p);
  s16x4 b = __builtin_amdgcn_ds_read_tr16_b64_v4i16((LAS s16x4*)(p + T));
  return __builtin_bit_cast(v4u, __builtin_shufflevector(a, b, 0, 1, 2, 3, 4, 5, 6, 7));
}
struct Base3 {
  u32 b[3];
  DI explicit Base3(LAS unsigned char* p) {
    b[0] = (u32)(size_t)p; b[1] = b[0] + 65536u; b[2] = b[0] + 131072u;
    asm volatile("" : "+v"(b[0]), "+v"(b[1]), "+v"(b[2]));
  }
  template <int OFF> DI LAS unsigned char* at() const { return (LAS unsigned char*)(size_t)b[OFF >> 16] + (OFF & 65535); }
};
DI void glds(rsrc_t rs, u32 voff, u32 soff, u32 ldsoff) {
  asm volatile("s_mov_b32 m0, %0\n\ts_nop 0\n\tbuffer_load_dwordx4 %1, %2, %3 offen lds" ::"s"(ldsoff), "v"(voff), "s"(rs), "s"(soff) : "m0", "memory");
}
template <int LOFF, int SOFF> DI void gldsi(rsrc_t rs, u32 voff, u32 sbase, u32 lbase) {
  u32 t;
  asm volatile("s_add_u32 m0, %1, %2\n\ts_add_u32 %0, %3, %4\n\tbuffer_load_dwordx4 %5, %6, %0 offen lds"
               : "=&s"(t) : "s"(lbase), "i"(LOFF), "s"(sbase), "i"(SOFF), "v"(voff), "s"(rs) : "m0", "scc", "memory");
}
DI void bar() { asm volatile("s_waitcnt lgkmcnt(0)\n\ts_barrier" ::: "memory"); }
DI void bar_raw() { asm volatile("s_barrier" ::: "memory"); }
DI void drain() { asm volatile("s_waitcnt vmcnt(0)" ::: "memory"); }
#define VMCNT(n) asm volatile("s_waitcnt vmcnt(" #n ")" ::: "memory")
#define RELOAD() asm volatile("" ::: "memory")
DI v4u ld128(rsrc_t rs, int voff, int soff) { return __builtin_amdgcn_raw_buffer_load_b128(rs, voff, soff, AUX_SC1); }
DI v2u ld64(rsrc_t rs, int voff, int soff) { return __builtin_amdgcn_raw_buffer_load_b64(rs, voff, soff, AUX_SC1); }
DI void st128(v4u v, rsrc_t rs, int voff, int soff) { __builtin_amdgcn_raw_buffer_store_b128(v, rs, voff + soff, 0, AUX_SC1); }
DI void st64(v2u v, rsrc_t rs, int voff, int soff) { __builtin_amdgcn_raw_buffer_store_b64(v, rs, voff, soff, AUX_SC1); }
template <int CTRL> DI float dpp_add(float v) {
  return v + __uint_as_float(__builtin_amdgcn_update_dpp(0u, __float_as_uint(v), CTRL, 0xf, 0xf, false));
}
constexpr int DPP_X1 = 0xB1, DPP_X2 = 0x4E, DPP_HM = 0x141, DPP_ROR4 = 0x124, DPP_ROR8 = 0x128;
DI float fold32(float x, float y) {
  auto r = __builtin_amdgcn_permlane32_swap(__float_as_uint(x), __float_as_uint(y), false, false);
  return __uint_as_float(r[0]) + __uint_as_float(r[1]);
}
DI float fold16(float x, float y) {
  auto r = __builtin_amdgcn_permlane16_swap(__float_as_uint(x), __float_as_uint(y), false, false);
  return __uint_as_float(r[0]) + __uint_as_float(r[1]);
}
DI void red8(const float (&a)[8], float& c0, float& c1) {
  const float b0 = fold32(a[0], a[4]), b1 = fold32(a[1], a[5]), b2 = fold32(a[2], a[6]), b3 = fold32(a[3], a[7]);
  c0 = fold16(b0, b2); c1 = fold16(b1, b3);
  c0 = dpp_add<DPP_X1>(c0); c1 = dpp_add<DPP_X1>(c1);
  c0 = dpp_add<DPP_X2>(c0); c1 = dpp_add<DPP_X2>(c1);
  c0 = dpp_add<DPP_ROR4>(c0); c1 = dpp_add<DPP_ROR4>(c1);
  c0 = dpp_add<DPP_ROR8>(c0); c1 = dpp_add<DPP_ROR8>(c1);
}
DI void red8q(const float (&a)[8], float& c0, float& c1) {
  const float b0 = fold32(a[0], a[4]), b1 = fold32(a[1], a[5]), b2 = fold32(a[2], a[6]), b3 = fold32(a[3], a[7]);
  c0 = fold16(b0, b2); c1 = fold16(b1, b3);
  c0 = dpp_add<DPP_ROR4>(c0); c1 = dpp_add<DPP_ROR4>(c1);
  c0 = dpp_add<DPP_ROR8>(c0); c1 = dpp_add<DPP_ROR8>(c1);
}
DI void publish(unsigned char* ws, int set, int idx, u32 tag, int lane) {
  drain();
  if (lane == 0) __hip_atomic_store((GAS u32*)(ws + OFF_FLAG + (set * 1024 + idx) * 128), tag, RLX_AGENT);
}
template <int N, int PRE> DI void waitn(unsigned char* ws, int set, int idx0, u32 tag, int lane) {
  if (PRE) __builtin_amdgcn_s_sleep(PRE);
  GAS u32* f = (GAS u32*)(ws + OFF_FLAG + (set * 1024 + idx0 + (lane & (N - 1))) * 128);
  for (u32 spins = 0; spins < SPIN_CAP; ++spins) {
    const u32 v = __hip_atomic_load(f, RLX_AGENT);
    if (__all(v == tag)) break;
    __builtin_amdgcn_s_sleep(1);
  }
  __builtin_amdgcn_fence(__ATOMIC_ACQUIRE, "wavefront");
  RELOAD();
}
DI void sig(LAS unsigned char* L, int i, int lane) {
  if (lane == 0) __hip_atomic_fetch_add((LAS u32*)(L + L_HINT) + i, 1u, __ATOMIC_RELAXED, __HIP_MEMORY_SCOPE_WORKGROUP);
}
DI void await(LAS unsigned char* L, int i, u32 n) {
  for (u32 spins = 0; spins < (1u << 20); ++spins) {
    const u32 v = __builtin_amdgcn_readfirstlane(__hip_atomic_load((LAS u32*)(L + L_HINT) + i, __ATOMIC_RELAXED, __HIP_MEMORY_SCOPE_WORKGROUP));
    if (v >= n) break;
    __builtin_amdgcn_s_sleep(1);
  }
  RELOAD();
}
DI void feat(const f32x16& P, u32 (&ph)[16]) {
  float p[16];
#pragma unroll
  for (int i = 0; i < 16; i += 2) { const u32 t = pk(P[i], P[i + 1]); p[i] = lo16(t); p[i + 1] = hi16(t); }
  float mx[4], mn[4];
#pragma unroll
  for (int i = 0; i < 4; ++i) {
    mx[i] = fmaxf(fmaxf(p[4 * i], p[4 * i + 1]), fmaxf(p[4 * i + 2], p[4 * i + 3]));
    mn[i] = fminf(fminf(p[4 * i], p[4 * i + 1]), fminf(p[4 * i + 2], p[4 * i + 3]));
  }
  constexpr float L2E = 1.4426950408889634f;
  const float mxl = fmaxf(fmaxf(mx[0], mx[1]), fmaxf(mx[2], mx[3])) * L2E, mnl = fminf(fminf(mn[0], mn[1]), fminf(mn[2], mn[3])) * L2E;
  float e1[16], e2[16];
#pragma unroll
  for (int i = 0; i < 16; ++i) { e1[i] = __builtin_amdgcn_exp2f(__builtin_fmaf(p[i], L2E, -mxl)); e2[i] = __builtin_amdgcn_exp2f(__builtin_fmaf(p[i], -L2E, mnl)); }
  float s1[4], s2[4];
#pragma unroll
  for (int i = 0; i < 4; ++i) {
    s1[i] = (e1[4 * i] + e1[4 * i + 1]) + (e1[4 * i + 2] + e1[4 * i + 3]);
    s2[i] = (e2[4 * i] + e2[4 * i + 1]) + (e2[4 * i + 2] + e2[4 * i + 3]);
  }
  const float r1 = rcp((s1[0] + s1[1]) + (s1[2] + s1[3])), r2 = rcp((s2[0] + s2[1]) + (s2[2] + s2[3]));
#pragma unroll
  for (int i = 0; i < 8; ++i) { ph[i] = pk(e1[2 * i] * r1, e1[2 * i + 1] * r1); ph[8 + i] = pk(e2[2 * i] * r2, e2[2 * i + 1] * r2); }
}

#define TSCLK() __builtin_amdgcn_s_memrealtime()
#define TS(i) do {} while (0)
#define TS_DUMP() do {} while (0)
#define TS_INIT() do {} while (0)

__global__ void __launch_bounds__(512, 2) mega(Params p) {
  extern __shared__ __attribute__((aligned(16))) unsigned char smem[];
  LAS unsigned char* L = (LAS unsigned char*)smem;
  TS_INIT();
  TS(0);
  const int tid = threadIdx.x, lane = tid & 63, w = __builtin_amdgcn_readfirstlane(tid >> 6);
  const int b = blockIdx.x, xcd = b & 7, rank = b >> 3, panel = xcd * 2 + (rank >> 4), mem = rank & 15;
  const int hh = lane >> 5, l31 = lane & 31;
  unsigned char* ws = p.ws;
  const int rowh = mem >> 3, colg = mem & 7;
  const int pair0 = mem < 11 ? 3 * mem : 33 + 2 * (mem - 11);
  const bool three = mem < 11;

  if (w >= 6) {
    const int v = w & 1;
    const rsrc_t rqkv = __builtin_amdgcn_make_buffer_rsrc((void*)p.wqkv, 0, D * 3 * D * 2, 0x00020000);
    const rsrc_t rwo = __builtin_amdgcn_make_buffer_rsrc((void*)p.wo, 0, D * D * 2, 0x00020000);
    const rsrc_t rgu = __builtin_amdgcn_make_buffer_rsrc((void*)(v ? p.wu : p.wg), 0, D * F * 2, 0x00020000);
    const rsrc_t rwd = __builtin_amdgcn_make_buffer_rsrc((void*)p.wd, 0, F * D * 2, 0x00020000);
    const u32 ldsb = (u32)(size_t)L;
    const u32 vq = (lane >> 2) * (3 * D * 2) + (lane & 3) * 16;
    const u32 vd = (lane >> 2) * (D * 2) + (lane & 3) * 16;
    const u32 vg = (lane >> 2) * (F * 2) + (((lane & 3) ^ (v << 1)) * 16);
    const u32 l1 = ldsb + v * 1024, l2 = ldsb + v * 2048;
    const u32 skv = (D + v * D + mem * 32) * 2, sq = v * 16 * (3 * D * 2) + mem * 64;
    const u32 so = 16 * v * 16 * (D * 2) + colg * 128, sd = 43 * v * 16 * (D * 2) + colg * 128;
    const u32 sg = pair0 * 64, sg2 = sg + (three ? 128 : 64);
#define LD_KV(S0, S1) static_for<S0, S1>([&](auto S) { gldsi<slot(P_KV + 2 * S.value), S.value * 16 * (3 * D * 2)>(rqkv, vq, skv, l1); })
#define LD_Q() static_for<0, 16>([&](auto S) { gldsi<slot(P_Q + 2 * S.value), 2 * S.value * 16 * (3 * D * 2)>(rqkv, vq, sq, l1); })
#define LD_DN(RS, SB, P0, T0, T1)                                                                    \
  static_for<T0, T1>([&](auto T) {                                                                   \
    gldsi<slot((P0) + 4 * T.value), T.value * 16 * (D * 2)>(RS, vd, SB, l2);                         \
    gldsi<slot((P0) + 4 * T.value) + 1024, T.value * 16 * (D * 2) + 64>(RS, vd, SB, l2);             \
  })
#define LD_GU(S0, S1)                                             \
  static_for<S0, S1>([&](auto S) {                                                                   \
    gldsi<slot(P_GU + 6 * S.value), S.value * 16 * (F * 2)>(rgu, vg, sg, l1);                        \
    gldsi<slot(P_GU + 6 * S.value + 2), S.value * 16 * (F * 2) + 64>(rgu, vg, sg, l1);               \
    gldsi<slot(P_GU + 6 * S.value + 4), S.value * 16 * (F * 2)>(rgu, vg, sg2, l1);                   \
  })
    if (lane < 16) *((LAS u32*)(L + L_HINT) + lane) = 0u;
    bar();
    LD_KV(0, 32);
    TS(1);
    await(L, 1, 4);
    LD_Q();
    VMCNT(16);
    TS(2);
    bar_raw();
    await(L, 2, 4);
    LD_DN(rwo, so, P_WO, 0, 10);
    VMCNT(20);
    TS(3);
    bar_raw();
    await(L, 3, 4);
    LD_DN(rwo, so, P_WO, 10, 16);
    LD_GU(0, 6);
    VMCNT(18);
    TS(4);
    bar_raw();
    await(L, 4, 4);
    LD_GU(6, 12);
    VMCNT(0);
    TS(5);
    bar_raw();
    await(L, 5, 4);
    LD_GU(12, 22);
    VMCNT(0);
    TS(6);
    bar_raw();
    TS(11);
    LD_GU(22, 32);
    LD_DN(rwd, sd, P_WD, 0, 1);
    TS(12);
    VMCNT(2);
    TS(7);
    bar_raw();
    LD_DN(rwd, sd, P_WD, 1, 17);
    VMCNT(0);
    TS(8);
    bar_raw();
    await(L, 6, 4);
    LD_DN(rwd, sd, P_WD, 17, 34);
    VMCNT(0);
    TS(9);
    bar_raw();
    LD_DN(rwd, sd, P_WD, 34, 43);
    VMCNT(0);
    TS(10);
    bar_raw();
    bar_raw();
    TS_DUMP();
    return;
  }

  const rsrc_t rs = __builtin_amdgcn_make_buffer_rsrc(ws, 0, WS_BYTES, 0x00020000);
  const u32 epoch = __builtin_amdgcn_raw_buffer_load_b32(rs, 0, OFF_EPOCH, AUX_SC1);

  if (w >= 4) {
    const int a = w & 1;
    u32 tag;
    {
      const bool on = rank >= 16 || a == 0;
      const int col0 = rank < 16 ? (xcd * 16 + rank) * 8 : 1024 + (xcd * 16 + rank - 16) * 16 + a * 8;
      v4u wm[8]; u32 cv[8];
      if (on) {
#pragma unroll
        for (int i = 0; i < 8; ++i) cv[i] = p.c[64 * i + lane];
#pragma unroll
        for (int i = 0; i < 8; ++i) wm[i] = *(const GAS v4u*)(p.wmod + (size_t)(64 * i + lane) * (6 * D) + col0);
      }
      SB0();
      bar_raw();
      TS(10);
      tag = epoch + 1u;
      if (on) {
        float sl[8], acc[8];
#pragma unroll
        for (int i = 0; i < 8; ++i) { const float c = lo16(cv[i]); sl[i] = rb(c * rcp(1.f + __expf(-c))); acc[i] = 0.f; }
        TS(11);
#pragma unroll
        for (int i = 0; i < 8; ++i)
#pragma unroll
          for (int j = 0; j < 4; ++j) { acc[2 * j] += sl[i] * lo16(wm[i][j]); acc[2 * j + 1] += sl[i] * hi16(wm[i][j]); }
        float c0, c1;
        TS(12);
        red8(acc, c0, c1);
        if ((lane & 15) == 0) __hip_atomic_store((GAS u64*)(ws + OFF_MODG + (col0 / 2 + (lane >> 4)) * 8), ((u64)tag << 32) | pk(c0, c1), RLX_AGENT);
      }
      if (a == 0) sig(L, 0, lane);
    }
    TS(1);
    bar();
    await(L, 2, 4);
    {
      const int loc = rank * 2 + a, task = xcd * 43 + loc, kp = task / 86, col0 = (task - kp * 86) * 32;
      const bool on = loc < 43;
      const u16* W = (col0 < F ? p.wg + col0 : p.wu + (col0 - F)) + (size_t)(128 * kp + 2 * (lane >> 2)) * F + (lane & 3) * 8;
      v4u wm[8];
      if (on) {
#pragma unroll
        for (int i = 0; i < 8; ++i) wm[i] = *(const GAS v4u*)(W + (size_t)(32 * (i >> 1) + (i & 1)) * F);
      }
      SB0();
      bar_raw();
      const int vec = lane >> 4, vo = (vec == 0 ? 2 : vec == 1 ? 4 : 5) * D * 4 + (colg * 64 + (lane & 15) * 4) * 4;
      v2u g[8];
      v4u gv;
      for (u32 spins = 0; spins < SPIN_CAP; ++spins) {
        RELOAD();
        gv = ld128(rs, vo, OFF_MODG);
#pragma unroll
        for (int i = 0; i < 8; ++i) g[i] = ld64(rs, ((kp & 3) * 64 + (lane >> 2)) * 8 + (i & 3) * 128, OFF_MODG + (3 + (i >> 2)) * D * 4);
        bool ok = (gv.y == tag) & (gv.w == tag);
#pragma unroll
        for (int i = 0; i < 8; ++i) ok &= g[i].y == tag;
        if (__all(ok)) break;
      }
      f32x4 v = {lo16(gv.x), hi16(gv.x), lo16(gv.z), hi16(gv.z)};
      if (vec == 1) v = f32x4{rb(1.f + v[0]), rb(1.f + v[1]), rb(1.f + v[2]), rb(1.f + v[3])};
      if (a == 0 && lane < 48) *(LAS f32x4*)(L + L_VEC + lane * 16) = v;
      if (on) {
        float a1[8], a2[8];
#pragma unroll
        for (int j = 0; j < 8; ++j) { a1[j] = 0.f; a2[j] = 0.f; }
#pragma unroll
        for (int i = 0; i < 8; ++i) {
          const u32 sbw = g[i >> 1].x, cbw = g[4 + (i >> 1)].x;
          const float sb = (i & 1) ? hi16(sbw) : lo16(sbw), sc = rb(1.f + ((i & 1) ? hi16(cbw) : lo16(cbw)));
#pragma unroll
          for (int j = 0; j < 4; ++j) {
            a1[2 * j] += sc * lo16(wm[i][j]); a1[2 * j + 1] += sc * hi16(wm[i][j]);
            a2[2 * j] += sb * lo16(wm[i][j]); a2[2 * j + 1] += sb * hi16(wm[i][j]);
          }
        }
        float c10, c11, c20, c21;
        red8q(a1, c10, c11); red8q(a2, c20, c21);
        const int k = (lane >> 2) & 3;
        const float val = k == 0 ? c10 : k == 1 ? c20 : k == 2 ? c11 : c21;
        __hip_atomic_store((GAS u64*)(ws + OFF_CG + kp * CG_PART + (col0 + 8 * (lane & 3) + 2 * (lane >> 4)) * 16 + k * 8), ((u64)tag << 32) | __float_as_uint(val), RLX_AGENT);
      }
    }
    TS(2);
    {
      const int idx0 = (mem * 4 + 2 * a) * 16;
      waitn<32, 0>(ws, FL_KV, idx0, tag, lane);
      TS(3);
      v4u e[32], zz[2];
#pragma unroll
      for (int i = 0; i < 32; ++i) e[i] = ld128(rs, lane * 16 + (i & 3) * 1024, OFF_KVE + idx0 * 1024 + (i >> 2) * 4096);
      zz[0] = ld128(rs, lane * 16, OFF_KVZ + idx0 * 64);
      zz[1] = ld128(rs, lane * 16 + 1024, OFF_KVZ + idx0 * 64);
      SB0();
#pragma unroll
      for (int q = 0; q < 2; ++q) {
        f32x4 s0 = __builtin_bit_cast(f32x4, e[16 * q]), s1 = __builtin_bit_cast(f32x4, e[16 * q + 1]);
#pragma unroll
        for (int i = 2; i < 16; i += 2) { s0 += __builtin_bit_cast(f32x4, e[16 * q + i]); s1 += __builtin_bit_cast(f32x4, e[16 * q + i + 1]); }
        s0 += s1;
        *(LAS v2u*)(L + L_KVF + (2 * a + q) * 512 + l31 * 16 + hh * 8) = v2u{pk(s0[0], s0[1]), pk(s0[2], s0[3])};
      }
      f32x4 z;
#pragma unroll
      for (int j = 0; j < 4; ++j) {
        float t = fold32(__uint_as_float(zz[0][j]), __uint_as_float(zz[1][j]));
        t = fold16(t, t);
        t = dpp_add<DPP_ROR4>(t);
        z[j] = dpp_add<DPP_ROR8>(t);
      }
      if (l31 < 4) *(LAS v2u*)(L + L_ZF + (2 * a + hh) * 32 + ((lane & 1) ^ 1) * 16 + ((lane >> 1) & 1) * 8) = v2u{pk(z[0], z[1]), pk(z[2], z[3])};
    }
    TS(4);
    bar();
    await(L, 4, 4);
    {
      const int ntile = three ? 6 : 4;
      v4u g[8];
      int off[2];
#pragma unroll
      for (int i = 0; i < 2; ++i) {
        int tile = 4 * i + 2 * a + hh; tile = tile < ntile ? tile : 0;
        off[i] = (32 * pair0 + 16 * tile + (lane & 15) + ((lane >> 4) & 1) * F) * 16;
      }
      for (u32 spins = 0; spins < SPIN_CAP; ++spins) {
        RELOAD();
        bool ok = true;
#pragma unroll
        for (int i = 0; i < 8; ++i) g[i] = ld128(rs, off[i >> 2], OFF_CG + (i & 3) * CG_PART);
#pragma unroll
        for (int i = 0; i < 8; ++i) ok &= (g[i].y == tag) & (g[i].w == tag);
        if (__all(ok)) break;
      }
#pragma unroll
      for (int i = 0; i < 2; ++i) {
        const float c1 = (__uint_as_float(g[4 * i].x) + __uint_as_float(g[4 * i + 1].x)) + (__uint_as_float(g[4 * i + 2].x) + __uint_as_float(g[4 * i + 3].x));
        const float c2 = (__uint_as_float(g[4 * i].z) + __uint_as_float(g[4 * i + 1].z)) + (__uint_as_float(g[4 * i + 2].z) + __uint_as_float(g[4 * i + 3].z));
        const float h1 = rb(c1), m1 = rb(c1 - h1), l1 = c1 - h1 - m1, h2 = rb(c2), m2 = rb(c2 - h2), l2 = c2 - h2 - m2;
        const int tile = 4 * i + 2 * a + hh;
        if (tile < 6) *(LAS v4u*)(L + L_CF + tile * 512 + l31 * 16) = v4u{pk(h1, m1), pk(l1, h2), pk(m2, l2), 0u};
      }
    }
    TS(5);
    bar();
    TS(6);
    bar();
    bar_raw();
    bar_raw();
    bar_raw();
    bar_raw();
    bar_raw();
    TS_DUMP();
    return;
  }

  __builtin_amdgcn_s_setprio(2);
  const int s2 = w & 1, kh = w >> 1;
  v4u aphi0, aphi1;
  u32 tag;
  {
    v4u xv[8];
    const u16* xr = p.x + (size_t)(panel * 128 + mem * 8 + (lane >> 3)) * D + (lane & 7) * 8;
#pragma unroll
    for (int i = 0; i < 8; ++i) xv[i] = *(const GAS v4u*)(xr + 64 * ((i + 2 * w) & 7));
    const int hs = (l31 >> 2) & 1, f = (l31 & 3) + 4 * (l31 >> 3);
    const u16* wp = p.wphi + (2 * mem + hs) * 256 + 4 * hh * 16 + f;
    u32 e[8];
#pragma unroll
    for (int j = 0; j < 8; ++j) e[j] = wp[(8 * (j >> 2) + (j & 3)) * 16];
    SB0();
    bar_raw();
    tag = epoch + 1u;
    float s[4] = {0.f, 0.f, 0.f, 0.f};
#pragma unroll
    for (int i = 0; i < 8; ++i)
#pragma unroll
      for (int j = 0; j < 4; ++j) s[j] += lo16(xv[i][j]) + hi16(xv[i][j]);
    float sm = (s[0] + s[1]) + (s[2] + s[3]);
    sm = dpp_add<DPP_X1>(sm); sm = dpp_add<DPP_X2>(sm); sm = dpp_add<DPP_HM>(sm);
    const float mean = sm * (1.f / D);
    float q[4] = {0.f, 0.f, 0.f, 0.f};
#pragma unroll
    for (int i = 0; i < 8; ++i)
#pragma unroll
      for (int j = 0; j < 4; ++j) { const float u = lo16(xv[i][j]) - mean, v = hi16(xv[i][j]) - mean; q[j] += u * u + v * v; }
    float qq = (q[0] + q[1]) + (q[2] + q[3]);
    qq = dpp_add<DPP_X1>(qq); qq = dpp_add<DPP_X2>(qq); qq = dpp_add<DPP_HM>(qq);
    const float m1 = rb(mean), r1 = rcp(rb(__builtin_amdgcn_sqrtf(rb(rb(qq * (1.f / D)) + 1e-5f))));
    float xn[16];
#pragma unroll
    for (int i = 0; i < 2; ++i)
#pragma unroll
      for (int j = 0; j < 4; ++j) {
        const u32 d = pk(lo16(xv[i][j]) - m1, hi16(xv[i][j]) - m1);
        const u32 t = pk(lo16(d) * r1, hi16(d) * r1);
        xn[8 * i + 2 * j] = lo16(t); xn[8 * i + 2 * j + 1] = hi16(t);
      }
    const v4u av = {e[0] | (e[1] << 16), e[2] | (e[3] << 16), e[4] | (e[5] << 16), e[6] | (e[7] << 16)};
    const v4u z = {0, 0, 0, 0};
    aphi0 = hs == 0 ? av : z; aphi1 = hs == 1 ? av : z;
    TS(1);
    await(L, 0, 1);
    __builtin_amdgcn_s_sleep(PRE_MOD);
    TS(21);
    v4u g;
    for (u32 spins = 0; spins < SPIN_CAP; ++spins) {
      RELOAD();
      g = ld128(rs, l31 * 16 + hh * 2048, OFF_MODG + w * 512);
      if (__all((g.y == tag) & (g.w == tag))) break;
      __builtin_amdgcn_s_sleep(2);
    }
    *(LAS v2u*)(L + L_T + w * 4096 + lane * 8) = v2u{g.x, g.z};
    v4u sav[2], cav[2];
#pragma unroll
    for (int i = 0; i < 2; ++i) {
      sav[i] = *(LAS v4u*)(L + L_T + w * 4096 + i * 128 + (lane & 7) * 16);
      cav[i] = *(LAS v4u*)(L + L_T + w * 4096 + 256 + i * 128 + (lane & 7) * 16);
    }
    TS(2);
    const int strip = panel * 4 + (mem >> 2), r = 8 * (mem & 3) + (lane >> 3);
#pragma unroll
    for (int i = 0; i < 2; ++i) {
      v4u o;
#pragma unroll
      for (int j = 0; j < 4; ++j) {
        const u32 sa = sav[i][j], ca = cav[i][j];
        const u32 sc = pk(1.f + lo16(ca), 1.f + hi16(ca));
        const u32 t = pk(xn[8 * i + 2 * j] * lo16(sc), xn[8 * i + 2 * j + 1] * hi16(sc));
        o[j] = pk(lo16(t) + lo16(sa), hi16(t) + hi16(sa));
      }
      st128(o, rs, (4 * i + ((lane & 7) >> 1)) * 1024 + (r + 32 * (lane & 1)) * 16, OFF_H + strip * 32768 + w * 8192);
    }
    publish(ws, FL_H, strip * 16 + (mem & 3) * 4 + w, tag, lane);
    sig(L, 1, lane);
  }
  TS(3);

  const int strip1 = panel * 4 + w;
  const int trq = (lane & 15) >> 2, trp = lane & 3, trb = (lane >> 4) & 1;
  const f32x16 zero16 = {0.f, 0.f, 0.f, 0.f, 0.f, 0.f, 0.f, 0.f, 0.f, 0.f, 0.f, 0.f, 0.f, 0.f, 0.f, 0.f};
  u32 phq[16];
  v4u wb[PD];
  {
    const Base3 wnat(L + (8 * hh + trq) * 64 + trb * 32 + trp * 8);
    waitn<16, PRE_FL>(ws, FL_H, strip1 * 16, tag, lane);
    TS(4);
    v4u a[32];
#pragma unroll
    for (int s = 0; s < 32; ++s) a[s] = ld128(rs, lane * 16 + (s & 3) * 1024, OFF_H + strip1 * 32768 + (s >> 2) * 4096), SB0();
    SB0();
    bar_raw();
    static_for<0, PD>([&](auto U) { wb[U.value] = ldw<256>(wnat.at<slot(P_KV + U.value)>()); });
    SB0();
    f32x16 kacc = zero16, vacc = zero16;
    static_for<0, 64>([&](auto U) {
      constexpr int u = U.value;
      const v4u cur = wb[u % PD];
      if constexpr (u + PD < 64) wb[u % PD] = ldw<256>(wnat.at<slot(P_KV + u + PD)>());
      if constexpr (u & 1) mfma_pin(a[u >> 1], cur, vacc); else mfma_pin(cur, a[u >> 1], kacc);
      if constexpr (u == 56) sig(L, 2, lane);
      SB0();
    });
    TS(5);
    f32x16 P = mfma(aphi0, pack8<0>(kacc), zero16);
    P = mfma(aphi1, pack8<8>(kacc), P);
    const v4u vb0 = pack8<0>(vacc), vb1 = pack8<8>(vacc);
    u32 phk[16];
    feat(P, phk);
    LAS unsigned char* T = L + L_T + w * 4096;
#pragma unroll
    for (int c = 0; c < 4; ++c)
      *(LAS v4u*)(T + lane * 64 + ((c ^ ((lane >> 1) & 3)) * 16)) = v4u{phk[4 * c], phk[4 * c + 1], phk[4 * c + 2], phk[4 * c + 3]};
    const v4u ones = {0x3F803F80u, 0x3F803F80u, 0x3F803F80u, 0x3F803F80u};
    LAS unsigned char* tr = T + (4 * hh + trq) * 64 + (((2 * trb + (trp >> 1)) ^ ((2 * hh + (trq >> 1)) & 3)) * 16) + 8 * (trp & 1);
    const v4u t0 = ldw<512>(tr), t1 = ldw<512>(tr + 1024), t2 = ldw<512>(tr + 2048), t3 = ldw<512>(tr + 3072);
    f32x16 D0 = mfma(t0, trb ? ones : vb0, zero16);
    f32x16 D1 = mfma(t2, trb ? vb0 : ones, zero16);
    D0 = mfma(t1, trb ? ones : vb1, D0);
    D1 = mfma(t3, trb ? vb1 : ones, D1);
#pragma unroll
    for (int g = 0; g < 4; ++g) {
      f32x4 e, z;
#pragma unroll
      for (int j = 0; j < 4; ++j) { e[j] = trb ? D1[4 * g + j] : D0[4 * g + j]; z[j] = trb ? D0[4 * g + j] : D1[4 * g + j]; }
      *(LAS f32x4*)(T + g * 1024 + lane * 16) = e;
      if ((lane & 15) == 0) *(LAS f32x4*)(L + L_Z + w * 256 + g * 64 + (lane >> 4) * 16) = z;
    }
    TS(6);
    bar();
    {
      f32x4 e = *(LAS f32x4*)(L + L_T + w * 1024 + lane * 16), z = *(LAS f32x4*)(L + L_Z + w * 64 + (lane & 3) * 16);
#pragma unroll
      for (int v = 1; v < 4; ++v) {
        e += *(LAS f32x4*)(L + L_T + v * 4096 + w * 1024 + lane * 16);
        z += *(LAS f32x4*)(L + L_Z + v * 256 + w * 64 + (lane & 3) * 16);
      }
      const int idx = (mem * 4 + w) * 16 + panel;
      st128(__builtin_bit_cast(v4u, e), rs, lane * 16, OFF_KVE + idx * 1024);
      if (lane < 4) st128(__builtin_bit_cast(v4u, z), rs, lane * 16, OFF_KVZ + idx * 64);
      static_for<0, PD>([&](auto U) { wb[U.value] = ldw<256>(wnat.at<slot(P_Q + U.value)>()); });
      publish(ws, FL_KV, idx, tag, lane);
      sig(L, 3, lane);
    }
    TS(7);
    f32x16 qacc = zero16;
    static_for<0, 32>([&](auto U) {
      constexpr int u = U.value;
      const v4u cur = wb[u % PD];
      if constexpr (u + PD < 32) wb[u % PD] = ldw<256>(wnat.at<slot(P_Q + u + PD)>());
      mfma_pin(cur, a[u], qacc);
      SB0();
    });
    P = mfma(aphi0, pack8<0>(qacc), zero16);
    P = mfma(aphi1, pack8<8>(qacc), P);
    feat(P, phq);
  }
  const int strip2 = panel * 4 + rowh * 2 + s2, ncol = colg * 64 + kh * 32;
  v2u xres[4];
  {
    const u16* xr = p.x + (size_t)(strip2 * 32 + l31) * D + ncol + 4 * hh;
#pragma unroll
    for (int i = 0; i < 4; ++i) xres[i] = *(const GAS v2u*)(xr + 8 * i);
  }
  TS(8);
  bar_raw();
  TS(9);
  {
    const bool on = (l31 >> 4) == hh;
    const v4u zv = {0, 0, 0, 0};
    f32x16 num = zero16, den = zero16;
    v4u ak[4], az[4];
#pragma unroll
    for (int s4 = 0; s4 < 4; ++s4) { ak[s4] = *(LAS v4u*)(L + L_KVF + s4 * 512 + l31 * 16); az[s4] = *(LAS v4u*)(L + L_ZF + s4 * 32 + hh * 16); }
#pragma unroll
    for (int s4 = 0; s4 < 4; ++s4) den = mfma(on ? az[s4] : zv, v4u{phq[4 * s4], phq[4 * s4 + 1], phq[4 * s4 + 2], phq[4 * s4 + 3]}, den);
#pragma unroll
    for (int s4 = 0; s4 < 4; ++s4) num = mfma(on ? ak[s4] : zv, v4u{phq[4 * s4], phq[4 * s4 + 1], phq[4 * s4 + 2], phq[4 * s4 + 3]}, num);
    const float d0 = rcp(rb(rb(den[0]) + 1e-6f)), d1 = rcp(rb(rb(den[8]) + 1e-6f));
    const v4u n0 = pack8<0>(num), n1 = pack8<8>(num);
    v4u o0, o1;
#pragma unroll
    for (int j = 0; j < 4; ++j) { o0[j] = pk(lo16(n0[j]) * d0, hi16(n0[j]) * d0); o1[j] = pk(lo16(n1[j]) * d1, hi16(n1[j]) * d1); }
    st128(o0, rs, lane * 16, OFF_O + strip1 * 32768 + (2 * mem) * 1024);
    st128(o1, rs, lane * 16 + 1024, OFF_O + strip1 * 32768 + (2 * mem) * 1024);
  }
#define WDN_BASES() const Base3 wown(L + (4 * hh + trq) * 64 + trb * 32 + trp * 8 + 3 * kh * 1024), woth(L + (4 * hh + trq) * 64 + trb * 32 + trp * 8 + (kh + 1) * 1024)
#define WDN(base, u) ldw<512>((((u) & 1) ? wown : woth).template at<slot((base) + 4 * ((u) >> 1))>())
  u32 x1p[8];
  {
    WDN_BASES();
    static_for<0, PD>([&](auto U) { wb[U.value] = WDN(P_WO, U.value); });
    publish(ws, FL_O, strip1 * 16 + mem, tag, lane);
    sig(L, 4, lane);
    TS(10);
    waitn<8, PRE_FL>(ws, FL_O, strip2 * 16 + kh * 8, tag, lane);
    TS(11);
    v4u a[16];
#pragma unroll
    for (int t = 0; t < 16; ++t) a[t] = ld128(rs, lane * 16 + (t & 3) * 1024, OFF_O + strip2 * 32768 + kh * 16384 + (t >> 2) * 4096), SB0();
    SB0();
    f32x16 c0 = zero16, c1 = zero16;
    static_for<0, 32>([&](auto U) {
      constexpr int u = U.value;
      const v4u cur = wb[u % PD];
      if constexpr (u + PD < 32) wb[u % PD] = WDN(P_WO, u + PD);
      if constexpr (u & 1) mfma_pin(cur, a[u >> 1], c0); else mfma_pin(cur, a[u >> 1], c1);
      SB0();
    });
#pragma unroll
    for (int g = 0; g < 4; ++g) *(LAS f32x4*)(L + L_T + w * 4096 + g * 1024 + lane * 16) = f32x4{c1[4 * g], c1[4 * g + 1], c1[4 * g + 2], c1[4 * g + 3]};
    f32x4 ga[4], sc[4];
#pragma unroll
    for (int g = 0; g < 4; ++g) {
      ga[g] = *(LAS f32x4*)(L + L_VEC + (kh * 32 + 8 * g + 4 * hh) * 4);
      sc[g] = *(LAS f32x4*)(L + L_VEC + 256 + (kh * 32 + 8 * g + 4 * hh) * 4);
    }
    TS(12);
    bar();
    float x1[16], sp[4];
    u32 yp[8];
#pragma unroll
    for (int g = 0; g < 4; ++g) {
      const f32x4 o = *(LAS f32x4*)(L + L_T + (w ^ 2) * 4096 + g * 1024 + lane * 16);
#pragma unroll
      for (int j = 0; j < 2; ++j) {
        const u32 y = pk(c0[4 * g + 2 * j] + o[2 * j], c0[4 * g + 2 * j + 1] + o[2 * j + 1]);
        const u32 t = pk(ga[g][2 * j] * lo16(y), ga[g][2 * j + 1] * hi16(y));
        const u32 r = pk(lo16(xres[g][j]) + lo16(t), hi16(xres[g][j]) + hi16(t));
        x1p[2 * g + j] = r;
        x1[4 * g + 2 * j] = lo16(r); x1[4 * g + 2 * j + 1] = hi16(r);
        yp[2 * g + j] = pk(lo16(r) * sc[g][2 * j], hi16(r) * sc[g][2 * j + 1]);
      }
      sp[g] = (x1[4 * g] + x1[4 * g + 1]) + (x1[4 * g + 2] + x1[4 * g + 3]);
    }
    const int tile = 2 * colg + kh;
    st128(v4u{yp[0], yp[1], yp[2], yp[3]}, rs, lane * 16, OFF_X1 + strip2 * 32768 + tile * 2048);
    st128(v4u{yp[4], yp[5], yp[6], yp[7]}, rs, lane * 16 + 1024, OFF_X1 + strip2 * 32768 + tile * 2048);
    const float ss = (sp[0] + sp[1]) + (sp[2] + sp[3]);
    const float mt = fold32(ss, ss) * (1.f / 32.f);
    float q[4];
#pragma unroll
    for (int g = 0; g < 4; ++g) {
      const float d0 = x1[4 * g] - mt, d1 = x1[4 * g + 1] - mt, d2 = x1[4 * g + 2] - mt, d3 = x1[4 * g + 3] - mt;
      q[g] = (d0 * d0 + d1 * d1) + (d2 * d2 + d3 * d3);
    }
    const float qs = (q[0] + q[1]) + (q[2] + q[3]);
    const float qt = fold32(qs, qs);
    if (lane < 32) st64(v2u{__float_as_uint(mt), __float_as_uint(qt)}, rs, lane * 8, OFF_ST + strip2 * 4096 + tile * 256);
  }
  asm volatile("" : "+v"(x1p[0]), "+v"(x1p[1]), "+v"(x1p[2]), "+v"(x1p[3]), "+v"(x1p[4]), "+v"(x1p[5]), "+v"(x1p[6]), "+v"(x1p[7]));
#define GU_S(u) ((u) < 144 ? (u) / 6 : 24 + ((u) - 144) % 8)
#define GU_I(u) ((u) < 144 ? (u) % 6 : ((u) - 144) / 8)
#define WGU(u) ldw<512>(((GU_I(u) & 1) ? wy : wx).template at<slot(P_GU + 6 * GU_S(u) + (GU_I(u) & 6))>())
  {
    const Base3 wx(L + (4 * hh + trq) * 64 + trp * 8 + trb * (1024 + 32)), wy(L + (4 * hh + trq) * 64 + trp * 8 + (trb ? 1024 : 32));
    static_for<0, PD>([&](auto U) { wb[U.value] = WGU(U.value); });
    publish(ws, FL_X, strip2 * 16 + 2 * colg + kh, tag, lane);
    sig(L, 5, lane);
    TS(13);
    waitn<16, PRE_FL>(ws, FL_X, strip1 * 16, tag, lane);
    TS(14);
    constexpr int AW = 16;
    v4u a[AW];
#define LDX(s) ld128(rs, lane * 16 + ((s) & 3) * 1024, OFF_X1 + strip1 * 32768 + ((s) >> 2) * 4096)
#pragma unroll
    for (int s = 0; s < AW; ++s) a[s] = LDX(s), SB0();
    v2u st[8];
    f32x16 acc[6];
#pragma unroll
    for (int i = 0; i < 6; ++i) acc[i] = zero16;
    float rr = 0.f;
    v4u bcor = {0, 0, 0, 0};
    v4u ho, cf;
    float tg0 = 0.f, tg1 = 0.f, tu0 = 0.f, tu1 = 0.f;
    u32 pg = 0, pu = 0;
    auto epi = [&](auto Ic, auto Kc) {
      constexpr int I = decltype(Ic)::value, k = decltype(Kc)::value, j = k >> 1;
      const f32x16& A = acc[I];
      if constexpr ((k & 1) == 0) {
        tg0 = A[2 * j] * rr; tg1 = A[2 * j + 1] * rr; tu0 = A[8 + 2 * j] * rr; tu1 = A[9 + 2 * j] * rr;
        pg = pk(tg0, tg1); pu = pk(tu0, tu1);
      } else {
        ho[j] = pk(lo16(pg) * lo16(pu), hi16(pg) * hi16(pu));
        if constexpr (j == 3) { if (I < 4 || three) st128(ho, rs, lane * 16, OFF_G + (strip1 * 86 + 2 * pair0 + I) * 1024); }
      }
    };
    static_for<0, 192>([&](auto Uc) {
      constexpr int u = decltype(Uc)::value, s = GU_S(u), i = GU_I(u);
      if constexpr (u == 6) TS(22);
      if constexpr (u == 60) { TS(23); bar_raw(); TS(24); }
      if constexpr (u == 132 - PD - 1) { TS(25); bar_raw(); TS(26); }
      const v4u cur = wb[u % PD];
      if constexpr (u + PD < 192) wb[u % PD] = WGU(u + PD);
      if constexpr (u >= 144 && s == 27) cf = *(LAS v4u*)(L + L_CF + i * 512 + l31 * 16);
      mfma_pin(cur, a[s % AW], acc[i]);
      if constexpr (u < 144 && i == 5 && s + AW < 32) a[s % AW] = LDX(s + AW);
      if constexpr (u >= 100 && u < 108) st[u - 100] = ld64(rs, l31 * 8 + hh * 2048 + (u - 100) * 256, OFF_ST + strip1 * 4096);
      if constexpr (u == 132) {
        float ms = 0.f;
#pragma unroll
        for (int t = 0; t < 8; ++t) ms += __uint_as_float(st[t].x);
        tg0 = fold32(ms, ms) * (1.f / 16.f);
      }
      if constexpr (u == 134 || u == 136) {
        float q = 0.f;
#pragma unroll
        for (int t = 2 * (u - 134); t < 2 * (u - 134) + 4; ++t) { const float d = __uint_as_float(st[t].x) - tg0; q += __uint_as_float(st[t].y) + 32.f * d * d; }
        if constexpr (u == 134) tg1 = q; else tg1 += q;
      }
      if constexpr (u == 138) tg1 = rb(__builtin_amdgcn_sqrtf(rb(rb(fold32(tg1, tg1) * (1.f / D)) + 1e-5f)));
      if constexpr (u == 140) {
        rr = rcp(tg1);
        if (hh == 0) bcor = v4u{pk(-tg0, -tg0), pk(-tg0, tg1), pk(tg1, tg1), 0u};
      }
      if constexpr (u >= 144 && s == 31) mfma_pin(cf, bcor, acc[i]);
      if constexpr (u >= 153) epi(IC<(u - 153) / 8>{}, IC<(u - 153) % 8>{});
      SB0();
      if constexpr (u == 143) TS(15);
    });
    bar_raw();
    epi(IC<4>{}, IC<7>{});
    static_for<0, 8>([&](auto Kc) { epi(IC<5>{}, Kc); });
    TS(16);
  }
  {
    WDN_BASES();
    static_for<0, PD>([&](auto U) { wb[U.value] = WDN(P_WD, U.value); });
    publish(ws, FL_G, strip1 * 16 + mem, tag, lane);
    sig(L, 6, lane);
    TS(17);
    waitn<16, PRE_FL>(ws, FL_G, strip2 * 16, tag, lane);
    TS(18);
    v4u a[43];
#pragma unroll
    for (int t = 0; t < 43; ++t) a[t] = ld128(rs, lane * 16 + (t & 3) * 1024, OFF_G + (strip2 * 86 + kh * 43) * 1024 + (t >> 2) * 4096), SB0();
    SB0();
    f32x16 c0 = zero16, c1 = zero16;
    static_for<0, 86>([&](auto U) {
      constexpr int u = U.value;
      if constexpr (u == 24) bar_raw();
      if constexpr (u == 68 - PD - 1) bar_raw();
      const v4u cur = wb[u % PD];
      if constexpr (u + PD < 86) wb[u % PD] = WDN(P_WD, u + PD);
      if constexpr (u & 1) mfma_pin(cur, a[u >> 1], c0); else mfma_pin(cur, a[u >> 1], c1);
      SB0();
    });
#pragma unroll
    for (int g = 0; g < 4; ++g) *(LAS f32x4*)(L + L_T + w * 4096 + g * 1024 + lane * 16) = f32x4{c1[4 * g], c1[4 * g + 1], c1[4 * g + 2], c1[4 * g + 3]};
    f32x4 gb[4];
#pragma unroll
    for (int g = 0; g < 4; ++g) gb[g] = *(LAS f32x4*)(L + L_VEC + 512 + (kh * 32 + 8 * g + 4 * hh) * 4);
    TS(19);
    bar();
    u16* orow = p.out + (size_t)(strip2 * 32 + l31) * D + ncol + 4 * hh;
#pragma unroll
    for (int g = 0; g < 4; ++g) {
      const f32x4 o = *(LAS f32x4*)(L + L_T + (w ^ 2) * 4096 + g * 1024 + lane * 16);
      v2u r;
#pragma unroll
      for (int j = 0; j < 2; ++j) {
        const u32 y = pk(c0[4 * g + 2 * j] + o[2 * j], c0[4 * g + 2 * j + 1] + o[2 * j + 1]);
        const u32 t = pk(gb[g][2 * j] * lo16(y), gb[g][2 * j + 1] * hi16(y));
        r[j] = pk(lo16(x1p[2 * g + j]) + lo16(t), hi16(x1p[2 * g + j]) + hi16(t));
      }
      *(GAS v2u*)(orow + 8 * g) = r;
    }
  }
  if (b == 0 && tid == 0) __hip_atomic_store((GAS u32*)(ws + OFF_EPOCH), tag, RLX_AGENT);
}
}

extern "C" void kernel_launch(void* const* d_in, const int* in_sizes, int n_in, void* d_out, int out_size, void* d_ws,
                              size_t ws_size, hipStream_t stream) {
  using namespace mk;
  Params p;
  p.x = (const u16*)d_in[0]; p.wqkv = (const u16*)d_in[1]; p.wphi = (const u16*)d_in[2]; p.wo = (const u16*)d_in[3];
  p.wg = (const u16*)d_in[5]; p.wu = (const u16*)d_in[6]; p.wd = (const u16*)d_in[7]; p.c = (const u16*)d_in[8];
  p.wmod = (const u16*)d_in[9]; p.out = (u16*)d_out; p.ws = (unsigned char*)d_ws;
  (void)hipFuncSetAttribute((const void*)mega, hipFuncAttributeMaxDynamicSharedMemorySize, LDS_BYTES);
  mega<<<256, 512, LDS_BYTES, stream>>>(p);
}
```

```cpp
#define MK_LEAK 2
#define MK_LEAKB 0
#include <hip/hip_runtime.h>

namespace mk {
typedef unsigned short u16;
typedef unsigned int u32;
typedef unsigned long long u64;
typedef u32 v4u __attribute__((ext_vector_type(4)));
typedef u32 v2u __attribute__((ext_vector_type(2)));
typedef float f32x2 __attribute__((ext_vector_type(2)));
typedef float f32x4 __attribute__((ext_vector_type(4)));
typedef float f32x16 __attribute__((ext_vector_type(16)));
typedef __bf16 bf16x2 __attribute__((ext_vector_type(2)));
typedef __bf16 bf16x8 __attribute__((ext_vector_type(8)));
typedef short s16x4 __attribute__((ext_vector_type(4)));
#define LAS __attribute__((address_space(3)))
#define GAS __attribute__((address_space(1)))
#define DI __device__ __forceinline__
#define RLX_AGENT __ATOMIC_RELAXED, __HIP_MEMORY_SCOPE_AGENT
#define SB0() __builtin_amdgcn_sched_barrier(0)
typedef __amdgpu_buffer_rsrc_t rsrc_t;

constexpr int D = 512, F = 1376;
constexpr int OFF_EPOCH = 0;
constexpr int OFF_MODG = 4096;
constexpr int OFF_FLAG = 32768;
constexpr int FL_H = 0, FL_KV = 1, FL_O = 2, FL_X = 3, FL_G = 4;
constexpr int OFF_CG = 768 << 10;
constexpr int CG_PART = 2 * F * 16;
constexpr int OFF_H = 1 << 20;
constexpr int OFF_O = 3 << 20;
constexpr int OFF_X1 = 5 << 20;
constexpr int OFF_ST = 7 << 20;
constexpr int OFF_KVZ = (7 << 20) + (512 << 10);
constexpr int OFF_KVE = 8 << 20;
constexpr int OFF_G = 9 << 20;
constexpr int WS_BYTES = 15 << 20;
constexpr int OFF_TS = 32 << 20;
constexpr int RING = 136;
constexpr int L_T = RING * 1024;
constexpr int L_Z = L_T + 16384;
constexpr int L_KVF = L_Z + 1024;
constexpr int L_ZF = L_KVF + 2048;
constexpr int L_VEC = L_ZF + 256;
constexpr int L_CF = L_VEC + 768;
constexpr int L_HINT = L_CF + 3072;
constexpr int LDS_BYTES = L_HINT + 64;
static_assert(LDS_BYTES <= 163840, "LDS");
constexpr int P_KV = 0, P_Q = 64, P_WO = 96, P_GU = 160, P_WD = 352;
constexpr int slot(int n) { return (n % RING) * 1024; }
constexpr int AUX_SC1 = 16;
constexpr u32 SPIN_CAP = 1u << 16;
constexpr int PRE_MOD = 12, PRE_FL = 14;
constexpr int PD = 5;

struct Params {
  const u16 *x, *wqkv, *wphi, *wo, *wg, *wu, *wd, *c, *wmod;
  u16* out;
  unsigned char* ws;
};

template <int V> struct IC { static constexpr int value = V; };
template <int I, int N, class Fn> DI void static_for(Fn&& f) {
  if constexpr (I < N) { f(IC<I>{}); static_for<I + 1, N>(f); }
}
DI float lo16(u32 v) { return __uint_as_float(v << 16); }
DI float hi16(u32 v) { return __uint_as_float(v & 0xffff0000u); }
DI u32 pk(float a, float b) { f32x2 v = {a, b}; return __builtin_bit_cast(u32, __builtin_convertvector(v, bf16x2)); }
DI float rb(float a) { return lo16(pk(a, 0.f)); }
DI float rcp(float a) { return __builtin_amdgcn_rcpf(a); }
DI f32x16 mfma(v4u a, v4u b, f32x16 c) {
  return __builtin_amdgcn_mfma_f32_32x32x16_bf16(__builtin_bit_cast(bf16x8, a), __builtin_bit_cast(bf16x8, b), c, 0, 0, 0);
}
DI void mfma_pin(v4u a, v4u b, f32x16& c) {
  c = mfma(a, b, c);
  asm volatile("" : "+v"(c));
}
template <int O> DI v4u pack8(const f32x16& x) {
  v4u r; r.x = pk(x[O], x[O + 1]); r.y = pk(x[O + 2], x[O + 3]); r.z = pk(x[O + 4], x[O + 5]); r.w = pk(x[O + 6], x[O + 7]); return r;
}
template <int T> DI v4u ldw(LAS unsigned char* p) {
  s16x4 a = __builtin_amdgcn_ds_read_tr16_b64_v4i16((LAS s16x4*)p);
  s16x4 b = __builtin_amdgcn_ds_read_tr16_b64_v4i16((LAS s16x4*)(p + T));
  return __builtin_bit_cast(v4u, __builtin_shufflevector(a, b, 0, 1, 2, 3, 4, 5, 6, 7));
}
struct Base3 {
  u32 b[3];
  DI explicit Base3(LAS unsigned char* p) {
    b[0] = (u32)(size_t)p; b[1] = b[0] + 65536u; b[2] = b[0] + 131072u;
    asm volatile("" : "+v"(b[0]), "+v"(b[1]), "+v"(b[2]));
  }
  template <int OFF> DI LAS unsigned char* at() const { return (LAS unsigned char*)(size_t)b[OFF >> 16] + (OFF & 65535); }
};
DI void glds(rsrc_t rs, u32 voff, u32 soff, u32 ldsoff) {
  asm volatile("s_mov_b32 m0, %0\n\ts_nop 0\n\tbuffer_load_dwordx4 %1, %2, %3 offen lds" ::"s"(ldsoff), "v"(voff), "s"(rs), "s"(soff) : "m0", "memory");
}
template <int LOFF, int SOFF> DI void gldsi(rsrc_t rs, u32 voff, u32 sbase, u32 lbase) {
  u32 t;
  asm volatile("s_add_u32 m0, %1, %2\n\ts_add_u32 %0, %3, %4\n\tbuffer_load_dwordx4 %5, %6, %0 offen lds"
               : "=&s"(t) : "s"(lbase), "i"(LOFF), "s"(sbase), "i"(SOFF), "v"(voff), "s"(rs) : "m0", "scc", "memory");
}
DI void bar() { asm volatile("s_waitcnt lgkmcnt(0)\n\ts_barrier" ::: "memory"); }
DI void bar_raw() { asm volatile("s_barrier" ::: "memory"); }
DI void drain() { asm volatile("s_waitcnt vmcnt(0)" ::: "memory"); }
#define VMCNT(n) asm volatile("s_waitcnt vmcnt(" #n ")" ::: "memory")
#define RELOAD() asm volatile("" ::: "memory")
DI v4u ld128(rsrc_t rs, int voff, int soff) { return __builtin_amdgcn_raw_buffer_load_b128(rs, voff, soff, AUX_SC1); }
DI v2u ld64(rsrc_t rs, int voff, int soff) { return __builtin_amdgcn_raw_buffer_load_b64(rs, voff, soff, AUX_SC1); }
DI void st128(v4u v, rsrc_t rs, int voff, int soff) { __builtin_amdgcn_raw_buffer_store_b128(v, rs, voff + soff, 0, AUX_SC1); }
DI void st64(v2u v, rsrc_t rs, int voff, int soff) { __builtin_amdgcn_raw_buffer_store_b64(v, rs, voff, soff, AUX_SC1); }
template <int CTRL> DI float dpp_add(float v) {
  return v + __uint_as_float(__builtin_amdgcn_update_dpp(0u, __float_as_uint(v), CTRL, 0xf, 0xf, false));
}
constexpr int DPP_X1 = 0xB1, DPP_X2 = 0x4E, DPP_HM = 0x141, DPP_ROR4 = 0x124, DPP_ROR8 = 0x128;
DI float fold32(float x, float y) {
  auto r = __builtin_amdgcn_permlane32_swap(__float_as_uint(x), __float_as_uint(y), false, false);
  return __uint_as_float(r[0]) + __uint_as_float(r[1]);
}
DI float fold16(float x, float y) {
  auto r = __builtin_amdgcn_permlane16_swap(__float_as_uint(x), __float_as_uint(y), false, false);
  return __uint_as_float(r[0]) + __uint_as_float(r[1]);
}
DI void red8(const float (&a)[8], float& c0, float& c1) {
  const float b0 = fold32(a[0], a[4]), b1 = fold32(a[1], a[5]), b2 = fold32(a[2], a[6]), b3 = fold32(a[3], a[7]);
  c0 = fold16(b0, b2); c1 = fold16(b1, b3);
  c0 = dpp_add<DPP_X1>(c0); c1 = dpp_add<DPP_X1>(c1);
  c0 = dpp_add<DPP_X2>(c0); c1 = dpp_add<DPP_X2>(c1);
  c0 = dpp_add<DPP_ROR4>(c0); c1 = dpp_add<DPP_ROR4>(c1);
  c0 = dpp_add<DPP_ROR8>(c0); c1 = dpp_add<DPP_ROR8>(c1);
}
DI void red8q(const float (&a)[8], float& c0, float& c1) {
  const float b0 = fold32(a[0], a[4]), b1 = fold32(a[1], a[5]), b2 = fold32(a[2], a[6]), b3 = fold32(a[3], a[7]);
  c0 = fold16(b0, b2); c1 = fold16(b1, b3);
  c0 = dpp_add<DPP_ROR4>(c0); c1 = dpp_add<DPP_ROR4>(c1);
  c0 = dpp_add<DPP_ROR8>(c0); c1 = dpp_add<DPP_ROR8>(c1);
}
DI void publish(unsigned char* ws, int set, int idx, u32 tag, int lane) {
  drain();
  if (lane == 0) __hip_atomic_store((GAS u32*)(ws + OFF_FLAG + (set * 1024 + idx) * 128), tag, RLX_AGENT);
}
template <int N, int PRE> DI void waitn(unsigned char* ws, int set, int idx0, u32 tag, int lane) {
  if (PRE) __builtin_amdgcn_s_sleep(PRE);
  GAS u32* f = (GAS u32*)(ws + OFF_FLAG + (set * 1024 + idx0 + (lane & (N - 1))) * 128);
  for (u32 spins = 0; spins < SPIN_CAP; ++spins) {
    const u32 v = __hip_atomic_load(f, RLX_AGENT);
    if (__all(v == tag)) break;
    __builtin_amdgcn_s_sleep(1);
  }
  __builtin_amdgcn_fence(__ATOMIC_ACQUIRE, "wavefront");
  RELOAD();
}
DI void sig(LAS unsigned char* L, int i, int lane) {
  if (lane == 0) __hip_atomic_fetch_add((LAS u32*)(L + L_HINT) + i, 1u, __ATOMIC_RELAXED, __HIP_MEMORY_SCOPE_WORKGROUP);
}
DI void await(LAS unsigned char* L, int i, u32 n) {
  for (u32 spins = 0; spins < (1u << 20); ++spins) {
    const u32 v = __builtin_amdgcn_readfirstlane(__hip_atomic_load((LAS u32*)(L + L_HINT) + i, __ATOMIC_RELAXED, __HIP_MEMORY_SCOPE_WORKGROUP));
    if (v >= n) break;
    __builtin_amdgcn_s_sleep(1);
  }
  RELOAD();
}
DI void feat(const f32x16& P, u32 (&ph)[16]) {
  float p[16];
#pragma unroll
  for (int i = 0; i < 16; i += 2) { const u32 t = pk(P[i], P[i + 1]); p[i] = lo16(t); p[i + 1] = hi16(t); }
  float mx[4], mn[4];
#pragma unroll
  for (int i = 0; i < 4; ++i) {
    mx[i] = fmaxf(fmaxf(p[4 * i], p[4 * i + 1]), fmaxf(p[4 * i + 2], p[4 * i + 3]));
    mn[i] = fminf(fminf(p[4 * i], p[4 * i + 1]), fminf(p[4 * i + 2], p[4 * i + 3]));
  }
  constexpr float L2E = 1.4426950408889634f;
  const float mxl = fmaxf(fmaxf(mx[0], mx[1]), fmaxf(mx[2], mx[3])) * L2E, mnl = fminf(fminf(mn[0], mn[1]), fminf(mn[2], mn[3])) * L2E;
  float e1[16], e2[16];
#pragma unroll
  for (int i = 0; i < 16; ++i) { e1[i] = __builtin_amdgcn_exp2f(__builtin_fmaf(p[i], L2E, -mxl)); e2[i] = __builtin_amdgcn_exp2f(__builtin_fmaf(p[i], -L2E, mnl)); }
  float s1[4], s2[4];
#pragma unroll
  for (int i = 0; i < 4; ++i) {
    s1[i] = (e1[4 * i] + e1[4 * i + 1]) + (e1[4 * i + 2] + e1[4 * i + 3]);
    s2[i] = (e2[4 * i] + e2[4 * i + 1]) + (e2[4 * i + 2] + e2[4 * i + 3]);
  }
  const float r1 = rcp((s1[0] + s1[1]) + (s1[2] + s1[3])), r2 = rcp((s2[0] + s2[1]) + (s2[2] + s2[3]));
#pragma unroll
  for (int i = 0; i < 8; ++i) { ph[i] = pk(e1[2 * i] * r1, e1[2 * i + 1] * r1); ph[8 + i] = pk(e2[2 * i] * r2, e2[2 * i + 1] * r2); }
}

#define TSCLK() __builtin_amdgcn_s_memrealtime()
#define TS(i) do { if ((i) == 0) { lk0 = __builtin_amdgcn_s_memrealtime(); __builtin_amdgcn_s_waitcnt(0xC07F); } if ((i) == MK_LEAK) { SB0(); lk1 = __builtin_amdgcn_s_memrealtime(); __builtin_amdgcn_s_waitcnt(0xC07F); SB0(); } } while (0)
#define TS_DUMP() do {} while (0)
#define TS_INIT() u64 lk0 = 0, lk1 = 0

__global__ void __launch_bounds__(512, 2) mega(Params p) {
  extern __shared__ __attribute__((aligned(16))) unsigned char smem[];
  LAS unsigned char* L = (LAS unsigned char*)smem;
  TS_INIT();
  TS(0);
  const int tid = threadIdx.x, lane = tid & 63, w = __builtin_amdgcn_readfirstlane(tid >> 6);
  const int b = blockIdx.x, xcd = b & 7, rank = b >> 3, panel = xcd * 2 + (rank >> 4), mem = rank & 15;
  const int hh = lane >> 5, l31 = lane & 31;
  unsigned char* ws = p.ws;
  const int rowh = mem >> 3, colg = mem & 7;
  const int pair0 = mem < 11 ? 3 * mem : 33 + 2 * (mem - 11);
  const bool three = mem < 11;

  if (w >= 6) {
    const int v = w & 1;
    const rsrc_t rqkv = __builtin_amdgcn_make_buffer_rsrc((void*)p.wqkv, 0, D * 3 * D * 2, 0x00020000);
    const rsrc_t rwo = __builtin_amdgcn_make_buffer_rsrc((void*)p.wo, 0, D * D * 2, 0x00020000);
    const rsrc_t rgu = __builtin_amdgcn_make_buffer_rsrc((void*)(v ? p.wu : p.wg), 0, D * F * 2, 0x00020000);
    const rsrc_t rwd = __builtin_amdgcn_make_buffer_rsrc((void*)p.wd, 0, F * D * 2, 0x00020000);
    const u32 ldsb = (u32)(size_t)L;
    const u32 vq = (lane >> 2) * (3 * D * 2) + (lane & 3) * 16;
    const u32 vd = (lane >> 2) * (D * 2) + (lane & 3) * 16;
    const u32 vg = (lane >> 2) * (F * 2) + (((lane & 3) ^ (v << 1)) * 16);
    const u32 l1 = ldsb + v * 1024, l2 = ldsb + v * 2048;
    const u32 skv = (D + v * D + mem * 32) * 2, sq = v * 16 * (3 * D * 2) + mem * 64;
    const u32 so = 16 * v * 16 * (D * 2) + colg * 128, sd = 43 * v * 16 * (D * 2) + colg * 128;
    const u32 sg = pair0 * 64, sg2 = sg + (three ? 128 : 64);
#define LD_KV(S0, S1) static_for<S0, S1>([&](auto S) { gldsi<slot(P_KV + 2 * S.value), S.value * 16 * (3 * D * 2)>(rqkv, vq, skv, l1); })
#define LD_Q() static_for<0, 16>([&](auto S) { gldsi<slot(P_Q + 2 * S.value), 2 * S.value * 16 * (3 * D * 2)>(rqkv, vq, sq, l1); })
#define LD_DN(RS, SB, P0, T0, T1)                                                                    \
  static_for<T0, T1>([&](auto T) {                                                                   \
    gldsi<slot((P0) + 4 * T.value), T.value * 16 * (D * 2)>(RS, vd, SB, l2);                         \
    gldsi<slot((P0) + 4 * T.value) + 1024, T.value * 16 * (D * 2) + 64>(RS, vd, SB, l2);             \
  })
#define LD_GU(S0, S1)                                             \
  static_for<S0, S1>([&](auto S) {                                                                   \
    gldsi<slot(P_GU + 6 * S.value), S.value * 16 * (F * 2)>(rgu, vg, sg, l1);                        \
    gldsi<slot(P_GU + 6 * S.value + 2), S.value * 16 * (F * 2) + 64>(rgu, vg, sg, l1);               \
    gldsi<slot(P_GU + 6 * S.value + 4), S.value * 16 * (F * 2)>(rgu, vg, sg2, l1);                   \
  })
    if (lane < 16) *((LAS u32*)(L + L_HINT) + lane) = 0u;
    bar();
    LD_KV(0, 32);
    TS(1);
    await(L, 1, 4);
    LD_Q();
    VMCNT(16);
    TS(2);
    bar_raw();
    await(L, 2, 4);
    LD_DN(rwo, so, P_WO, 0, 10);
    VMCNT(20);
    TS(3);
    bar_raw();
    await(L, 3, 4);
    LD_DN(rwo, so, P_WO, 10, 16);
    LD_GU(0, 6);
    VMCNT(18);
    TS(4);
    bar_raw();
    await(L, 4, 4);
    LD_GU(6, 12);
    VMCNT(0);
    TS(5);
    bar_raw();
    await(L, 5, 4);
    LD_GU(12, 22);
    VMCNT(0);
    TS(6);
    bar_raw();
    TS(11);
    LD_GU(22, 32);
    LD_DN(rwd, sd, P_WD, 0, 1);
    TS(12);
    VMCNT(2);
    TS(7);
    bar_raw();
    LD_DN(rwd, sd, P_WD, 1, 17);
    VMCNT(0);
    TS(8);
    bar_raw();
    await(L, 6, 4);
    LD_DN(rwd, sd, P_WD, 17, 34);
    VMCNT(0);
    TS(9);
    bar_raw();
    LD_DN(rwd, sd, P_WD, 34, 43);
    VMCNT(0);
    TS(10);
    bar_raw();
    bar_raw();
    TS_DUMP();
    return;
  }

  const rsrc_t rs = __builtin_amdgcn_make_buffer_rsrc(ws, 0, WS_BYTES, 0x00020000);
  const u32 epoch = __builtin_amdgcn_raw_buffer_load_b32(rs, 0, OFF_EPOCH, AUX_SC1);

  if (w >= 4) {
    const int a = w & 1;
    u32 tag;
    {
      const bool on = rank >= 16 || a == 0;
      const int col0 = rank < 16 ? (xcd * 16 + rank) * 8 : 1024 + (xcd * 16 + rank - 16) * 16 + a * 8;
      v4u wm[8]; u32 cv[8];
      if (on) {
#pragma unroll
        for (int i = 0; i < 8; ++i) cv[i] = p.c[64 * i + lane];
#pragma unroll
        for (int i = 0; i < 8; ++i) wm[i] = *(const GAS v4u*)(p.wmod + (size_t)(64 * i + lane) * (6 * D) + col0);
      }
      SB0();
      bar_raw();
      TS(10);
      tag = epoch + 1u;
      if (on) {
        float sl[8], acc[8];
#pragma unroll
        for (int i = 0; i < 8; ++i) { const float c = lo16(cv[i]); sl[i] = rb(c * rcp(1.f + __expf(-c))); acc[i] = 0.f; }
        TS(11);
#pragma unroll
        for (int i = 0; i < 8; ++i)
#pragma unroll
          for (int j = 0; j < 4; ++j) { acc[2 * j] += sl[i] * lo16(wm[i][j]); acc[2 * j + 1] += sl[i] * hi16(wm[i][j]); }
        float c0, c1;
        TS(12);
        red8(acc, c0, c1);
        if ((lane & 15) == 0) __hip_atomic_store((GAS u64*)(ws + OFF_MODG + (col0 / 2 + (lane >> 4)) * 8), ((u64)tag << 32) | pk(c0, c1), RLX_AGENT);
      }
      if (a == 0) sig(L, 0, lane);
    }
    TS(1);
    bar();
    await(L, 2, 4);
    {
      const int loc = rank * 2 + a, task = xcd * 43 + loc, kp = task / 86, col0 = (task - kp * 86) * 32;
      const bool on = loc < 43;
      const u16* W = (col0 < F ? p.wg + col0 : p.wu + (col0 - F)) + (size_t)(128 * kp + 2 * (lane >> 2)) * F + (lane & 3) * 8;
      v4u wm[8];
      if (on) {
#pragma unroll
        for (int i = 0; i < 8; ++i) wm[i] = *(const GAS v4u*)(W + (size_t)(32 * (i >> 1) + (i & 1)) * F);
      }
      SB0();
      bar_raw();
      const int vec = lane >> 4, vo = (vec == 0 ? 2 : vec == 1 ? 4 : 5) * D * 4 + (colg * 64 + (lane & 15) * 4) * 4;
      v2u g[8];
      v4u gv;
      for (u32 spins = 0; spins < SPIN_CAP; ++spins) {
        RELOAD();
        gv = ld128(rs, vo, OFF_MODG);
#pragma unroll
        for (int i = 0; i < 8; ++i) g[i] = ld64(rs, ((kp & 3) * 64 + (lane >> 2)) * 8 + (i & 3) * 128, OFF_MODG + (3 + (i >> 2)) * D * 4);
        bool ok = (gv.y == tag) & (gv.w == tag);
#pragma unroll
        for (int i = 0; i < 8; ++i) ok &= g[i].y == tag;
        if (__all(ok)) break;
      }
      f32x4 v = {lo16(gv.x), hi16(gv.x), lo16(gv.z), hi16(gv.z)};
      if (vec == 1) v = f32x4{rb(1.f + v[0]), rb(1.f + v[1]), rb(1.f + v[2]), rb(1.f + v[3])};
      if (a == 0 && lane < 48) *(LAS f32x4*)(L + L_VEC + lane * 16) = v;
      if (on) {
        float a1[8], a2[8];
#pragma unroll
        for (int j = 0; j < 8; ++j) { a1[j] = 0.f; a2[j] = 0.f; }
#pragma unroll
        for (int i = 0; i < 8; ++i) {
          const u32 sbw = g[i >> 1].x, cbw = g[4 + (i >> 1)].x;
          const float sb = (i & 1) ? hi16(sbw) : lo16(sbw), sc = rb(1.f + ((i & 1) ? hi16(cbw) : lo16(cbw)));
#pragma unroll
          for (int j = 0; j < 4; ++j) {
            a1[2 * j] += sc * lo16(wm[i][j]); a1[2 * j + 1] += sc * hi16(wm[i][j]);
            a2[2 * j] += sb * lo16(wm[i][j]); a2[2 * j + 1] += sb * hi16(wm[i][j]);
          }
        }
        float c10, c11, c20, c21;
        red8q(a1, c10, c11); red8q(a2, c20, c21);
        const int k = (lane >> 2) & 3;
        const float val = k == 0 ? c10 : k == 1 ? c20 : k == 2 ? c11 : c21;
        __hip_atomic_store((GAS u64*)(ws + OFF_CG + kp * CG_PART + (col0 + 8 * (lane & 3) + 2 * (lane >> 4)) * 16 + k * 8), ((u64)tag << 32) | __float_as_uint(val), RLX_AGENT);
      }
    }
    TS(2);
    {
      const int idx0 = (mem * 4 + 2 * a) * 16;
      waitn<32, 0>(ws, FL_KV, idx0, tag, lane);
      TS(3);
      v4u e[32], zz[2];
#pragma unroll
      for (int i = 0; i < 32; ++i) e[i] = ld128(rs, lane * 16 + (i & 3) * 1024, OFF_KVE + idx0 * 1024 + (i >> 2) * 4096);
      zz[0] = ld128(rs, lane * 16, OFF_KVZ + idx0 * 64);
      zz[1] = ld128(rs, lane * 16 + 1024, OFF_KVZ + idx0 * 64);
      SB0();
#pragma unroll
      for (int q = 0; q < 2; ++q) {
        f32x4 s0 = __builtin_bit_cast(f32x4, e[16 * q]), s1 = __builtin_bit_cast(f32x4, e[16 * q + 1]);
#pragma unroll
        for (int i = 2; i < 16; i += 2) { s0 += __builtin_bit_cast(f32x4, e[16 * q + i]); s1 += __builtin_bit_cast(f32x4, e[16 * q + i + 1]); }
        s0 += s1;
        *(LAS v2u*)(L + L_KVF + (2 * a + q) * 512 + l31 * 16 + hh * 8) = v2u{pk(s0[0], s0[1]), pk(s0[2], s0[3])};
      }
      f32x4 z;
#pragma unroll
      for (int j = 0; j < 4; ++j) {
        float t = fold32(__uint_as_float(zz[0][j]), __uint_as_float(zz[1][j]));
        t = fold16(t, t);
        t = dpp_add<DPP_ROR4>(t);
        z[j] = dpp_add<DPP_ROR8>(t);
      }
      if (l31 < 4) *(LAS v2u*)(L + L_ZF + (2 * a + hh) * 32 + ((lane & 1) ^ 1) * 16 + ((lane >> 1) & 1) * 8) = v2u{pk(z[0], z[1]), pk(z[2], z[3])};
    }
    TS(4);
    bar();
    await(L, 4, 4);
    {
      const int ntile = three ? 6 : 4;
      v4u g[8];
      int off[2];
#pragma unroll
      for (int i = 0; i < 2; ++i) {
        int tile = 4 * i + 2 * a + hh; tile = tile < ntile ? tile : 0;
        off[i] = (32 * pair0 + 16 * tile + (lane & 15) + ((lane >> 4) & 1) * F) * 16;
      }
      for (u32 spins = 0; spins < SPIN_CAP; ++spins) {
        RELOAD();
        bool ok = true;
#pragma unroll
        for (int i = 0; i < 8; ++i) g[i] = ld128(rs, off[i >> 2], OFF_CG + (i & 3) * CG_PART);
#pragma unroll
        for (int i = 0; i < 8; ++i) ok &= (g[i].y == tag) & (g[i].w == tag);
        if (__all(ok)) break;
      }
#pragma unroll
      for (int i = 0; i < 2; ++i) {
        const float c1 = (__uint_as_float(g[4 * i].x) + __uint_as_float(g[4 * i + 1].x)) + (__uint_as_float(g[4 * i + 2].x) + __uint_as_float(g[4 * i + 3].x));
        const float c2 = (__uint_as_float(g[4 * i].z) + __uint_as_float(g[4 * i + 1].z)) + (__uint_as_float(g[4 * i + 2].z) + __uint_as_float(g[4 * i + 3].z));
        const float h1 = rb(c1), m1 = rb(c1 - h1), l1 = c1 - h1 - m1, h2 = rb(c2), m2 = rb(c2 - h2), l2 = c2 - h2 - m2;
        const int tile = 4 * i + 2 * a + hh;
        if (tile < 6) *(LAS v4u*)(L + L_CF + tile * 512 + l31 * 16) = v4u{pk(h1, m1), pk(l1, h2), pk(m2, l2), 0u};
      }
    }
    TS(5);
    bar();
    TS(6);
    bar();
    bar_raw();
    bar_raw();
    bar_raw();
    bar_raw();
    bar_raw();
    TS_DUMP();
    return;
  }

  __builtin_amdgcn_s_setprio(2);
  const int s2 = w & 1, kh = w >> 1;
  v4u aphi0, aphi1;
  u32 tag;
  {
    v4u xv[8];
    const u16* xr = p.x + (size_t)(panel * 128 + mem * 8 + (lane >> 3)) * D + (lane & 7) * 8;
#pragma unroll
    for (int i = 0; i < 8; ++i) xv[i] = *(const GAS v4u*)(xr + 64 * ((i + 2 * w) & 7));
    const int hs = (l31 >> 2) & 1, f = (l31 & 3) + 4 * (l31 >> 3);
    const u16* wp = p.wphi + (2 * mem + hs) * 256 + 4 * hh * 16 + f;
    u32 e[8];
#pragma unroll
    for (int j = 0; j < 8; ++j) e[j] = wp[(8 * (j >> 2) + (j & 3)) * 16];
    SB0();
    bar_raw();
    tag = epoch + 1u;
    float s[4] = {0.f, 0.f, 0.f, 0.f};
#pragma unroll
    for (int i = 0; i < 8; ++i)
#pragma unroll
      for (int j = 0; j < 4; ++j) s[j] += lo16(xv[i][j]) + hi16(xv[i][j]);
    float sm = (s[0] + s[1]) + (s[2] + s[3]);
    sm = dpp_add<DPP_X1>(sm); sm = dpp_add<DPP_X2>(sm); sm = dpp_add<DPP_HM>(sm);
    const float mean = sm * (1.f / D);
    float q[4] = {0.f, 0.f, 0.f, 0.f};
#pragma unroll
    for (int i = 0; i < 8; ++i)
#pragma unroll
      for (int j = 0; j < 4; ++j) { const float u = lo16(xv[i][j]) - mean, v = hi16(xv[i][j]) - mean; q[j] += u * u + v * v; }
    float qq = (q[0] + q[1]) + (q[2] + q[3]);
    qq = dpp_add<DPP_X1>(qq); qq = dpp_add<DPP_X2>(qq); qq = dpp_add<DPP_HM>(qq);
    const float m1 = rb(mean), r1 = rcp(rb(__builtin_amdgcn_sqrtf(rb(rb(qq * (1.f / D)) + 1e-5f))));
    float xn[16];
#pragma unroll
    for (int i = 0; i < 2; ++i)
#pragma unroll
      for (int j = 0; j < 4; ++j) {
        const u32 d = pk(lo16(xv[i][j]) - m1, hi16(xv[i][j]) - m1);
        const u32 t = pk(lo16(d) * r1, hi16(d) * r1);
        xn[8 * i + 2 * j] = lo16(t); xn[8 * i + 2 * j + 1] = hi16(t);
      }
    const v4u av = {e[0] | (e[1] << 16), e[2] | (e[3] << 16), e[4] | (e[5] << 16), e[6] | (e[7] << 16)};
    const v4u z = {0, 0, 0, 0};
    aphi0 = hs == 0 ? av : z; aphi1 = hs == 1 ? av : z;
    TS(1);
    await(L, 0, 1);
    __builtin_amdgcn_s_sleep(PRE_MOD);
    TS(21);
    v4u g;
    for (u32 spins = 0; spins < SPIN_CAP; ++spins) {
      RELOAD();
      g = ld128(rs, l31 * 16 + hh * 2048, OFF_MODG + w * 512);
      if (__all((g.y == tag) & (g.w == tag))) break;
      __builtin_amdgcn_s_sleep(2);
    }
    *(LAS v2u*)(L + L_T + w * 4096 + lane * 8) = v2u{g.x, g.z};
    v4u sav[2], cav[2];
#pragma unroll
    for (int i = 0; i < 2; ++i) {
      sav[i] = *(LAS v4u*)(L + L_T + w * 4096 + i * 128 + (lane & 7) * 16);
      cav[i] = *(LAS v4u*)(L + L_T + w * 4096 + 256 + i * 128 + (lane & 7) * 16);
    }
    TS(2);
    const int strip = panel * 4 + (mem >> 2), r = 8 * (mem & 3) + (lane >> 3);
#pragma unroll
    for (int i = 0; i < 2; ++i) {
      v4u o;
#pragma unroll
      for (int j = 0; j < 4; ++j) {
        const u32 sa = sav[i][j], ca = cav[i][j];
        const u32 sc = pk(1.f + lo16(ca), 1.f + hi16(ca));
        const u32 t = pk(xn[8 * i + 2 * j] * lo16(sc), xn[8 * i + 2 * j + 1] * hi16(sc));
        o[j] = pk(lo16(t) + lo16(sa), hi16(t) + hi16(sa));
      }
      st128(o, rs, (4 * i + ((lane & 7) >> 1)) * 1024 + (r + 32 * (lane & 1)) * 16, OFF_H + strip * 32768 + w * 8192);
    }
    publish(ws, FL_H, strip * 16 + (mem & 3) * 4 + w, tag, lane);
    sig(L, 1, lane);
  }
  TS(3);

  const int strip1 = panel * 4 + w;
  const int trq = (lane & 15) >> 2, trp = lane & 3, trb = (lane >> 4) & 1;
  const f32x16 zero16 = {0.f, 0.f, 0.f, 0.f, 0.f, 0.f, 0.f, 0.f, 0.f, 0.f, 0.f, 0.f, 0.f, 0.f, 0.f, 0.f};
  u32 phq[16];
  v4u wb[PD];
  {
    const Base3 wnat(L + (8 * hh + trq) * 64 + trb * 32 + trp * 8);
    waitn<16, PRE_FL>(ws, FL_H, strip1 * 16, tag, lane);
    TS(4);
    v4u a[32];
#pragma unroll
    for (int s = 0; s < 32; ++s) a[s] = ld128(rs, lane * 16 + (s & 3) * 1024, OFF_H + strip1 * 32768 + (s >> 2) * 4096), SB0();
    SB0();
    bar_raw();
    static_for<0, PD>([&](auto U) { wb[U.value] = ldw<256>(wnat.at<slot(P_KV + U.value)>()); });
    SB0();
    f32x16 kacc = zero16, vacc = zero16;
    static_for<0, 64>([&](auto U) {
      constexpr int u = U.value;
      const v4u cur = wb[u % PD];
      if constexpr (u + PD < 64) wb[u % PD] = ldw<256>(wnat.at<slot(P_KV + u + PD)>());
      if constexpr (u & 1) mfma_pin(a[u >> 1], cur, vacc); else mfma_pin(cur, a[u >> 1], kacc);
      if constexpr (u == 56) sig(L, 2, lane);
      SB0();
    });
    TS(5);
    f32x16 P = mfma(aphi0, pack8<0>(kacc), zero16);
    P = mfma(aphi1, pack8<8>(kacc), P);
    const v4u vb0 = pack8<0>(vacc), vb1 = pack8<8>(vacc);
    u32 phk[16];
    feat(P, phk);
    LAS unsigned char* T = L + L_T + w * 4096;
#pragma unroll
    for (int c = 0; c < 4; ++c)
      *(LAS v4u*)(T + lane * 64 + ((c ^ ((lane >> 1) & 3)) * 16)) = v4u{phk[4 * c], phk[4 * c + 1], phk[4 * c + 2], phk[4 * c + 3]};
    const v4u ones = {0x3F803F80u, 0x3F803F80u, 0x3F803F80u, 0x3F803F80u};
    LAS unsigned char* tr = T + (4 * hh + trq) * 64 + (((2 * trb + (trp >> 1)) ^ ((2 * hh + (trq >> 1)) & 3)) * 16) + 8 * (trp & 1);
    const v4u t0 = ldw<512>(tr), t1 = ldw<512>(tr + 1024), t2 = ldw<512>(tr + 2048), t3 = ldw<512>(tr + 3072);
    f32x16 D0 = mfma(t0, trb ? ones : vb0, zero16);
    f32x16 D1 = mfma(t2, trb ? vb0 : ones, zero16);
    D0 = mfma(t1, trb ? ones : vb1, D0);
    D1 = mfma(t3, trb ? vb1 : ones, D1);
#pragma unroll
    for (int g = 0; g < 4; ++g) {
      f32x4 e, z;
#pragma unroll
      for (int j = 0; j < 4; ++j) { e[j] = trb ? D1[4 * g + j] : D0[4 * g + j]; z[j] = trb ? D0[4 * g + j] : D1[4 * g + j]; }
      *(LAS f32x4*)(T + g * 1024 + lane * 16) = e;
      if ((lane & 15) == 0) *(LAS f32x4*)(L + L_Z + w * 256 + g * 64 + (lane >> 4) * 16) = z;
    }
    TS(6);
    bar();
    {
      f32x4 e = *(LAS f32x4*)(L + L_T + w * 1024 + lane * 16), z = *(LAS f32x4*)(L + L_Z + w * 64 + (lane & 3) * 16);
#pragma unroll
      for (int v = 1; v < 4; ++v) {
        e += *(LAS f32x4*)(L + L_T + v * 4096 + w * 1024 + lane * 16);
        z += *(LAS f32x4*)(L + L_Z + v * 256 + w * 64 + (lane & 3) * 16);
      }
      const int idx = (mem * 4 + w) * 16 + panel;
      st128(__builtin_bit_cast(v4u, e), rs, lane * 16, OFF_KVE + idx * 1024);
      if (lane < 4) st128(__builtin_bit_cast(v4u, z), rs, lane * 16, OFF_KVZ + idx * 64);
      static_for<0, PD>([&](auto U) { wb[U.value] = ldw<256>(wnat.at<slot(P_Q + U.value)>()); });
      publish(ws, FL_KV, idx, tag, lane);
      sig(L, 3, lane);
    }
    TS(7);
    f32x16 qacc = zero16;
    static_for<0, 32>([&](auto U) {
      constexpr int u = U.value;
      const v4u cur = wb[u % PD];
      if constexpr (u + PD < 32) wb[u % PD] = ldw<256>(wnat.at<slot(P_Q + u + PD)>());
      mfma_pin(cur, a[u], qacc);
      SB0();
    });
    P = mfma(aphi0, pack8<0>(qacc), zero16);
    P = mfma(aphi1, pack8<8>(qacc), P);
    feat(P, phq);
  }
  const int strip2 = panel * 4 + rowh * 2 + s2, ncol = colg * 64 + kh * 32;
  v2u xres[4];
  {
    const u16* xr = p.x + (size_t)(strip2 * 32 + l31) * D + ncol + 4 * hh;
#pragma unroll
    for (int i = 0; i < 4; ++i) xres[i] = *(const GAS v2u*)(xr + 8 * i);
  }
  TS(8);
  bar_raw();
  TS(9);
  {
    const bool on = (l31 >> 4) == hh;
    const v4u zv = {0, 0, 0, 0};
    f32x16 num = zero16, den = zero16;
    v4u ak[4], az[4];
#pragma unroll
    for (int s4 = 0; s4 < 4; ++s4) { ak[s4] = *(LAS v4u*)(L + L_KVF + s4 * 512 + l31 * 16); az[s4] = *(LAS v4u*)(L + L_ZF + s4 * 32 + hh * 16); }
#pragma unroll
    for (int s4 = 0; s4 < 4; ++s4) den = mfma(on ? az[s4] : zv, v4u{phq[4 * s4], phq[4 * s4 + 1], phq[4 * s4 + 2], phq[4 * s4 + 3]}, den);
#pragma unroll
    for (int s4 = 0; s4 < 4; ++s4) num = mfma(on ? ak[s4] : zv, v4u{phq[4 * s4], phq[4 * s4 + 1], phq[4 * s4 + 2], phq[4 * s4 + 3]}, num);
    const float d0 = rcp(rb(rb(den[0]) + 1e-6f)), d1 = rcp(rb(rb(den[8]) + 1e-6f));
    const v4u n0 = pack8<0>(num), n1 = pack8<8>(num);
    v4u o0, o1;
#pragma unroll
    for (int j = 0; j < 4; ++j) { o0[j] = pk(lo16(n0[j]) * d0, hi16(n0[j]) * d0); o1[j] = pk(lo16(n1[j]) * d1, hi16(n1[j]) * d1); }
    st128(o0, rs, lane * 16, OFF_O + strip1 * 32768 + (2 * mem) * 1024);
    st128(o1, rs, lane * 16 + 1024, OFF_O + strip1 * 32768 + (2 * mem) * 1024);
  }
#define WDN_BASES() const Base3 wown(L + (4 * hh + trq) * 64 + trb * 32 + trp * 8 + 3 * kh * 1024), woth(L + (4 * hh + trq) * 64 + trb * 32 + trp * 8 + (kh + 1) * 1024)
#define WDN(base, u) ldw<512>((((u) & 1) ? wown : woth).template at<slot((base) + 4 * ((u) >> 1))>())
  u32 x1p[8];
  {
    WDN_BASES();
    static_for<0, PD>([&](auto U) { wb[U.value] = WDN(P_WO, U.value); });
    publish(ws, FL_O, strip1 * 16 + mem, tag, lane);
    sig(L, 4, lane);
    TS(10);
    waitn<8, PRE_FL>(ws, FL_O, strip2 * 16 + kh * 8, tag, lane);
    TS(11);
    v4u a[16];
#pragma unroll
    for (int t = 0; t < 16; ++t) a[t] = ld128(rs, lane * 16 + (t & 3) * 1024, OFF_O + strip2 * 32768 + kh * 16384 + (t >> 2) * 4096), SB0();
    SB0();
    f32x16 c0 = zero16, c1 = zero16;
    static_for<0, 32>([&](auto U) {
      constexpr int u = U.value;
      const v4u cur = wb[u % PD];
      if constexpr (u + PD < 32) wb[u % PD] = WDN(P_WO, u + PD);
      if constexpr (u & 1) mfma_pin(cur, a[u >> 1], c0); else mfma_pin(cur, a[u >> 1], c1);
      SB0();
    });
#pragma unroll
    for (int g = 0; g < 4; ++g) *(LAS f32x4*)(L + L_T + w * 4096 + g * 1024 + lane * 16) = f32x4{c1[4 * g], c1[4 * g + 1], c1[4 * g + 2], c1[4 * g + 3]};
    f32x4 ga[4], sc[4];
#pragma unroll
    for (int g = 0; g < 4; ++g) {
      ga[g] = *(LAS f32x4*)(L + L_VEC + (kh * 32 + 8 * g + 4 * hh) * 4);
      sc[g] = *(LAS f32x4*)(L + L_VEC + 256 + (kh * 32 + 8 * g + 4 * hh) * 4);
    }
    TS(12);
    bar();
    float x1[16], sp[4];
    u32 yp[8];
#pragma unroll
    for (int g = 0; g < 4; ++g) {
      const f32x4 o = *(LAS f32x4*)(L + L_T + (w ^ 2) * 4096 + g * 1024 + lane * 16);
#pragma unroll
      for (int j = 0; j < 2; ++j) {
        const u32 y = pk(c0[4 * g + 2 * j] + o[2 * j], c0[4 * g + 2 * j + 1] + o[2 * j + 1]);
        const u32 t = pk(ga[g][2 * j] * lo16(y), ga[g][2 * j + 1] * hi16(y));
        const u32 r = pk(lo16(xres[g][j]) + lo16(t), hi16(xres[g][j]) + hi16(t));
        x1p[2 * g + j] = r;
        x1[4 * g + 2 * j] = lo16(r); x1[4 * g + 2 * j + 1] = hi16(r);
        yp[2 * g + j] = pk(lo16(r) * sc[g][2 * j], hi16(r) * sc[g][2 * j + 1]);
      }
      sp[g] = (x1[4 * g] + x1[4 * g + 1]) + (x1[4 * g + 2] + x1[4 * g + 3]);
    }
    const int tile = 2 * colg + kh;
    st128(v4u{yp[0], yp[1], yp[2], yp[3]}, rs, lane * 16, OFF_X1 + strip2 * 32768 + tile * 2048);
    st128(v4u{yp[4], yp[5], yp[6], yp[7]}, rs, lane * 16 + 1024, OFF_X1 + strip2 * 32768 + tile * 2048);
    const float ss = (sp[0] + sp[1]) + (sp[2] + sp[3]);
    const float mt = fold32(ss, ss) * (1.f / 32.f);
    float q[4];
#pragma unroll
    for (int g = 0; g < 4; ++g) {
      const float d0 = x1[4 * g] - mt, d1 = x1[4 * g + 1] - mt, d2 = x1[4 * g + 2] - mt, d3 = x1[4 * g + 3] - mt;
      q[g] = (d0 * d0 + d1 * d1) + (d2 * d2 + d3 * d3);
    }
    const float qs = (q[0] + q[1]) + (q[2] + q[3]);
    const float qt = fold32(qs, qs);
    if (lane < 32) st64(v2u{__float_as_uint(mt), __float_as_uint(qt)}, rs, lane * 8, OFF_ST + strip2 * 4096 + tile * 256);
  }
  asm volatile("" : "+v"(x1p[0]), "+v"(x1p[1]), "+v"(x1p[2]), "+v"(x1p[3]), "+v"(x1p[4]), "+v"(x1p[5]), "+v"(x1p[6]), "+v"(x1p[7]));
#define GU_S(u) ((u) < 144 ? (u) / 6 : 24 + ((u) - 144) % 8)
#define GU_I(u) ((u) < 144 ? (u) % 6 : ((u) - 144) / 8)
#define WGU(u) ldw<512>(((GU_I(u) & 1) ? wy : wx).template at<slot(P_GU + 6 * GU_S(u) + (GU_I(u) & 6))>())
  {
    const Base3 wx(L + (4 * hh + trq) * 64 + trp * 8 + trb * (1024 + 32)), wy(L + (4 * hh + trq) * 64 + trp * 8 + (trb ? 1024 : 32));
    static_for<0, PD>([&](auto U) { wb[U.value] = WGU(U.value); });
    publish(ws, FL_X, strip2 * 16 + 2 * colg + kh, tag, lane);
    sig(L, 5, lane);
    TS(13);
    waitn<16, PRE_FL>(ws, FL_X, strip1 * 16, tag, lane);
    TS(14);
    constexpr int AW = 16;
    v4u a[AW];
#define LDX(s) ld128(rs, lane * 16 + ((s) & 3) * 1024, OFF_X1 + strip1 * 32768 + ((s) >> 2) * 4096)
#pragma unroll
    for (int s = 0; s < AW; ++s) a[s] = LDX(s), SB0();
    v2u st[8];
    f32x16 acc[6];
#pragma unroll
    for (int i = 0; i < 6; ++i) acc[i] = zero16;
    float rr = 0.f;
    v4u bcor = {0, 0, 0, 0};
    v4u ho, cf;
    float tg0 = 0.f, tg1 = 0.f, tu0 = 0.f, tu1 = 0.f;
    u32 pg = 0, pu = 0;
    auto epi = [&](auto Ic, auto Kc) {
      constexpr int I = decltype(Ic)::value, k = decltype(Kc)::value, j = k >> 1;
      const f32x16& A = acc[I];
      if constexpr ((k & 1) == 0) {
        tg0 = A[2 * j] * rr; tg1 = A[2 * j + 1] * rr; tu0 = A[8 + 2 * j] * rr; tu1 = A[9 + 2 * j] * rr;
        pg = pk(tg0, tg1); pu = pk(tu0, tu1);
      } else {
        ho[j] = pk(lo16(pg) * lo16(pu), hi16(pg) * hi16(pu));
        if constexpr (j == 3) { if (I < 4 || three) st128(ho, rs, lane * 16, OFF_G + (strip1 * 86 + 2 * pair0 + I) * 1024); }
      }
    };
    static_for<0, 192>([&](auto Uc) {
      constexpr int u = decltype(Uc)::value, s = GU_S(u), i = GU_I(u);
      if constexpr (u == 6) TS(22);
      if constexpr (u == 60) { TS(23); bar_raw(); TS(24); }
      if constexpr (u == 132 - PD - 1) { TS(25); bar_raw(); TS(26); }
      const v4u cur = wb[u % PD];
      if constexpr (u + PD < 192) wb[u % PD] = WGU(u + PD);
      if constexpr (u >= 144 && s == 27) cf = *(LAS v4u*)(L + L_CF + i * 512 + l31 * 16);
      mfma_pin(cur, a[s % AW], acc[i]);
      if constexpr (u < 144 && i == 5 && s + AW < 32) a[s % AW] = LDX(s + AW);
      if constexpr (u >= 100 && u < 108) st[u - 100] = ld64(rs, l31 * 8 + hh * 2048 + (u - 100) * 256, OFF_ST + strip1 * 4096);
      if constexpr (u == 132) {
        float ms = 0.f;
#pragma unroll
        for (int t = 0; t < 8; ++t) ms += __uint_as_float(st[t].x);
        tg0 = fold32(ms, ms) * (1.f / 16.f);
      }
      if constexpr (u == 134 || u == 136) {
        float q = 0.f;
#pragma unroll
        for (int t = 2 * (u - 134); t < 2 * (u - 134) + 4; ++t) { const float d = __uint_as_float(st[t].x) - tg0; q += __uint_as_float(st[t].y) + 32.f * d * d; }
        if constexpr (u == 134) tg1 = q; else tg1 += q;
      }
      if constexpr (u == 138) tg1 = rb(__builtin_amdgcn_sqrtf(rb(rb(fold32(tg1, tg1) * (1.f / D)) + 1e-5f)));
      if constexpr (u == 140) {
        rr = rcp(tg1);
        if (hh == 0) bcor = v4u{pk(-tg0, -tg0), pk(-tg0, tg1), pk(tg1, tg1), 0u};
      }
      if constexpr (u >= 144 && s == 31) mfma_pin(cf, bcor, acc[i]);
      if constexpr (u >= 153) epi(IC<(u - 153) / 8>{}, IC<(u - 153) % 8>{});
      SB0();
      if constexpr (u == 143) TS(15);
    });
    bar_raw();
    epi(IC<4>{}, IC<7>{});
    static_for<0, 8>([&](auto Kc) { epi(IC<5>{}, Kc); });
    TS(16);
  }
  {
    WDN_BASES();
    static_for<0, PD>([&](auto U) { wb[U.value] = WDN(P_WD, U.value); });
    publish(ws, FL_G, strip1 * 16 + mem, tag, lane);
    sig(L, 6, lane);
    TS(17);
    waitn<16, PRE_FL>(ws, FL_G, strip2 * 16, tag, lane);
    TS(18);
    v4u a[43];
#pragma unroll
    for (int t = 0; t < 43; ++t) a[t] = ld128(rs, lane * 16 + (t & 3) * 1024, OFF_G + (strip2 * 86 + kh * 43) * 1024 + (t >> 2) * 4096), SB0();
    SB0();
    f32x16 c0 = zero16, c1 = zero16;
    static_for<0, 86>([&](auto U) {
      constexpr int u = U.value;
      if constexpr (u == 24) bar_raw();
      if constexpr (u == 68 - PD - 1) bar_raw();
      const v4u cur = wb[u % PD];
      if constexpr (u + PD < 86) wb[u % PD] = WDN(P_WD, u + PD);
      if constexpr (u & 1) mfma_pin(cur, a[u >> 1], c0); else mfma_pin(cur, a[u >> 1], c1);
      SB0();
    });
#pragma unroll
    for (int g = 0; g < 4; ++g) *(LAS f32x4*)(L + L_T + w * 4096 + g * 1024 + lane * 16) = f32x4{c1[4 * g], c1[4 * g + 1], c1[4 * g + 2], c1[4 * g + 3]};
    f32x4 gb[4];
#pragma unroll
    for (int g = 0; g < 4; ++g) gb[g] = *(LAS f32x4*)(L + L_VEC + 512 + (kh * 32 + 8 * g + 4 * hh) * 4);
    TS(19);
    bar();
    u16* orow = p.out + (size_t)(strip2 * 32 + l31) * D + ncol + 4 * hh;
#pragma unroll
    for (int g = 0; g < 4; ++g) {
      const f32x4 o = *(LAS f32x4*)(L + L_T + (w ^ 2) * 4096 + g * 1024 + lane * 16);
      v2u r;
#pragma unroll
      for (int j = 0; j < 2; ++j) {
        const u32 y = pk(c0[4 * g + 2 * j] + o[2 * j], c0[4 * g + 2 * j + 1] + o[2 * j + 1]);
        const u32 t = pk(gb[g][2 * j] * lo16(y), gb[g][2 * j + 1] * hi16(y));
        r[j] = pk(lo16(x1p[2 * g + j]) + lo16(t), hi16(x1p[2 * g + j]) + hi16(t));
      }
      *(GAS v2u*)(orow + 8 * g) = r;
    }
  }
  if (b == 0 && tid == 0) __hip_atomic_store((GAS u32*)(ws + OFF_EPOCH), tag, RLX_AGENT);
  if (b == MK_LEAKB && w == 0) {
    const u64 te = __builtin_amdgcn_s_memrealtime();
    const u64 goal = MK_LEAK == 99 ? lk0 + 10000 : MK_LEAK == 98 ? te + 4 * (te - lk0) : te + 4 * (lk1 - lk0);
    while (__builtin_amdgcn_s_memrealtime() < goal) __builtin_amdgcn_s_sleep(1);
  }
}
}

extern "C" void kernel_launch(void* const* d_in, const int* in_sizes, int n_in, void* d_out, int out_size, void* d_ws,
                              size_t ws_size, hipStream_t stream) {
  using namespace mk;
  Params p;
  p.x = (const u16*)d_in[0]; p.wqkv = (const u16*)d_in[1]; p.wphi = (const u16*)d_in[2]; p.wo = (const u16*)d_in[3];
  p.wg = (const u16*)d_in[5]; p.wu = (const u16*)d_in[6]; p.wd = (const u16*)d_in[7]; p.c = (const u16*)d_in[8];
  p.wmod = (const u16*)d_in[9]; p.out = (u16*)d_out; p.ws = (unsigned char*)d_ws;
  (void)hipFuncSetAttribute((const void*)mega, hipFuncAttributeMaxDynamicSharedMemorySize, LDS_BYTES);
  mega<<<256, 512, LDS_BYTES, stream>>>(p);
}
```

```cpp
#include <hip/hip_runtime.h>

namespace mk {
typedef unsigned short u16;
typedef unsigned int u32;
typedef unsigned long long u64;
typedef u32 v4u __attribute__((ext_vector_type(4)));
typedef u32 v2u __attribute__((ext_vector_type(2)));
typedef float f32x2 __attribute__((ext_vector_type(2)));
typedef float f32x4 __attribute__((ext_vector_type(4)));
typedef float f32x16 __attribute__((ext_vector_type(16)));
typedef __bf16 bf16x2 __attribute__((ext_vector_type(2)));
typedef __bf16 bf16x8 __attribute__((ext_vector_type(8)));
typedef short s16x4 __attribute__((ext_vector_type(4)));
#define LAS __attribute__((address_space(3)))
#define GAS __attribute__((address_space(1)))
#define DI __device__ __forceinline__
#define RLX_AGENT __ATOMIC_RELAXED, __HIP_MEMORY_SCOPE_AGENT
#define SB0() __builtin_amdgcn_sched_barrier(0)
typedef __amdgpu_buffer_rsrc_t rsrc_t;

constexpr int D = 512, F = 1376;
constexpr int OFF_EPOCH = 0;
constexpr int OFF_MODG = 64 << 10;
constexpr int OFF_KVZ = 128 << 10;
constexpr int OFF_CG = 192 << 10;
constexpr int CG_PART = 2 * F * 16;
constexpr int OFF_ARR = 384 << 10;
constexpr int OFF_MODR = 512 << 10;
constexpr int OFF_KVE = 1 << 20;
constexpr int OFF_PAN = 2 << 20;
constexpr int PN_H = 0;
constexpr int PN_O = 128 << 10;
constexpr int PN_X1 = 256 << 10;
constexpr int PN_G = 384 << 10;
constexpr int PN_ST = 768 << 10;
constexpr int PN_FL = 800 << 10;
constexpr int FL_H = 0, FL_O = 4, FL_X = 8, FL_G = 12, FL_KV = 384;
constexpr int PSTR = (848 << 10) + 4224;
constexpr int WS_BYTES = 16 << 20;
constexpr int OFF_TS = 32 << 20;
constexpr int RING = 136;
constexpr int L_T = RING * 1024, TW = 4352;
constexpr int L_KVF = L_T + 4 * TW;
constexpr int L_ZF = L_KVF + 2048;
constexpr int L_VEC = L_ZF + 256;
constexpr int L_CF = L_VEC + 768;
constexpr int L_HINT = L_CF + 3072;
constexpr int LDS_BYTES = L_HINT + 128;
static_assert(LDS_BYTES <= 163840, "LDS");
constexpr int P_KV = 0, P_Q = 64, P_WO = 96, P_GU = 160, P_WD = 352;
constexpr int H_CP = 6, H_PP = 7;
constexpr int pair_of(int t) { return ((t & 15) < 11 ? 3 * (t & 15) : 2 * (t & 15) + 11) + (t >> 4); }
constexpr int slot(int n) { return (n % RING) * 1024; }
constexpr int AUX_SC1 = 16;
constexpr u32 SPIN_CAP = 1u << 16;
#define PRE_MOD 8
#define HI_DELAY 8
#define POLL_GAP 6
#define PRE_FL 4
#define KV1 32
#define PGU 4
#define PKV 0
#define GPUB 29
#define LPRIO 0
#define APRIO 1
#define TMOD 0
#define CW 1
#define DW 2
#define CSLP 3
#define PG2 2
#define W0 12
constexpr int PD = 5;
constexpr int CODE_KB = 36;

#define NBV(x) (x)
struct Params {
  const u16 *x, *wqkv, *wphi, *wo, *wg, *wu, *wd, *c, *wmod;
  u16* out;
  unsigned char* ws;
};

template <int V> struct IC { static constexpr int value = V; };
template <int I, int N, class Fn> DI void static_for(Fn&& f) {
  if constexpr (I < N) { f(IC<I>{}); static_for<I + 1, N>(f); }
}
DI float lo16(u32 v) { return __uint_as_float(v << 16); }
DI float hi16(u32 v) { return __uint_as_float(v & 0xffff0000u); }
DI u32 pk(float a, float b) { f32x2 v = {a, b}; return __builtin_bit_cast(u32, __builtin_convertvector(v, bf16x2)); }
DI float rb(float a) { return lo16(pk(a, 0.f)); }
DI float rcp(float a) { return __builtin_amdgcn_rcpf(a); }
DI f32x16 mfma(v4u a, v4u b, f32x16 c) {
  return __builtin_amdgcn_mfma_f32_32x32x16_bf16(__builtin_bit_cast(bf16x8, a), __builtin_bit_cast(bf16x8, b), c, 0, 0, 0);
}
DI void mfma_pin(v4u a, v4u b, f32x16& c) {
  c = mfma(a, b, c);
  asm volatile("" : "+v"(c));
}
template <int O> DI v4u pack8(const f32x16& x) {
  v4u r; r.x = pk(x[O], x[O + 1]); r.y = pk(x[O + 2], x[O + 3]); r.z = pk(x[O + 4], x[O + 5]); r.w = pk(x[O + 6], x[O + 7]); return r;
}
template <int T> DI v4u ldw(LAS unsigned char* p) {
  s16x4 a = __builtin_amdgcn_ds_read_tr16_b64_v4i16((LAS s16x4*)p);
  s16x4 b = __builtin_amdgcn_ds_read_tr16_b64_v4i16((LAS s16x4*)(p + T));
  return __builtin_bit_cast(v4u, __builtin_shufflevector(a, b, 0, 1, 2, 3, 4, 5, 6, 7));
}
struct Base3 {
  u32 b[3];
  DI explicit Base3(LAS unsigned char* p) {
    b[0] = (u32)(size_t)p; b[1] = b[0] + 65536u; b[2] = b[0] + 131072u;
    asm volatile("" : "+v"(b[0]), "+v"(b[1]), "+v"(b[2]));
  }
  template <int OFF> DI LAS unsigned char* at() const { return (LAS unsigned char*)(size_t)b[OFF >> 16] + (OFF & 65535); }
};
template <int LOFF, int SOFF> DI void gldsi(rsrc_t rs, u32 voff, u32 sbase, u32 lbase) {
  u32 t;
  asm volatile("s_add_u32 m0, %1, %2\n\ts_add_u32 %0, %3, %4\n\tbuffer_load_dwordx4 %5, %6, %0 offen lds"
               : "=&s"(t) : "s"(lbase), "i"(LOFF), "s"(sbase), "i"(SOFF), "v"(voff), "s"(rs) : "m0", "scc", "memory");
}
DI void bar() { asm volatile("s_waitcnt lgkmcnt(0)\n\ts_barrier" ::: "memory"); }
DI void bar_raw() { asm volatile("s_barrier" ::: "memory"); }
DI void drain() { asm volatile("s_waitcnt vmcnt(0)" ::: "memory"); }
#define VMCNT(n) asm volatile("s_waitcnt vmcnt(" #n ")" ::: "memory")
#define RELOAD() asm volatile("" ::: "memory")
DI v4u ld128(rsrc_t rs, int voff, int soff) { return __builtin_amdgcn_raw_buffer_load_b128(rs, voff, soff, AUX_SC1); }
DI v2u ld64(rsrc_t rs, int voff, int soff) { return __builtin_amdgcn_raw_buffer_load_b64(rs, voff, soff, AUX_SC1); }
DI void st128(v4u v, rsrc_t rs, int voff, int soff) { __builtin_amdgcn_raw_buffer_store_b128(v, rs, voff + soff, 0, AUX_SC1); }
DI void st64(v2u v, rsrc_t rs, int voff, int soff) { __builtin_amdgcn_raw_buffer_store_b64(v, rs, voff, soff, AUX_SC1); }
template <int S> DI v4u ldfrag(rsrc_t rs, int voff, int soff) { return ld128(rs, voff + (S & 3) * 1024, soff + (S >> 2) * 4096); }
template <int W, int N> DI void ldfrags(v4u (&a)[N], rsrc_t rs, int voff, int soff) {
  static_for<0, W>([&](auto S) { a[S.value] = ldfrag<S.value>(rs, voff, soff); SB0(); });
}
template <int CTRL> DI float dpp_add(float v) {
  return v + __uint_as_float(__builtin_amdgcn_update_dpp(0u, __float_as_uint(v), CTRL, 0xf, 0xf, false));
}
constexpr int DPP_X1 = 0xB1, DPP_X2 = 0x4E, DPP_HM = 0x141, DPP_ROR4 = 0x124, DPP_ROR8 = 0x128;
DI float fold32(float x, float y) {
  auto r = __builtin_amdgcn_permlane32_swap(__float_as_uint(x), __float_as_uint(y), false, false);
  return __uint_as_float(r[0]) + __uint_as_float(r[1]);
}
DI float fold16(float x, float y) {
  auto r = __builtin_amdgcn_permlane16_swap(__float_as_uint(x), __float_as_uint(y), false, false);
  return __uint_as_float(r[0]) + __uint_as_float(r[1]);
}
DI void red8q(const float (&a)[8], float& c0, float& c1) {
  const float b0 = fold32(a[0], a[4]), b1 = fold32(a[1], a[5]), b2 = fold32(a[2], a[6]), b3 = fold32(a[3], a[7]);
  c0 = fold16(b0, b2); c1 = fold16(b1, b3);
  c0 = dpp_add<DPP_ROR4>(c0); c1 = dpp_add<DPP_ROR4>(c1);
  c0 = dpp_add<DPP_ROR8>(c0); c1 = dpp_add<DPP_ROR8>(c1);
}
DI void red8(const float (&a)[8], float& c0, float& c1) {
  red8q(a, c0, c1);
  c0 = dpp_add<DPP_X1>(c0); c1 = dpp_add<DPP_X1>(c1);
  c0 = dpp_add<DPP_X2>(c0); c1 = dpp_add<DPP_X2>(c1);
}
DI void publish(rsrc_t rs, int word, int bit, int lane) {
  drain();
  (void)__builtin_amdgcn_raw_ptr_buffer_atomic_add_i32(1 << bit, rs, lane == 0 ? 0 : WS_BYTES, OFF_ARR + word * 128, 0);
}
DI u32 ask(rsrc_t rs, int word, int lane) { return __builtin_amdgcn_raw_ptr_buffer_atomic_add_i32(0, rs, lane == 0 ? 0 : WS_BYTES, OFF_ARR + word * 128, 0); }
DI void spin(rsrc_t rs, int word, u32 m, int lane) {
#pragma nounroll
  for (u32 spins = 0; spins < SPIN_CAP; ++spins)
    if ((__builtin_amdgcn_readfirstlane(ask(rs, word, lane)) & m) == m) break;
}
DI void poll(u32 f, rsrc_t rs, int word, u32 m, int lane) {
  if (__builtin_expect((__builtin_amdgcn_readfirstlane(f) & m) != m, 0)) spin(rs, word, m, lane);
  __builtin_amdgcn_fence(__ATOMIC_ACQUIRE, "wavefront");
  RELOAD();
}
DI void waitn(rsrc_t rs, int word, u32 m, int lane) {
  if (PRE_FL) __builtin_amdgcn_s_sleep(PRE_FL);
  spin(rs, word, m, lane);
  __builtin_amdgcn_fence(__ATOMIC_ACQUIRE, "wavefront");
  RELOAD();
}
DI void sig(LAS unsigned char* L, int i, int k) { *(volatile LAS u32*)(L + L_HINT + 16 * i + 4 * k) = 1u; }
template <int N> DI void await(LAS unsigned char* L, int i) {
#pragma nounroll
  for (u32 spins = 0; spins < (1u << 20); ++spins) {
    const v4u v = *(volatile LAS v4u*)(L + L_HINT + 16 * i);
    if (__builtin_amdgcn_readfirstlane(N == 1 ? v.x : (v.x & v.y & v.z & v.w))) break;
    __builtin_amdgcn_s_sleep(1);
  }
  RELOAD();
}
DI void mark(LAS unsigned char* L, int i, int k, u32 val) { *(volatile LAS u32*)(L + L_HINT + 16 * i + 4 * k) = val; }
DI v4u peek(LAS unsigned char* L, int i) { return *(volatile LAS v4u*)(L + L_HINT + 16 * i); }
DI u32 umin(u32 a, u32 b) { return a < b ? a : b; }
DI void await_cp(LAS unsigned char* L, u32 k) {
#pragma nounroll
  for (u32 spins = 0; spins < (1u << 20); ++spins) {
    const v4u v = peek(L, H_CP);
    if (__builtin_amdgcn_readfirstlane(umin(umin(v.x, v.y), umin(v.z, v.w))) >= k) break;
    __builtin_amdgcn_s_sleep(CSLP);
  }
  RELOAD();
}
template <bool AUX> DI u32 pp_min(v4u v) {
  u32 m = umin(v.x, v.y);
  if (AUX) m = umin(m, umin(v.z, v.w));
  return __builtin_amdgcn_readfirstlane(m);
}
template <bool AUX> DI void need_pp(LAS unsigned char* L, v4u v, u32 k) {
  if (__builtin_expect(pp_min<AUX>(v) < k, 0)) {
#pragma nounroll
    for (u32 spins = 0; spins < (1u << 20); ++spins)
      if (pp_min<AUX>(peek(L, H_PP)) >= k) break;
  }
  RELOAD();
}
DI void feat(const f32x16& P, u32 (&ph)[16]) {
#pragma clang fp contract(off)
  constexpr float L2E = 1.4426950408889634f;
  f32x2 p[8];
#pragma unroll
  for (int i = 0; i < 8; ++i) { const u32 t = pk(P[2 * i], P[2 * i + 1]); p[i] = f32x2{lo16(t), hi16(t)} * L2E; }
  float mx[4], mn[4];
#pragma unroll
  for (int i = 0; i < 4; ++i) {
    mx[i] = fmaxf(fmaxf(p[2 * i].x, p[2 * i].y), fmaxf(p[2 * i + 1].x, p[2 * i + 1].y));
    mn[i] = fminf(fminf(p[2 * i].x, p[2 * i].y), fminf(p[2 * i + 1].x, p[2 * i + 1].y));
  }
  const float mxl = fmaxf(fmaxf(mx[0], mx[1]), fmaxf(mx[2], mx[3])), mnl = fminf(fminf(mn[0], mn[1]), fminf(mn[2], mn[3]));
  f32x2 e1[8], e2[8];
#pragma unroll
  for (int i = 0; i < 8; ++i) { e1[i] = p[i] - mxl; e2[i] = mnl - p[i]; }
  SB0();
#pragma unroll
  for (int i = 0; i < 8; ++i) {
    e1[i] = f32x2{__builtin_amdgcn_exp2f(e1[i].x), __builtin_amdgcn_exp2f(e1[i].y)};
    e2[i] = f32x2{__builtin_amdgcn_exp2f(e2[i].x), __builtin_amdgcn_exp2f(e2[i].y)};
  }
  const f32x2 s1 = ((e1[0] + e1[1]) + (e1[2] + e1[3])) + ((e1[4] + e1[5]) + (e1[6] + e1[7]));
  const f32x2 s2 = ((e2[0] + e2[1]) + (e2[2] + e2[3])) + ((e2[4] + e2[5]) + (e2[6] + e2[7]));
  const float r1 = rcp(s1.x + s1.y), r2 = rcp(s2.x + s2.y);
#pragma unroll
  for (int i = 0; i < 8; ++i) { const f32x2 u = e1[i] * r1, v = e2[i] * r2; ph[i] = pk(u.x, u.y); ph[8 + i] = pk(v.x, v.y); }
}

#define TSCLK() __builtin_amdgcn_s_memrealtime()
#define TS(i) do {} while (0)
#define TS_DUMP() do {} while (0)
#define TS_INIT() do {} while (0)

__global__ void __launch_bounds__(512, 2) mega(Params p) {
  extern __shared__ __attribute__((aligned(16))) unsigned char smem[];
  LAS unsigned char* L = (LAS unsigned char*)smem;
  TS_INIT();
  TS(0);
  u64 pc0;
  asm volatile("s_getpc_b64 %0" : "=s"(pc0));
  const int tid = threadIdx.x, lane = tid & 63;
  int w = __builtin_amdgcn_readfirstlane(tid >> 6);
  const int b = blockIdx.x, xcd = b & 7, rank = b >> 3, panel = (xcd >> 1) * 4 + (rank >> 3), mem = (xcd & 1) * 8 + (rank & 7);
  const int hh = lane >> 5, l31 = lane & 31;
  unsigned char* ws = p.ws;
  const int rowh = mem & 1, colg = mem >> 1;
  const int pair0 = min(3 * mem, 2 * mem + 11);
  const bool three = mem < 11;
  const rsrc_t rs = __builtin_amdgcn_make_buffer_rsrc(ws, 0, WS_BYTES, 0x00020000);
  const int pb = OFF_PAN + panel * PSTR, fw = panel * 24;
  const int mcg = xcd * 8 + (rank >> 2), mkp = rank & 3;
  const rsrc_t rqkv = __builtin_amdgcn_make_buffer_rsrc((void*)p.wqkv, 0, D * 3 * D * 2, 0x00020000);
  const rsrc_t rwo = __builtin_amdgcn_make_buffer_rsrc((void*)p.wo, 0, D * D * 2, 0x00020000);
  const rsrc_t rgu = __builtin_amdgcn_make_buffer_rsrc((void*)((w & 1) ? p.wu : p.wg), 0, D * F * 2, 0x00020000);
  const rsrc_t rwd = __builtin_amdgcn_make_buffer_rsrc((void*)p.wd, 0, F * D * 2, 0x00020000);

  v4u r0[4];
  u32 cv0[4], epoch;
  u32 vq = (lane >> 2) * (3 * D * 2) + (lane & 3) * 16;
  asm volatile("" : "+v"(vq));
  {
    const u32 ld = (u32)(size_t)L + w * 1024;
    if (w <= 4) {
      const rsrc_t r1 = __builtin_amdgcn_make_buffer_rsrc((void*)(w == 4 ? p.wmod : p.x), 0, w == 4 ? D * 6 * D * 2 : 2048 * D * 2, 0x00020000);
      const int vo = w == 4 ? (128 * mkp + 4 * (lane >> 1)) * (6 * D * 2) + mcg * 32 + (lane & 1) * 16
                            : (panel * 128 + mem * 8 + (lane >> 3)) * (D * 2) + (lane & 7) * 16 + 256 * w;
#pragma unroll
      for (int i = 0; i < 4; ++i) r0[i] = __builtin_amdgcn_raw_buffer_load_b128(r1, vo, w == 4 ? i * (6 * D * 2) : i < 2 ? i * 128 : 1 << 30, 0);
      SB0();
    }
    {
      const rsrc_t r2 = __builtin_amdgcn_make_buffer_rsrc((void*)p.c, 0, w == 4 ? D * 2 : 0, 0x00020000);
      const v2u c2 = __builtin_amdgcn_raw_buffer_load_b64(r2, (128 * mkp + 4 * (lane >> 1)) * 2, 0, 0);
      cv0[0] = c2.x; cv0[1] = c2.y;
      epoch = __builtin_amdgcn_raw_buffer_load_b32(rs, 0, OFF_EPOCH, AUX_SC1);
      __builtin_amdgcn_raw_buffer_store_b32(0u, rs, (w == 4 && lane < 2) ? lane * 128 : WS_BYTES, OFF_ARR + b * 256, AUX_SC1);
      SB0();
    }
    if (w >= 5) {
      const rsrc_t rc = __builtin_amdgcn_make_buffer_rsrc((void*)pc0, 0, CODE_KB * 1024, 0x00020000);
      gldsi<slot(128), 0>(rc, lane < CODE_KB / 6 ? (u32)lane * 128u : 1u << 30, (u32)(((rank & 15) * 3 + w - 5) * (CODE_KB / 6) * 128), ld);
      const u32 tq = (rank * 16 + (lane >> 2)) * (3 * D * 2) + (xcd & 1) * 512 + (lane & 3) * 128;
      if (w != 5) gldsi<slot(128), 0>(rqkv, tq, (w - 5) * (D * 2), ld);
      else {
        gldsi<slot(128), 0>(rs, lane < 2 ? (u32)(pb + lane * (848 << 10)) : 1u << 30, 0u, ld);
        const rsrc_t rp = __builtin_amdgcn_make_buffer_rsrc((void*)p.wphi, 0, 32 * 256 * 2, 0x00020000);
        gldsi<L_CF + 2048, 0>(rp, lane * 16, mem * 1024, (u32)(size_t)L);
      }
    }
    if (tid < 32) *((LAS u32*)(L + L_HINT) + tid) = 0u;
    TS(27);
    bar();
    asm volatile("" : "+s"(w));
    if (w >= 6) {
      u32 lo = (u32)(size_t)L + (w & 1) * 1024, so = (D + (w & 1) * D + mem * 32) * 2;
#pragma nounroll
      for (int i = 0; i < KV1 / 2; ++i) {
        gldsi<0, 0>(rqkv, vq, so, lo);
        gldsi<2048, 16 * (3 * D * 2)>(rqkv, vq, so, lo);
        lo += 4096; so += 32 * (3 * D * 2);
      }
    }
    if (w == 4) {
      __builtin_amdgcn_s_setprio(3);
      TS(10);
      float sl[4], acc[8];
#pragma unroll
      for (int i = 0; i < 4; ++i) { const float c = (i & 1) ? hi16(cv0[i >> 1]) : lo16(cv0[i >> 1]); sl[i] = rb(c * rcp(1.f + __expf(-c))); }
      u32 tag = epoch;
      asm volatile("" : "+v"(tag));
      tag += 1u;
      TS(11);
#pragma unroll
      for (int j = 0; j < 8; ++j) acc[j] = 0.f;
#pragma unroll
      for (int i = 0; i < 4; ++i)
#pragma unroll
        for (int j = 0; j < 4; ++j) { acc[2 * j] += sl[i] * lo16(r0[i][j]); acc[2 * j + 1] += sl[i] * hi16(r0[i][j]); }
      float c0, c1;
      TS(12);
      red8q(acc, c0, c1);
      c0 = dpp_add<DPP_X2>(c0); c1 = dpp_add<DPP_X2>(c1);
      const int go = ((lane >> 1) & 7) * 32768 + (8 * (lane & 1) + 2 * (lane >> 4)) * 8;
      const v4u gv = {__float_as_uint(c0), tag, __float_as_uint(c1), tag};
      drain();
      st128(gv, rs, go, OFF_MODR + mkp * 8192 + mcg * 128);
      st128(gv, rs, go, OFF_MODR + 8 * 32768 + mkp * 8192 + mcg * 128);
      sig(L, 0, 0);
    }
    asm volatile("" : "+s"(w));
  }

  if (w == 4 || w == 5) {
    const int a = w & 1;
    u32 tag;
    tag = epoch;
    asm volatile("" : "+v"(tag));
    tag += 1u;
    drain();
    TS(1);
    bar();
    __builtin_amdgcn_s_setprio(APRIO);
    await<4>(L, 2);
    const int kp = xcd >> 1, cpair = (xcd & 1) * 24 + rank;
    const bool con = rank < ((xcd & 1) ? 19 : 24);
    const int col0 = a * F + cpair * 32;
    v4u wm[8];
#pragma unroll
    for (int i = 0; i < 8; ++i)
      wm[i] = __builtin_amdgcn_raw_buffer_load_b128(rgu, con ? (128 * kp + 2 * (lane >> 2)) * (F * 2) + cpair * 64 + (lane & 3) * 16 : 1 << 30, (32 * (i >> 1) + (i & 1)) * (F * 2), 0);
    SB0();
    if (a == 1) {
      v4u wn[8]; u32 cv[8];
      const int row0 = 128 * mkp + (lane >> 2);
#pragma unroll
      for (int i = 0; i < 8; ++i) cv[i] = p.c[row0 + 16 * i];
#pragma unroll
      for (int i = 0; i < 8; ++i) wn[i] = *(const GAS v4u*)(p.wmod + (size_t)(row0 + 16 * i) * (6 * D) + 1024 + mcg * 32 + (lane & 3) * 8);
      SB0();
      bar_raw();
      SB0();
      float acc[8];
#pragma unroll
      for (int j = 0; j < 8; ++j) acc[j] = 0.f;
#pragma unroll
      for (int i = 0; i < 8; ++i) {
        const float c = lo16(cv[i]), sl = rb(c * rcp(1.f + __expf(-c)));
#pragma unroll
        for (int j = 0; j < 4; ++j) { acc[2 * j] += sl * lo16(wn[i][j]); acc[2 * j + 1] += sl * hi16(wn[i][j]); }
      }
      float c0, c1;
      red8q(acc, c0, c1);
      st128(v4u{__float_as_uint(c0), tag, __float_as_uint(c1), tag}, rs, (lane & 12) == 0 ? (8 * (lane & 3) + 2 * (lane >> 4)) * 8 : WS_BYTES,
            OFF_MODG + mkp * 16384 + mcg * 256);
    } else {
      bar_raw();
    }
    bar_raw();
    TS(2);
    {
      const int idx0 = (mem * 4 + 2 * a) * 16;
      v4u e[32], zz[2];
      {
        u32 todo = 0xFFu;
        for (u32 spins = 0; spins < SPIN_CAP && todo; ++spins) {
          const u32 bad = ~__builtin_amdgcn_readfirstlane(ask(rs, FL_KV + mem * 2 + a, lane));
          __builtin_amdgcn_fence(__ATOMIC_ACQUIRE, "wavefront");
          RELOAD();
          static_for<0, 8>([&](auto G) {
            constexpr int g = G.value;
            if (((todo >> g) & 1u) && !(bad & (0x00030003u << (2 * g)))) {
              todo &= ~(1u << g);
              e[2 * g] = ld128(rs, lane * 16, OFF_KVE + (idx0 + 2 * g) * 1024);
              e[2 * g + 1] = ld128(rs, lane * 16, OFF_KVE + (idx0 + 2 * g + 1) * 1024);
              e[16 + 2 * g] = ld128(rs, lane * 16, OFF_KVE + (idx0 + 16 + 2 * g) * 1024);
              e[17 + 2 * g] = ld128(rs, lane * 16, OFF_KVE + (idx0 + 17 + 2 * g) * 1024);
            }
          });
        }
      }
      TS(3);
      zz[0] = ldfrag<0>(rs, lane * 16, OFF_KVZ + idx0 * 64);
      zz[1] = ldfrag<1>(rs, lane * 16, OFF_KVZ + idx0 * 64);
      SB0();
#pragma unroll
      for (int q = 0; q < 2; ++q) {
        f32x4 s0 = __builtin_bit_cast(f32x4, e[16 * q]), s1 = __builtin_bit_cast(f32x4, e[16 * q + 1]);
#pragma unroll
        for (int i = 2; i < 16; i += 2) { s0 += __builtin_bit_cast(f32x4, e[16 * q + i]); s1 += __builtin_bit_cast(f32x4, e[16 * q + i + 1]); }
        s0 += s1;
        *(LAS v2u*)(L + L_KVF + (2 * a + q) * 512 + l31 * 16 + hh * 8) = v2u{pk(s0[0], s0[1]), pk(s0[2], s0[3])};
      }
      f32x4 z;
#pragma unroll
      for (int j = 0; j < 4; ++j) {
        float t = fold32(__uint_as_float(zz[0][j]), __uint_as_float(zz[1][j]));
        t = fold16(t, t);
        t = dpp_add<DPP_ROR4>(t);
        z[j] = dpp_add<DPP_ROR8>(t);
      }
      if (l31 < 4) *(LAS v2u*)(L + L_ZF + (2 * a + hh) * 32 + ((lane & 1) ^ 1) * 16 + ((lane >> 1) & 1) * 8) = v2u{pk(z[0], z[1]), pk(z[2], z[3])};
    }
    TS(4);
    bar();
    u32 gsb[4], gsc[4];
    {
      const int vo = ((hh ? 2 : 3 * a) * D + colg * 64 + 2 * l31) * 8, ro = (D + 128 * kp + 2 * lane) * 8;
      v4u g[12];
      for (u32 spins = 0; spins < SPIN_CAP; ++spins) {
        RELOAD();
#pragma unroll
        for (int i = 0; i < 12; ++i) g[i] = ld128(rs, i < 4 ? vo : ro + (i >> 3) * (D * 8), OFF_MODG + (i & 3) * 16384);
        bool ok = true;
#pragma unroll
        for (int i = 0; i < 12; ++i) ok &= (g[i].y == tag) & (g[i].w == tag);
        if (__all(ok)) break;
      }
      u32 pr[3];
#pragma unroll
      for (int t = 0; t < 3; ++t)
        pr[t] = pk((__uint_as_float(g[4 * t].x) + __uint_as_float(g[4 * t + 1].x)) + (__uint_as_float(g[4 * t + 2].x) + __uint_as_float(g[4 * t + 3].x)),
                   (__uint_as_float(g[4 * t].z) + __uint_as_float(g[4 * t + 1].z)) + (__uint_as_float(g[4 * t + 2].z) + __uint_as_float(g[4 * t + 3].z)));
      const u32 v1 = pk(1.f + lo16(pr[0]), 1.f + hi16(pr[0])), vv = hh ? v1 : pr[0];
      *(LAS f32x2*)(L + L_VEC + (hh ? 1 : 2 * a) * 256 + l31 * 8) = f32x2{lo16(vv), hi16(vv)};
      const u32 sc = pk(1.f + lo16(pr[2]), 1.f + hi16(pr[2]));
#pragma unroll
      for (int i = 0; i < 4; ++i) {
        gsb[i] = __builtin_amdgcn_ds_bpermute((16 * i + (lane >> 2)) * 4, pr[1]);
        gsc[i] = __builtin_amdgcn_ds_bpermute((16 * i + (lane >> 2)) * 4, sc);
      }
    }
    if (con) {
      float a1[8], a2[8];
#pragma unroll
      for (int j = 0; j < 8; ++j) { a1[j] = 0.f; a2[j] = 0.f; }
#pragma unroll
      for (int i = 0; i < 8; ++i) {
        const float sb = (i & 1) ? hi16(gsb[i >> 1]) : lo16(gsb[i >> 1]), sc = (i & 1) ? hi16(gsc[i >> 1]) : lo16(gsc[i >> 1]);
#pragma unroll
        for (int j = 0; j < 4; ++j) {
          a1[2 * j] += sc * lo16(wm[i][j]); a1[2 * j + 1] += sc * hi16(wm[i][j]);
          a2[2 * j] += sb * lo16(wm[i][j]); a2[2 * j + 1] += sb * hi16(wm[i][j]);
        }
      }
      float c10, c11, c20, c21;
      red8q(a1, c10, c11); red8q(a2, c20, c21);
      const int k = (lane >> 2) & 3;
      const float val = k == 0 ? c10 : k == 1 ? c20 : k == 2 ? c11 : c21;
      st64(v2u{__float_as_uint(val), tag}, rs, (col0 + 8 * (lane & 3) + 2 * (lane >> 4)) * 16 + k * 8, OFF_CG + kp * CG_PART);
    }
    TS(5);
    bar();
    {
      const int ntile = three ? 6 : 4;
      v4u cg[8];
      int off[2];
#pragma unroll
      for (int i = 0; i < 2; ++i) {
        int tile = 4 * i + 2 * a + hh; tile = tile < ntile ? tile : 0;
        off[i] = (32 * pair0 + 16 * tile + (lane & 15) + ((lane >> 4) & 1) * F) * 16;
      }
      for (u32 spins = 0; spins < SPIN_CAP; ++spins) {
        RELOAD();
        bool ok = true;
#pragma unroll
        for (int i = 0; i < 8; ++i) cg[i] = ld128(rs, off[i >> 2], OFF_CG + (i & 3) * CG_PART);
#pragma unroll
        for (int i = 0; i < 8; ++i) ok &= (cg[i].y == tag) & (cg[i].w == tag);
        if (__all(ok)) break;
      }
#pragma unroll
      for (int i = 0; i < 2; ++i) {
        const float c1 = (__uint_as_float(cg[4 * i].x) + __uint_as_float(cg[4 * i + 1].x)) + (__uint_as_float(cg[4 * i + 2].x) + __uint_as_float(cg[4 * i + 3].x));
        const float c2 = (__uint_as_float(cg[4 * i].z) + __uint_as_float(cg[4 * i + 1].z)) + (__uint_as_float(cg[4 * i + 2].z) + __uint_as_float(cg[4 * i + 3].z));
        const float h1 = rb(c1), m1 = rb(c1 - h1), l1 = c1 - h1 - m1, h2 = rb(c2), m2 = rb(c2 - h2), l2 = c2 - h2 - m2;
        const int tile = 4 * i + 2 * a + hh;
        if (tile < 6) *(LAS v4u*)(L + L_CF + tile * 512 + l31 * 16) = v4u{pk(h1, m1), pk(l1, h2), pk(m2, l2), 0u};
      }
    }
    await_cp(L, 1);
    {
      v4u st[16];
#pragma unroll
      for (int t = 0; t < 16; ++t) st[t] = ld128(rs, l31 * 16 + hh * 8192 + t * 512, pb + PN_ST + 2 * a * 8192);
      for (u32 spins = 0; spins < SPIN_CAP; ++spins) {
        bool ok = true;
#pragma unroll
        for (int t = 0; t < 16; ++t) ok &= (st[t].y == tag) & (st[t].w == tag);
        if (__all(ok)) break;
        RELOAD();
#pragma unroll
        for (int t = 0; t < 16; ++t) st[t] = ld128(rs, l31 * 16 + hh * 8192 + t * 512, pb + PN_ST + 2 * a * 8192);
      }
      float ms = 0.f;
#pragma unroll
      for (int t = 0; t < 16; ++t) ms += __uint_as_float(st[t].x);
      const float mean = ms * (1.f / 16.f);
      float q = 0.f;
#pragma unroll
      for (int t = 0; t < 16; ++t) { const float d = __uint_as_float(st[t].x) - mean; q += __uint_as_float(st[t].z) + 32.f * d * d; }
      const float den = rb(__builtin_amdgcn_sqrtf(rb(rb(q * (1.f / D)) + 1e-5f)));
      *(LAS v4u*)(L + L_KVF + a * 1024 + lane * 16) = v4u{pk(-mean, -mean), pk(-mean, den), pk(den, den), __float_as_uint(rcp(den * den))};
    }
    RELOAD();
    mark(L, H_PP, 2 + a, 5u);
    TS(6);
    {
      v4u wt[58];
      const u32 vd = (lane >> 2) * (D * 2) + (lane & 3) * 16, sd = a * 16 * (D * 2) + colg * 128;
      const Base3 wdst(L + a * 2048 + lane * 16);
      static_for<28, 86>([&](auto J) {
        wt[J.value - 28] = __builtin_amdgcn_raw_buffer_load_b128(rwd, NBV(vd + (J.value & 1) * 64), sd + pair_of(J.value >> 1) * 32 * (D * 2), 0); SB0(); });
      TS(7);
#define ST_WT(T0, T1, k) do { static_for<2 * (T0), 2 * (T1)>([&](auto J) { \
      *(LAS v4u*)wdst.template at<slot(P_WD + 4 * (J.value >> 1)) + (J.value & 1) * 1024>() = wt[J.value - 28]; }); \
      RELOAD(); mark(L, H_PP, 2 + a, k); RELOAD(); } while (0)
      await_cp(L, 4);
      ST_WT(14, 18, 6u);
      await_cp(L, 5);
      ST_WT(18, 26, 7u);
      await_cp(L, 6);
      ST_WT(26, 34, 8u);
      TS(8);
      await_cp(L, 7);
      ST_WT(34, 43, 9u);
      TS(9);
    }
    TS_DUMP();
    return;
  }

  if (w >= 6) {
    const int v = w & 1;
    __builtin_amdgcn_s_setprio(LPRIO);
    const u32 ldsb = (u32)(size_t)L;
    const u32 l1 = ldsb + v * 1024, l2 = ldsb + v * 2048;
    const u32 skv = (D + v * D + mem * 32) * 2, sq = v * 16 * (3 * D * 2) + mem * 64;
    const u32 so = 16 * v * 16 * (D * 2) + colg * 128, sd = v * 16 * (D * 2) + colg * 128;
    const u32 sg = pair0 * 64, sg2 = sg + (three ? 128 : 64);
#define LD_KV(S0, S1, PC) static_for<S0, S1>([&](auto S) { gldsi<slot(P_KV + 2 * S.value), S.value * 16 * (3 * D * 2)>(rqkv, vq, skv, l1); PACE(PC); })
#define PACE(n) do { if ((n) > 0) __builtin_amdgcn_s_sleep(n); } while (0)
#define LD_Q(S0, S1, PC) static_for<S0, S1>([&](auto S) { gldsi<slot(P_Q + 2 * S.value), 2 * S.value * 16 * (3 * D * 2)>(rqkv, vq, sq, l1); PACE(PC); })
#define LD_DN(RS, SB, P0, T0, T1, PC)                                                                    \
  static_for<T0, T1>([&](auto T) {                                                                   \
    gldsi<slot((P0) + 4 * T.value), T.value * 16 * (D * 2)>(RS, vd, SB, l2);                         \
    gldsi<slot((P0) + 4 * T.value) + 1024, T.value * 16 * (D * 2) + 64>(RS, vd, SB, l2); PACE(PC);   \
  })
#define LD_GU(S0, S1, PC)                                                                                \
  static_for<S0, S1>([&](auto S) {                                                                   \
    gldsi<slot(P_GU + 6 * S.value), S.value * 16 * (F * 2)>(rgu, vg, sg, l1);                        \
    gldsi<slot(P_GU + 6 * S.value + 2), S.value * 16 * (F * 2) + 64>(rgu, vg, sg, l1);               \
    gldsi<slot(P_GU + 6 * S.value + 4), S.value * 16 * (F * 2)>(rgu, vg, sg2, l1); PACE(PC);         \
  })
    TS(1);
    VMCNT(0);
    SB0();
    int ln;
    asm volatile("v_mbcnt_lo_u32_b32 %0, -1, 0\n\tv_mbcnt_hi_u32_b32 %0, -1, %0" : "=v"(ln));
    const u32 vd = (ln >> 2) * (D * 2) + (ln & 3) * 16;
    const u32 vg = (ln >> 2) * (F * 2) + (((ln & 3) ^ (v << 1)) * 16);
    TS(2);
    await<4>(L, 3);
    LD_KV(KV1, 32, PKV);
    VMCNT(0);
    TS(11);
    bar_raw();
    v4u wt[48];
    const Base3 wdst1(L + v * 1024 + ln * 16);
#define GUN(s_, P_) (P_GU + ((s_) < 16 ? 6 * (s_) + 2 * (P_) : 96 + 32 * (P_) + 2 * ((s_) - 16)))
#define GUR(s_, P_) __builtin_amdgcn_raw_buffer_load_b128(rgu, NBV(vg), ((P_) == 2 ? sg2 : sg + (P_) * 64) + (s_) * 16 * (F * 2), 0)
#define GUW(s_, P_, r_) *(LAS v4u*)wdst1.template at<slot(GUN(s_, P_))>() = wt[r_]
#define LD_GU2(S0, S1, PC) static_for<S0, S1>([&](auto S) { constexpr int s_ = S.value; \
      gldsi<slot(GUN(s_, 0)), s_ * 16 * (F * 2)>(rgu, vg, sg, l1); SB0(); \
      wt[2 * (s_ - 16)] = GUR(s_, 1); wt[2 * (s_ - 16) + 1] = GUR(s_, 2); PACE(PC); SB0(); })
#define LD_WD(T0, T1) static_for<T0, T1>([&](auto T) { \
      gldsi<slot(P_WD + 4 * T.value), pair_of(T.value) * 32 * (D * 2)>(rwd, vd, sd, l2); \
      gldsi<slot(P_WD + 4 * T.value) + 1024, pair_of(T.value) * 32 * (D * 2) + 64>(rwd, vd, sd, l2); })
#define DONE(k) do { RELOAD(); mark(L, H_PP, v, k); RELOAD(); } while (0)
    await<4>(L, 2);
    gldsi<L_CF, 0>(v ? rwo : rqkv, (rank * 16 + (ln >> 2)) * (v ? D * 2 : 3 * D * 2) + (xcd & 1) * 512 + (ln & 3) * 128, 0u, l1);
    LD_Q(0, 16, 0);
    TS(3);
    bar_raw();
    VMCNT(0);
    bar_raw();
    gldsi<L_CF, 0>(rwd, v ? 1u << 30 : ((xcd >> 1) * 32 + rank + 128 * (ln >> 2)) * (D * 2) + (xcd & 1) * 512 + (ln & 3) * 128, 0u, l1);
    LD_DN(rwo, so, P_WO, 0, 16, 1);
    LD_GU(0, 6, PGU);
    VMCNT(18);
    TS(4);
    bar_raw();
    LD_GU(6, 12, PGU);
    await<4>(L, 4);
    static_for<0, 12>([&](auto J) { wt[32 + J.value] = GUR(12 + J.value / 3, J.value % 3); SB0(); });
    static_for<16, 20>([&](auto S) { constexpr int s_ = S.value;
      wt[28 + s_] = GUR(s_, 0); wt[2 * (s_ - 16)] = GUR(s_, 1); wt[2 * (s_ - 16) + 1] = GUR(s_, 2); SB0(); });
    VMCNT(24);
    TS(5);
    bar_raw();
    static_for<0, 12>([&](auto J) { GUW(12 + J.value / 3, J.value % 3, 32 + J.value); });
    static_for<16, 20>([&](auto S) { GUW(S.value, 0, 28 + S.value); });
    DONE(1);
    LD_GU2(20, 32, PG2);
    VMCNT(0);
    DONE(3);
    TS(6);
    await_cp(L, 1);
    {
      const u32 tdn = ((2 * rank + v) * 16 + (ln >> 2)) * (D * 2) + (xcd & 1) * 512 + (ln & 3) * 128;
      __builtin_amdgcn_s_waitcnt(0x0F70);
      __builtin_amdgcn_s_sleep(4);
      gldsi<L_T, 0>(rwd, tdn, 0u, l1);
      gldsi<L_T, 1024 * (D * 2)>(rwd, tdn, 0u, l1);
    }
    static_for<16, 32>([&](auto S) { GUW(S.value, 1, 2 * (S.value - 16)); });
    DONE(4);
    await_cp(L, 2);
    static_for<16, 32>([&](auto S) { GUW(S.value, 2, 2 * (S.value - 16) + 1); });
    DONE(5);
    TS(7);
    await_cp(L, 3);
    LD_WD(0, 10);
    await_cp(L, 4);
    LD_WD(10, 14);
    VMCNT(0);
    DONE(100);
    TS(8);
    TS_DUMP();
    return;
  }

  __builtin_amdgcn_s_setprio(2);
  const int s2 = w & 1, kh = w >> 1;
  const int strip2 = rowh * 2 + s2, ncol = colg * 64 + kh * 32;
  v4u aphi0, aphi1;
  u32 tag;
  {
    float xf[16];
#pragma unroll
    for (int i = 0; i < 8; ++i) { xf[2 * i] = lo16(r0[i >> 2][i & 3]); xf[2 * i + 1] = hi16(r0[i >> 2][i & 3]); }
    float sm = (((xf[0] + xf[1]) + (xf[2] + xf[3])) + ((xf[4] + xf[5]) + (xf[6] + xf[7]))) + (((xf[8] + xf[9]) + (xf[10] + xf[11])) + ((xf[12] + xf[13]) + (xf[14] + xf[15])));
    sm = dpp_add<DPP_X1>(sm); sm = dpp_add<DPP_X2>(sm); sm = dpp_add<DPP_HM>(sm);
    const float mw = sm * (1.f / 128.f);
    float q[4];
#pragma unroll
    for (int i = 0; i < 4; ++i) {
      const float d0 = xf[4 * i] - mw, d1 = xf[4 * i + 1] - mw, d2 = xf[4 * i + 2] - mw, d3 = xf[4 * i + 3] - mw;
      q[i] = (d0 * d0 + d1 * d1) + (d2 * d2 + d3 * d3);
    }
    float qq = (q[0] + q[1]) + (q[2] + q[3]);
    qq = dpp_add<DPP_X1>(qq); qq = dpp_add<DPP_X2>(qq); qq = dpp_add<DPP_HM>(qq);
    *(volatile LAS f32x2*)(L + L_KVF + (lane >> 3) * 32 + w * 8) = f32x2{mw, qq};
    sig(L, 1, w);
    await<4>(L, 1);
    const f32x4 pa = *(LAS f32x4*)(L + L_KVF + (lane >> 3) * 32), pc = *(LAS f32x4*)(L + L_KVF + (lane >> 3) * 32 + 16);
    const float mean = ((pa[0] + pa[2]) + (pc[0] + pc[2])) * 0.25f;
    const float f0 = pa[0] - mean, f1 = pa[2] - mean, f2 = pc[0] - mean, f3 = pc[2] - mean;
    const float m2 = ((pa[1] + pa[3]) + (pc[1] + pc[3])) + 128.f * ((f0 * f0 + f1 * f1) + (f2 * f2 + f3 * f3));
    const float m1 = rb(mean), r1 = rcp(rb(__builtin_amdgcn_sqrtf(rb(rb(m2 * (1.f / D)) + 1e-5f))));
    float xn[16];
#pragma unroll
    for (int i = 0; i < 8; ++i) {
      const u32 d = pk(xf[2 * i] - m1, xf[2 * i + 1] - m1);
      const u32 t = pk(lo16(d) * r1, hi16(d) * r1);
      xn[2 * i] = lo16(t); xn[2 * i + 1] = hi16(t);
    }
#pragma unroll
    for (int i = 0; i < 16; ++i) asm volatile("" : "+v"(xn[i]));
    tag = __builtin_amdgcn_readfirstlane(epoch) + 1u;
    TS(1);
    await<1>(L, 0);
    __builtin_amdgcn_s_sleep(PRE_MOD);
    TS(21);
    v4u g[8];
    {
      u32 todo = 0xFFu;
      for (u32 spins = 0; spins < SPIN_CAP && todo; ++spins) {
        RELOAD();
        static_for<0, 8>([&](auto I) {
          constexpr int i = I.value;
          if ((todo >> i) & 1u) g[i] = ld128(rs, lane * 16, OFF_MODR + panel * 32768 + (i & 3) * 8192 + (i >> 2) * 4096 + w * 1024);
        });
        todo = 0;
        static_for<0, 8>([&](auto I) { if (__builtin_amdgcn_ballot_w64((g[I.value].y != tag) | (g[I.value].w != tag))) todo |= 1u << I.value; });
      }
    }
    {
      u32 pr[2];
#pragma unroll
      for (int t = 0; t < 2; ++t)
        pr[t] = pk((__uint_as_float(g[4 * t].x) + __uint_as_float(g[4 * t + 1].x)) + (__uint_as_float(g[4 * t + 2].x) + __uint_as_float(g[4 * t + 3].x)),
                   (__uint_as_float(g[4 * t].z) + __uint_as_float(g[4 * t + 1].z)) + (__uint_as_float(g[4 * t + 2].z) + __uint_as_float(g[4 * t + 3].z)));
      *(LAS u32*)(L + L_T + w * TW + lane * 4) = pr[0];
      *(LAS u32*)(L + L_T + w * TW + 256 + lane * 4) = pk(1.f + lo16(pr[1]), 1.f + hi16(pr[1]));
    }
    v4u sav[2], cav[2];
#pragma unroll
    for (int i = 0; i < 2; ++i) {
      sav[i] = *(LAS v4u*)(L + L_T + w * TW + i * 128 + (lane & 7) * 16);
      cav[i] = *(LAS v4u*)(L + L_T + w * TW + 256 + i * 128 + (lane & 7) * 16);
    }
    sig(L, 3, w);
    TS(2);
    const int strip = mem >> 2, r = 8 * (mem & 3) + (lane >> 3);
#pragma unroll
    for (int i = 0; i < 2; ++i) {
      v4u o;
#pragma unroll
      for (int j = 0; j < 4; ++j) {
        const u32 sa = sav[i][j], sc = cav[i][j];
        const u32 t = pk(xn[8 * i + 2 * j] * lo16(sc), xn[8 * i + 2 * j + 1] * hi16(sc));
        o[j] = pk(lo16(t) + lo16(sa), hi16(t) + hi16(sa));
      }
      st128(o, rs, (4 * i + ((lane & 7) >> 1)) * 1024 + (r + 32 * (lane & 1)) * 16, pb + PN_H + strip * 32768 + w * 8192);
    }
    await<4>(L, 3);
    publish(rs, fw + FL_H + strip, (mem & 3) * 4 + w, lane);
  }
  TS(3);

  const int trq = (lane & 15) >> 2, trp = lane & 3, trb = (lane >> 4) & 1;
  const f32x16 zero16 = {0.f, 0.f, 0.f, 0.f, 0.f, 0.f, 0.f, 0.f, 0.f, 0.f, 0.f, 0.f, 0.f, 0.f, 0.f, 0.f};
  u32 phq[16];
  v4u wb[PD];
  {
    const Base3 wnat(L + (8 * hh + trq) * 64 + trb * 32 + trp * 8);
    waitn(rs, fw + FL_H + w, 0xFFFFu, lane);
    TS(4);
    v4u a[32];
    ldfrags<W0>(a, rs, lane * 16, pb + PN_H + w * 32768);
    bar_raw();
    {
      const int hs = (l31 >> 2) & 1;
      const LAS u16* wp = (const LAS u16*)(L + L_CF + 2048) + hs * 256 + 4 * hh * 16 + (l31 & 3) + 4 * (l31 >> 3);
      u32 e[8];
#pragma unroll
      for (int j = 0; j < 8; ++j) e[j] = wp[(8 * (j >> 2) + (j & 3)) * 16];
      const v4u av = {e[0] | (e[1] << 16), e[2] | (e[3] << 16), e[4] | (e[5] << 16), e[6] | (e[7] << 16)};
      const v4u z = {0, 0, 0, 0};
      aphi0 = hs == 0 ? av : z; aphi1 = hs == 1 ? av : z;
      asm volatile("" : "+v"(aphi0), "+v"(aphi1));
    }
    static_for<0, PD>([&](auto U) { wb[U.value] = ldw<256>(wnat.at<slot(P_KV + U.value)>()); });
    SB0();
    f32x16 kacc = zero16, vacc = zero16;
    static_for<0, 64>([&](auto U) {
      constexpr int u = U.value;
      const v4u cur = wb[u % PD];
      if constexpr (u + PD < 64) wb[u % PD] = ldw<256>(wnat.at<slot(P_KV + u + PD)>());
      if constexpr (u & 1) mfma_pin(a[u >> 1], cur, vacc); else mfma_pin(cur, a[u >> 1], kacc);
      if constexpr (!(u & 1) && (u >> 1) + W0 < 32) a[(u >> 1) + W0] = ldfrag<(u >> 1) + W0>(rs, lane * 16, pb + PN_H + w * 32768);
      if constexpr (u == 2 * (32 - W0)) sig(L, 2, w);
      SB0();
    });
    TS(5);
    f32x16 P = mfma(aphi0, pack8<0>(kacc), zero16);
    P = mfma(aphi1, pack8<8>(kacc), P);
    const v4u vb0 = pack8<0>(vacc), vb1 = pack8<8>(vacc);
    u32 phk[16];
    feat(P, phk);
    LAS unsigned char* T = L + L_T + w * TW;
#pragma unroll
    for (int c = 0; c < 4; ++c)
      *(LAS v4u*)(T + lane * 64 + ((c ^ ((lane >> 1) & 3)) * 16)) = v4u{phk[4 * c], phk[4 * c + 1], phk[4 * c + 2], phk[4 * c + 3]};
    const v4u ones = {0x3F803F80u, 0x3F803F80u, 0x3F803F80u, 0x3F803F80u};
    LAS unsigned char* tr = T + (4 * hh + trq) * 64 + (((2 * trb + (trp >> 1)) ^ ((2 * hh + (trq >> 1)) & 3)) * 16) + 8 * (trp & 1);
    const v4u t0 = ldw<512>(tr), t1 = ldw<512>(tr + 1024), t2 = ldw<512>(tr + 2048), t3 = ldw<512>(tr + 3072);
    f32x16 D0 = mfma(t0, trb ? ones : vb0, zero16);
    f32x16 D1 = mfma(t2, trb ? vb0 : ones, zero16);
    D0 = mfma(t1, trb ? ones : vb1, D0);
    D1 = mfma(t3, trb ? vb1 : ones, D1);
    {
      LAS unsigned char* pe = T + lane * 16;
      LAS unsigned char* pz = T + 1024 + (lane >> 4) * 16;
      LAS unsigned char* p0 = trb ? pz : pe;
      LAS unsigned char* p1 = trb ? pe : pz;
#pragma unroll
      for (int g = 0; g < 4; ++g) *(LAS f32x4*)(p0 + g * 1088) = f32x4{D0[4 * g], D0[4 * g + 1], D0[4 * g + 2], D0[4 * g + 3]};
#pragma unroll
      for (int g = 0; g < 4; ++g) *(LAS f32x4*)(p1 + g * 1088) = f32x4{D1[4 * g], D1[4 * g + 1], D1[4 * g + 2], D1[4 * g + 3]};
    }
    TS(6);
    bar();
    {
      f32x4 e[4], z[4];
#pragma unroll
      for (int v = 0; v < 4; ++v) {
        e[v] = *(LAS f32x4*)(L + L_T + v * TW + w * 1088 + lane * 16);
        z[v] = *(LAS f32x4*)(L + L_T + v * TW + w * 1088 + 1024 + (lane & 3) * 16);
      }
      SB0();
      const int idx = (mem * 4 + w) * 16 + panel;
      st128(__builtin_bit_cast(v4u, (e[0] + e[1]) + (e[2] + e[3])), rs, lane * 16, OFF_KVE + idx * 1024);
      st128(__builtin_bit_cast(v4u, (z[0] + z[1]) + (z[2] + z[3])), rs, lane < 4 ? lane * 16 : WS_BYTES, OFF_KVZ + idx * 64);
      publish(rs, FL_KV + mem * 2 + (w >> 1), (w & 1) * 16 + panel, lane);
      bar_raw();
      static_for<0, PD>([&](auto U) { wb[U.value] = ldw<256>(wnat.at<slot(P_Q + U.value)>()); });
    }
    TS(7);
    f32x16 qacc = zero16;
    static_for<0, 32>([&](auto U) {
      constexpr int u = U.value;
      const v4u cur = wb[u % PD];
      if constexpr (u + PD < 32) wb[u % PD] = ldw<256>(wnat.at<slot(P_Q + u + PD)>());
      mfma_pin(cur, a[u], qacc);
      SB0();
    });
    P = mfma(aphi0, pack8<0>(qacc), zero16);
    P = mfma(aphi1, pack8<8>(qacc), P);
    feat(P, phq);
#pragma unroll
    for (int i = 0; i < 16; ++i) asm volatile("" : "+v"(phq[i]));
  }
  v2u xres[4];
  {
    const u16* xr = p.x + (size_t)(panel * 128 + strip2 * 32 + l31) * D + ncol + 4 * hh;
#pragma unroll
    for (int i = 0; i < 4; ++i) xres[i] = *(const GAS v2u*)(xr + 8 * i);
  }
  TS(8);
  bar_raw();
  TS(9);
  {
    const bool on = (l31 >> 4) == hh;
    const v4u zv = {0, 0, 0, 0};
    f32x16 num = zero16, den = zero16;
    v4u ak[4], az[4];
#pragma unroll
    for (int s4 = 0; s4 < 4; ++s4) { az[s4] = *(LAS v4u*)(L + L_ZF + s4 * 32 + hh * 16); ak[s4] = *(LAS v4u*)(L + L_KVF + s4 * 512 + l31 * 16); }
    SB0();
#pragma unroll
    for (int s4 = 0; s4 < 4; ++s4) den = mfma(on ? az[s4] : zv, v4u{phq[4 * s4], phq[4 * s4 + 1], phq[4 * s4 + 2], phq[4 * s4 + 3]}, den);
#pragma unroll
    for (int s4 = 0; s4 < 4; ++s4) num = mfma(on ? ak[s4] : zv, v4u{phq[4 * s4], phq[4 * s4 + 1], phq[4 * s4 + 2], phq[4 * s4 + 3]}, num);
    const float d0 = rcp(rb(rb(den[0]) + 1e-6f)), d1 = rcp(rb(rb(den[8]) + 1e-6f));
    const v4u n0 = pack8<0>(num), n1 = pack8<8>(num);
    v4u o0, o1;
#pragma unroll
    for (int j = 0; j < 4; ++j) { o0[j] = pk(lo16(n0[j]) * d0, hi16(n0[j]) * d0); o1[j] = pk(lo16(n1[j]) * d1, hi16(n1[j]) * d1); }
    st128(o0, rs, lane * 16, pb + PN_O + w * 32768 + (2 * mem) * 1024);
    st128(o1, rs, lane * 16 + 1024, pb + PN_O + w * 32768 + (2 * mem) * 1024);
  }
#define WDN_BASES() const Base3 wown(L + (4 * hh + trq) * 64 + trb * 32 + trp * 8 + 3 * kh * 1024), woth(L + (4 * hh + trq) * 64 + trb * 32 + trp * 8 + (kh + 1) * 1024)
#define WDN(base, u) ldw<512>((((u) & 1) ? wown : woth).template at<slot((base) + 4 * ((u) >> 1))>())
  u32 x1p[8];
  {
    WDN_BASES();
    static_for<0, PD>([&](auto U) { wb[U.value] = WDN(P_WO, U.value); });
    publish(rs, fw + FL_O + w, mem, lane);
    TS(10);
    waitn(rs, fw + FL_O + strip2, 0xFFu << (kh * 8), lane);
    TS(11);
    v4u a[16];
    ldfrags<16>(a, rs, lane * 16, pb + PN_O + strip2 * 32768 + kh * 16384);
    sig(L, 4, w);
    SB0();
    f32x16 c0 = zero16, c1 = zero16;
    static_for<0, 32>([&](auto U) {
      constexpr int u = U.value;
      const v4u cur = wb[u % PD];
      if constexpr (u + PD < 32) wb[u % PD] = WDN(P_WO, u + PD);
      if constexpr (u & 1) mfma_pin(cur, a[u >> 1], c0); else mfma_pin(cur, a[u >> 1], c1);
      SB0();
    });
#pragma unroll
    for (int g = 0; g < 4; ++g) *(LAS f32x4*)(L + L_T + w * TW + g * 1024 + lane * 16) = f32x4{c1[4 * g], c1[4 * g + 1], c1[4 * g + 2], c1[4 * g + 3]};
    TS(12);
    bar();
    f32x4 o[4], ga[4], sc[4];
#pragma unroll
    for (int g = 0; g < 4; ++g) {
      o[g] = *(LAS f32x4*)(L + L_T + (w ^ 2) * TW + g * 1024 + lane * 16);
      ga[g] = *(LAS f32x4*)(L + L_VEC + (kh * 32 + 8 * g + 4 * hh) * 4);
      sc[g] = *(LAS f32x4*)(L + L_VEC + 256 + (kh * 32 + 8 * g + 4 * hh) * 4);
    }
    SB0();
    float x1[16], sp[4];
    u32 yp[8];
#pragma unroll
    for (int g = 0; g < 4; ++g) {
#pragma unroll
      for (int j = 0; j < 2; ++j) {
        const u32 y = pk(c0[4 * g + 2 * j] + o[g][2 * j], c0[4 * g + 2 * j + 1] + o[g][2 * j + 1]);
        const u32 t = pk(ga[g][2 * j] * lo16(y), ga[g][2 * j + 1] * hi16(y));
        const u32 r = pk(lo16(xres[g][j]) + lo16(t), hi16(xres[g][j]) + hi16(t));
        x1p[2 * g + j] = r;
        x1[4 * g + 2 * j] = lo16(r); x1[4 * g + 2 * j + 1] = hi16(r);
        yp[2 * g + j] = pk(lo16(r) * sc[g][2 * j], hi16(r) * sc[g][2 * j + 1]);
      }
    }
    const int tile = 2 * colg + kh;
    st128(v4u{yp[0], yp[1], yp[2], yp[3]}, rs, lane * 16, pb + PN_X1 + strip2 * 32768 + tile * 2048);
    st128(v4u{yp[4], yp[5], yp[6], yp[7]}, rs, lane * 16 + 1024, pb + PN_X1 + strip2 * 32768 + tile * 2048);
    SB0();
#pragma unroll
    for (int g = 0; g < 4; ++g) sp[g] = (x1[4 * g] + x1[4 * g + 1]) + (x1[4 * g + 2] + x1[4 * g + 3]);
    const float ss = (sp[0] + sp[1]) + (sp[2] + sp[3]);
    const float mt = fold32(ss, ss) * (1.f / 32.f);
    float q[4];
#pragma unroll
    for (int g = 0; g < 4; ++g) {
      const float d0 = x1[4 * g] - mt, d1 = x1[4 * g + 1] - mt, d2 = x1[4 * g + 2] - mt, d3 = x1[4 * g + 3] - mt;
      q[g] = (d0 * d0 + d1 * d1) + (d2 * d2 + d3 * d3);
    }
    const float qs = (q[0] + q[1]) + (q[2] + q[3]);
    const float qt = fold32(qs, qs);
    v4u sv = {__float_as_uint(mt), tag, __float_as_uint(qt), tag};
    asm volatile("" : "+v"(sv));
    publish(rs, fw + FL_X + strip2, tile, lane);
    st128(sv, rs, lane < 32 ? lane * 16 : WS_BYTES, pb + PN_ST + strip2 * 8192 + tile * 512);
  }
  asm volatile("" : "+v"(x1p[0]), "+v"(x1p[1]), "+v"(x1p[2]), "+v"(x1p[3]), "+v"(x1p[4]), "+v"(x1p[5]), "+v"(x1p[6]), "+v"(x1p[7]));
#define GU_S(u) ((u) < 96 ? (u) / 6 : 16 + (((u) - 96) % 32) / 2)
#define GU_I(u) ((u) < 96 ? (u) % 6 : 2 * (((u) - 96) / 32) + ((u) & 1))
#define WGU(u) ldw<512>((((u) & 1) ? wy : wx).template at<slot(P_GU + ((u) & ~1))>())
  {
    const Base3 wx(L + (4 * hh + trq) * 64 + trp * 8 + trb * (1024 + 32)), wy(L + (4 * hh + trq) * 64 + trp * 8 + (trb ? 1024 : 32));
    static_for<0, PD>([&](auto U) { wb[U.value] = WGU(U.value); });
    TS(13);
    waitn(rs, fw + FL_X + w, 0xFFFFu, lane);
    TS(14);
    constexpr int AW = 20;
    v4u a[AW];
    ldfrags<8>(a, rs, lane * 16, pb + PN_X1 + w * 32768);
    f32x16 acc[6];
#pragma unroll
    for (int i = 0; i < 6; ++i) acc[i] = zero16;
    float rr = 0.f;
    v4u bcor = {0, 0, 0, 0};
    v4u ho, cf0, cf1, pp;
    float t0 = 0.f, t1 = 0.f;
    const int go = pb + PN_G + (w * 96 + mem) * 1024;
    auto epi = [&](auto Ic, auto Kc) {
      constexpr int I = decltype(Ic)::value, k = decltype(Kc)::value, j = k >> 1;
      const f32x16& A = acc[I];
      if constexpr ((k & 1) == 0) { t0 = A[2 * j] * rr; t1 = A[2 * j + 1] * rr; asm volatile("" : "+v"(t0)); t0 *= A[8 + 2 * j]; }
      else {
        t1 *= A[9 + 2 * j];
        ho[j] = pk(t0, t1);
        if constexpr (j == 3) st128(ho, rs, (I < 4 || three) ? lane * 16 : WS_BYTES, go + ((I & 1) * 48 + (I >> 1) * 16) * 1024);
      }
    };
    static_for<0, 192>([&](auto Uc) {
      constexpr int u = decltype(Uc)::value, s = GU_S(u), i = GU_I(u), q = (u - 96) % 32;
      if constexpr (u == 6) TS(22);
      if constexpr (u == 60) TS(23);
      if constexpr (u == 96) TS(24);
      if constexpr (u == 128) TS(25);
      if constexpr (u == 160) TS(26);
      if constexpr (u == 56) mark(L, H_CP, w, 2u);
      if constexpr (u == 96 || u == 128 || u == 160) mark(L, H_CP, w, u / 32);
      if constexpr (u == 62 || u == 94 || u == 112) pp = peek(L, H_PP);
      if constexpr (u == 66) need_pp<false>(L, pp, 1u);
      if constexpr (u == 98) need_pp<false>(L, pp, 3u);
      if constexpr (u == 116) need_pp<true>(L, pp, 5u);
      const v4u cur = wb[u % PD];
      if constexpr (u + PD < 192) wb[u % PD] = WGU(u + PD);
      if constexpr (u >= 96 && q == 24) cf0 = *(LAS v4u*)(L + L_CF + i * 512 + l31 * 16);
      if constexpr (u >= 96 && q == 25) cf1 = *(LAS v4u*)(L + L_CF + i * 512 + l31 * 16);
      mfma_pin(cur, a[s % AW], acc[i]);
      if constexpr (u < 96 && i == 5 && s + AW < 32) a[s % AW] = ldfrag<s + AW>(rs, lane * 16, pb + PN_X1 + w * 32768);
      if constexpr (u < 2 * (AW - 8) && (u & 1)) a[8 + (u >> 1)] = ldfrag<8 + (u >> 1)>(rs, lane * 16, pb + PN_X1 + w * 32768);
      if constexpr (u == 2 * (AW - 8)) mark(L, H_CP, w, 1u);
      if constexpr (u == 117) bcor = *(LAS v4u*)(L + L_KVF + w * 512 + l31 * 16);
      if constexpr (u == 123) { rr = __uint_as_float(bcor.w); bcor = v4u{hh ? 0u : bcor.x, hh ? 0u : bcor.y, hh ? 0u : bcor.z, 0u}; }
      if constexpr (u >= 96 && q == 30) mfma_pin(cf0, bcor, acc[i]);
      if constexpr (u >= 96 && q == 31) mfma_pin(cf1, bcor, acc[i]);
      if constexpr (u >= 128 && q >= 1 && q < 17) epi(IC<(i & 6) - 2 + (q - 1) / 8>{}, IC<(q - 1) % 8>{});
      if constexpr (u == 164) publish(rs, fw + FL_G + w * 3, mem, lane);
      SB0();
    });
    mark(L, H_CP, w, 6u);
    TS(15);
    pp = peek(L, H_PP);
    static_for<0, 7>([&](auto Kc) { epi(IC<4>{}, Kc); });
    SB0();
    publish(rs, fw + FL_G + w * 3 + 1, mem, lane);
    SB0();
    static_for<7, 16>([&](auto Kc) { epi(IC<4 + Kc.value / 8>{}, IC<Kc.value % 8>{}); });
    need_pp<true>(L, pp, 6u);
    TS(16);
  }
  {
    WDN_BASES();
    static_for<0, PD>([&](auto U) { wb[U.value] = WDN(P_WD, U.value); });
    const int fi = fw + FL_G + strip2 * 3;
    u32 fl = ask(rs, fi, lane);
    publish(rs, fw + FL_G + w * 3 + 2, mem, lane);
    TS(17);
    poll(fl, rs, fi, 0xFFFFu, lane);
    TS(18);
    v4u a[43], pp;
    const int gi = pb + PN_G + (strip2 * 96 + kh * 48) * 1024;
    fl = ask(rs, fi + 1, lane);
    SB0();
    ldfrags<W0>(a, rs, lane * 16, gi);
    f32x16 c0 = zero16, c1 = zero16;
    static_for<0, 86>([&](auto U) {
      constexpr int u = U.value;
      if constexpr (u == 18) mark(L, H_CP, w, 7u);
      if constexpr (u == 26 || u == 42 || u == 58) pp = peek(L, H_PP);
      if constexpr (u == 30 || u == 46 || u == 62) need_pp<true>(L, pp, 7u + (u - 30) / 16);
      if constexpr (u == 2 * (16 - W0)) poll(fl, rs, fi + 1, 0xFFFFu, lane);
      if constexpr (u == 2 * (32 - W0) - 26) fl = ask(rs, fi + 2, lane);
      if constexpr (u == 2 * (32 - W0)) poll(fl, rs, fi + 2, 0x7FFu, lane);
      const v4u cur = wb[u % PD];
      if constexpr (u + PD < 86) wb[u % PD] = WDN(P_WD, u + PD);
      if constexpr (u & 1) mfma_pin(cur, a[u >> 1], c0); else mfma_pin(cur, a[u >> 1], c1);
      if constexpr (!(u & 1) && (u >> 1) + W0 < 43) a[(u >> 1) + W0] = ldfrag<(u >> 1) + W0>(rs, lane * 16, gi);
      SB0();
    });
#pragma unroll
    for (int g = 0; g < 4; ++g) *(LAS f32x4*)(L + L_T + w * TW + g * 1024 + lane * 16) = f32x4{c1[4 * g], c1[4 * g + 1], c1[4 * g + 2], c1[4 * g + 3]};
    f32x4 gb[4];
#pragma unroll
    for (int g = 0; g < 4; ++g) gb[g] = *(LAS f32x4*)(L + L_VEC + 512 + (kh * 32 + 8 * g + 4 * hh) * 4);
    TS(19);
    bar();
    f32x4 o[4];
#pragma unroll
    for (int g = 0; g < 4; ++g) o[g] = *(LAS f32x4*)(L + L_T + (w ^ 2) * TW + g * 1024 + lane * 16);
    SB0();
    u16* orow = p.out + (size_t)(panel * 128 + strip2 * 32 + l31) * D + ncol + 4 * hh;
#pragma unroll
    for (int g = 0; g < 4; ++g) {
      v2u r;
#pragma unroll
      for (int j = 0; j < 2; ++j) {
        r[j] = pk(lo16(x1p[2 * g + j]) + gb[g][2 * j] * (c0[4 * g + 2 * j] + o[g][2 * j]),
                  hi16(x1p[2 * g + j]) + gb[g][2 * j + 1] * (c0[4 * g + 2 * j + 1] + o[g][2 * j + 1]));
      }
      *(GAS v2u*)(orow + 8 * g) = r;
    }
  }
  if (b == 0 && tid == 0) __hip_atomic_store((GAS u32*)(ws + OFF_EPOCH), tag, RLX_AGENT);
}
}

extern "C" void kernel_launch(void* const* d_in, const int* in_sizes, int n_in, void* d_out, int out_size, void* d_ws,
                              size_t ws_size, hipStream_t stream) {
  using namespace mk;
  Params p;
  p.x = (const u16*)d_in[0]; p.wqkv = (const u16*)d_in[1]; p.wphi = (const u16*)d_in[2]; p.wo = (const u16*)d_in[3];
  p.wg = (const u16*)d_in[5]; p.wu = (const u16*)d_in[6]; p.wd = (const u16*)d_in[7]; p.c = (const u16*)d_in[8];
  p.wmod = (const u16*)d_in[9]; p.out = (u16*)d_out; p.ws = (unsigned char*)d_ws;
  (void)hipFuncSetAttribute((const void*)mega, hipFuncAttributeMaxDynamicSharedMemorySize, LDS_BYTES);
  mega<<<256, 512, LDS_BYTES, stream>>>(p);
}
```

```cpp
#include <hip/hip_runtime.h>

namespace mk {
typedef unsigned short u16;
typedef unsigned int u32;
typedef unsigned long long u64;
typedef u32 v4u __attribute__((ext_vector_type(4)));
typedef u32 v2u __attribute__((ext_vector_type(2)));
typedef float f32x2 __attribute__((ext_vector_type(2)));
typedef float f32x4 __attribute__((ext_vector_type(4)));
typedef float f32x16 __attribute__((ext_vector_type(16)));
typedef __bf16 bf16x2 __attribute__((ext_vector_type(2)));
typedef __bf16 bf16x8 __attribute__((ext_vector_type(8)));
typedef short s16x4 __attribute__((ext_vector_type(4)));
#define LAS __attribute__((address_space(3)))
#define GAS __attribute__((address_space(1)))
#define DI __device__ __forceinline__
#define RLX_AGENT __ATOMIC_RELAXED, __HIP_MEMORY_SCOPE_AGENT
#define SB0() __builtin_amdgcn_sched_barrier(0)
typedef __amdgpu_buffer_rsrc_t rsrc_t;

constexpr int D = 512, F = 1376;
constexpr int OFF_EPOCH = 0;
constexpr int OFF_MODG = 64 << 10;
constexpr int OFF_KVZ = 128 << 10;
constexpr int OFF_CG = 192 << 10;
constexpr int CG_PART = 2 * F * 16;
constexpr int OFF_ARR = 384 << 10;
constexpr int OFF_MODR = 512 << 10;
constexpr int OFF_KVE = 1 << 20;
constexpr int OFF_PAN = 2 << 20;
constexpr int PN_H = 0;
constexpr int PN_O = 128 << 10;
constexpr int PN_X1 = 256 << 10;
constexpr int PN_G = 384 << 10;
constexpr int PN_ST = 768 << 10;
constexpr int PN_FL = 800 << 10;
constexpr int FL_H = 0, FL_O = 4, FL_X = 8, FL_G = 12, FL_KV = 384;
constexpr int PSTR = (848 << 10) + 4224;
constexpr int WS_BYTES = 16 << 20;
constexpr int OFF_TS = 32 << 20;
constexpr int RING = 136;
constexpr int L_T = RING * 1024, TW = 4352;
constexpr int L_KVF = L_T + 4 * TW;
constexpr int L_ZF = L_KVF + 2048;
constexpr int L_VEC = L_ZF + 256;
constexpr int L_CF = L_VEC + 768;
constexpr int L_HINT = L_CF + 3072;
constexpr int LDS_BYTES = L_HINT + 128;
static_assert(LDS_BYTES <= 163840, "LDS");
constexpr int P_KV = 0, P_Q = 64, P_WO = 96, P_GU = 160, P_WD = 352;
constexpr int H_CP = 6, H_PP = 7;
constexpr int pair_of(int t) { return ((t & 15) < 11 ? 3 * (t & 15) : 2 * (t & 15) + 11) + (t >> 4); }
constexpr int slot(int n) { return (n % RING) * 1024; }
constexpr int AUX_SC1 = 16;
constexpr u32 SPIN_CAP = 1u << 16;
#define PRE_MOD 8
#define HI_DELAY 8
#define POLL_GAP 6
#define PRE_FL 4
#define KV1 32
#define PGU 4
#define PKV 0
#define GPUB 29
#define LPRIO 0
#define APRIO 1
#define TMOD 0
#define CW 1
#define DW 2
#define CSLP 3
#define W0 12
constexpr int PD = 5;
constexpr int CODE_KB = 36;

#define NBV(x) (x)
struct Params {
  const u16 *x, *wqkv, *wphi, *wo, *wg, *wu, *wd, *c, *wmod;
  u16* out;
  unsigned char* ws;
};

template <int V> struct IC { static constexpr int value = V; };
template <int I, int N, class Fn> DI void static_for(Fn&& f) {
  if constexpr (I < N) { f(IC<I>{}); static_for<I + 1, N>(f); }
}
DI float lo16(u32 v) { return __uint_as_float(v << 16); }
DI float hi16(u32 v) { return __uint_as_float(v & 0xffff0000u); }
DI u32 pk(float a, float b) { f32x2 v = {a, b}; return __builtin_bit_cast(u32, __builtin_convertvector(v, bf16x2)); }
DI float rb(float a) { return lo16(pk(a, 0.f)); }
DI float rcp(float a) { return __builtin_amdgcn_rcpf(a); }
DI f32x16 mfma(v4u a, v4u b, f32x16 c) {
  return __builtin_amdgcn_mfma_f32_32x32x16_bf16(__builtin_bit_cast(bf16x8, a), __builtin_bit_cast(bf16x8, b), c, 0, 0, 0);
}
DI void mfma_pin(v4u a, v4u b, f32x16& c) {
  c = mfma(a, b, c);
  asm volatile("" : "+v"(c));
}
template <int O> DI v4u pack8(const f32x16& x) {
  v4u r; r.x = pk(x[O], x[O + 1]); r.y = pk(x[O + 2], x[O + 3]); r.z = pk(x[O + 4], x[O + 5]); r.w = pk(x[O + 6], x[O + 7]); return r;
}
template <int T> DI v4u ldw(LAS unsigned char* p) {
  s16x4 a = __builtin_amdgcn_ds_read_tr16_b64_v4i16((LAS s16x4*)p);
  s16x4 b = __builtin_amdgcn_ds_read_tr16_b64_v4i16((LAS s16x4*)(p + T));
  return __builtin_bit_cast(v4u, __builtin_shufflevector(a, b, 0, 1, 2, 3, 4, 5, 6, 7));
}
struct Base3 {
  u32 b[3];
  DI explicit Base3(LAS unsigned char* p) {
    b[0] = (u32)(size_t)p; b[1] = b[0] + 65536u; b[2] = b[0] + 131072u;
    asm volatile("" : "+v"(b[0]), "+v"(b[1]), "+v"(b[2]));
  }
  template <int OFF> DI LAS unsigned char* at() const { return (LAS unsigned char*)(size_t)b[OFF >> 16] + (OFF & 65535); }
};
template <int LOFF, int SOFF> DI void gldsi(rsrc_t rs, u32 voff, u32 sbase, u32 lbase) {
  u32 t;
  asm volatile("s_add_u32 m0, %1, %2\n\ts_add_u32 %0, %3, %4\n\tbuffer_load_dwordx4 %5, %6, %0 offen lds"
               : "=&s"(t) : "s"(lbase), "i"(LOFF), "s"(sbase), "i"(SOFF), "v"(voff), "s"(rs) : "m0", "scc", "memory");
}
DI void bar() { asm volatile("s_waitcnt lgkmcnt(0)\n\ts_barrier" ::: "memory"); }
DI void bar_raw() { asm volatile("s_barrier" ::: "memory"); }
DI void drain() { asm volatile("s_waitcnt vmcnt(0)" ::: "memory"); }
#define VMCNT(n) asm volatile("s_waitcnt vmcnt(" #n ")" ::: "memory")
#define RELOAD() asm volatile("" ::: "memory")
DI v4u ld128(rsrc_t rs, int voff, int soff) { return __builtin_amdgcn_raw_buffer_load_b128(rs, voff, soff, AUX_SC1); }
DI v2u ld64(rsrc_t rs, int voff, int soff) { return __builtin_amdgcn_raw_buffer_load_b64(rs, voff, soff, AUX_SC1); }
DI void st128(v4u v, rsrc_t rs, int voff, int soff) { __builtin_amdgcn_raw_buffer_store_b128(v, rs, voff + soff, 0, AUX_SC1); }
DI void st64(v2u v, rsrc_t rs, int voff, int soff) { __builtin_amdgcn_raw_buffer_store_b64(v, rs, voff, soff, AUX_SC1); }
template <int S> DI v4u ldfrag(rsrc_t rs, int voff, int soff) { return ld128(rs, voff + (S & 3) * 1024, soff + (S >> 2) * 4096); }
template <int W, int N> DI void ldfrags(v4u (&a)[N], rsrc_t rs, int voff, int soff) {
  static_for<0, W>([&](auto S) { a[S.value] = ldfrag<S.value>(rs, voff, soff); SB0(); });
}
template <int CTRL> DI float dpp_add(float v) {
  return v + __uint_as_float(__builtin_amdgcn_update_dpp(0u, __float_as_uint(v), CTRL, 0xf, 0xf, false));
}
constexpr int DPP_X1 = 0xB1, DPP_X2 = 0x4E, DPP_HM = 0x141, DPP_ROR4 = 0x124, DPP_ROR8 = 0x128;
DI float fold32(float x, float y) {
  auto r = __builtin_amdgcn_permlane32_swap(__float_as_uint(x), __float_as_uint(y), false, false);
  return __uint_as_float(r[0]) + __uint_as_float(r[1]);
}
DI float fold16(float x, float y) {
  auto r = __builtin_amdgcn_permlane16_swap(__float_as_uint(x), __float_as_uint(y), false, false);
  return __uint_as_float(r[0]) + __uint_as_float(r[1]);
}
DI void red8q(const float (&a)[8], float& c0, float& c1) {
  const float b0 = fold32(a[0], a[4]), b1 = fold32(a[1], a[5]), b2 = fold32(a[2], a[6]), b3 = fold32(a[3], a[7]);
  c0 = fold16(b0, b2); c1 = fold16(b1, b3);
  c0 = dpp_add<DPP_ROR4>(c0); c1 = dpp_add<DPP_ROR4>(c1);
  c0 = dpp_add<DPP_ROR8>(c0); c1 = dpp_add<DPP_ROR8>(c1);
}
DI void red8(const float (&a)[8], float& c0, float& c1) {
  red8q(a, c0, c1);
  c0 = dpp_add<DPP_X1>(c0); c1 = dpp_add<DPP_X1>(c1);
  c0 = dpp_add<DPP_X2>(c0); c1 = dpp_add<DPP_X2>(c1);
}
DI void publish(rsrc_t rs, int word, int bit, int lane) {
  drain();
  (void)__builtin_amdgcn_raw_ptr_buffer_atomic_add_i32(1 << bit, rs, lane == 0 ? 0 : WS_BYTES, OFF_ARR + word * 128, 0);
}
DI u32 ask(rsrc_t rs, int word, int lane) { return __builtin_amdgcn_raw_ptr_buffer_atomic_add_i32(0, rs, lane == 0 ? 0 : WS_BYTES, OFF_ARR + word * 128, 0); }
DI void spin(rsrc_t rs, int word, u32 m, int lane) {
#pragma nounroll
  for (u32 spins = 0; spins < SPIN_CAP; ++spins)
    if ((__builtin_amdgcn_readfirstlane(ask(rs, word, lane)) & m) == m) break;
}
DI void poll(u32 f, rsrc_t rs, int word, u32 m, int lane) {
  if (__builtin_expect((__builtin_amdgcn_readfirstlane(f) & m) != m, 0)) spin(rs, word, m, lane);
  __builtin_amdgcn_fence(__ATOMIC_ACQUIRE, "wavefront");
  RELOAD();
}
DI void waitn(rsrc_t rs, int word, u32 m, int lane) {
  if (PRE_FL) __builtin_amdgcn_s_sleep(PRE_FL);
  spin(rs, word, m, lane);
  __builtin_amdgcn_fence(__ATOMIC_ACQUIRE, "wavefront");
  RELOAD();
}
DI void sig(LAS unsigned char* L, int i, int k) { *(volatile LAS u32*)(L + L_HINT + 16 * i + 4 * k) = 1u; }
template <int N> DI void await(LAS unsigned char* L, int i) {
#pragma nounroll
  for (u32 spins = 0; spins < (1u << 20); ++spins) {
    const v4u v = *(volatile LAS v4u*)(L + L_HINT + 16 * i);
    if (__builtin_amdgcn_readfirstlane(N == 1 ? v.x : (v.x & v.y & v.z & v.w))) break;
    __builtin_amdgcn_s_sleep(1);
  }
  RELOAD();
}
DI void mark(LAS unsigned char* L, int i, int k, u32 val) { *(volatile LAS u32*)(L + L_HINT + 16 * i + 4 * k) = val; }
DI v4u peek(LAS unsigned char* L, int i) { return *(volatile LAS v4u*)(L + L_HINT + 16 * i); }
DI u32 umin(u32 a, u32 b) { return a < b ? a : b; }
DI void await_cp(LAS unsigned char* L, u32 k) {
#pragma nounroll
  for (u32 spins = 0; spins < (1u << 20); ++spins) {
    const v4u v = peek(L, H_CP);
    if (__builtin_amdgcn_readfirstlane(umin(umin(v.x, v.y), umin(v.z, v.w))) >= k) break;
    __builtin_amdgcn_s_sleep(CSLP);
  }
  RELOAD();
}
template <bool AUX> DI u32 pp_min(v4u v) {
  u32 m = umin(v.x, v.y);
  if (AUX) m = umin(m, umin(v.z, v.w));
  return __builtin_amdgcn_readfirstlane(m);
}
template <bool AUX> DI void need_pp(LAS unsigned char* L, v4u v, u32 k) {
  if (__builtin_expect(pp_min<AUX>(v) < k, 0)) {
#pragma nounroll
    for (u32 spins = 0; spins < (1u << 20); ++spins)
      if (pp_min<AUX>(peek(L, H_PP)) >= k) break;
  }
  RELOAD();
}
DI void feat(const f32x16& P, u32 (&ph)[16]) {
#pragma clang fp contract(off)
  constexpr float L2E = 1.4426950408889634f;
  f32x2 p[8];
#pragma unroll
  for (int i = 0; i < 8; ++i) { const u32 t = pk(P[2 * i], P[2 * i + 1]); p[i] = f32x2{lo16(t), hi16(t)} * L2E; }
  float mx[4], mn[4];
#pragma unroll
  for (int i = 0; i < 4; ++i) {
    mx[i] = fmaxf(fmaxf(p[2 * i].x, p[2 * i].y), fmaxf(p[2 * i + 1].x, p[2 * i + 1].y));
    mn[i] = fminf(fminf(p[2 * i].x, p[2 * i].y), fminf(p[2 * i + 1].x, p[2 * i + 1].y));
  }
  const float mxl = fmaxf(fmaxf(mx[0], mx[1]), fmaxf(mx[2], mx[3])), mnl = fminf(fminf(mn[0], mn[1]), fminf(mn[2], mn[3]));
  f32x2 e1[8], e2[8];
#pragma unroll
  for (int i = 0; i < 8; ++i) { e1[i] = p[i] - mxl; e2[i] = mnl - p[i]; }
  SB0();
#pragma unroll
  for (int i = 0; i < 8; ++i) {
    e1[i] = f32x2{__builtin_amdgcn_exp2f(e1[i].x), __builtin_amdgcn_exp2f(e1[i].y)};
    e2[i] = f32x2{__builtin_amdgcn_exp2f(e2[i].x), __builtin_amdgcn_exp2f(e2[i].y)};
  }
  const f32x2 s1 = ((e1[0] + e1[1]) + (e1[2] + e1[3])) + ((e1[4] + e1[5]) + (e1[6] + e1[7]));
  const f32x2 s2 = ((e2[0] + e2[1]) + (e2[2] + e2[3])) + ((e2[4] + e2[5]) + (e2[6] + e2[7]));
  const float r1 = rcp(s1.x + s1.y), r2 = rcp(s2.x + s2.y);
#pragma unroll
  for (int i = 0; i < 8; ++i) { const f32x2 u = e1[i] * r1, v = e2[i] * r2; ph[i] = pk(u.x, u.y); ph[8 + i] = pk(v.x, v.y); }
}

#define TSCLK() __builtin_amdgcn_s_memrealtime()
#define TS(i) do {} while (0)
#define TS_DUMP() do {} while (0)
#define TS_INIT() do {} while (0)

__global__ void __launch_bounds__(512, 2) mega(Params p) {
  extern __shared__ __attribute__((aligned(16))) unsigned char smem[];
  LAS unsigned char* L = (LAS unsigned char*)smem;
  TS_INIT();
  TS(0);
  u64 pc0;
  asm volatile("s_getpc_b64 %0" : "=s"(pc0));
  const int tid = threadIdx.x, lane = tid & 63;
  int w = __builtin_amdgcn_readfirstlane(tid >> 6);
  const int b = blockIdx.x, xcd = b & 7, rank = b >> 3, panel = (xcd >> 1) * 4 + (rank >> 3), mem = (xcd & 1) * 8 + (rank & 7);
  const int hh = lane >> 5, l31 = lane & 31;
  unsigned char* ws = p.ws;
  const int rowh = mem & 1, colg = mem >> 1;
  const int pair0 = min(3 * mem, 2 * mem + 11);
  const bool three = mem < 11;
  const rsrc_t rs = __builtin_amdgcn_make_buffer_rsrc(ws, 0, WS_BYTES, 0x00020000);
  const int pb = OFF_PAN + panel * PSTR, fw = panel * 24;
  const int mcg = xcd * 8 + (rank >> 2), mkp = rank & 3;
  const rsrc_t rqkv = __builtin_amdgcn_make_buffer_rsrc((void*)p.wqkv, 0, D * 3 * D * 2, 0x00020000);
  const rsrc_t rwo = __builtin_amdgcn_make_buffer_rsrc((void*)p.wo, 0, D * D * 2, 0x00020000);
  const rsrc_t rgu = __builtin_amdgcn_make_buffer_rsrc((void*)((w & 1) ? p.wu : p.wg), 0, D * F * 2, 0x00020000);
  const rsrc_t rwd = __builtin_amdgcn_make_buffer_rsrc((void*)p.wd, 0, F * D * 2, 0x00020000);

  v4u r0[4];
  u32 cv0[4], epoch;
  u32 vq = (lane >> 2) * (3 * D * 2) + (lane & 3) * 16;
  asm volatile("" : "+v"(vq));
  {
    const u32 ld = (u32)(size_t)L + w * 1024;
    if (w <= 4) {
      const rsrc_t r1 = __builtin_amdgcn_make_buffer_rsrc((void*)(w == 4 ? p.wmod : p.x), 0, w == 4 ? D * 6 * D * 2 : 2048 * D * 2, 0x00020000);
      const int vo = w == 4 ? (128 * mkp + 4 * (lane >> 1)) * (6 * D * 2) + mcg * 32 + (lane & 1) * 16
                            : (panel * 128 + mem * 8 + (lane >> 3)) * (D * 2) + (lane & 7) * 16 + 256 * w;
#pragma unroll
      for (int i = 0; i < 4; ++i) r0[i] = __builtin_amdgcn_raw_buffer_load_b128(r1, vo, w == 4 ? i * (6 * D * 2) : i < 2 ? i * 128 : 1 << 30, 0);
      SB0();
    }
    {
      const rsrc_t r2 = __builtin_amdgcn_make_buffer_rsrc((void*)p.c, 0, w == 4 ? D * 2 : 0, 0x00020000);
      const v2u c2 = __builtin_amdgcn_raw_buffer_load_b64(r2, (128 * mkp + 4 * (lane >> 1)) * 2, 0, 0);
      cv0[0] = c2.x; cv0[1] = c2.y;
      epoch = __builtin_amdgcn_raw_buffer_load_b32(rs, 0, OFF_EPOCH, AUX_SC1);
      __builtin_amdgcn_raw_buffer_store_b32(0u, rs, (w == 4 && lane < 2) ? lane * 128 : WS_BYTES, OFF_ARR + b * 256, AUX_SC1);
      SB0();
    }
    if (w >= 5) {
      const rsrc_t rc = __builtin_amdgcn_make_buffer_rsrc((void*)pc0, 0, CODE_KB * 1024, 0x00020000);
      gldsi<slot(128), 0>(rc, lane < CODE_KB / 6 ? (u32)lane * 128u : 1u << 30, (u32)(((rank & 15) * 3 + w - 5) * (CODE_KB / 6) * 128), ld);
      const u32 tq = (rank * 16 + (lane >> 2)) * (3 * D * 2) + (xcd & 1) * 512 + (lane & 3) * 128;
      if (w != 5) gldsi<slot(128), 0>(rqkv, tq, (w - 5) * (D * 2), ld);
      else {
        gldsi<slot(128), 0>(rs, lane < 2 ? (u32)(pb + lane * (848 << 10)) : 1u << 30, 0u, ld);
        const rsrc_t rp = __builtin_amdgcn_make_buffer_rsrc((void*)p.wphi, 0, 32 * 256 * 2, 0x00020000);
        gldsi<L_CF + 2048, 0>(rp, lane * 16, mem * 1024, (u32)(size_t)L);
      }
    }
    if (tid < 32) *((LAS u32*)(L + L_HINT) + tid) = 0u;
    TS(27);
    bar();
    asm volatile("" : "+s"(w));
    if (w >= 6) {
      u32 lo = (u32)(size_t)L + (w & 1) * 1024, so = (D + (w & 1) * D + mem * 32) * 2;
#pragma nounroll
      for (int i = 0; i < KV1 / 2; ++i) {
        gldsi<0, 0>(rqkv, vq, so, lo);
        gldsi<2048, 16 * (3 * D * 2)>(rqkv, vq, so, lo);
        lo += 4096; so += 32 * (3 * D * 2);
      }
    }
    if (w == 4) {
      __builtin_amdgcn_s_setprio(3);
      TS(10);
      float sl[4], acc[8];
#pragma unroll
      for (int i = 0; i < 4; ++i) { const float c = (i & 1) ? hi16(cv0[i >> 1]) : lo16(cv0[i >> 1]); sl[i] = rb(c * rcp(1.f + __expf(-c))); }
      u32 tag = epoch;
      asm volatile("" : "+v"(tag));
      tag += 1u;
      TS(11);
#pragma unroll
      for (int j = 0; j < 8; ++j) acc[j] = 0.f;
#pragma unroll
      for (int i = 0; i < 4; ++i)
#pragma unroll
        for (int j = 0; j < 4; ++j) { acc[2 * j] += sl[i] * lo16(r0[i][j]); acc[2 * j + 1] += sl[i] * hi16(r0[i][j]); }
      float c0, c1;
      TS(12);
      red8q(acc, c0, c1);
      c0 = dpp_add<DPP_X2>(c0); c1 = dpp_add<DPP_X2>(c1);
      const int go = ((lane >> 1) & 7) * 32768 + (8 * (lane & 1) + 2 * (lane >> 4)) * 8;
      const v4u gv = {__float_as_uint(c0), tag, __float_as_uint(c1), tag};
      drain();
      st128(gv, rs, go, OFF_MODR + mkp * 8192 + mcg * 128);
      st128(gv, rs, go, OFF_MODR + 8 * 32768 + mkp * 8192 + mcg * 128);
      sig(L, 0, 0);
    }
    asm volatile("" : "+s"(w));
  }

  if (w == 4 || w == 5) {
    const int a = w & 1;
    u32 tag;
    tag = epoch;
    asm volatile("" : "+v"(tag));
    tag += 1u;
    drain();
    TS(1);
    bar();
    __builtin_amdgcn_s_setprio(APRIO);
    await<4>(L, 2);
    const int kp = xcd >> 1, cpair = (xcd & 1) * 24 + rank;
    const bool con = rank < ((xcd & 1) ? 19 : 24);
    const int col0 = a * F + cpair * 32;
    v4u wm[8];
#pragma unroll
    for (int i = 0; i < 8; ++i)
      wm[i] = __builtin_amdgcn_raw_buffer_load_b128(rgu, con ? (128 * kp + 2 * (lane >> 2)) * (F * 2) + cpair * 64 + (lane & 3) * 16 : 1 << 30, (32 * (i >> 1) + (i & 1)) * (F * 2), 0);
    SB0();
    if (a == 1) {
      v4u wn[8]; u32 cv[8];
      const int row0 = 128 * mkp + (lane >> 2);
#pragma unroll
      for (int i = 0; i < 8; ++i) cv[i] = p.c[row0 + 16 * i];
#pragma unroll
      for (int i = 0; i < 8; ++i) wn[i] = *(const GAS v4u*)(p.wmod + (size_t)(row0 + 16 * i) * (6 * D) + 1024 + mcg * 32 + (lane & 3) * 8);
      SB0();
      bar_raw();
      SB0();
      float acc[8];
#pragma unroll
      for (int j = 0; j < 8; ++j) acc[j] = 0.f;
#pragma unroll
      for (int i = 0; i < 8; ++i) {
        const float c = lo16(cv[i]), sl = rb(c * rcp(1.f + __expf(-c)));
#pragma unroll
        for (int j = 0; j < 4; ++j) { acc[2 * j] += sl * lo16(wn[i][j]); acc[2 * j + 1] += sl * hi16(wn[i][j]); }
      }
      float c0, c1;
      red8q(acc, c0, c1);
      st128(v4u{__float_as_uint(c0), tag, __float_as_uint(c1), tag}, rs, (lane & 12) == 0 ? (8 * (lane & 3) + 2 * (lane >> 4)) * 8 : WS_BYTES,
            OFF_MODG + mkp * 16384 + mcg * 256);
    } else {
      bar_raw();
    }
    bar_raw();
    TS(2);
    {
      const int idx0 = (mem * 4 + 2 * a) * 16;
      v4u e[32], zz[2];
      {
        u32 todo = 0xFFu;
        for (u32 spins = 0; spins < SPIN_CAP && todo; ++spins) {
          const u32 bad = ~__builtin_amdgcn_readfirstlane(ask(rs, FL_KV + mem * 2 + a, lane));
          __builtin_amdgcn_fence(__ATOMIC_ACQUIRE, "wavefront");
          RELOAD();
          static_for<0, 8>([&](auto G) {
            constexpr int g = G.value;
            if (((todo >> g) & 1u) && !(bad & (0x00030003u << (2 * g)))) {
              todo &= ~(1u << g);
              e[2 * g] = ld128(rs, lane * 16, OFF_KVE + (idx0 + 2 * g) * 1024);
              e[2 * g + 1] = ld128(rs, lane * 16, OFF_KVE + (idx0 + 2 * g + 1) * 1024);
              e[16 + 2 * g] = ld128(rs, lane * 16, OFF_KVE + (idx0 + 16 + 2 * g) * 1024);
              e[17 + 2 * g] = ld128(rs, lane * 16, OFF_KVE + (idx0 + 17 + 2 * g) * 1024);
            }
          });
        }
      }
      TS(3);
      zz[0] = ldfrag<0>(rs, lane * 16, OFF_KVZ + idx0 * 64);
      zz[1] = ldfrag<1>(rs, lane * 16, OFF_KVZ + idx0 * 64);
      SB0();
#pragma unroll
      for (int q = 0; q < 2; ++q) {
        f32x4 s0 = __builtin_bit_cast(f32x4, e[16 * q]), s1 = __builtin_bit_cast(f32x4, e[16 * q + 1]);
#pragma unroll
        for (int i = 2; i < 16; i += 2) { s0 += __builtin_bit_cast(f32x4, e[16 * q + i]); s1 += __builtin_bit_cast(f32x4, e[16 * q + i + 1]); }
        s0 += s1;
        *(LAS v2u*)(L + L_KVF + (2 * a + q) * 512 + l31 * 16 + hh * 8) = v2u{pk(s0[0], s0[1]), pk(s0[2], s0[3])};
      }
      f32x4 z;
#pragma unroll
      for (int j = 0; j < 4; ++j) {
        float t = fold32(__uint_as_float(zz[0][j]), __uint_as_float(zz[1][j]));
        t = fold16(t, t);
        t = dpp_add<DPP_ROR4>(t);
        z[j] = dpp_add<DPP_ROR8>(t);
      }
      if (l31 < 4) *(LAS v2u*)(L + L_ZF + (2 * a + hh) * 32 + ((lane & 1) ^ 1) * 16 + ((lane >> 1) & 1) * 8) = v2u{pk(z[0], z[1]), pk(z[2], z[3])};
    }
    TS(4);
    bar();
    u32 gsb[4], gsc[4];
    {
      const int vo = ((hh ? 2 : 3 * a) * D + colg * 64 + 2 * l31) * 8, ro = (D + 128 * kp + 2 * lane) * 8;
      v4u g[12];
      for (u32 spins = 0; spins < SPIN_CAP; ++spins) {
        RELOAD();
#pragma unroll
        for (int i = 0; i < 12; ++i) g[i] = ld128(rs, i < 4 ? vo : ro + (i >> 3) * (D * 8), OFF_MODG + (i & 3) * 16384);
        bool ok = true;
#pragma unroll
        for (int i = 0; i < 12; ++i) ok &= (g[i].y == tag) & (g[i].w == tag);
        if (__all(ok)) break;
      }
      u32 pr[3];
#pragma unroll
      for (int t = 0; t < 3; ++t)
        pr[t] = pk((__uint_as_float(g[4 * t].x) + __uint_as_float(g[4 * t + 1].x)) + (__uint_as_float(g[4 * t + 2].x) + __uint_as_float(g[4 * t + 3].x)),
                   (__uint_as_float(g[4 * t].z) + __uint_as_float(g[4 * t + 1].z)) + (__uint_as_float(g[4 * t + 2].z) + __uint_as_float(g[4 * t + 3].z)));
      const u32 v1 = pk(1.f + lo16(pr[0]), 1.f + hi16(pr[0])), vv = hh ? v1 : pr[0];
      *(LAS f32x2*)(L + L_VEC + (hh ? 1 : 2 * a) * 256 + l31 * 8) = f32x2{lo16(vv), hi16(vv)};
      const u32 sc = pk(1.f + lo16(pr[2]), 1.f + hi16(pr[2]));
#pragma unroll
      for (int i = 0; i < 4; ++i) {
        gsb[i] = __builtin_amdgcn_ds_bpermute((16 * i + (lane >> 2)) * 4, pr[1]);
        gsc[i] = __builtin_amdgcn_ds_bpermute((16 * i + (lane >> 2)) * 4, sc);
      }
    }
    if (con) {
      float a1[8], a2[8];
#pragma unroll
      for (int j = 0; j < 8; ++j) { a1[j] = 0.f; a2[j] = 0.f; }
#pragma unroll
      for (int i = 0; i < 8; ++i) {
        const float sb = (i & 1) ? hi16(gsb[i >> 1]) : lo16(gsb[i >> 1]), sc = (i & 1) ? hi16(gsc[i >> 1]) : lo16(gsc[i >> 1]);
#pragma unroll
        for (int j = 0; j < 4; ++j) {
          a1[2 * j] += sc * lo16(wm[i][j]); a1[2 * j + 1] += sc * hi16(wm[i][j]);
          a2[2 * j] += sb * lo16(wm[i][j]); a2[2 * j + 1] += sb * hi16(wm[i][j]);
        }
      }
      float c10, c11, c20, c21;
      red8q(a1, c10, c11); red8q(a2, c20, c21);
      const int k = (lane >> 2) & 3;
      const float val = k == 0 ? c10 : k == 1 ? c20 : k == 2 ? c11 : c21;
      st64(v2u{__float_as_uint(val), tag}, rs, (col0 + 8 * (lane & 3) + 2 * (lane >> 4)) * 16 + k * 8, OFF_CG + kp * CG_PART);
    }
    TS(5);
    bar();
    {
      const int ntile = three ? 6 : 4;
      v4u cg[8];
      int off[2];
#pragma unroll
      for (int i = 0; i < 2; ++i) {
        int tile = 4 * i + 2 * a + hh; tile = tile < ntile ? tile : 0;
        off[i] = (32 * pair0 + 16 * tile + (lane & 15) + ((lane >> 4) & 1) * F) * 16;
      }
      for (u32 spins = 0; spins < SPIN_CAP; ++spins) {
        RELOAD();
        bool ok = true;
#pragma unroll
        for (int i = 0; i < 8; ++i) cg[i] = ld128(rs, off[i >> 2], OFF_CG + (i & 3) * CG_PART);
#pragma unroll
        for (int i = 0; i < 8; ++i) ok &= (cg[i].y == tag) & (cg[i].w == tag);
        if (__all(ok)) break;
      }
#pragma unroll
      for (int i = 0; i < 2; ++i) {
        const float c1 = (__uint_as_float(cg[4 * i].x) + __uint_as_float(cg[4 * i + 1].x)) + (__uint_as_float(cg[4 * i + 2].x) + __uint_as_float(cg[4 * i + 3].x));
        const float c2 = (__uint_as_float(cg[4 * i].z) + __uint_as_float(cg[4 * i + 1].z)) + (__uint_as_float(cg[4 * i + 2].z) + __uint_as_float(cg[4 * i + 3].z));
        const float h1 = rb(c1), m1 = rb(c1 - h1), l1 = c1 - h1 - m1, h2 = rb(c2), m2 = rb(c2 - h2), l2 = c2 - h2 - m2;
        const int tile = 4 * i + 2 * a + hh;
        if (tile < 6) *(LAS v4u*)(L + L_CF + tile * 512 + l31 * 16) = v4u{pk(h1, m1), pk(l1, h2), pk(m2, l2), 0u};
      }
    }
    await_cp(L, 1);
    {
      v4u st[16];
#pragma unroll
      for (int t = 0; t < 16; ++t) st[t] = ld128(rs, l31 * 16 + hh * 8192 + t * 512, pb + PN_ST + 2 * a * 8192);
      for (u32 spins = 0; spins < SPIN_CAP; ++spins) {
        bool ok = true;
#pragma unroll
        for (int t = 0; t < 16; ++t) ok &= (st[t].y == tag) & (st[t].w == tag);
        if (__all(ok)) break;
        RELOAD();
#pragma unroll
        for (int t = 0; t < 16; ++t) st[t] = ld128(rs, l31 * 16 + hh * 8192 + t * 512, pb + PN_ST + 2 * a * 8192);
      }
      float ms = 0.f;
#pragma unroll
      for (int t = 0; t < 16; ++t) ms += __uint_as_float(st[t].x);
      const float mean = ms * (1.f / 16.f);
      float q = 0.f;
#pragma unroll
      for (int t = 0; t < 16; ++t) { const float d = __uint_as_float(st[t].x) - mean; q += __uint_as_float(st[t].z) + 32.f * d * d; }
      const float den = rb(__builtin_amdgcn_sqrtf(rb(rb(q * (1.f / D)) + 1e-5f)));
      *(LAS v4u*)(L + L_KVF + a * 1024 + lane * 16) = v4u{pk(-mean, -mean), pk(-mean, den), pk(den, den), __float_as_uint(rcp(den * den))};
    }
    RELOAD();
    mark(L, H_PP, 2 + a, 5u);
    TS(6);
    {
      v4u wt[58];
      const u32 vd = (lane >> 2) * (D * 2) + (lane & 3) * 16, sd = a * 16 * (D * 2) + colg * 128;
      const Base3 wdst(L + a * 2048 + lane * 16);
      static_for<28, 86>([&](auto J) {
        wt[J.value - 28] = __builtin_amdgcn_raw_buffer_load_b128(rwd, NBV(vd + (J.value & 1) * 64), sd + pair_of(J.value >> 1) * 32 * (D * 2), 0); SB0(); });
      TS(7);
#define ST_WT(T0, T1, k) do { static_for<2 * (T0), 2 * (T1)>([&](auto J) { \
      *(LAS v4u*)wdst.template at<slot(P_WD + 4 * (J.value >> 1)) + (J.value & 1) * 1024>() = wt[J.value - 28]; }); \
      RELOAD(); mark(L, H_PP, 2 + a, k); RELOAD(); } while (0)
      await_cp(L, 4);
      ST_WT(14, 18, 6u);
      await_cp(L, 5);
      ST_WT(18, 26, 7u);
      await_cp(L, 6);
      ST_WT(26, 34, 8u);
      TS(8);
      await_cp(L, 7);
      ST_WT(34, 43, 9u);
      TS(9);
    }
    TS_DUMP();
    return;
  }

  if (w >= 6) {
    const int v = w & 1;
    __builtin_amdgcn_s_setprio(LPRIO);
    const u32 ldsb = (u32)(size_t)L;
    const u32 l1 = ldsb + v * 1024, l2 = ldsb + v * 2048;
    const u32 skv = (D + v * D + mem * 32) * 2, sq = v * 16 * (3 * D * 2) + mem * 64;
    const u32 so = 16 * v * 16 * (D * 2) + colg * 128, sd = v * 16 * (D * 2) + colg * 128;
    const u32 sg = pair0 * 64, sg2 = sg + (three ? 128 : 64);
#define LD_KV(S0, S1, PC) static_for<S0, S1>([&](auto S) { gldsi<slot(P_KV + 2 * S.value), S.value * 16 * (3 * D * 2)>(rqkv, vq, skv, l1); PACE(PC); })
#define PACE(n) do { if ((n) > 0) __builtin_amdgcn_s_sleep(n); } while (0)
#define LD_Q(S0, S1, PC) static_for<S0, S1>([&](auto S) { gldsi<slot(P_Q + 2 * S.value), 2 * S.value * 16 * (3 * D * 2)>(rqkv, vq, sq, l1); PACE(PC); })
#define LD_DN(RS, SB, P0, T0, T1, PC)                                                                    \
  static_for<T0, T1>([&](auto T) {                                                                   \
    gldsi<slot((P0) + 4 * T.value), T.value * 16 * (D * 2)>(RS, vd, SB, l2);                         \
    gldsi<slot((P0) + 4 * T.value) + 1024, T.value * 16 * (D * 2) + 64>(RS, vd, SB, l2); PACE(PC);   \
  })
#define LD_GU(S0, S1, PC)                                                                                \
  static_for<S0, S1>([&](auto S) {                                                                   \
    gldsi<slot(P_GU + 6 * S.value), S.value * 16 * (F * 2)>(rgu, vg, sg, l1);                        \
    gldsi<slot(P_GU + 6 * S.value + 2), S.value * 16 * (F * 2) + 64>(rgu, vg, sg, l1);               \
    gldsi<slot(P_GU + 6 * S.value + 4), S.value * 16 * (F * 2)>(rgu, vg, sg2, l1); PACE(PC);         \
  })
    TS(1);
    VMCNT(0);
    SB0();
    int ln;
    asm volatile("v_mbcnt_lo_u32_b32 %0, -1, 0\n\tv_mbcnt_hi_u32_b32 %0, -1, %0" : "=v"(ln));
    const u32 vd = (ln >> 2) * (D * 2) + (ln & 3) * 16;
    const u32 vg = (ln >> 2) * (F * 2) + (((ln & 3) ^ (v << 1)) * 16);
    TS(2);
    await<4>(L, 3);
    LD_KV(KV1, 32, PKV);
    VMCNT(0);
    TS(11);
    bar_raw();
    v4u wt[48];
    const Base3 wdst1(L + v * 1024 + ln * 16);
#define GUN(s_, P_) (P_GU + ((s_) < 16 ? 6 * (s_) + 2 * (P_) : 96 + 32 * (P_) + 2 * ((s_) - 16)))
#define GUR(s_, P_) __builtin_amdgcn_raw_buffer_load_b128(rgu, NBV(vg), ((P_) == 2 ? sg2 : sg + (P_) * 64) + (s_) * 16 * (F * 2), 0)
#define GUW(s_, P_, r_) *(LAS v4u*)wdst1.template at<slot(GUN(s_, P_))>() = wt[r_]
#define LD_GU2(S0, S1, PC) static_for<S0, S1>([&](auto S) { constexpr int s_ = S.value; \
      gldsi<slot(GUN(s_, 0)), s_ * 16 * (F * 2)>(rgu, vg, sg, l1); SB0(); \
      wt[2 * (s_ - 16)] = GUR(s_, 1); wt[2 * (s_ - 16) + 1] = GUR(s_, 2); PACE(PC); SB0(); })
#define LD_WD(T0, T1) static_for<T0, T1>([&](auto T) { \
      gldsi<slot(P_WD + 4 * T.value), pair_of(T.value) * 32 * (D * 2)>(rwd, vd, sd, l2); \
      gldsi<slot(P_WD + 4 * T.value) + 1024, pair_of(T.value) * 32 * (D * 2) + 64>(rwd, vd, sd, l2); })
#define DONE(k) do { RELOAD(); mark(L, H_PP, v, k); RELOAD(); } while (0)
    await<4>(L, 2);
    gldsi<L_CF, 0>(v ? rwo : rqkv, (rank * 16 + (ln >> 2)) * (v ? D * 2 : 3 * D * 2) + (xcd & 1) * 512 + (ln & 3) * 128, 0u, l1);
    LD_Q(0, 16, 0);
    TS(3);
    bar_raw();
    VMCNT(0);
    bar_raw();
    gldsi<L_CF, 0>(rwd, v ? 1u << 30 : ((xcd >> 1) * 32 + rank + 128 * (ln >> 2)) * (D * 2) + (xcd & 1) * 512 + (ln & 3) * 128, 0u, l1);
    LD_DN(rwo, so, P_WO, 0, 16, 1);
    LD_GU(0, 6, PGU);
    VMCNT(18);
    TS(4);
    bar_raw();
    LD_GU(6, 12, PGU);
    await<4>(L, 4);
    static_for<0, 12>([&](auto J) { wt[32 + J.value] = GUR(12 + J.value / 3, J.value % 3); SB0(); });
    static_for<16, 20>([&](auto S) { constexpr int s_ = S.value;
      wt[28 + s_] = GUR(s_, 0); wt[2 * (s_ - 16)] = GUR(s_, 1); wt[2 * (s_ - 16) + 1] = GUR(s_, 2); SB0(); });
    VMCNT(24);
    TS(5);
    bar_raw();
    static_for<0, 12>([&](auto J) { GUW(12 + J.value / 3, J.value % 3, 32 + J.value); });
    static_for<16, 20>([&](auto S) { GUW(S.value, 0, 28 + S.value); });
    DONE(1);
    LD_GU2(20, 24, PGU);
    await_cp(L, 1);
    LD_GU2(24, 32, 0);
    {
      const u32 tdn = ((2 * rank + v) * 16 + (ln >> 2)) * (D * 2) + (xcd & 1) * 512 + (ln & 3) * 128;
      gldsi<L_T, 0>(rwd, tdn, 0u, l1);
      gldsi<L_T, 1024 * (D * 2)>(rwd, tdn, 0u, l1);
    }
    VMCNT(26);
    DONE(2);
    __builtin_amdgcn_s_waitcnt(0x0F72);
    VMCNT(2);
    DONE(3);
    TS(6);
    await_cp(L, 2);
    static_for<16, 32>([&](auto S) { GUW(S.value, 1, 2 * (S.value - 16)); });
    DONE(4);
    static_for<16, 32>([&](auto S) { GUW(S.value, 2, 2 * (S.value - 16) + 1); });
    DONE(5);
    TS(7);
    await_cp(L, 3);
    LD_WD(0, 10);
    await_cp(L, 4);
    LD_WD(10, 14);
    VMCNT(0);
    DONE(100);
    TS(8);
    TS_DUMP();
    return;
  }

  __builtin_amdgcn_s_setprio(2);
  const int s2 = w & 1, kh = w >> 1;
  const int strip2 = rowh * 2 + s2, ncol = colg * 64 + kh * 32;
  v4u aphi0, aphi1;
  u32 tag;
  {
    float xf[16];
#pragma unroll
    for (int i = 0; i < 8; ++i) { xf[2 * i] = lo16(r0[i >> 2][i & 3]); xf[2 * i + 1] = hi16(r0[i >> 2][i & 3]); }
    float sm = (((xf[0] + xf[1]) + (xf[2] + xf[3])) + ((xf[4] + xf[5]) + (xf[6] + xf[7]))) + (((xf[8] + xf[9]) + (xf[10] + xf[11])) + ((xf[12] + xf[13]) + (xf[14] + xf[15])));
    sm = dpp_add<DPP_X1>(sm); sm = dpp_add<DPP_X2>(sm); sm = dpp_add<DPP_HM>(sm);
    const float mw = sm * (1.f / 128.f);
    float q[4];
#pragma unroll
    for (int i = 0; i < 4; ++i) {
      const float d0 = xf[4 * i] - mw, d1 = xf[4 * i + 1] - mw, d2 = xf[4 * i + 2] - mw, d3 = xf[4 * i + 3] - mw;
      q[i] = (d0 * d0 + d1 * d1) + (d2 * d2 + d3 * d3);
    }
    float qq = (q[0] + q[1]) + (q[2] + q[3]);
    qq = dpp_add<DPP_X1>(qq); qq = dpp_add<DPP_X2>(qq); qq = dpp_add<DPP_HM>(qq);
    *(volatile LAS f32x2*)(L + L_KVF + (lane >> 3) * 32 + w * 8) = f32x2{mw, qq};
    sig(L, 1, w);
    await<4>(L, 1);
    const f32x4 pa = *(LAS f32x4*)(L + L_KVF + (lane >> 3) * 32), pc = *(LAS f32x4*)(L + L_KVF + (lane >> 3) * 32 + 16);
    const float mean = ((pa[0] + pa[2]) + (pc[0] + pc[2])) * 0.25f;
    const float f0 = pa[0] - mean, f1 = pa[2] - mean, f2 = pc[0] - mean, f3 = pc[2] - mean;
    const float m2 = ((pa[1] + pa[3]) + (pc[1] + pc[3])) + 128.f * ((f0 * f0 + f1 * f1) + (f2 * f2 + f3 * f3));
    const float m1 = rb(mean), r1 = rcp(rb(__builtin_amdgcn_sqrtf(rb(rb(m2 * (1.f / D)) + 1e-5f))));
    float xn[16];
#pragma unroll
    for (int i = 0; i < 8; ++i) {
      const u32 d = pk(xf[2 * i] - m1, xf[2 * i + 1] - m1);
      const u32 t = pk(lo16(d) * r1, hi16(d) * r1);
      xn[2 * i] = lo16(t); xn[2 * i + 1] = hi16(t);
    }
#pragma unroll
    for (int i = 0; i < 16; ++i) asm volatile("" : "+v"(xn[i]));
    tag = __builtin_amdgcn_readfirstlane(epoch) + 1u;
    TS(1);
    await<1>(L, 0);
    __builtin_amdgcn_s_sleep(PRE_MOD);
    TS(21);
    v4u g[8];
    {
      u32 todo = 0xFFu;
      for (u32 spins = 0; spins < SPIN_CAP && todo; ++spins) {
        RELOAD();
        static_for<0, 8>([&](auto I) {
          constexpr int i = I.value;
          if ((todo >> i) & 1u) g[i] = ld128(rs, lane * 16, OFF_MODR + panel * 32768 + (i & 3) * 8192 + (i >> 2) * 4096 + w * 1024);
        });
        todo = 0;
        static_for<0, 8>([&](auto I) { if (__builtin_amdgcn_ballot_w64((g[I.value].y != tag) | (g[I.value].w != tag))) todo |= 1u << I.value; });
      }
    }
    {
      u32 pr[2];
#pragma unroll
      for (int t = 0; t < 2; ++t)
        pr[t] = pk((__uint_as_float(g[4 * t].x) + __uint_as_float(g[4 * t + 1].x)) + (__uint_as_float(g[4 * t + 2].x) + __uint_as_float(g[4 * t + 3].x)),
                   (__uint_as_float(g[4 * t].z) + __uint_as_float(g[4 * t + 1].z)) + (__uint_as_float(g[4 * t + 2].z) + __uint_as_float(g[4 * t + 3].z)));
      *(LAS u32*)(L + L_T + w * TW + lane * 4) = pr[0];
      *(LAS u32*)(L + L_T + w * TW + 256 + lane * 4) = pk(1.f + lo16(pr[1]), 1.f + hi16(pr[1]));
    }
    v4u sav[2], cav[2];
#pragma unroll
    for (int i = 0; i < 2; ++i) {
      sav[i] = *(LAS v4u*)(L + L_T + w * TW + i * 128 + (lane & 7) * 16);
      cav[i] = *(LAS v4u*)(L + L_T + w * TW + 256 + i * 128 + (lane & 7) * 16);
    }
    sig(L, 3, w);
    TS(2);
    const int strip = mem >> 2, r = 8 * (mem & 3) + (lane >> 3);
#pragma unroll
    for (int i = 0; i < 2; ++i) {
      v4u o;
#pragma unroll
      for (int j = 0; j < 4; ++j) {
        const u32 sa = sav[i][j], sc = cav[i][j];
        const u32 t = pk(xn[8 * i + 2 * j] * lo16(sc), xn[8 * i + 2 * j + 1] * hi16(sc));
        o[j] = pk(lo16(t) + lo16(sa), hi16(t) + hi16(sa));
      }
      st128(o, rs, (4 * i + ((lane & 7) >> 1)) * 1024 + (r + 32 * (lane & 1)) * 16, pb + PN_H + strip * 32768 + w * 8192);
    }
    await<4>(L, 3);
    publish(rs, fw + FL_H + strip, (mem & 3) * 4 + w, lane);
  }
  TS(3);

  const int trq = (lane & 15) >> 2, trp = lane & 3, trb = (lane >> 4) & 1;
  const f32x16 zero16 = {0.f, 0.f, 0.f, 0.f, 0.f, 0.f, 0.f, 0.f, 0.f, 0.f, 0.f, 0.f, 0.f, 0.f, 0.f, 0.f};
  u32 phq[16];
  v4u wb[PD];
  {
    const Base3 wnat(L + (8 * hh + trq) * 64 + trb * 32 + trp * 8);
    waitn(rs, fw + FL_H + w, 0xFFFFu, lane);
    TS(4);
    v4u a[32];
    ldfrags<W0>(a, rs, lane * 16, pb + PN_H + w * 32768);
    bar_raw();
    {
      const int hs = (l31 >> 2) & 1;
      const LAS u16* wp = (const LAS u16*)(L + L_CF + 2048) + hs * 256 + 4 * hh * 16 + (l31 & 3) + 4 * (l31 >> 3);
      u32 e[8];
#pragma unroll
      for (int j = 0; j < 8; ++j) e[j] = wp[(8 * (j >> 2) + (j & 3)) * 16];
      const v4u av = {e[0] | (e[1] << 16), e[2] | (e[3] << 16), e[4] | (e[5] << 16), e[6] | (e[7] << 16)};
      const v4u z = {0, 0, 0, 0};
      aphi0 = hs == 0 ? av : z; aphi1 = hs == 1 ? av : z;
      asm volatile("" : "+v"(aphi0), "+v"(aphi1));
    }
    static_for<0, PD>([&](auto U) { wb[U.value] = ldw<256>(wnat.at<slot(P_KV + U.value)>()); });
    SB0();
    f32x16 kacc = zero16, vacc = zero16;
    static_for<0, 64>([&](auto U) {
      constexpr int u = U.value;
      const v4u cur = wb[u % PD];
      if constexpr (u + PD < 64) wb[u % PD] = ldw<256>(wnat.at<slot(P_KV + u + PD)>());
      if constexpr (u & 1) mfma_pin(a[u >> 1], cur, vacc); else mfma_pin(cur, a[u >> 1], kacc);
      if constexpr (!(u & 1) && (u >> 1) + W0 < 32) a[(u >> 1) + W0] = ldfrag<(u >> 1) + W0>(rs, lane * 16, pb + PN_H + w * 32768);
      if constexpr (u == 2 * (32 - W0)) sig(L, 2, w);
      SB0();
    });
    TS(5);
    f32x16 P = mfma(aphi0, pack8<0>(kacc), zero16);
    P = mfma(aphi1, pack8<8>(kacc), P);
    const v4u vb0 = pack8<0>(vacc), vb1 = pack8<8>(vacc);
    u32 phk[16];
    feat(P, phk);
    LAS unsigned char* T = L + L_T + w * TW;
#pragma unroll
    for (int c = 0; c < 4; ++c)
      *(LAS v4u*)(T + lane * 64 + ((c ^ ((lane >> 1) & 3)) * 16)) = v4u{phk[4 * c], phk[4 * c + 1], phk[4 * c + 2], phk[4 * c + 3]};
    const v4u ones = {0x3F803F80u, 0x3F803F80u, 0x3F803F80u, 0x3F803F80u};
    LAS unsigned char* tr = T + (4 * hh + trq) * 64 + (((2 * trb + (trp >> 1)) ^ ((2 * hh + (trq >> 1)) & 3)) * 16) + 8 * (trp & 1);
    const v4u t0 = ldw<512>(tr), t1 = ldw<512>(tr + 1024), t2 = ldw<512>(tr + 2048), t3 = ldw<512>(tr + 3072);
    f32x16 D0 = mfma(t0, trb ? ones : vb0, zero16);
    f32x16 D1 = mfma(t2, trb ? vb0 : ones, zero16);
    D0 = mfma(t1, trb ? ones : vb1, D0);
    D1 = mfma(t3, trb ? vb1 : ones, D1);
    {
      LAS unsigned char* pe = T + lane * 16;
      LAS unsigned char* pz = T + 1024 + (lane >> 4) * 16;
      LAS unsigned char* p0 = trb ? pz : pe;
      LAS unsigned char* p1 = trb ? pe : pz;
#pragma unroll
      for (int g = 0; g < 4; ++g) *(LAS f32x4*)(p0 + g * 1088) = f32x4{D0[4 * g], D0[4 * g + 1], D0[4 * g + 2], D0[4 * g + 3]};
#pragma unroll
      for (int g = 0; g < 4; ++g) *(LAS f32x4*)(p1 + g * 1088) = f32x4{D1[4 * g], D1[4 * g + 1], D1[4 * g + 2], D1[4 * g + 3]};
    }
    TS(6);
    bar();
    {
      f32x4 e[4], z[4];
#pragma unroll
      for (int v = 0; v < 4; ++v) {
        e[v] = *(LAS f32x4*)(L + L_T + v * TW + w * 1088 + lane * 16);
        z[v] = *(LAS f32x4*)(L + L_T + v * TW + w * 1088 + 1024 + (lane & 3) * 16);
      }
      SB0();
      const int idx = (mem * 4 + w) * 16 + panel;
      st128(__builtin_bit_cast(v4u, (e[0] + e[1]) + (e[2] + e[3])), rs, lane * 16, OFF_KVE + idx * 1024);
      st128(__builtin_bit_cast(v4u, (z[0] + z[1]) + (z[2] + z[3])), rs, lane < 4 ? lane * 16 : WS_BYTES, OFF_KVZ + idx * 64);
      publish(rs, FL_KV + mem * 2 + (w >> 1), (w & 1) * 16 + panel, lane);
      bar_raw();
      static_for<0, PD>([&](auto U) { wb[U.value] = ldw<256>(wnat.at<slot(P_Q + U.value)>()); });
    }
    TS(7);
    f32x16 qacc = zero16;
    static_for<0, 32>([&](auto U) {
      constexpr int u = U.value;
      const v4u cur = wb[u % PD];
      if constexpr (u + PD < 32) wb[u % PD] = ldw<256>(wnat.at<slot(P_Q + u + PD)>());
      mfma_pin(cur, a[u], qacc);
      SB0();
    });
    P = mfma(aphi0, pack8<0>(qacc), zero16);
    P = mfma(aphi1, pack8<8>(qacc), P);
    feat(P, phq);
#pragma unroll
    for (int i = 0; i < 16; ++i) asm volatile("" : "+v"(phq[i]));
  }
  v2u xres[4];
  {
    const u16* xr = p.x + (size_t)(panel * 128 + strip2 * 32 + l31) * D + ncol + 4 * hh;
#pragma unroll
    for (int i = 0; i < 4; ++i) xres[i] = *(const GAS v2u*)(xr + 8 * i);
  }
  TS(8);
  bar_raw();
  TS(9);
  {
    const bool on = (l31 >> 4) == hh;
    const v4u zv = {0, 0, 0, 0};
    f32x16 num = zero16, den = zero16;
    v4u ak[4], az[4];
#pragma unroll
    for (int s4 = 0; s4 < 4; ++s4) { az[s4] = *(LAS v4u*)(L + L_ZF + s4 * 32 + hh * 16); ak[s4] = *(LAS v4u*)(L + L_KVF + s4 * 512 + l31 * 16); }
    SB0();
#pragma unroll
    for (int s4 = 0; s4 < 4; ++s4) den = mfma(on ? az[s4] : zv, v4u{phq[4 * s4], phq[4 * s4 + 1], phq[4 * s4 + 2], phq[4 * s4 + 3]}, den);
#pragma unroll
    for (int s4 = 0; s4 < 4; ++s4) num = mfma(on ? ak[s4] : zv, v4u{phq[4 * s4], phq[4 * s4 + 1], phq[4 * s4 + 2], phq[4 * s4 + 3]}, num);
    const float d0 = rcp(rb(rb(den[0]) + 1e-6f)), d1 = rcp(rb(rb(den[8]) + 1e-6f));
    const v4u n0 = pack8<0>(num), n1 = pack8<8>(num);
    v4u o0, o1;
#pragma unroll
    for (int j = 0; j < 4; ++j) { o0[j] = pk(lo16(n0[j]) * d0, hi16(n0[j]) * d0); o1[j] = pk(lo16(n1[j]) * d1, hi16(n1[j]) * d1); }
    st128(o0, rs, lane * 16, pb + PN_O + w * 32768 + (2 * mem) * 1024);
    st128(o1, rs, lane * 16 + 1024, pb + PN_O + w * 32768 + (2 * mem) * 1024);
  }
#define WDN_BASES() const Base3 wown(L + (4 * hh + trq) * 64 + trb * 32 + trp * 8 + 3 * kh * 1024), woth(L + (4 * hh + trq) * 64 + trb * 32 + trp * 8 + (kh + 1) * 1024)
#define WDN(base, u) ldw<512>((((u) & 1) ? wown : woth).template at<slot((base) + 4 * ((u) >> 1))>())
  u32 x1p[8];
  {
    WDN_BASES();
    static_for<0, PD>([&](auto U) { wb[U.value] = WDN(P_WO, U.value); });
    publish(rs, fw + FL_O + w, mem, lane);
    TS(10);
    waitn(rs, fw + FL_O + strip2, 0xFFu << (kh * 8), lane);
    TS(11);
    v4u a[16];
    ldfrags<16>(a, rs, lane * 16, pb + PN_O + strip2 * 32768 + kh * 16384);
    sig(L, 4, w);
    SB0();
    f32x16 c0 = zero16, c1 = zero16;
    static_for<0, 32>([&](auto U) {
      constexpr int u = U.value;
      const v4u cur = wb[u % PD];
      if constexpr (u + PD < 32) wb[u % PD] = WDN(P_WO, u + PD);
      if constexpr (u & 1) mfma_pin(cur, a[u >> 1], c0); else mfma_pin(cur, a[u >> 1], c1);
      SB0();
    });
#pragma unroll
    for (int g = 0; g < 4; ++g) *(LAS f32x4*)(L + L_T + w * TW + g * 1024 + lane * 16) = f32x4{c1[4 * g], c1[4 * g + 1], c1[4 * g + 2], c1[4 * g + 3]};
    TS(12);
    bar();
    f32x4 o[4], ga[4], sc[4];
#pragma unroll
    for (int g = 0; g < 4; ++g) {
      o[g] = *(LAS f32x4*)(L + L_T + (w ^ 2) * TW + g * 1024 + lane * 16);
      ga[g] = *(LAS f32x4*)(L + L_VEC + (kh * 32 + 8 * g + 4 * hh) * 4);
      sc[g] = *(LAS f32x4*)(L + L_VEC + 256 + (kh * 32 + 8 * g + 4 * hh) * 4);
    }
    SB0();
    float x1[16], sp[4];
    u32 yp[8];
#pragma unroll
    for (int g = 0; g < 4; ++g) {
#pragma unroll
      for (int j = 0; j < 2; ++j) {
        const u32 y = pk(c0[4 * g + 2 * j] + o[g][2 * j], c0[4 * g + 2 * j + 1] + o[g][2 * j + 1]);
        const u32 t = pk(ga[g][2 * j] * lo16(y), ga[g][2 * j + 1] * hi16(y));
        const u32 r = pk(lo16(xres[g][j]) + lo16(t), hi16(xres[g][j]) + hi16(t));
        x1p[2 * g + j] = r;
        x1[4 * g + 2 * j] = lo16(r); x1[4 * g + 2 * j + 1] = hi16(r);
        yp[2 * g + j] = pk(lo16(r) * sc[g][2 * j], hi16(r) * sc[g][2 * j + 1]);
      }
    }
    const int tile = 2 * colg + kh;
    st128(v4u{yp[0], yp[1], yp[2], yp[3]}, rs, lane * 16, pb + PN_X1 + strip2 * 32768 + tile * 2048);
    st128(v4u{yp[4], yp[5], yp[6], yp[7]}, rs, lane * 16 + 1024, pb + PN_X1 + strip2 * 32768 + tile * 2048);
    SB0();
#pragma unroll
    for (int g = 0; g < 4; ++g) sp[g] = (x1[4 * g] + x1[4 * g + 1]) + (x1[4 * g + 2] + x1[4 * g + 3]);
    const float ss = (sp[0] + sp[1]) + (sp[2] + sp[3]);
    const float mt = fold32(ss, ss) * (1.f / 32.f);
    float q[4];
#pragma unroll
    for (int g = 0; g < 4; ++g) {
      const float d0 = x1[4 * g] - mt, d1 = x1[4 * g + 1] - mt, d2 = x1[4 * g + 2] - mt, d3 = x1[4 * g + 3] - mt;
      q[g] = (d0 * d0 + d1 * d1) + (d2 * d2 + d3 * d3);
    }
    const float qs = (q[0] + q[1]) + (q[2] + q[3]);
    const float qt = fold32(qs, qs);
    v4u sv = {__float_as_uint(mt), tag, __float_as_uint(qt), tag};
    asm volatile("" : "+v"(sv));
    publish(rs, fw + FL_X + strip2, tile, lane);
    st128(sv, rs, lane < 32 ? lane * 16 : WS_BYTES, pb + PN_ST + strip2 * 8192 + tile * 512);
  }
  asm volatile("" : "+v"(x1p[0]), "+v"(x1p[1]), "+v"(x1p[2]), "+v"(x1p[3]), "+v"(x1p[4]), "+v"(x1p[5]), "+v"(x1p[6]), "+v"(x1p[7]));
#define GU_S(u) ((u) < 96 ? (u) / 6 : 16 + (((u) - 96) % 32) / 2)
#define GU_I(u) ((u) < 96 ? (u) % 6 : 2 * (((u) - 96) / 32) + ((u) & 1))
#define WGU(u) ldw<512>((((u) & 1) ? wy : wx).template at<slot(P_GU + ((u) & ~1))>())
  {
    const Base3 wx(L + (4 * hh + trq) * 64 + trp * 8 + trb * (1024 + 32)), wy(L + (4 * hh + trq) * 64 + trp * 8 + (trb ? 1024 : 32));
    static_for<0, PD>([&](auto U) { wb[U.value] = WGU(U.value); });
    TS(13);
    waitn(rs, fw + FL_X + w, 0xFFFFu, lane);
    TS(14);
    constexpr int AW = 20;
    v4u a[AW];
    ldfrags<8>(a, rs, lane * 16, pb + PN_X1 + w * 32768);
    f32x16 acc[6];
#pragma unroll
    for (int i = 0; i < 6; ++i) acc[i] = zero16;
    float rr = 0.f;
    v4u bcor = {0, 0, 0, 0};
    v4u ho, cf0, cf1, pp;
    float t0 = 0.f, t1 = 0.f;
    const int go = pb + PN_G + (w * 96 + mem) * 1024;
    auto epi = [&](auto Ic, auto Kc) {
      constexpr int I = decltype(Ic)::value, k = decltype(Kc)::value, j = k >> 1;
      const f32x16& A = acc[I];
      if constexpr ((k & 1) == 0) { t0 = A[2 * j] * rr; t1 = A[2 * j + 1] * rr; asm volatile("" : "+v"(t0)); t0 *= A[8 + 2 * j]; }
      else {
        t1 *= A[9 + 2 * j];
        ho[j] = pk(t0, t1);
        if constexpr (j == 3) st128(ho, rs, (I < 4 || three) ? lane * 16 : WS_BYTES, go + ((I & 1) * 48 + (I >> 1) * 16) * 1024);
      }
    };
    static_for<0, 192>([&](auto Uc) {
      constexpr int u = decltype(Uc)::value, s = GU_S(u), i = GU_I(u), q = (u - 96) % 32;
      if constexpr (u == 6) TS(22);
      if constexpr (u == 60) TS(23);
      if constexpr (u == 96) TS(24);
      if constexpr (u == 128) TS(25);
      if constexpr (u == 160) TS(26);
      if constexpr (u == 56) mark(L, H_CP, w, 2u);
      if constexpr (u == 96 || u == 128 || u == 160) mark(L, H_CP, w, u / 32);
      if constexpr (u == 62 || u == 94 || u == 102 || u == 112 || u == 118 || u == 150) pp = peek(L, H_PP);
      if constexpr (u == 66) need_pp<false>(L, pp, 1u);
      if constexpr (u == 98) need_pp<false>(L, pp, 2u);
      if constexpr (u == 106) need_pp<false>(L, pp, 3u);
      if constexpr (u == 116) need_pp<true>(L, pp, 3u);
      if constexpr (u == 122) need_pp<false>(L, pp, 4u);
      if constexpr (u == 154) need_pp<false>(L, pp, 5u);
      const v4u cur = wb[u % PD];
      if constexpr (u + PD < 192) wb[u % PD] = WGU(u + PD);
      if constexpr (u >= 96 && q == 24) cf0 = *(LAS v4u*)(L + L_CF + i * 512 + l31 * 16);
      if constexpr (u >= 96 && q == 25) cf1 = *(LAS v4u*)(L + L_CF + i * 512 + l31 * 16);
      mfma_pin(cur, a[s % AW], acc[i]);
      if constexpr (u < 96 && i == 5 && s + AW < 32) a[s % AW] = ldfrag<s + AW>(rs, lane * 16, pb + PN_X1 + w * 32768);
      if constexpr (u < 2 * (AW - 8) && (u & 1)) a[8 + (u >> 1)] = ldfrag<8 + (u >> 1)>(rs, lane * 16, pb + PN_X1 + w * 32768);
      if constexpr (u == 2 * (AW - 8)) mark(L, H_CP, w, 1u);
      if constexpr (u == 117) bcor = *(LAS v4u*)(L + L_KVF + w * 512 + l31 * 16);
      if constexpr (u == 123) { rr = __uint_as_float(bcor.w); bcor = v4u{hh ? 0u : bcor.x, hh ? 0u : bcor.y, hh ? 0u : bcor.z, 0u}; }
      if constexpr (u >= 96 && q == 30) mfma_pin(cf0, bcor, acc[i]);
      if constexpr (u >= 96 && q == 31) mfma_pin(cf1, bcor, acc[i]);
      if constexpr (u >= 128 && q >= 1 && q < 17) epi(IC<(i & 6) - 2 + (q - 1) / 8>{}, IC<(q - 1) % 8>{});
      if constexpr (u == 164) publish(rs, fw + FL_G + w * 3, mem, lane);
      SB0();
    });
    mark(L, H_CP, w, 6u);
    TS(15);
    pp = peek(L, H_PP);
    static_for<0, 7>([&](auto Kc) { epi(IC<4>{}, Kc); });
    SB0();
    publish(rs, fw + FL_G + w * 3 + 1, mem, lane);
    SB0();
    static_for<7, 16>([&](auto Kc) { epi(IC<4 + Kc.value / 8>{}, IC<Kc.value % 8>{}); });
    need_pp<true>(L, pp, 6u);
    TS(16);
  }
  {
    WDN_BASES();
    static_for<0, PD>([&](auto U) { wb[U.value] = WDN(P_WD, U.value); });
    const int fi = fw + FL_G + strip2 * 3;
    u32 fl = ask(rs, fi, lane);
    publish(rs, fw + FL_G + w * 3 + 2, mem, lane);
    TS(17);
    poll(fl, rs, fi, 0xFFFFu, lane);
    TS(18);
    v4u a[43], pp;
    const int gi = pb + PN_G + (strip2 * 96 + kh * 48) * 1024;
    fl = ask(rs, fi + 1, lane);
    SB0();
    ldfrags<W0>(a, rs, lane * 16, gi);
    f32x16 c0 = zero16, c1 = zero16;
    static_for<0, 86>([&](auto U) {
      constexpr int u = U.value;
      if constexpr (u == 18) mark(L, H_CP, w, 7u);
      if constexpr (u == 26 || u == 42 || u == 58) pp = peek(L, H_PP);
      if constexpr (u == 30 || u == 46 || u == 62) need_pp<true>(L, pp, 7u + (u - 30) / 16);
      if constexpr (u == 2 * (16 - W0)) poll(fl, rs, fi + 1, 0xFFFFu, lane);
      if constexpr (u == 2 * (32 - W0) - 26) fl = ask(rs, fi + 2, lane);
      if constexpr (u == 2 * (32 - W0)) poll(fl, rs, fi + 2, 0x7FFu, lane);
      const v4u cur = wb[u % PD];
      if constexpr (u + PD < 86) wb[u % PD] = WDN(P_WD, u + PD);
      if constexpr (u & 1) mfma_pin(cur, a[u >> 1], c0); else mfma_pin(cur, a[u >> 1], c1);
      if constexpr (!(u & 1) && (u >> 1) + W0 < 43) a[(u >> 1) + W0] = ldfrag<(u >> 1) + W0>(rs, lane * 16, gi);
      SB0();
    });
#pragma unroll
    for (int g = 0; g < 4; ++g) *(LAS f32x4*)(L + L_T + w * TW + g * 1024 + lane * 16) = f32x4{c1[4 * g], c1[4 * g + 1], c1[4 * g + 2], c1[4 * g + 3]};
    f32x4 gb[4];
#pragma unroll
    for (int g = 0; g < 4; ++g) gb[g] = *(LAS f32x4*)(L + L_VEC + 512 + (kh * 32 + 8 * g + 4 * hh) * 4);
    TS(19);
    bar();
    f32x4 o[4];
#pragma unroll
    for (int g = 0; g < 4; ++g) o[g] = *(LAS f32x4*)(L + L_T + (w ^ 2) * TW + g * 1024 + lane * 16);
    SB0();
    u16* orow = p.out + (size_t)(panel * 128 + strip2 * 32 + l31) * D + ncol + 4 * hh;
#pragma unroll
    for (int g = 0; g < 4; ++g) {
      v2u r;
#pragma unroll
      for (int j = 0; j < 2; ++j) {
        r[j] = pk(lo16(x1p[2 * g + j]) + gb[g][2 * j] * (c0[4 * g + 2 * j] + o[g][2 * j]),
                  hi16(x1p[2 * g + j]) + gb[g][2 * j + 1] * (c0[4 * g + 2 * j + 1] + o[g][2 * j + 1]));
      }
      *(GAS v2u*)(orow + 8 * g) = r;
    }
  }
  if (b == 0 && tid == 0) __hip_atomic_store((GAS u32*)(ws + OFF_EPOCH), tag, RLX_AGENT);
}
}

extern "C" void kernel_launch(void* const* d_in, const int* in_sizes, int n_in, void* d_out, int out_size, void* d_ws,
                              size_t ws_size, hipStream_t stream) {
  using namespace mk;
  Params p;
  p.x = (const u16*)d_in[0]; p.wqkv = (const u16*)d_in[1]; p.wphi = (const u16*)d_in[2]; p.wo = (const u16*)d_in[3];
  p.wg = (const u16*)d_in[5]; p.wu = (const u16*)d_in[6]; p.wd = (const u16*)d_in[7]; p.c = (const u16*)d_in[8];
  p.wmod = (const u16*)d_in[9]; p.out = (u16*)d_out; p.ws = (unsigned char*)d_ws;
  (void)hipFuncSetAttribute((const void*)mega, hipFuncAttributeMaxDynamicSharedMemorySize, LDS_BYTES);
  mega<<<256, 512, LDS_BYTES, stream>>>(p);
}
```

```cpp
#define MK_LEAK 2
#define MK_LEAKB 77
#include <hip/hip_runtime.h>

namespace mk {
typedef unsigned short u16;
typedef unsigned int u32;
typedef unsigned long long u64;
typedef u32 v4u __attribute__((ext_vector_type(4)));
typedef u32 v2u __attribute__((ext_vector_type(2)));
typedef float f32x2 __attribute__((ext_vector_type(2)));
typedef float f32x4 __attribute__((ext_vector_type(4)));
typedef float f32x16 __attribute__((ext_vector_type(16)));
typedef __bf16 bf16x2 __attribute__((ext_vector_type(2)));
typedef __bf16 bf16x8 __attribute__((ext_vector_type(8)));
typedef short s16x4 __attribute__((ext_vector_type(4)));
#define LAS __attribute__((address_space(3)))
#define GAS __attribute__((address_space(1)))
#define DI __device__ __forceinline__
#define RLX_AGENT __ATOMIC_RELAXED, __HIP_MEMORY_SCOPE_AGENT
#define SB0() __builtin_amdgcn_sched_barrier(0)
typedef __amdgpu_buffer_rsrc_t rsrc_t;

constexpr int D = 512, F = 1376;
constexpr int OFF_EPOCH = 0;
constexpr int OFF_MODG = 64 << 10;
constexpr int OFF_KVZ = 128 << 10;
constexpr int OFF_CG = 192 << 10;
constexpr int CG_PART = 2 * F * 16;
constexpr int OFF_ARR = 384 << 10;
constexpr int OFF_MODR = 512 << 10;
constexpr int OFF_KVE = 1 << 20;
constexpr int OFF_PAN = 2 << 20;
constexpr int PN_H = 0;
constexpr int PN_O = 128 << 10;
constexpr int PN_X1 = 256 << 10;
constexpr int PN_G = 384 << 10;
constexpr int PN_ST = 768 << 10;
constexpr int PN_FL = 800 << 10;
constexpr int FL_H = 0, FL_O = 4, FL_X = 8, FL_G = 12, FL_KV = 384;
constexpr int PSTR = (848 << 10) + 4224;
constexpr int WS_BYTES = 16 << 20;
constexpr int OFF_TS = 32 << 20;
constexpr int RING = 136;
constexpr int L_T = RING * 1024, TW = 4352;
constexpr int L_KVF = L_T + 4 * TW;
constexpr int L_ZF = L_KVF + 2048;
constexpr int L_VEC = L_ZF + 256;
constexpr int L_CF = L_VEC + 768;
constexpr int L_HINT = L_CF + 3072;
constexpr int LDS_BYTES = L_HINT + 128;
static_assert(LDS_BYTES <= 163840, "LDS");
constexpr int P_KV = 0, P_Q = 64, P_WO = 96, P_GU = 160, P_WD = 352;
constexpr int H_CP = 6, H_PP = 7;
constexpr int pair_of(int t) { return ((t & 15) < 11 ? 3 * (t & 15) : 2 * (t & 15) + 11) + (t >> 4); }
constexpr int slot(int n) { return (n % RING) * 1024; }
constexpr int AUX_SC1 = 16;
constexpr u32 SPIN_CAP = 1u << 16;
#define PRE_MOD 8
#define HI_DELAY 8
#define POLL_GAP 6
#define PRE_FL 4
#define KV1 32
#define PGU 4
#define PKV 0
#define GPUB 29
#define LPRIO 0
#define APRIO 1
#define TMOD 0
#define CW 1
#define DW 2
#define CSLP 3
#define PG2 2
#define W0 12
constexpr int PD = 5;
constexpr int CODE_KB = 36;

#define NBV(x) (x)
struct Params {
  const u16 *x, *wqkv, *wphi, *wo, *wg, *wu, *wd, *c, *wmod;
  u16* out;
  unsigned char* ws;
};

template <int V> struct IC { static constexpr int value = V; };
template <int I, int N, class Fn> DI void static_for(Fn&& f) {
  if constexpr (I < N) { f(IC<I>{}); static_for<I + 1, N>(f); }
}
DI float lo16(u32 v) { return __uint_as_float(v << 16); }
DI float hi16(u32 v) { return __uint_as_float(v & 0xffff0000u); }
DI u32 pk(float a, float b) { f32x2 v = {a, b}; return __builtin_bit_cast(u32, __builtin_convertvector(v, bf16x2)); }
DI float rb(float a) { return lo16(pk(a, 0.f)); }
DI float rcp(float a) { return __builtin_amdgcn_rcpf(a); }
DI f32x16 mfma(v4u a, v4u b, f32x16 c) {
  return __builtin_amdgcn_mfma_f32_32x32x16_bf16(__builtin_bit_cast(bf16x8, a), __builtin_bit_cast(bf16x8, b), c, 0, 0, 0);
}
DI void mfma_pin(v4u a, v4u b, f32x16& c) {
  c = mfma(a, b, c);
  asm volatile("" : "+v"(c));
}
template <int O> DI v4u pack8(const f32x16& x) {
  v4u r; r.x = pk(x[O], x[O + 1]); r.y = pk(x[O + 2], x[O + 3]); r.z = pk(x[O + 4], x[O + 5]); r.w = pk(x[O + 6], x[O + 7]); return r;
}
template <int T> DI v4u ldw(LAS unsigned char* p) {
  s16x4 a = __builtin_amdgcn_ds_read_tr16_b64_v4i16((LAS s16x4*)p);
  s16x4 b = __builtin_amdgcn_ds_read_tr16_b64_v4i16((LAS s16x4*)(p + T));
  return __builtin_bit_cast(v4u, __builtin_shufflevector(a, b, 0, 1, 2, 3, 4, 5, 6, 7));
}
struct Base3 {
  u32 b[3];
  DI explicit Base3(LAS unsigned char* p) {
    b[0] = (u32)(size_t)p; b[1] = b[0] + 65536u; b[2] = b[0] + 131072u;
    asm volatile("" : "+v"(b[0]), "+v"(b[1]), "+v"(b[2]));
  }
  template <int OFF> DI LAS unsigned char* at() const { return (LAS unsigned char*)(size_t)b[OFF >> 16] + (OFF & 65535); }
};
template <int LOFF, int SOFF> DI void gldsi(rsrc_t rs, u32 voff, u32 sbase, u32 lbase) {
  u32 t;
  asm volatile("s_add_u32 m0, %1, %2\n\ts_add_u32 %0, %3, %4\n\tbuffer_load_dwordx4 %5, %6, %0 offen lds"
               : "=&s"(t) : "s"(lbase), "i"(LOFF), "s"(sbase), "i"(SOFF), "v"(voff), "s"(rs) : "m0", "scc", "memory");
}
DI void bar() { asm volatile("s_waitcnt lgkmcnt(0)\n\ts_barrier" ::: "memory"); }
DI void bar_raw() { asm volatile("s_barrier" ::: "memory"); }
DI void drain() { asm volatile("s_waitcnt vmcnt(0)" ::: "memory"); }
#define VMCNT(n) asm volatile("s_waitcnt vmcnt(" #n ")" ::: "memory")
#define RELOAD() asm volatile("" ::: "memory")
DI v4u ld128(rsrc_t rs, int voff, int soff) { return __builtin_amdgcn_raw_buffer_load_b128(rs, voff, soff, AUX_SC1); }
DI v2u ld64(rsrc_t rs, int voff, int soff) { return __builtin_amdgcn_raw_buffer_load_b64(rs, voff, soff, AUX_SC1); }
DI void st128(v4u v, rsrc_t rs, int voff, int soff) { __builtin_amdgcn_raw_buffer_store_b128(v, rs, voff + soff, 0, AUX_SC1); }
DI void st64(v2u v, rsrc_t rs, int voff, int soff) { __builtin_amdgcn_raw_buffer_store_b64(v, rs, voff, soff, AUX_SC1); }
template <int S> DI v4u ldfrag(rsrc_t rs, int voff, int soff) { return ld128(rs, voff + (S & 3) * 1024, soff + (S >> 2) * 4096); }
template <int W, int N> DI void ldfrags(v4u (&a)[N], rsrc_t rs, int voff, int soff) {
  static_for<0, W>([&](auto S) { a[S.value] = ldfrag<S.value>(rs, voff, soff); SB0(); });
}
template <int CTRL> DI float dpp_add(float v) {
  return v + __uint_as_float(__builtin_amdgcn_update_dpp(0u, __float_as_uint(v), CTRL, 0xf, 0xf, false));
}
constexpr int DPP_X1 = 0xB1, DPP_X2 = 0x4E, DPP_HM = 0x141, DPP_ROR4 = 0x124, DPP_ROR8 = 0x128;
DI float fold32(float x, float y) {
  auto r = __builtin_amdgcn_permlane32_swap(__float_as_uint(x), __float_as_uint(y), false, false);
  return __uint_as_float(r[0]) + __uint_as_float(r[1]);
}
DI float fold16(float x, float y) {
  auto r = __builtin_amdgcn_permlane16_swap(__float_as_uint(x), __float_as_uint(y), false, false);
  return __uint_as_float(r[0]) + __uint_as_float(r[1]);
}
DI void red8q(const float (&a)[8], float& c0, float& c1) {
  const float b0 = fold32(a[0], a[4]), b1 = fold32(a[1], a[5]), b2 = fold32(a[2], a[6]), b3 = fold32(a[3], a[7]);
  c0 = fold16(b0, b2); c1 = fold16(b1, b3);
  c0 = dpp_add<DPP_ROR4>(c0); c1 = dpp_add<DPP_ROR4>(c1);
  c0 = dpp_add<DPP_ROR8>(c0); c1 = dpp_add<DPP_ROR8>(c1);
}
DI void red8(const float (&a)[8], float& c0, float& c1) {
  red8q(a, c0, c1);
  c0 = dpp_add<DPP_X1>(c0); c1 = dpp_add<DPP_X1>(c1);
  c0 = dpp_add<DPP_X2>(c0); c1 = dpp_add<DPP_X2>(c1);
}
DI void publish(rsrc_t rs, int word, int bit, int lane) {
  drain();
  (void)__builtin_amdgcn_raw_ptr_buffer_atomic_add_i32(1 << bit, rs, lane == 0 ? 0 : WS_BYTES, OFF_ARR + word * 128, 0);
}
DI u32 ask(rsrc_t rs, int word, int lane) { return __builtin_amdgcn_raw_ptr_buffer_atomic_add_i32(0, rs, lane == 0 ? 0 : WS_BYTES, OFF_ARR + word * 128, 0); }
DI void spin(rsrc_t rs, int word, u32 m, int lane) {
#pragma nounroll
  for (u32 spins = 0; spins < SPIN_CAP; ++spins)
    if ((__builtin_amdgcn_readfirstlane(ask(rs, word, lane)) & m) == m) break;
}
DI void poll(u32 f, rsrc_t rs, int word, u32 m, int lane) {
  if (__builtin_expect((__builtin_amdgcn_readfirstlane(f) & m) != m, 0)) spin(rs, word, m, lane);
  __builtin_amdgcn_fence(__ATOMIC_ACQUIRE, "wavefront");
  RELOAD();
}
DI void waitn(rsrc_t rs, int word, u32 m, int lane) {
  if (PRE_FL) __builtin_amdgcn_s_sleep(PRE_FL);
  spin(rs, word, m, lane);
  __builtin_amdgcn_fence(__ATOMIC_ACQUIRE, "wavefront");
  RELOAD();
}
DI void sig(LAS unsigned char* L, int i, int k) { *(volatile LAS u32*)(L + L_HINT + 16 * i + 4 * k) = 1u; }
template <int N> DI void await(LAS unsigned char* L, int i) {
#pragma nounroll
  for (u32 spins = 0; spins < (1u << 20); ++spins) {
    const v4u v = *(volatile LAS v4u*)(L + L_HINT + 16 * i);
    if (__builtin_amdgcn_readfirstlane(N == 1 ? v.x : (v.x & v.y & v.z & v.w))) break;
    __builtin_amdgcn_s_sleep(1);
  }
  RELOAD();
}
DI void mark(LAS unsigned char* L, int i, int k, u32 val) { *(volatile LAS u32*)(L + L_HINT + 16 * i + 4 * k) = val; }
DI v4u peek(LAS unsigned char* L, int i) { return *(volatile LAS v4u*)(L + L_HINT + 16 * i); }
DI u32 umin(u32 a, u32 b) { return a < b ? a : b; }
DI void await_cp(LAS unsigned char* L, u32 k) {
#pragma nounroll
  for (u32 spins = 0; spins < (1u << 20); ++spins) {
    const v4u v = peek(L, H_CP);
    if (__builtin_amdgcn_readfirstlane(umin(umin(v.x, v.y), umin(v.z, v.w))) >= k) break;
    __builtin_amdgcn_s_sleep(CSLP);
  }
  RELOAD();
}
template <bool AUX> DI u32 pp_min(v4u v) {
  u32 m = umin(v.x, v.y);
  if (AUX) m = umin(m, umin(v.z, v.w));
  return __builtin_amdgcn_readfirstlane(m);
}
template <bool AUX> DI void need_pp(LAS unsigned char* L, v4u v, u32 k) {
  if (__builtin_expect(pp_min<AUX>(v) < k, 0)) {
#pragma nounroll
    for (u32 spins = 0; spins < (1u << 20); ++spins)
      if (pp_min<AUX>(peek(L, H_PP)) >= k) break;
  }
  RELOAD();
}
DI void feat(const f32x16& P, u32 (&ph)[16]) {
#pragma clang fp contract(off)
  constexpr float L2E = 1.4426950408889634f;
  f32x2 p[8];
#pragma unroll
  for (int i = 0; i < 8; ++i) { const u32 t = pk(P[2 * i], P[2 * i + 1]); p[i] = f32x2{lo16(t), hi16(t)} * L2E; }
  float mx[4], mn[4];
#pragma unroll
  for (int i = 0; i < 4; ++i) {
    mx[i] = fmaxf(fmaxf(p[2 * i].x, p[2 * i].y), fmaxf(p[2 * i + 1].x, p[2 * i + 1].y));
    mn[i] = fminf(fminf(p[2 * i].x, p[2 * i].y), fminf(p[2 * i + 1].x, p[2 * i + 1].y));
  }
  const float mxl = fmaxf(fmaxf(mx[0], mx[1]), fmaxf(mx[2], mx[3])), mnl = fminf(fminf(mn[0], mn[1]), fminf(mn[2], mn[3]));
  f32x2 e1[8], e2[8];
#pragma unroll
  for (int i = 0; i < 8; ++i) { e1[i] = p[i] - mxl; e2[i] = mnl - p[i]; }
  SB0();
#pragma unroll
  for (int i = 0; i < 8; ++i) {
    e1[i] = f32x2{__builtin_amdgcn_exp2f(e1[i].x), __builtin_amdgcn_exp2f(e1[i].y)};
    e2[i] = f32x2{__builtin_amdgcn_exp2f(e2[i].x), __builtin_amdgcn_exp2f(e2[i].y)};
  }
  const f32x2 s1 = ((e1[0] + e1[1]) + (e1[2] + e1[3])) + ((e1[4] + e1[5]) + (e1[6] + e1[7]));
  const f32x2 s2 = ((e2[0] + e2[1]) + (e2[2] + e2[3])) + ((e2[4] + e2[5]) + (e2[6] + e2[7]));
  const float r1 = rcp(s1.x + s1.y), r2 = rcp(s2.x + s2.y);
#pragma unroll
  for (int i = 0; i < 8; ++i) { const f32x2 u = e1[i] * r1, v = e2[i] * r2; ph[i] = pk(u.x, u.y); ph[8 + i] = pk(v.x, v.y); }
}

#define TSCLK() __builtin_amdgcn_s_memrealtime()
#define TS(i) do { if ((i) == 0) { lk0 = __builtin_amdgcn_s_memrealtime(); __builtin_amdgcn_s_waitcnt(0xC07F); } if ((i) == MK_LEAK) { SB0(); lk1 = __builtin_amdgcn_s_memrealtime(); __builtin_amdgcn_s_waitcnt(0xC07F); SB0(); } } while (0)
#define TS_DUMP() do {} while (0)
#define TS_INIT() u64 lk0 = 0, lk1 = 0

__global__ void __launch_bounds__(512, 2) mega(Params p) {
  extern __shared__ __attribute__((aligned(16))) unsigned char smem[];
  LAS unsigned char* L = (LAS unsigned char*)smem;
  TS_INIT();
  TS(0);
  u64 pc0;
  asm volatile("s_getpc_b64 %0" : "=s"(pc0));
  const int tid = threadIdx.x, lane = tid & 63;
  int w = __builtin_amdgcn_readfirstlane(tid >> 6);
  const int b = blockIdx.x, xcd = b & 7, rank = b >> 3, panel = (xcd >> 1) * 4 + (rank >> 3), mem = (xcd & 1) * 8 + (rank & 7);
  const int hh = lane >> 5, l31 = lane & 31;
  unsigned char* ws = p.ws;
  const int rowh = mem & 1, colg = mem >> 1;
  const int pair0 = min(3 * mem, 2 * mem + 11);
  const bool three = mem < 11;
  const rsrc_t rs = __builtin_amdgcn_make_buffer_rsrc(ws, 0, WS_BYTES, 0x00020000);
  const int pb = OFF_PAN + panel * PSTR, fw = panel * 24;
  const int mcg = xcd * 8 + (rank >> 2), mkp = rank & 3;
  const rsrc_t rqkv = __builtin_amdgcn_make_buffer_rsrc((void*)p.wqkv, 0, D * 3 * D * 2, 0x00020000);
  const rsrc_t rwo = __builtin_amdgcn_make_buffer_rsrc((void*)p.wo, 0, D * D * 2, 0x00020000);
  const rsrc_t rgu = __builtin_amdgcn_make_buffer_rsrc((void*)((w & 1) ? p.wu : p.wg), 0, D * F * 2, 0x00020000);
  const rsrc_t rwd = __builtin_amdgcn_make_buffer_rsrc((void*)p.wd, 0, F * D * 2, 0x00020000);

  v4u r0[4];
  u32 cv0[4], epoch;
  u32 vq = (lane >> 2) * (3 * D * 2) + (lane & 3) * 16;
  asm volatile("" : "+v"(vq));
  {
    const u32 ld = (u32)(size_t)L + w * 1024;
    if (w <= 4) {
      const rsrc_t r1 = __builtin_amdgcn_make_buffer_rsrc((void*)(w == 4 ? p.wmod : p.x), 0, w == 4 ? D * 6 * D * 2 : 2048 * D * 2, 0x00020000);
      const int vo = w == 4 ? (128 * mkp + 4 * (lane >> 1)) * (6 * D * 2) + mcg * 32 + (lane & 1) * 16
                            : (panel * 128 + mem * 8 + (lane >> 3)) * (D * 2) + (lane & 7) * 16 + 256 * w;
#pragma unroll
      for (int i = 0; i < 4; ++i) r0[i] = __builtin_amdgcn_raw_buffer_load_b128(r1, vo, w == 4 ? i * (6 * D * 2) : i < 2 ? i * 128 : 1 << 30, 0);
      SB0();
    }
    {
      const rsrc_t r2 = __builtin_amdgcn_make_buffer_rsrc((void*)p.c, 0, w == 4 ? D * 2 : 0, 0x00020000);
      const v2u c2 = __builtin_amdgcn_raw_buffer_load_b64(r2, (128 * mkp + 4 * (lane >> 1)) * 2, 0, 0);
      cv0[0] = c2.x; cv0[1] = c2.y;
      epoch = __builtin_amdgcn_raw_buffer_load_b32(rs, 0, OFF_EPOCH, AUX_SC1);
      __builtin_amdgcn_raw_buffer_store_b32(0u, rs, (w == 4 && lane < 2) ? lane * 128 : WS_BYTES, OFF_ARR + b * 256, AUX_SC1);
      SB0();
    }
    if (w >= 5) {
      const rsrc_t rc = __builtin_amdgcn_make_buffer_rsrc((void*)pc0, 0, CODE_KB * 1024, 0x00020000);
      gldsi<slot(128), 0>(rc, lane < CODE_KB / 6 ? (u32)lane * 128u : 1u << 30, (u32)(((rank & 15) * 3 + w - 5) * (CODE_KB / 6) * 128), ld);
      const u32 tq = (rank * 16 + (lane >> 2)) * (3 * D * 2) + (xcd & 1) * 512 + (lane & 3) * 128;
      if (w != 5) gldsi<slot(128), 0>(rqkv, tq, (w - 5) * (D * 2), ld);
      else {
        gldsi<slot(128), 0>(rs, lane < 2 ? (u32)(pb + lane * (848 << 10)) : 1u << 30, 0u, ld);
        const rsrc_t rp = __builtin_amdgcn_make_buffer_rsrc((void*)p.wphi, 0, 32 * 256 * 2, 0x00020000);
        gldsi<L_CF + 2048, 0>(rp, lane * 16, mem * 1024, (u32)(size_t)L);
      }
    }
    if (tid < 32) *((LAS u32*)(L + L_HINT) + tid) = 0u;
    TS(27);
    bar();
    asm volatile("" : "+s"(w));
    if (w >= 6) {
      u32 lo = (u32)(size_t)L + (w & 1) * 1024, so = (D + (w & 1) * D + mem * 32) * 2;
#pragma nounroll
      for (int i = 0; i < KV1 / 2; ++i) {
        gldsi<0, 0>(rqkv, vq, so, lo);
        gldsi<2048, 16 * (3 * D * 2)>(rqkv, vq, so, lo);
        lo += 4096; so += 32 * (3 * D * 2);
      }
    }
    if (w == 4) {
      __builtin_amdgcn_s_setprio(3);
      TS(10);
      float sl[4], acc[8];
#pragma unroll
      for (int i = 0; i < 4; ++i) { const float c = (i & 1) ? hi16(cv0[i >> 1]) : lo16(cv0[i >> 1]); sl[i] = rb(c * rcp(1.f + __expf(-c))); }
      u32 tag = epoch;
      asm volatile("" : "+v"(tag));
      tag += 1u;
      TS(11);
#pragma unroll
      for (int j = 0; j < 8; ++j) acc[j] = 0.f;
#pragma unroll
      for (int i = 0; i < 4; ++i)
#pragma unroll
        for (int j = 0; j < 4; ++j) { acc[2 * j] += sl[i] * lo16(r0[i][j]); acc[2 * j + 1] += sl[i] * hi16(r0[i][j]); }
      float c0, c1;
      TS(12);
      red8q(acc, c0, c1);
      c0 = dpp_add<DPP_X2>(c0); c1 = dpp_add<DPP_X2>(c1);
      const int go = ((lane >> 1) & 7) * 32768 + (8 * (lane & 1) + 2 * (lane >> 4)) * 8;
      const v4u gv = {__float_as_uint(c0), tag, __float_as_uint(c1), tag};
      drain();
      st128(gv, rs, go, OFF_MODR + mkp * 8192 + mcg * 128);
      st128(gv, rs, go, OFF_MODR + 8 * 32768 + mkp * 8192 + mcg * 128);
      sig(L, 0, 0);
    }
    asm volatile("" : "+s"(w));
  }

  if (w == 4 || w == 5) {
    const int a = w & 1;
    u32 tag;
    tag = epoch;
    asm volatile("" : "+v"(tag));
    tag += 1u;
    drain();
    TS(1);
    bar();
    __builtin_amdgcn_s_setprio(APRIO);
    await<4>(L, 2);
    const int kp = xcd >> 1, cpair = (xcd & 1) * 24 + rank;
    const bool con = rank < ((xcd & 1) ? 19 : 24);
    const int col0 = a * F + cpair * 32;
    v4u wm[8];
#pragma unroll
    for (int i = 0; i < 8; ++i)
      wm[i] = __builtin_amdgcn_raw_buffer_load_b128(rgu, con ? (128 * kp + 2 * (lane >> 2)) * (F * 2) + cpair * 64 + (lane & 3) * 16 : 1 << 30, (32 * (i >> 1) + (i & 1)) * (F * 2), 0);
    SB0();
    if (a == 1) {
      v4u wn[8]; u32 cv[8];
      const int row0 = 128 * mkp + (lane >> 2);
#pragma unroll
      for (int i = 0; i < 8; ++i) cv[i] = p.c[row0 + 16 * i];
#pragma unroll
      for (int i = 0; i < 8; ++i) wn[i] = *(const GAS v4u*)(p.wmod + (size_t)(row0 + 16 * i) * (6 * D) + 1024 + mcg * 32 + (lane & 3) * 8);
      SB0();
      bar_raw();
      SB0();
      float acc[8];
#pragma unroll
      for (int j = 0; j < 8; ++j) acc[j] = 0.f;
#pragma unroll
      for (int i = 0; i < 8; ++i) {
        const float c = lo16(cv[i]), sl = rb(c * rcp(1.f + __expf(-c)));
#pragma unroll
        for (int j = 0; j < 4; ++j) { acc[2 * j] += sl * lo16(wn[i][j]); acc[2 * j + 1] += sl * hi16(wn[i][j]); }
      }
      float c0, c1;
      red8q(acc, c0, c1);
      st128(v4u{__float_as_uint(c0), tag, __float_as_uint(c1), tag}, rs, (lane & 12) == 0 ? (8 * (lane & 3) + 2 * (lane >> 4)) * 8 : WS_BYTES,
            OFF_MODG + mkp * 16384 + mcg * 256);
    } else {
      bar_raw();
    }
    bar_raw();
    TS(2);
    {
      const int idx0 = (mem * 4 + 2 * a) * 16;
      v4u e[32], zz[2];
      {
        u32 todo = 0xFFu;
        for (u32 spins = 0; spins < SPIN_CAP && todo; ++spins) {
          const u32 bad = ~__builtin_amdgcn_readfirstlane(ask(rs, FL_KV + mem * 2 + a, lane));
          __builtin_amdgcn_fence(__ATOMIC_ACQUIRE, "wavefront");
          RELOAD();
          static_for<0, 8>([&](auto G) {
            constexpr int g = G.value;
            if (((todo >> g) & 1u) && !(bad & (0x00030003u << (2 * g)))) {
              todo &= ~(1u << g);
              e[2 * g] = ld128(rs, lane * 16, OFF_KVE + (idx0 + 2 * g) * 1024);
              e[2 * g + 1] = ld128(rs, lane * 16, OFF_KVE + (idx0 + 2 * g + 1) * 1024);
              e[16 + 2 * g] = ld128(rs, lane * 16, OFF_KVE + (idx0 + 16 + 2 * g) * 1024);
              e[17 + 2 * g] = ld128(rs, lane * 16, OFF_KVE + (idx0 + 17 + 2 * g) * 1024);
            }
          });
        }
      }
      TS(3);
      zz[0] = ldfrag<0>(rs, lane * 16, OFF_KVZ + idx0 * 64);
      zz[1] = ldfrag<1>(rs, lane * 16, OFF_KVZ + idx0 * 64);
      SB0();
#pragma unroll
      for (int q = 0; q < 2; ++q) {
        f32x4 s0 = __builtin_bit_cast(f32x4, e[16 * q]), s1 = __builtin_bit_cast(f32x4, e[16 * q + 1]);
#pragma unroll
        for (int i = 2; i < 16; i += 2) { s0 += __builtin_bit_cast(f32x4, e[16 * q + i]); s1 += __builtin_bit_cast(f32x4, e[16 * q + i + 1]); }
        s0 += s1;
        *(LAS v2u*)(L + L_KVF + (2 * a + q) * 512 + l31 * 16 + hh * 8) = v2u{pk(s0[0], s0[1]), pk(s0[2], s0[3])};
      }
      f32x4 z;
#pragma unroll
      for (int j = 0; j < 4; ++j) {
        float t = fold32(__uint_as_float(zz[0][j]), __uint_as_float(zz[1][j]));
        t = fold16(t, t);
        t = dpp_add<DPP_ROR4>(t);
        z[j] = dpp_add<DPP_ROR8>(t);
      }
      if (l31 < 4) *(LAS v2u*)(L + L_ZF + (2 * a + hh) * 32 + ((lane & 1) ^ 1) * 16 + ((lane >> 1) & 1) * 8) = v2u{pk(z[0], z[1]), pk(z[2], z[3])};
    }
    TS(4);
    bar();
    u32 gsb[4], gsc[4];
    {
      const int vo = ((hh ? 2 : 3 * a) * D + colg * 64 + 2 * l31) * 8, ro = (D + 128 * kp + 2 * lane) * 8;
      v4u g[12];
      for (u32 spins = 0; spins < SPIN_CAP; ++spins) {
        RELOAD();
#pragma unroll
        for (int i = 0; i < 12; ++i) g[i] = ld128(rs, i < 4 ? vo : ro + (i >> 3) * (D * 8), OFF_MODG + (i & 3) * 16384);
        bool ok = true;
#pragma unroll
        for (int i = 0; i < 12; ++i) ok &= (g[i].y == tag) & (g[i].w == tag);
        if (__all(ok)) break;
      }
      u32 pr[3];
#pragma unroll
      for (int t = 0; t < 3; ++t)
        pr[t] = pk((__uint_as_float(g[4 * t].x) + __uint_as_float(g[4 * t + 1].x)) + (__uint_as_float(g[4 * t + 2].x) + __uint_as_float(g[4 * t + 3].x)),
                   (__uint_as_float(g[4 * t].z) + __uint_as_float(g[4 * t + 1].z)) + (__uint_as_float(g[4 * t + 2].z) + __uint_as_float(g[4 * t + 3].z)));
      const u32 v1 = pk(1.f + lo16(pr[0]), 1.f + hi16(pr[0])), vv = hh ? v1 : pr[0];
      *(LAS f32x2*)(L + L_VEC + (hh ? 1 : 2 * a) * 256 + l31 * 8) = f32x2{lo16(vv), hi16(vv)};
      const u32 sc = pk(1.f + lo16(pr[2]), 1.f + hi16(pr[2]));
#pragma unroll
      for (int i = 0; i < 4; ++i) {
        gsb[i] = __builtin_amdgcn_ds_bpermute((16 * i + (lane >> 2)) * 4, pr[1]);
        gsc[i] = __builtin_amdgcn_ds_bpermute((16 * i + (lane >> 2)) * 4, sc);
      }
    }
    if (con) {
      float a1[8], a2[8];
#pragma unroll
      for (int j = 0; j < 8; ++j) { a1[j] = 0.f; a2[j] = 0.f; }
#pragma unroll
      for (int i = 0; i < 8; ++i) {
        const float sb = (i & 1) ? hi16(gsb[i >> 1]) : lo16(gsb[i >> 1]), sc = (i & 1) ? hi16(gsc[i >> 1]) : lo16(gsc[i >> 1]);
#pragma unroll
        for (int j = 0; j < 4; ++j) {
          a1[2 * j] += sc * lo16(wm[i][j]); a1[2 * j + 1] += sc * hi16(wm[i][j]);
          a2[2 * j] += sb * lo16(wm[i][j]); a2[2 * j + 1] += sb * hi16(wm[i][j]);
        }
      }
      float c10, c11, c20, c21;
      red8q(a1, c10, c11); red8q(a2, c20, c21);
      const int k = (lane >> 2) & 3;
      const float val = k == 0 ? c10 : k == 1 ? c20 : k == 2 ? c11 : c21;
      st64(v2u{__float_as_uint(val), tag}, rs, (col0 + 8 * (lane & 3) + 2 * (lane >> 4)) * 16 + k * 8, OFF_CG + kp * CG_PART);
    }
    TS(5);
    bar();
    {
      const int ntile = three ? 6 : 4;
      v4u cg[8];
      int off[2];
#pragma unroll
      for (int i = 0; i < 2; ++i) {
        int tile = 4 * i + 2 * a + hh; tile = tile < ntile ? tile : 0;
        off[i] = (32 * pair0 + 16 * tile + (lane & 15) + ((lane >> 4) & 1) * F) * 16;
      }
      for (u32 spins = 0; spins < SPIN_CAP; ++spins) {
        RELOAD();
        bool ok = true;
#pragma unroll
        for (int i = 0; i < 8; ++i) cg[i] = ld128(rs, off[i >> 2], OFF_CG + (i & 3) * CG_PART);
#pragma unroll
        for (int i = 0; i < 8; ++i) ok &= (cg[i].y == tag) & (cg[i].w == tag);
        if (__all(ok)) break;
      }
#pragma unroll
      for (int i = 0; i < 2; ++i) {
        const float c1 = (__uint_as_float(cg[4 * i].x) + __uint_as_float(cg[4 * i + 1].x)) + (__uint_as_float(cg[4 * i + 2].x) + __uint_as_float(cg[4 * i + 3].x));
        const float c2 = (__uint_as_float(cg[4 * i].z) + __uint_as_float(cg[4 * i + 1].z)) + (__uint_as_float(cg[4 * i + 2].z) + __uint_as_float(cg[4 * i + 3].z));
        const float h1 = rb(c1), m1 = rb(c1 - h1), l1 = c1 - h1 - m1, h2 = rb(c2), m2 = rb(c2 - h2), l2 = c2 - h2 - m2;
        const int tile = 4 * i + 2 * a + hh;
        if (tile < 6) *(LAS v4u*)(L + L_CF + tile * 512 + l31 * 16) = v4u{pk(h1, m1), pk(l1, h2), pk(m2, l2), 0u};
      }
    }
    await_cp(L, 1);
    {
      v4u st[16];
#pragma unroll
      for (int t = 0; t < 16; ++t) st[t] = ld128(rs, l31 * 16 + hh * 8192 + t * 512, pb + PN_ST + 2 * a * 8192);
      for (u32 spins = 0; spins < SPIN_CAP; ++spins) {
        bool ok = true;
#pragma unroll
        for (int t = 0; t < 16; ++t) ok &= (st[t].y == tag) & (st[t].w == tag);
        if (__all(ok)) break;
        RELOAD();
#pragma unroll
        for (int t = 0; t < 16; ++t) st[t] = ld128(rs, l31 * 16 + hh * 8192 + t * 512, pb + PN_ST + 2 * a * 8192);
      }
      float ms = 0.f;
#pragma unroll
      for (int t = 0; t < 16; ++t) ms += __uint_as_float(st[t].x);
      const float mean = ms * (1.f / 16.f);
      float q = 0.f;
#pragma unroll
      for (int t = 0; t < 16; ++t) { const float d = __uint_as_float(st[t].x) - mean; q += __uint_as_float(st[t].z) + 32.f * d * d; }
      const float den = rb(__builtin_amdgcn_sqrtf(rb(rb(q * (1.f / D)) + 1e-5f)));
      *(LAS v4u*)(L + L_KVF + a * 1024 + lane * 16) = v4u{pk(-mean, -mean), pk(-mean, den), pk(den, den), __float_as_uint(rcp(den * den))};
    }
    RELOAD();
    mark(L, H_PP, 2 + a, 5u);
    TS(6);
    {
      v4u wt[58];
      const u32 vd = (lane >> 2) * (D * 2) + (lane & 3) * 16, sd = a * 16 * (D * 2) + colg * 128;
      const Base3 wdst(L + a * 2048 + lane * 16);
      static_for<28, 86>([&](auto J) {
        wt[J.value - 28] = __builtin_amdgcn_raw_buffer_load_b128(rwd, NBV(vd + (J.value & 1) * 64), sd + pair_of(J.value >> 1) * 32 * (D * 2), 0); SB0(); });
      TS(7);
#define ST_WT(T0, T1, k) do { static_for<2 * (T0), 2 * (T1)>([&](auto J) { \
      *(LAS v4u*)wdst.template at<slot(P_WD + 4 * (J.value >> 1)) + (J.value & 1) * 1024>() = wt[J.value - 28]; }); \
      RELOAD(); mark(L, H_PP, 2 + a, k); RELOAD(); } while (0)
      await_cp(L, 4);
      ST_WT(14, 18, 6u);
      await_cp(L, 5);
      ST_WT(18, 26, 7u);
      await_cp(L, 6);
      ST_WT(26, 34, 8u);
      TS(8);
      await_cp(L, 7);
      ST_WT(34, 43, 9u);
      TS(9);
    }
    TS_DUMP();
    return;
  }

  if (w >= 6) {
    const int v = w & 1;
    __builtin_amdgcn_s_setprio(LPRIO);
    const u32 ldsb = (u32)(size_t)L;
    const u32 l1 = ldsb + v * 1024, l2 = ldsb + v * 2048;
    const u32 skv = (D + v * D + mem * 32) * 2, sq = v * 16 * (3 * D * 2) + mem * 64;
    const u32 so = 16 * v * 16 * (D * 2) + colg * 128, sd = v * 16 * (D * 2) + colg * 128;
    const u32 sg = pair0 * 64, sg2 = sg + (three ? 128 : 64);
#define LD_KV(S0, S1, PC) static_for<S0, S1>([&](auto S) { gldsi<slot(P_KV + 2 * S.value), S.value * 16 * (3 * D * 2)>(rqkv, vq, skv, l1); PACE(PC); })
#define PACE(n) do { if ((n) > 0) __builtin_amdgcn_s_sleep(n); } while (0)
#define LD_Q(S0, S1, PC) static_for<S0, S1>([&](auto S) { gldsi<slot(P_Q + 2 * S.value), 2 * S.value * 16 * (3 * D * 2)>(rqkv, vq, sq, l1); PACE(PC); })
#define LD_DN(RS, SB, P0, T0, T1, PC)                                                                    \
  static_for<T0, T1>([&](auto T) {                                                                   \
    gldsi<slot((P0) + 4 * T.value), T.value * 16 * (D * 2)>(RS, vd, SB, l2);                         \
    gldsi<slot((P0) + 4 * T.value) + 1024, T.value * 16 * (D * 2) + 64>(RS, vd, SB, l2); PACE(PC);   \
  })
#define LD_GU(S0, S1, PC)                                                                                \
  static_for<S0, S1>([&](auto S) {                                                                   \
    gldsi<slot(P_GU + 6 * S.value), S.value * 16 * (F * 2)>(rgu, vg, sg, l1);                        \
    gldsi<slot(P_GU + 6 * S.value + 2), S.value * 16 * (F * 2) + 64>(rgu, vg, sg, l1);               \
    gldsi<slot(P_GU + 6 * S.value + 4), S.value * 16 * (F * 2)>(rgu, vg, sg2, l1); PACE(PC);         \
  })
    TS(1);
    VMCNT(0);
    SB0();
    int ln;
    asm volatile("v_mbcnt_lo_u32_b32 %0, -1, 0\n\tv_mbcnt_hi_u32_b32 %0, -1, %0" : "=v"(ln));
    const u32 vd = (ln >> 2) * (D * 2) + (ln & 3) * 16;
    const u32 vg = (ln >> 2) * (F * 2) + (((ln & 3) ^ (v << 1)) * 16);
    TS(2);
    await<4>(L, 3);
    LD_KV(KV1, 32, PKV);
    VMCNT(0);
    TS(11);
    bar_raw();
    v4u wt[48];
    const Base3 wdst1(L + v * 1024 + ln * 16);
#define GUN(s_, P_) (P_GU + ((s_) < 16 ? 6 * (s_) + 2 * (P_) : 96 + 32 * (P_) + 2 * ((s_) - 16)))
#define GUR(s_, P_) __builtin_amdgcn_raw_buffer_load_b128(rgu, NBV(vg), ((P_) == 2 ? sg2 : sg + (P_) * 64) + (s_) * 16 * (F * 2), 0)
#define GUW(s_, P_, r_) *(LAS v4u*)wdst1.template at<slot(GUN(s_, P_))>() = wt[r_]
#define LD_GU2(S0, S1, PC) static_for<S0, S1>([&](auto S) { constexpr int s_ = S.value; \
      gldsi<slot(GUN(s_, 0)), s_ * 16 * (F * 2)>(rgu, vg, sg, l1); SB0(); \
      wt[2 * (s_ - 16)] = GUR(s_, 1); wt[2 * (s_ - 16) + 1] = GUR(s_, 2); PACE(PC); SB0(); })
#define LD_WD(T0, T1) static_for<T0, T1>([&](auto T) { \
      gldsi<slot(P_WD + 4 * T.value), pair_of(T.value) * 32 * (D * 2)>(rwd, vd, sd, l2); \
      gldsi<slot(P_WD + 4 * T.value) + 1024, pair_of(T.value) * 32 * (D * 2) + 64>(rwd, vd, sd, l2); })
#define DONE(k) do { RELOAD(); mark(L, H_PP, v, k); RELOAD(); } while (0)
    await<4>(L, 2);
    gldsi<L_CF, 0>(v ? rwo : rqkv, (rank * 16 + (ln >> 2)) * (v ? D * 2 : 3 * D * 2) + (xcd & 1) * 512 + (ln & 3) * 128, 0u, l1);
    LD_Q(0, 16, 0);
    TS(3);
    bar_raw();
    VMCNT(0);
    bar_raw();
    gldsi<L_CF, 0>(rwd, v ? 1u << 30 : ((xcd >> 1) * 32 + rank + 128 * (ln >> 2)) * (D * 2) + (xcd & 1) * 512 + (ln & 3) * 128, 0u, l1);
    LD_DN(rwo, so, P_WO, 0, 16, 1);
    LD_GU(0, 6, PGU);
    VMCNT(18);
    TS(4);
    bar_raw();
    LD_GU(6, 12, PGU);
    await<4>(L, 4);
    static_for<0, 12>([&](auto J) { wt[32 + J.value] = GUR(12 + J.value / 3, J.value % 3); SB0(); });
    static_for<16, 20>([&](auto S) { constexpr int s_ = S.value;
      wt[28 + s_] = GUR(s_, 0); wt[2 * (s_ - 16)] = GUR(s_, 1); wt[2 * (s_ - 16) + 1] = GUR(s_, 2); SB0(); });
    VMCNT(24);
    TS(5);
    bar_raw();
    static_for<0, 12>([&](auto J) { GUW(12 + J.value / 3, J.value % 3, 32 + J.value); });
    static_for<16, 20>([&](auto S) { GUW(S.value, 0, 28 + S.value); });
    DONE(1);
    LD_GU2(20, 32, PG2);
    VMCNT(0);
    DONE(3);
    TS(6);
    await_cp(L, 1);
    {
      const u32 tdn = ((2 * rank + v) * 16 + (ln >> 2)) * (D * 2) + (xcd & 1) * 512 + (ln & 3) * 128;
      __builtin_amdgcn_s_waitcnt(0x0F70);
      __builtin_amdgcn_s_sleep(4);
      gldsi<L_T, 0>(rwd, tdn, 0u, l1);
      gldsi<L_T, 1024 * (D * 2)>(rwd, tdn, 0u, l1);
    }
    static_for<16, 32>([&](auto S) { GUW(S.value, 1, 2 * (S.value - 16)); });
    DONE(4);
    await_cp(L, 2);
    static_for<16, 32>([&](auto S) { GUW(S.value, 2, 2 * (S.value - 16) + 1); });
    DONE(5);
    TS(7);
    await_cp(L, 3);
    LD_WD(0, 10);
    await_cp(L, 4);
    LD_WD(10, 14);
    VMCNT(0);
    DONE(100);
    TS(8);
    TS_DUMP();
    return;
  }

  __builtin_amdgcn_s_setprio(2);
  const int s2 = w & 1, kh = w >> 1;
  const int strip2 = rowh * 2 + s2, ncol = colg * 64 + kh * 32;
  v4u aphi0, aphi1;
  u32 tag;
  {
    float xf[16];
#pragma unroll
    for (int i = 0; i < 8; ++i) { xf[2 * i] = lo16(r0[i >> 2][i & 3]); xf[2 * i + 1] = hi16(r0[i >> 2][i & 3]); }
    float sm = (((xf[0] + xf[1]) + (xf[2] + xf[3])) + ((xf[4] + xf[5]) + (xf[6] + xf[7]))) + (((xf[8] + xf[9]) + (xf[10] + xf[11])) + ((xf[12] + xf[13]) + (xf[14] + xf[15])));
    sm = dpp_add<DPP_X1>(sm); sm = dpp_add<DPP_X2>(sm); sm = dpp_add<DPP_HM>(sm);
    const float mw = sm * (1.f / 128.f);
    float q[4];
#pragma unroll
    for (int i = 0; i < 4; ++i) {
      const float d0 = xf[4 * i] - mw, d1 = xf[4 * i + 1] - mw, d2 = xf[4 * i + 2] - mw, d3 = xf[4 * i + 3] - mw;
      q[i] = (d0 * d0 + d1 * d1) + (d2 * d2 + d3 * d3);
    }
    float qq = (q[0] + q[1]) + (q[2] + q[3]);
    qq = dpp_add<DPP_X1>(qq); qq = dpp_add<DPP_X2>(qq); qq = dpp_add<DPP_HM>(qq);
    *(volatile LAS f32x2*)(L + L_KVF + (lane >> 3) * 32 + w * 8) = f32x2{mw, qq};
    sig(L, 1, w);
    await<4>(L, 1);
    const f32x4 pa = *(LAS f32x4*)(L + L_KVF + (lane >> 3) * 32), pc = *(LAS f32x4*)(L + L_KVF + (lane >> 3) * 32 + 16);
    const float mean = ((pa[0] + pa[2]) + (pc[0] + pc[2])) * 0.25f;
    const float f0 = pa[0] - mean, f1 = pa[2] - mean, f2 = pc[0] - mean, f3 = pc[2] - mean;
    const float m2 = ((pa[1] + pa[3]) + (pc[1] + pc[3])) + 128.f * ((f0 * f0 + f1 * f1) + (f2 * f2 + f3 * f3));
    const float m1 = rb(mean), r1 = rcp(rb(__builtin_amdgcn_sqrtf(rb(rb(m2 * (1.f / D)) + 1e-5f))));
    float xn[16];
#pragma unroll
    for (int i = 0; i < 8; ++i) {
      const u32 d = pk(xf[2 * i] - m1, xf[2 * i + 1] - m1);
      const u32 t = pk(lo16(d) * r1, hi16(d) * r1);
      xn[2 * i] = lo16(t); xn[2 * i + 1] = hi16(t);
    }
#pragma unroll
    for (int i = 0; i < 16; ++i) asm volatile("" : "+v"(xn[i]));
    tag = __builtin_amdgcn_readfirstlane(epoch) + 1u;
    TS(1);
    await<1>(L, 0);
    __builtin_amdgcn_s_sleep(PRE_MOD);
    TS(21);
    v4u g[8];
    {
      u32 todo = 0xFFu;
      for (u32 spins = 0; spins < SPIN_CAP && todo; ++spins) {
        RELOAD();
        static_for<0, 8>([&](auto I) {
          constexpr int i = I.value;
          if ((todo >> i) & 1u) g[i] = ld128(rs, lane * 16, OFF_MODR + panel * 32768 + (i & 3) * 8192 + (i >> 2) * 4096 + w * 1024);
        });
        todo = 0;
        static_for<0, 8>([&](auto I) { if (__builtin_amdgcn_ballot_w64((g[I.value].y != tag) | (g[I.value].w != tag))) todo |= 1u << I.value; });
      }
    }
    {
      u32 pr[2];
#pragma unroll
      for (int t = 0; t < 2; ++t)
        pr[t] = pk((__uint_as_float(g[4 * t].x) + __uint_as_float(g[4 * t + 1].x)) + (__uint_as_float(g[4 * t + 2].x) + __uint_as_float(g[4 * t + 3].x)),
                   (__uint_as_float(g[4 * t].z) + __uint_as_float(g[4 * t + 1].z)) + (__uint_as_float(g[4 * t + 2].z) + __uint_as_float(g[4 * t + 3].z)));
      *(LAS u32*)(L + L_T + w * TW + lane * 4) = pr[0];
      *(LAS u32*)(L + L_T + w * TW + 256 + lane * 4) = pk(1.f + lo16(pr[1]), 1.f + hi16(pr[1]));
    }
    v4u sav[2], cav[2];
#pragma unroll
    for (int i = 0; i < 2; ++i) {
      sav[i] = *(LAS v4u*)(L + L_T + w * TW + i * 128 + (lane & 7) * 16);
      cav[i] = *(LAS v4u*)(L + L_T + w * TW + 256 + i * 128 + (lane & 7) * 16);
    }
    sig(L, 3, w);
    TS(2);
    const int strip = mem >> 2, r = 8 * (mem & 3) + (lane >> 3);
#pragma unroll
    for (int i = 0; i < 2; ++i) {
      v4u o;
#pragma unroll
      for (int j = 0; j < 4; ++j) {
        const u32 sa = sav[i][j], sc = cav[i][j];
        const u32 t = pk(xn[8 * i + 2 * j] * lo16(sc), xn[8 * i + 2 * j + 1] * hi16(sc));
        o[j] = pk(lo16(t) + lo16(sa), hi16(t) + hi16(sa));
      }
      st128(o, rs, (4 * i + ((lane & 7) >> 1)) * 1024 + (r + 32 * (lane & 1)) * 16, pb + PN_H + strip * 32768 + w * 8192);
    }
    await<4>(L, 3);
    publish(rs, fw + FL_H + strip, (mem & 3) * 4 + w, lane);
  }
  TS(3);

  const int trq = (lane & 15) >> 2, trp = lane & 3, trb = (lane >> 4) & 1;
  const f32x16 zero16 = {0.f, 0.f, 0.f, 0.f, 0.f, 0.f, 0.f, 0.f, 0.f, 0.f, 0.f, 0.f, 0.f, 0.f, 0.f, 0.f};
  u32 phq[16];
  v4u wb[PD];
  {
    const Base3 wnat(L + (8 * hh + trq) * 64 + trb * 32 + trp * 8);
    waitn(rs, fw + FL_H + w, 0xFFFFu, lane);
    TS(4);
    v4u a[32];
    ldfrags<W0>(a, rs, lane * 16, pb + PN_H + w * 32768);
    bar_raw();
    {
      const int hs = (l31 >> 2) & 1;
      const LAS u16* wp = (const LAS u16*)(L + L_CF + 2048) + hs * 256 + 4 * hh * 16 + (l31 & 3) + 4 * (l31 >> 3);
      u32 e[8];
#pragma unroll
      for (int j = 0; j < 8; ++j) e[j] = wp[(8 * (j >> 2) + (j & 3)) * 16];
      const v4u av = {e[0] | (e[1] << 16), e[2] | (e[3] << 16), e[4] | (e[5] << 16), e[6] | (e[7] << 16)};
      const v4u z = {0, 0, 0, 0};
      aphi0 = hs == 0 ? av : z; aphi1 = hs == 1 ? av : z;
      asm volatile("" : "+v"(aphi0), "+v"(aphi1));
    }
    static_for<0, PD>([&](auto U) { wb[U.value] = ldw<256>(wnat.at<slot(P_KV + U.value)>()); });
    SB0();
    f32x16 kacc = zero16, vacc = zero16;
    static_for<0, 64>([&](auto U) {
      constexpr int u = U.value;
      const v4u cur = wb[u % PD];
      if constexpr (u + PD < 64) wb[u % PD] = ldw<256>(wnat.at<slot(P_KV + u + PD)>());
      if constexpr (u & 1) mfma_pin(a[u >> 1], cur, vacc); else mfma_pin(cur, a[u >> 1], kacc);
      if constexpr (!(u & 1) && (u >> 1) + W0 < 32) a[(u >> 1) + W0] = ldfrag<(u >> 1) + W0>(rs, lane * 16, pb + PN_H + w * 32768);
      if constexpr (u == 2 * (32 - W0)) sig(L, 2, w);
      SB0();
    });
    TS(5);
    f32x16 P = mfma(aphi0, pack8<0>(kacc), zero16);
    P = mfma(aphi1, pack8<8>(kacc), P);
    const v4u vb0 = pack8<0>(vacc), vb1 = pack8<8>(vacc);
    u32 phk[16];
    feat(P, phk);
    LAS unsigned char* T = L + L_T + w * TW;
#pragma unroll
    for (int c = 0; c < 4; ++c)
      *(LAS v4u*)(T + lane * 64 + ((c ^ ((lane >> 1) & 3)) * 16)) = v4u{phk[4 * c], phk[4 * c + 1], phk[4 * c + 2], phk[4 * c + 3]};
    const v4u ones = {0x3F803F80u, 0x3F803F80u, 0x3F803F80u, 0x3F803F80u};
    LAS unsigned char* tr = T + (4 * hh + trq) * 64 + (((2 * trb + (trp >> 1)) ^ ((2 * hh + (trq >> 1)) & 3)) * 16) + 8 * (trp & 1);
    const v4u t0 = ldw<512>(tr), t1 = ldw<512>(tr + 1024), t2 = ldw<512>(tr + 2048), t3 = ldw<512>(tr + 3072);
    f32x16 D0 = mfma(t0, trb ? ones : vb0, zero16);
    f32x16 D1 = mfma(t2, trb ? vb0 : ones, zero16);
    D0 = mfma(t1, trb ? ones : vb1, D0);
    D1 = mfma(t3, trb ? vb1 : ones, D1);
    {
      LAS unsigned char* pe = T + lane * 16;
      LAS unsigned char* pz = T + 1024 + (lane >> 4) * 16;
      LAS unsigned char* p0 = trb ? pz : pe;
      LAS unsigned char* p1 = trb ? pe : pz;
#pragma unroll
      for (int g = 0; g < 4; ++g) *(LAS f32x4*)(p0 + g * 1088) = f32x4{D0[4 * g], D0[4 * g + 1], D0[4 * g + 2], D0[4 * g + 3]};
#pragma unroll
      for (int g = 0; g < 4; ++g) *(LAS f32x4*)(p1 + g * 1088) = f32x4{D1[4 * g], D1[4 * g + 1], D1[4 * g + 2], D1[4 * g + 3]};
    }
    TS(6);
    bar();
    {
      f32x4 e[4], z[4];
#pragma unroll
      for (int v = 0; v < 4; ++v) {
        e[v] = *(LAS f32x4*)(L + L_T + v * TW + w * 1088 + lane * 16);
        z[v] = *(LAS f32x4*)(L + L_T + v * TW + w * 1088 + 1024 + (lane & 3) * 16);
      }
      SB0();
      const int idx = (mem * 4 + w) * 16 + panel;
      st128(__builtin_bit_cast(v4u, (e[0] + e[1]) + (e[2] + e[3])), rs, lane * 16, OFF_KVE + idx * 1024);
      st128(__builtin_bit_cast(v4u, (z[0] + z[1]) + (z[2] + z[3])), rs, lane < 4 ? lane * 16 : WS_BYTES, OFF_KVZ + idx * 64);
      publish(rs, FL_KV + mem * 2 + (w >> 1), (w & 1) * 16 + panel, lane);
      bar_raw();
      static_for<0, PD>([&](auto U) { wb[U.value] = ldw<256>(wnat.at<slot(P_Q + U.value)>()); });
    }
    TS(7);
    f32x16 qacc = zero16;
    static_for<0, 32>([&](auto U) {
      constexpr int u = U.value;
      const v4u cur = wb[u % PD];
      if constexpr (u + PD < 32) wb[u % PD] = ldw<256>(wnat.at<slot(P_Q + u + PD)>());
      mfma_pin(cur, a[u], qacc);
      SB0();
    });
    P = mfma(aphi0, pack8<0>(qacc), zero16);
    P = mfma(aphi1, pack8<8>(qacc), P);
    feat(P, phq);
#pragma unroll
    for (int i = 0; i < 16; ++i) asm volatile("" : "+v"(phq[i]));
  }
  v2u xres[4];
  {
    const u16* xr = p.x + (size_t)(panel * 128 + strip2 * 32 + l31) * D + ncol + 4 * hh;
#pragma unroll
    for (int i = 0; i < 4; ++i) xres[i] = *(const GAS v2u*)(xr + 8 * i);
  }
  TS(8);
  bar_raw();
  TS(9);
  {
    const bool on = (l31 >> 4) == hh;
    const v4u zv = {0, 0, 0, 0};
    f32x16 num = zero16, den = zero16;
    v4u ak[4], az[4];
#pragma unroll
    for (int s4 = 0; s4 < 4; ++s4) { az[s4] = *(LAS v4u*)(L + L_ZF + s4 * 32 + hh * 16); ak[s4] = *(LAS v4u*)(L + L_KVF + s4 * 512 + l31 * 16); }
    SB0();
#pragma unroll
    for (int s4 = 0; s4 < 4; ++s4) den = mfma(on ? az[s4] : zv, v4u{phq[4 * s4], phq[4 * s4 + 1], phq[4 * s4 + 2], phq[4 * s4 + 3]}, den);
#pragma unroll
    for (int s4 = 0; s4 < 4; ++s4) num = mfma(on ? ak[s4] : zv, v4u{phq[4 * s4], phq[4 * s4 + 1], phq[4 * s4 + 2], phq[4 * s4 + 3]}, num);
    const float d0 = rcp(rb(rb(den[0]) + 1e-6f)), d1 = rcp(rb(rb(den[8]) + 1e-6f));
    const v4u n0 = pack8<0>(num), n1 = pack8<8>(num);
    v4u o0, o1;
#pragma unroll
    for (int j = 0; j < 4; ++j) { o0[j] = pk(lo16(n0[j]) * d0, hi16(n0[j]) * d0); o1[j] = pk(lo16(n1[j]) * d1, hi16(n1[j]) * d1); }
    st128(o0, rs, lane * 16, pb + PN_O + w * 32768 + (2 * mem) * 1024);
    st128(o1, rs, lane * 16 + 1024, pb + PN_O + w * 32768 + (2 * mem) * 1024);
  }
#define WDN_BASES() const Base3 wown(L + (4 * hh + trq) * 64 + trb * 32 + trp * 8 + 3 * kh * 1024), woth(L + (4 * hh + trq) * 64 + trb * 32 + trp * 8 + (kh + 1) * 1024)
#define WDN(base, u) ldw<512>((((u) & 1) ? wown : woth).template at<slot((base) + 4 * ((u) >> 1))>())
  u32 x1p[8];
  {
    WDN_BASES();
    static_for<0, PD>([&](auto U) { wb[U.value] = WDN(P_WO, U.value); });
    publish(rs, fw + FL_O + w, mem, lane);
    TS(10);
    waitn(rs, fw + FL_O + strip2, 0xFFu << (kh * 8), lane);
    TS(11);
    v4u a[16];
    ldfrags<16>(a, rs, lane * 16, pb + PN_O + strip2 * 32768 + kh * 16384);
    sig(L, 4, w);
    SB0();
    f32x16 c0 = zero16, c1 = zero16;
    static_for<0, 32>([&](auto U) {
      constexpr int u = U.value;
      const v4u cur = wb[u % PD];
      if constexpr (u + PD < 32) wb[u % PD] = WDN(P_WO, u + PD);
      if constexpr (u & 1) mfma_pin(cur, a[u >> 1], c0); else mfma_pin(cur, a[u >> 1], c1);
      SB0();
    });
#pragma unroll
    for (int g = 0; g < 4; ++g) *(LAS f32x4*)(L + L_T + w * TW + g * 1024 + lane * 16) = f32x4{c1[4 * g], c1[4 * g + 1], c1[4 * g + 2], c1[4 * g + 3]};
    TS(12);
    bar();
    f32x4 o[4], ga[4], sc[4];
#pragma unroll
    for (int g = 0; g < 4; ++g) {
      o[g] = *(LAS f32x4*)(L + L_T + (w ^ 2) * TW + g * 1024 + lane * 16);
      ga[g] = *(LAS f32x4*)(L + L_VEC + (kh * 32 + 8 * g + 4 * hh) * 4);
      sc[g] = *(LAS f32x4*)(L + L_VEC + 256 + (kh * 32 + 8 * g + 4 * hh) * 4);
    }
    SB0();
    float x1[16], sp[4];
    u32 yp[8];
#pragma unroll
    for (int g = 0; g < 4; ++g) {
#pragma unroll
      for (int j = 0; j < 2; ++j) {
        const u32 y = pk(c0[4 * g + 2 * j] + o[g][2 * j], c0[4 * g + 2 * j + 1] + o[g][2 * j + 1]);
        const u32 t = pk(ga[g][2 * j] * lo16(y), ga[g][2 * j + 1] * hi16(y));
        const u32 r = pk(lo16(xres[g][j]) + lo16(t), hi16(xres[g][j]) + hi16(t));
        x1p[2 * g + j] = r;
        x1[4 * g + 2 * j] = lo16(r); x1[4 * g + 2 * j + 1] = hi16(r);
        yp[2 * g + j] = pk(lo16(r) * sc[g][2 * j], hi16(r) * sc[g][2 * j + 1]);
      }
    }
    const int tile = 2 * colg + kh;
    st128(v4u{yp[0], yp[1], yp[2], yp[3]}, rs, lane * 16, pb + PN_X1 + strip2 * 32768 + tile * 2048);
    st128(v4u{yp[4], yp[5], yp[6], yp[7]}, rs, lane * 16 + 1024, pb + PN_X1 + strip2 * 32768 + tile * 2048);
    SB0();
#pragma unroll
    for (int g = 0; g < 4; ++g) sp[g] = (x1[4 * g] + x1[4 * g + 1]) + (x1[4 * g + 2] + x1[4 * g + 3]);
    const float ss = (sp[0] + sp[1]) + (sp[2] + sp[3]);
    const float mt = fold32(ss, ss) * (1.f / 32.f);
    float q[4];
#pragma unroll
    for (int g = 0; g < 4; ++g) {
      const float d0 = x1[4 * g] - mt, d1 = x1[4 * g + 1] - mt, d2 = x1[4 * g + 2] - mt, d3 = x1[4 * g + 3] - mt;
      q[g] = (d0 * d0 + d1 * d1) + (d2 * d2 + d3 * d3);
    }
    const float qs = (q[0] + q[1]) + (q[2] + q[3]);
    const float qt = fold32(qs, qs);
    v4u sv = {__float_as_uint(mt), tag, __float_as_uint(qt), tag};
    asm volatile("" : "+v"(sv));
    publish(rs, fw + FL_X + strip2, tile, lane);
    st128(sv, rs, lane < 32 ? lane * 16 : WS_BYTES, pb + PN_ST + strip2 * 8192 + tile * 512);
  }
  asm volatile("" : "+v"(x1p[0]), "+v"(x1p[1]), "+v"(x1p[2]), "+v"(x1p[3]), "+v"(x1p[4]), "+v"(x1p[5]), "+v"(x1p[6]), "+v"(x1p[7]));
#define GU_S(u) ((u) < 96 ? (u) / 6 : 16 + (((u) - 96) % 32) / 2)
#define GU_I(u) ((u) < 96 ? (u) % 6 : 2 * (((u) - 96) / 32) + ((u) & 1))
#define WGU(u) ldw<512>((((u) & 1) ? wy : wx).template at<slot(P_GU + ((u) & ~1))>())
  {
    const Base3 wx(L + (4 * hh + trq) * 64 + trp * 8 + trb * (1024 + 32)), wy(L + (4 * hh + trq) * 64 + trp * 8 + (trb ? 1024 : 32));
    static_for<0, PD>([&](auto U) { wb[U.value] = WGU(U.value); });
    TS(13);
    waitn(rs, fw + FL_X + w, 0xFFFFu, lane);
    TS(14);
    constexpr int AW = 20;
    v4u a[AW];
    ldfrags<8>(a, rs, lane * 16, pb + PN_X1 + w * 32768);
    f32x16 acc[6];
#pragma unroll
    for (int i = 0; i < 6; ++i) acc[i] = zero16;
    float rr = 0.f;
    v4u bcor = {0, 0, 0, 0};
    v4u ho, cf0, cf1, pp;
    float t0 = 0.f, t1 = 0.f;
    const int go = pb + PN_G + (w * 96 + mem) * 1024;
    auto epi = [&](auto Ic, auto Kc) {
      constexpr int I = decltype(Ic)::value, k = decltype(Kc)::value, j = k >> 1;
      const f32x16& A = acc[I];
      if constexpr ((k & 1) == 0) { t0 = A[2 * j] * rr; t1 = A[2 * j + 1] * rr; asm volatile("" : "+v"(t0)); t0 *= A[8 + 2 * j]; }
      else {
        t1 *= A[9 + 2 * j];
        ho[j] = pk(t0, t1);
        if constexpr (j == 3) st128(ho, rs, (I < 4 || three) ? lane * 16 : WS_BYTES, go + ((I & 1) * 48 + (I >> 1) * 16) * 1024);
      }
    };
    static_for<0, 192>([&](auto Uc) {
      constexpr int u = decltype(Uc)::value, s = GU_S(u), i = GU_I(u), q = (u - 96) % 32;
      if constexpr (u == 6) TS(22);
      if constexpr (u == 60) TS(23);
      if constexpr (u == 96) TS(24);
      if constexpr (u == 128) TS(25);
      if constexpr (u == 160) TS(26);
      if constexpr (u == 56) mark(L, H_CP, w, 2u);
      if constexpr (u == 96 || u == 128 || u == 160) mark(L, H_CP, w, u / 32);
      if constexpr (u == 62 || u == 94 || u == 112) pp = peek(L, H_PP);
      if constexpr (u == 66) need_pp<false>(L, pp, 1u);
      if constexpr (u == 98) need_pp<false>(L, pp, 3u);
      if constexpr (u == 116) need_pp<true>(L, pp, 5u);
      const v4u cur = wb[u % PD];
      if constexpr (u + PD < 192) wb[u % PD] = WGU(u + PD);
      if constexpr (u >= 96 && q == 24) cf0 = *(LAS v4u*)(L + L_CF + i * 512 + l31 * 16);
      if constexpr (u >= 96 && q == 25) cf1 = *(LAS v4u*)(L + L_CF + i * 512 + l31 * 16);
      mfma_pin(cur, a[s % AW], acc[i]);
      if constexpr (u < 96 && i == 5 && s + AW < 32) a[s % AW] = ldfrag<s + AW>(rs, lane * 16, pb + PN_X1 + w * 32768);
      if constexpr (u < 2 * (AW - 8) && (u & 1)) a[8 + (u >> 1)] = ldfrag<8 + (u >> 1)>(rs, lane * 16, pb + PN_X1 + w * 32768);
      if constexpr (u == 2 * (AW - 8)) mark(L, H_CP, w, 1u);
      if constexpr (u == 117) bcor = *(LAS v4u*)(L + L_KVF + w * 512 + l31 * 16);
      if constexpr (u == 123) { rr = __uint_as_float(bcor.w); bcor = v4u{hh ? 0u : bcor.x, hh ? 0u : bcor.y, hh ? 0u : bcor.z, 0u}; }
      if constexpr (u >= 96 && q == 30) mfma_pin(cf0, bcor, acc[i]);
      if constexpr (u >= 96 && q == 31) mfma_pin(cf1, bcor, acc[i]);
      if constexpr (u >= 128 && q >= 1 && q < 17) epi(IC<(i & 6) - 2 + (q - 1) / 8>{}, IC<(q - 1) % 8>{});
      if constexpr (u == 164) publish(rs, fw + FL_G + w * 3, mem, lane);
      SB0();
    });
    mark(L, H_CP, w, 6u);
    TS(15);
    pp = peek(L, H_PP);
    static_for<0, 7>([&](auto Kc) { epi(IC<4>{}, Kc); });
    SB0();
    publish(rs, fw + FL_G + w * 3 + 1, mem, lane);
    SB0();
    static_for<7, 16>([&](auto Kc) { epi(IC<4 + Kc.value / 8>{}, IC<Kc.value % 8>{}); });
    need_pp<true>(L, pp, 6u);
    TS(16);
  }
  {
    WDN_BASES();
    static_for<0, PD>([&](auto U) { wb[U.value] = WDN(P_WD, U.value); });
    const int fi = fw + FL_G + strip2 * 3;
    u32 fl = ask(rs, fi, lane);
    publish(rs, fw + FL_G + w * 3 + 2, mem, lane);
    TS(17);
    poll(fl, rs, fi, 0xFFFFu, lane);
    TS(18);
    v4u a[43], pp;
    const int gi = pb + PN_G + (strip2 * 96 + kh * 48) * 1024;
    fl = ask(rs, fi + 1, lane);
    SB0();
    ldfrags<W0>(a, rs, lane * 16, gi);
    f32x16 c0 = zero16, c1 = zero16;
    static_for<0, 86>([&](auto U) {
      constexpr int u = U.value;
      if constexpr (u == 18) mark(L, H_CP, w, 7u);
      if constexpr (u == 26 || u == 42 || u == 58) pp = peek(L, H_PP);
      if constexpr (u == 30 || u == 46 || u == 62) need_pp<true>(L, pp, 7u + (u - 30) / 16);
      if constexpr (u == 2 * (16 - W0)) poll(fl, rs, fi + 1, 0xFFFFu, lane);
      if constexpr (u == 2 * (32 - W0) - 26) fl = ask(rs, fi + 2, lane);
      if constexpr (u == 2 * (32 - W0)) poll(fl, rs, fi + 2, 0x7FFu, lane);
      const v4u cur = wb[u % PD];
      if constexpr (u + PD < 86) wb[u % PD] = WDN(P_WD, u + PD);
      if constexpr (u & 1) mfma_pin(cur, a[u >> 1], c0); else mfma_pin(cur, a[u >> 1], c1);
      if constexpr (!(u & 1) && (u >> 1) + W0 < 43) a[(u >> 1) + W0] = ldfrag<(u >> 1) + W0>(rs, lane * 16, gi);
      SB0();
    });
#pragma unroll
    for (int g = 0; g < 4; ++g) *(LAS f32x4*)(L + L_T + w * TW + g * 1024 + lane * 16) = f32x4{c1[4 * g], c1[4 * g + 1], c1[4 * g + 2], c1[4 * g + 3]};
    f32x4 gb[4];
#pragma unroll
    for (int g = 0; g < 4; ++g) gb[g] = *(LAS f32x4*)(L + L_VEC + 512 + (kh * 32 + 8 * g + 4 * hh) * 4);
    TS(19);
    bar();
    f32x4 o[4];
#pragma unroll
    for (int g = 0; g < 4; ++g) o[g] = *(LAS f32x4*)(L + L_T + (w ^ 2) * TW + g * 1024 + lane * 16);
    SB0();
    u16* orow = p.out + (size_t)(panel * 128 + strip2 * 32 + l31) * D + ncol + 4 * hh;
#pragma unroll
    for (int g = 0; g < 4; ++g) {
      v2u r;
#pragma unroll
      for (int j = 0; j < 2; ++j) {
        r[j] = pk(lo16(x1p[2 * g + j]) + gb[g][2 * j] * (c0[4 * g + 2 * j] + o[g][2 * j]),
                  hi16(x1p[2 * g + j]) + gb[g][2 * j + 1] * (c0[4 * g + 2 * j + 1] + o[g][2 * j + 1]));
      }
      *(GAS v2u*)(orow + 8 * g) = r;
    }
  }
  if (b == 0 && tid == 0) __hip_atomic_store((GAS u32*)(ws + OFF_EPOCH), tag, RLX_AGENT);
  if (b == MK_LEAKB && w == 0) {
    const u64 te = __builtin_amdgcn_s_memrealtime();
    const u64 goal = lk0 + 5000 + 4 * ((MK_LEAK == 98 ? te : lk1) - lk0);
    while (__builtin_amdgcn_s_memrealtime() < goal) __builtin_amdgcn_s_sleep(1);
  }
}
}

extern "C" void kernel_launch(void* const* d_in, const int* in_sizes, int n_in, void* d_out, int out_size, void* d_ws,
                              size_t ws_size, hipStream_t stream) {
  using namespace mk;
  Params p;
  p.x = (const u16*)d_in[0]; p.wqkv = (const u16*)d_in[1]; p.wphi = (const u16*)d_in[2]; p.wo = (const u16*)d_in[3];
  p.wg = (const u16*)d_in[5]; p.wu = (const u16*)d_in[6]; p.wd = (const u16*)d_in[7]; p.c = (const u16*)d_in[8];
  p.wmod = (const u16*)d_in[9]; p.out = (u16*)d_out; p.ws = (unsigned char*)d_ws;
  (void)hipFuncSetAttribute((const void*)mega, hipFuncAttributeMaxDynamicSharedMemorySize, LDS_BYTES);
  mega<<<256, 512, LDS_BYTES, stream>>>(p);
}
```

```cpp
#define TLBT 1
#include <hip/hip_runtime.h>

namespace mk {
typedef unsigned short u16;
typedef unsigned int u32;
typedef unsigned long long u64;
typedef u32 v4u __attribute__((ext_vector_type(4)));
typedef u32 v2u __attribute__((ext_vector_type(2)));
typedef float f32x2 __attribute__((ext_vector_type(2)));
typedef float f32x4 __attribute__((ext_vector_type(4)));
typedef float f32x16 __attribute__((ext_vector_type(16)));
typedef __bf16 bf16x2 __attribute__((ext_vector_type(2)));
typedef __bf16 bf16x8 __attribute__((ext_vector_type(8)));
typedef short s16x4 __attribute__((ext_vector_type(4)));
#define LAS __attribute__((address_space(3)))
#define GAS __attribute__((address_space(1)))
#define DI __device__ __forceinline__
#define RLX_AGENT __ATOMIC_RELAXED, __HIP_MEMORY_SCOPE_AGENT
#define SB0() __builtin_amdgcn_sched_barrier(0)
typedef __amdgpu_buffer_rsrc_t rsrc_t;

constexpr int D = 512, F = 1376;
constexpr int OFF_EPOCH = 0;
constexpr int OFF_MODG = 64 << 10;
constexpr int OFF_KVZ = 128 << 10;
constexpr int OFF_CG = 192 << 10;
constexpr int CG_PART = 2 * F * 16;
constexpr int OFF_ARR = 384 << 10;
constexpr int OFF_MODR = 512 << 10;
constexpr int OFF_KVE = 1 << 20;
constexpr int OFF_PAN = 2 << 20;
constexpr int PN_H = 0;
constexpr int PN_O = 128 << 10;
constexpr int PN_X1 = 256 << 10;
constexpr int PN_G = 384 << 10;
constexpr int PN_ST = 768 << 10;
constexpr int PN_FL = 800 << 10;
constexpr int FL_H = 0, FL_O = 4, FL_X = 8, FL_G = 12, FL_KV = 384;
constexpr int PSTR = (848 << 10) + 4224;
constexpr int WS_BYTES = 16 << 20;
constexpr int OFF_TS = 32 << 20;
constexpr int RING = 136;
constexpr int L_T = RING * 1024, TW = 4352;
constexpr int L_KVF = L_T + 4 * TW;
constexpr int L_ZF = L_KVF + 2048;
constexpr int L_VEC = L_ZF + 256;
constexpr int L_CF = L_VEC + 768;
constexpr int L_HINT = L_CF + 3072;
constexpr int LDS_BYTES = L_HINT + 128;
static_assert(LDS_BYTES <= 163840, "LDS");
constexpr int P_KV = 0, P_Q = 64, P_WO = 96, P_GU = 160, P_WD = 352;
constexpr int H_CP = 6, H_PP = 7;
constexpr int pair_of(int t) { return ((t & 15) < 11 ? 3 * (t & 15) : 2 * (t & 15) + 11) + (t >> 4); }
constexpr int slot(int n) { return (n % RING) * 1024; }
constexpr int AUX_SC1 = 16;
constexpr u32 SPIN_CAP = 1u << 16;
#define PRE_MOD 8
#define HI_DELAY 8
#define POLL_GAP 6
#define PRE_FL 4
#define KV1 32
#define PGU 4
#define PKV 0
#define GPUB 29
#define LPRIO 0
#define APRIO 1
#define TMOD 0
#define CW 1
#define DW 2
#define CSLP 3
#define PG2 2
#define W0 12
constexpr int PD = 5;
constexpr int CODE_BYTES = 47000, CODE_KB = 48;

#define NBV(x) (x)
struct Params {
  const u16 *x, *wqkv, *wphi, *wo, *wg, *wu, *wd, *c, *wmod;
  u16* out;
  unsigned char* ws;
};

template <int V> struct IC { static constexpr int value = V; };
template <int I, int N, class Fn> DI void static_for(Fn&& f) {
  if constexpr (I < N) { f(IC<I>{}); static_for<I + 1, N>(f); }
}
DI float lo16(u32 v) { return __uint_as_float(v << 16); }
DI float hi16(u32 v) { return __uint_as_float(v & 0xffff0000u); }
DI u32 pk(float a, float b) { f32x2 v = {a, b}; return __builtin_bit_cast(u32, __builtin_convertvector(v, bf16x2)); }
DI float rb(float a) { return lo16(pk(a, 0.f)); }
DI float rcp(float a) { return __builtin_amdgcn_rcpf(a); }
DI f32x16 mfma(v4u a, v4u b, f32x16 c) {
  return __builtin_amdgcn_mfma_f32_32x32x16_bf16(__builtin_bit_cast(bf16x8, a), __builtin_bit_cast(bf16x8, b), c, 0, 0, 0);
}
DI void mfma_pin(v4u a, v4u b, f32x16& c) {
  c = mfma(a, b, c);
  asm volatile("" : "+v"(c));
}
template <int O> DI v4u pack8(const f32x16& x) {
  v4u r; r.x = pk(x[O], x[O + 1]); r.y = pk(x[O + 2], x[O + 3]); r.z = pk(x[O + 4], x[O + 5]); r.w = pk(x[O + 6], x[O + 7]); return r;
}
template <int T> DI v4u ldw(LAS unsigned char* p) {
  s16x4 a = __builtin_amdgcn_ds_read_tr16_b64_v4i16((LAS s16x4*)p);
  s16x4 b = __builtin_amdgcn_ds_read_tr16_b64_v4i16((LAS s16x4*)(p + T));
  return __builtin_bit_cast(v4u, __builtin_shufflevector(a, b, 0, 1, 2, 3, 4, 5, 6, 7));
}
struct Base3 {
  u32 b[3];
  DI explicit Base3(LAS unsigned char* p) {
    b[0] = (u32)(size_t)p; b[1] = b[0] + 65536u; b[2] = b[0] + 131072u;
    asm volatile("" : "+v"(b[0]), "+v"(b[1]), "+v"(b[2]));
  }
  template <int OFF> DI LAS unsigned char* at() const { return (LAS unsigned char*)(size_t)b[OFF >> 16] + (OFF & 65535); }
};
template <int LOFF, int SOFF> DI void gldsi(rsrc_t rs, u32 voff, u32 sbase, u32 lbase) {
  u32 t;
  asm volatile("s_add_u32 m0, %1, %2\n\ts_add_u32 %0, %3, %4\n\tbuffer_load_dwordx4 %5, %6, %0 offen lds"
               : "=&s"(t) : "s"(lbase), "i"(LOFF), "s"(sbase), "i"(SOFF), "v"(voff), "s"(rs) : "m0", "scc", "memory");
}
DI void bar() { asm volatile("s_waitcnt lgkmcnt(0)\n\ts_barrier" ::: "memory"); }
DI void bar_raw() { asm volatile("s_barrier" ::: "memory"); }
DI void drain() { asm volatile("s_waitcnt vmcnt(0)" ::: "memory"); }
#define VMCNT(n) asm volatile("s_waitcnt vmcnt(" #n ")" ::: "memory")
#define RELOAD() asm volatile("" ::: "memory")
DI v4u ld128(rsrc_t rs, int voff, int soff) { return __builtin_amdgcn_raw_buffer_load_b128(rs, voff, soff, AUX_SC1); }
DI v2u ld64(rsrc_t rs, int voff, int soff) { return __builtin_amdgcn_raw_buffer_load_b64(rs, voff, soff, AUX_SC1); }
DI void st128(v4u v, rsrc_t rs, int voff, int soff) { __builtin_amdgcn_raw_buffer_store_b128(v, rs, voff + soff, 0, AUX_SC1); }
DI void st64(v2u v, rsrc_t rs, int voff, int soff) { __builtin_amdgcn_raw_buffer_store_b64(v, rs, voff, soff, AUX_SC1); }
template <int S> DI v4u ldfrag(rsrc_t rs, int voff, int soff) { return ld128(rs, voff + (S & 3) * 1024, soff + (S >> 2) * 4096); }
template <int W, int N> DI void ldfrags(v4u (&a)[N], rsrc_t rs, int voff, int soff) {
  static_for<0, W>([&](auto S) { a[S.value] = ldfrag<S.value>(rs, voff, soff); SB0(); });
}
template <int CTRL> DI float dpp_add(float v) {
  return v + __uint_as_float(__builtin_amdgcn_update_dpp(0u, __float_as_uint(v), CTRL, 0xf, 0xf, false));
}
constexpr int DPP_X1 = 0xB1, DPP_X2 = 0x4E, DPP_HM = 0x141, DPP_ROR4 = 0x124, DPP_ROR8 = 0x128;
DI float fold32(float x, float y) {
  auto r = __builtin_amdgcn_permlane32_swap(__float_as_uint(x), __float_as_uint(y), false, false);
  return __uint_as_float(r[0]) + __uint_as_float(r[1]);
}
DI float fold16(float x, float y) {
  auto r = __builtin_amdgcn_permlane16_swap(__float_as_uint(x), __float_as_uint(y), false, false);
  return __uint_as_float(r[0]) + __uint_as_float(r[1]);
}
DI void red8q(const float (&a)[8], float& c0, float& c1) {
  const float b0 = fold32(a[0], a[4]), b1 = fold32(a[1], a[5]), b2 = fold32(a[2], a[6]), b3 = fold32(a[3], a[7]);
  c0 = fold16(b0, b2); c1 = fold16(b1, b3);
  c0 = dpp_add<DPP_ROR4>(c0); c1 = dpp_add<DPP_ROR4>(c1);
  c0 = dpp_add<DPP_ROR8>(c0); c1 = dpp_add<DPP_ROR8>(c1);
}
DI void red8(const float (&a)[8], float& c0, float& c1) {
  red8q(a, c0, c1);
  c0 = dpp_add<DPP_X1>(c0); c1 = dpp_add<DPP_X1>(c1);
  c0 = dpp_add<DPP_X2>(c0); c1 = dpp_add<DPP_X2>(c1);
}
DI void publish(rsrc_t rs, int word, int bit, int lane) {
  drain();
  (void)__builtin_amdgcn_raw_ptr_buffer_atomic_add_i32(1 << bit, rs, lane == 0 ? 0 : WS_BYTES, OFF_ARR + word * 128, 0);
}
DI u32 ask(rsrc_t rs, int word, int lane) { return __builtin_amdgcn_raw_ptr_buffer_atomic_add_i32(0, rs, lane == 0 ? 0 : WS_BYTES, OFF_ARR + word * 128, 0); }
DI void spin(rsrc_t rs, int word, u32 m, int lane) {
#pragma nounroll
  for (u32 spins = 0; spins < SPIN_CAP; ++spins)
    if ((__builtin_amdgcn_readfirstlane(ask(rs, word, lane)) & m) == m) break;
}
DI void poll(u32 f, rsrc_t rs, int word, u32 m, int lane) {
  if (__builtin_expect((__builtin_amdgcn_readfirstlane(f) & m) != m, 0)) spin(rs, word, m, lane);
  __builtin_amdgcn_fence(__ATOMIC_ACQUIRE, "wavefront");
  RELOAD();
}
DI void waitn(rsrc_t rs, int word, u32 m, int lane) {
  if (PRE_FL) __builtin_amdgcn_s_sleep(PRE_FL);
  spin(rs, word, m, lane);
  __builtin_amdgcn_fence(__ATOMIC_ACQUIRE, "wavefront");
  RELOAD();
}
DI void sig(LAS unsigned char* L, int i, int k) { *(volatile LAS u32*)(L + L_HINT + 16 * i + 4 * k) = 1u; }
template <int N> DI void await(LAS unsigned char* L, int i) {
#pragma nounroll
  for (u32 spins = 0; spins < (1u << 20); ++spins) {
    const v4u v = *(volatile LAS v4u*)(L + L_HINT + 16 * i);
    if (__builtin_amdgcn_readfirstlane(N == 1 ? v.x : (v.x & v.y & v.z & v.w))) break;
    __builtin_amdgcn_s_sleep(1);
  }
  RELOAD();
}
DI void mark(LAS unsigned char* L, int i, int k, u32 val) { *(volatile LAS u32*)(L + L_HINT + 16 * i + 4 * k) = val; }
DI v4u peek(LAS unsigned char* L, int i) { return *(volatile LAS v4u*)(L + L_HINT + 16 * i); }
DI u32 umin(u32 a, u32 b) { return a < b ? a : b; }
DI void await_cp(LAS unsigned char* L, u32 k) {
#pragma nounroll
  for (u32 spins = 0; spins < (1u << 20); ++spins) {
    const v4u v = peek(L, H_CP);
    if (__builtin_amdgcn_readfirstlane(umin(umin(v.x, v.y), umin(v.z, v.w))) >= k) break;
    __builtin_amdgcn_s_sleep(CSLP);
  }
  RELOAD();
}
template <bool AUX> DI u32 pp_min(v4u v) {
  u32 m = umin(v.x, v.y);
  if (AUX) m = umin(m, umin(v.z, v.w));
  return __builtin_amdgcn_readfirstlane(m);
}
template <bool AUX> DI void need_pp(LAS unsigned char* L, v4u v, u32 k) {
  if (__builtin_expect(pp_min<AUX>(v) < k, 0)) {
#pragma nounroll
    for (u32 spins = 0; spins < (1u << 20); ++spins)
      if (pp_min<AUX>(peek(L, H_PP)) >= k) break;
  }
  RELOAD();
}
DI void feat(const f32x16& P, u32 (&ph)[16]) {
#pragma clang fp contract(off)
  constexpr float L2E = 1.4426950408889634f;
  f32x2 p[8];
#pragma unroll
  for (int i = 0; i < 8; ++i) { const u32 t = pk(P[2 * i], P[2 * i + 1]); p[i] = f32x2{lo16(t), hi16(t)} * L2E; }
  float mx[4], mn[4];
#pragma unroll
  for (int i = 0; i < 4; ++i) {
    mx[i] = fmaxf(fmaxf(p[2 * i].x, p[2 * i].y), fmaxf(p[2 * i + 1].x, p[2 * i + 1].y));
    mn[i] = fminf(fminf(p[2 * i].x, p[2 * i].y), fminf(p[2 * i + 1].x, p[2 * i + 1].y));
  }
  const float mxl = fmaxf(fmaxf(mx[0], mx[1]), fmaxf(mx[2], mx[3])), mnl = fminf(fminf(mn[0], mn[1]), fminf(mn[2], mn[3]));
  f32x2 e1[8], e2[8];
#pragma unroll
  for (int i = 0; i < 8; ++i) { e1[i] = p[i] - mxl; e2[i] = mnl - p[i]; }
  SB0();
#pragma unroll
  for (int i = 0; i < 8; ++i) {
    e1[i] = f32x2{__builtin_amdgcn_exp2f(e1[i].x), __builtin_amdgcn_exp2f(e1[i].y)};
    e2[i] = f32x2{__builtin_amdgcn_exp2f(e2[i].x), __builtin_amdgcn_exp2f(e2[i].y)};
  }
  const f32x2 s1 = ((e1[0] + e1[1]) + (e1[2] + e1[3])) + ((e1[4] + e1[5]) + (e1[6] + e1[7]));
  const f32x2 s2 = ((e2[0] + e2[1]) + (e2[2] + e2[3])) + ((e2[4] + e2[5]) + (e2[6] + e2[7]));
  const float r1 = rcp(s1.x + s1.y), r2 = rcp(s2.x + s2.y);
#pragma unroll
  for (int i = 0; i < 8; ++i) { const f32x2 u = e1[i] * r1, v = e2[i] * r2; ph[i] = pk(u.x, u.y); ph[8 + i] = pk(v.x, v.y); }
}

#define TSCLK() __builtin_amdgcn_s_memrealtime()
#define TS(i) do {} while (0)
#define TS_DUMP() do {} while (0)
#define TS_INIT() do {} while (0)

__global__ void __launch_bounds__(512, 2) mega(Params p) {
  extern __shared__ __attribute__((aligned(16))) unsigned char smem[];
  LAS unsigned char* L = (LAS unsigned char*)smem;
  TS_INIT();
  TS(0);
  u64 pc0;
  asm volatile("s_getpc_b64 %0" : "=s"(pc0));
  const int tid = threadIdx.x, lane = tid & 63;
  int w = __builtin_amdgcn_readfirstlane(tid >> 6);
  const int b = blockIdx.x, xcd = b & 7, rank = b >> 3, panel = (xcd >> 1) * 4 + (rank >> 3), mem = (xcd & 1) * 8 + (rank & 7);
  const int hh = lane >> 5, l31 = lane & 31;
  unsigned char* ws = p.ws;
  const int rowh = mem & 1, colg = mem >> 1;
  const int pair0 = min(3 * mem, 2 * mem + 11);
  const bool three = mem < 11;
  const rsrc_t rs = __builtin_amdgcn_make_buffer_rsrc(ws, 0, WS_BYTES, 0x00020000);
  const int pb = OFF_PAN + panel * PSTR, fw = panel * 24;
  const int mcg = xcd * 8 + (rank >> 2), mkp = rank & 3;
  const rsrc_t rqkv = __builtin_amdgcn_make_buffer_rsrc((void*)p.wqkv, 0, D * 3 * D * 2, 0x00020000);
  const rsrc_t rwo = __builtin_amdgcn_make_buffer_rsrc((void*)p.wo, 0, D * D * 2, 0x00020000);
  const rsrc_t rgu = __builtin_amdgcn_make_buffer_rsrc((void*)((w & 1) ? p.wu : p.wg), 0, D * F * 2, 0x00020000);
  const rsrc_t rwd = __builtin_amdgcn_make_buffer_rsrc((void*)p.wd, 0, F * D * 2, 0x00020000);

  v4u r0[4];
  u32 cv0[4], epoch;
  u32 vq = (lane >> 2) * (3 * D * 2) + (lane & 3) * 16;
  asm volatile("" : "+v"(vq));
  {
    const u32 ld = (u32)(size_t)L + w * 1024;
    if (w <= 4) {
      const rsrc_t r1 = __builtin_amdgcn_make_buffer_rsrc((void*)(w == 4 ? p.wmod : p.x), 0, w == 4 ? D * 6 * D * 2 : 2048 * D * 2, 0x00020000);
      const int vo = w == 4 ? (128 * mkp + 4 * (lane >> 1)) * (6 * D * 2) + mcg * 32 + (lane & 1) * 16
                            : (panel * 128 + mem * 8 + (lane >> 3)) * (D * 2) + (lane & 7) * 16 + 256 * w;
#pragma unroll
      for (int i = 0; i < 4; ++i) r0[i] = __builtin_amdgcn_raw_buffer_load_b128(r1, vo, w == 4 ? i * (6 * D * 2) : i < 2 ? i * 128 : 1 << 30, 0);
      SB0();
    }
    {
      const rsrc_t r2 = __builtin_amdgcn_make_buffer_rsrc((void*)p.c, 0, w == 4 ? D * 2 : 0, 0x00020000);
      const v2u c2 = __builtin_amdgcn_raw_buffer_load_b64(r2, (128 * mkp + 4 * (lane >> 1)) * 2, 0, 0);
      cv0[0] = c2.x; cv0[1] = c2.y;
      epoch = __builtin_amdgcn_raw_buffer_load_b32(rs, 0, OFF_EPOCH, AUX_SC1);
      __builtin_amdgcn_raw_buffer_store_b32(0u, rs, (w == 4 && lane < 2) ? lane * 128 : WS_BYTES, OFF_ARR + b * 256, AUX_SC1);
      SB0();
    }
    if (w >= 5) {
      const rsrc_t rc = __builtin_amdgcn_make_buffer_rsrc((void*)pc0, 0, CODE_BYTES, 0x00020000);
      gldsi<slot(128), 0>(rc, lane < CODE_KB / 6 ? (u32)lane * 128u : 1u << 30, (u32)(((rank & 15) * 3 + w - 5) * (CODE_KB / 6) * 128), ld);
      const u32 tq = (rank * 16 + (lane >> 2)) * (3 * D * 2) + (xcd & 1) * 512 + (lane & 3) * 128;
      if (w != 5) gldsi<slot(128), 0>(rqkv, tq, (w - 5) * (D * 2), ld);
      else {
        gldsi<slot(128), 0>(rs, lane < 2 ? (u32)(pb + lane * (848 << 10)) : 1u << 30, 0u, ld);
        {
          const u32 t1 = lane == 0 ? (u32)rank << 16 : 1u << 30;
          const rsrc_t rg = __builtin_amdgcn_make_buffer_rsrc((void*)p.wg, 0, D * F * 2, 0x00020000);
          const rsrc_t ro = __builtin_amdgcn_make_buffer_rsrc((void*)p.out, 0, 2048 * D * 2, 0x00020000);
          gldsi<slot(128), 0>(rs, lane == 0 ? ((u32)rank << 16) + 65024u : lane < 3 ? (u32)(OFF_PAN + (xcd >> 1) * 4 * PSTR + ((rank * 2 + lane - 1) << 16)) : 1u << 30, 0u, ld);
          gldsi<slot(128), 0>(rg, t1, 0u, ld);
          gldsi<slot(128), 0>(rgu, t1, 0u, ld);
          gldsi<slot(128), 0>(rwd, t1, 0u, ld);
          gldsi<slot(128), 0>(rwo, t1, 0u, ld);
          gldsi<slot(128), 0>(ro, t1, 0u, ld);
        }
        const rsrc_t rp = __builtin_amdgcn_make_buffer_rsrc((void*)p.wphi, 0, 32 * 256 * 2, 0x00020000);
        gldsi<L_CF + 2048, 0>(rp, lane * 16, mem * 1024, (u32)(size_t)L);
      }
    }
    if (tid < 32) *((LAS u32*)(L + L_HINT) + tid) = 0u;
    TS(27);
    bar();
    asm volatile("" : "+s"(w));
    if (w >= 6) {
      u32 lo = (u32)(size_t)L + (w & 1) * 1024, so = (D + (w & 1) * D + mem * 32) * 2;
#pragma nounroll
      for (int i = 0; i < KV1 / 2; ++i) {
        gldsi<0, 0>(rqkv, vq, so, lo);
        gldsi<2048, 16 * (3 * D * 2)>(rqkv, vq, so, lo);
        lo += 4096; so += 32 * (3 * D * 2);
      }
    }
    if (w == 4) {
      __builtin_amdgcn_s_setprio(3);
      TS(10);
      float sl[4], acc[8];
#pragma unroll
      for (int i = 0; i < 4; ++i) { const float c = (i & 1) ? hi16(cv0[i >> 1]) : lo16(cv0[i >> 1]); sl[i] = rb(c * rcp(1.f + __expf(-c))); }
      u32 tag = epoch;
      asm volatile("" : "+v"(tag));
      tag += 1u;
      TS(11);
#pragma unroll
      for (int j = 0; j < 8; ++j) acc[j] = 0.f;
#pragma unroll
      for (int i = 0; i < 4; ++i)
#pragma unroll
        for (int j = 0; j < 4; ++j) { acc[2 * j] += sl[i] * lo16(r0[i][j]); acc[2 * j + 1] += sl[i] * hi16(r0[i][j]); }
      float c0, c1;
      TS(12);
      red8q(acc, c0, c1);
      c0 = dpp_add<DPP_X2>(c0); c1 = dpp_add<DPP_X2>(c1);
      const int go = ((lane >> 1) & 7) * 32768 + (8 * (lane & 1) + 2 * (lane >> 4)) * 8;
      const v4u gv = {__float_as_uint(c0), tag, __float_as_uint(c1), tag};
      drain();
      st128(gv, rs, go, OFF_MODR + mkp * 8192 + mcg * 128);
      st128(gv, rs, go, OFF_MODR + 8 * 32768 + mkp * 8192 + mcg * 128);
      sig(L, 0, 0);
    }
    asm volatile("" : "+s"(w));
  }

  if (w == 4 || w == 5) {
    const int a = w & 1;
    u32 tag;
    tag = epoch;
    asm volatile("" : "+v"(tag));
    tag += 1u;
    drain();
    TS(1);
    bar();
    __builtin_amdgcn_s_setprio(APRIO);
    await<4>(L, 2);
    const int kp = xcd >> 1, cpair = (xcd & 1) * 24 + rank;
    const bool con = rank < ((xcd & 1) ? 19 : 24);
    const int col0 = a * F + cpair * 32;
    v4u wm[8];
#pragma unroll
    for (int i = 0; i < 8; ++i)
      wm[i] = __builtin_amdgcn_raw_buffer_load_b128(rgu, con ? (128 * kp + 2 * (lane >> 2)) * (F * 2) + cpair * 64 + (lane & 3) * 16 : 1 << 30, (32 * (i >> 1) + (i & 1)) * (F * 2), 0);
    SB0();
    if (a == 1) {
      v4u wn[8]; u32 cv[8];
      const int row0 = 128 * mkp + (lane >> 2);
#pragma unroll
      for (int i = 0; i < 8; ++i) cv[i] = p.c[row0 + 16 * i];
#pragma unroll
      for (int i = 0; i < 8; ++i) wn[i] = *(const GAS v4u*)(p.wmod + (size_t)(row0 + 16 * i) * (6 * D) + 1024 + mcg * 32 + (lane & 3) * 8);
      SB0();
      bar_raw();
      SB0();
      float acc[8];
#pragma unroll
      for (int j = 0; j < 8; ++j) acc[j] = 0.f;
#pragma unroll
      for (int i = 0; i < 8; ++i) {
        const float c = lo16(cv[i]), sl = rb(c * rcp(1.f + __expf(-c)));
#pragma unroll
        for (int j = 0; j < 4; ++j) { acc[2 * j] += sl * lo16(wn[i][j]); acc[2 * j + 1] += sl * hi16(wn[i][j]); }
      }
      float c0, c1;
      red8q(acc, c0, c1);
      st128(v4u{__float_as_uint(c0), tag, __float_as_uint(c1), tag}, rs, (lane & 12) == 0 ? (8 * (lane & 3) + 2 * (lane >> 4)) * 8 : WS_BYTES,
            OFF_MODG + mkp * 16384 + mcg * 256);
    } else {
      bar_raw();
    }
    bar_raw();
    TS(2);
    {
      const int idx0 = (mem * 4 + 2 * a) * 16;
      v4u e[32], zz[2];
      {
        u32 todo = 0xFFu;
        for (u32 spins = 0; spins < SPIN_CAP && todo; ++spins) {
          const u32 bad = ~__builtin_amdgcn_readfirstlane(ask(rs, FL_KV + mem * 2 + a, lane));
          __builtin_amdgcn_fence(__ATOMIC_ACQUIRE, "wavefront");
          RELOAD();
          static_for<0, 8>([&](auto G) {
            constexpr int g = G.value;
            if (((todo >> g) & 1u) && !(bad & (0x00030003u << (2 * g)))) {
              todo &= ~(1u << g);
              e[2 * g] = ld128(rs, lane * 16, OFF_KVE + (idx0 + 2 * g) * 1024);
              e[2 * g + 1] = ld128(rs, lane * 16, OFF_KVE + (idx0 + 2 * g + 1) * 1024);
              e[16 + 2 * g] = ld128(rs, lane * 16, OFF_KVE + (idx0 + 16 + 2 * g) * 1024);
              e[17 + 2 * g] = ld128(rs, lane * 16, OFF_KVE + (idx0 + 17 + 2 * g) * 1024);
            }
          });
        }
      }
      TS(3);
      zz[0] = ldfrag<0>(rs, lane * 16, OFF_KVZ + idx0 * 64);
      zz[1] = ldfrag<1>(rs, lane * 16, OFF_KVZ + idx0 * 64);
      SB0();
#pragma unroll
      for (int q = 0; q < 2; ++q) {
        f32x4 s0 = __builtin_bit_cast(f32x4, e[16 * q]), s1 = __builtin_bit_cast(f32x4, e[16 * q + 1]);
#pragma unroll
        for (int i = 2; i < 16; i += 2) { s0 += __builtin_bit_cast(f32x4, e[16 * q + i]); s1 += __builtin_bit_cast(f32x4, e[16 * q + i + 1]); }
        s0 += s1;
        *(LAS v2u*)(L + L_KVF + (2 * a + q) * 512 + l31 * 16 + hh * 8) = v2u{pk(s0[0], s0[1]), pk(s0[2], s0[3])};
      }
      f32x4 z;
#pragma unroll
      for (int j = 0; j < 4; ++j) {
        float t = fold32(__uint_as_float(zz[0][j]), __uint_as_float(zz[1][j]));
        t = fold16(t, t);
        t = dpp_add<DPP_ROR4>(t);
        z[j] = dpp_add<DPP_ROR8>(t);
      }
      if (l31 < 4) *(LAS v2u*)(L + L_ZF + (2 * a + hh) * 32 + ((lane & 1) ^ 1) * 16 + ((lane >> 1) & 1) * 8) = v2u{pk(z[0], z[1]), pk(z[2], z[3])};
    }
    TS(4);
    bar();
    u32 gsb[4], gsc[4];
    {
      const int vo = ((hh ? 2 : 3 * a) * D + colg * 64 + 2 * l31) * 8, ro = (D + 128 * kp + 2 * lane) * 8;
      v4u g[12];
      for (u32 spins = 0; spins < SPIN_CAP; ++spins) {
        RELOAD();
#pragma unroll
        for (int i = 0; i < 12; ++i) g[i] = ld128(rs, i < 4 ? vo : ro + (i >> 3) * (D * 8), OFF_MODG + (i & 3) * 16384);
        bool ok = true;
#pragma unroll
        for (int i = 0; i < 12; ++i) ok &= (g[i].y == tag) & (g[i].w == tag);
        if (__all(ok)) break;
      }
      u32 pr[3];
#pragma unroll
      for (int t = 0; t < 3; ++t)
        pr[t] = pk((__uint_as_float(g[4 * t].x) + __uint_as_float(g[4 * t + 1].x)) + (__uint_as_float(g[4 * t + 2].x) + __uint_as_float(g[4 * t + 3].x)),
                   (__uint_as_float(g[4 * t].z) + __uint_as_float(g[4 * t + 1].z)) + (__uint_as_float(g[4 * t + 2].z) + __uint_as_float(g[4 * t + 3].z)));
      const u32 v1 = pk(1.f + lo16(pr[0]), 1.f + hi16(pr[0])), vv = hh ? v1 : pr[0];
      *(LAS f32x2*)(L + L_VEC + (hh ? 1 : 2 * a) * 256 + l31 * 8) = f32x2{lo16(vv), hi16(vv)};
      const u32 sc = pk(1.f + lo16(pr[2]), 1.f + hi16(pr[2]));
#pragma unroll
      for (int i = 0; i < 4; ++i) {
        gsb[i] = __builtin_amdgcn_ds_bpermute((16 * i + (lane >> 2)) * 4, pr[1]);
        gsc[i] = __builtin_amdgcn_ds_bpermute((16 * i + (lane >> 2)) * 4, sc);
      }
    }
    if (con) {
      float a1[8], a2[8];
#pragma unroll
      for (int j = 0; j < 8; ++j) { a1[j] = 0.f; a2[j] = 0.f; }
#pragma unroll
      for (int i = 0; i < 8; ++i) {
        const float sb = (i & 1) ? hi16(gsb[i >> 1]) : lo16(gsb[i >> 1]), sc = (i & 1) ? hi16(gsc[i >> 1]) : lo16(gsc[i >> 1]);
#pragma unroll
        for (int j = 0; j < 4; ++j) {
          a1[2 * j] += sc * lo16(wm[i][j]); a1[2 * j + 1] += sc * hi16(wm[i][j]);
          a2[2 * j] += sb * lo16(wm[i][j]); a2[2 * j + 1] += sb * hi16(wm[i][j]);
        }
      }
      float c10, c11, c20, c21;
      red8q(a1, c10, c11); red8q(a2, c20, c21);
      const int k = (lane >> 2) & 3;
      const float val = k == 0 ? c10 : k == 1 ? c20 : k == 2 ? c11 : c21;
      st64(v2u{__float_as_uint(val), tag}, rs, (col0 + 8 * (lane & 3) + 2 * (lane >> 4)) * 16 + k * 8, OFF_CG + kp * CG_PART);
    }
    TS(5);
    bar();
    {
      const int ntile = three ? 6 : 4;
      v4u cg[8];
      int off[2];
#pragma unroll
      for (int i = 0; i < 2; ++i) {
        int tile = 4 * i + 2 * a + hh; tile = tile < ntile ? tile : 0;
        off[i] = (32 * pair0 + 16 * tile + (lane & 15) + ((lane >> 4) & 1) * F) * 16;
      }
      for (u32 spins = 0; spins < SPIN_CAP; ++spins) {
        RELOAD();
        bool ok = true;
#pragma unroll
        for (int i = 0; i < 8; ++i) cg[i] = ld128(rs, off[i >> 2], OFF_CG + (i & 3) * CG_PART);
#pragma unroll
        for (int i = 0; i < 8; ++i) ok &= (cg[i].y == tag) & (cg[i].w == tag);
        if (__all(ok)) break;
      }
#pragma unroll
      for (int i = 0; i < 2; ++i) {
        const float c1 = (__uint_as_float(cg[4 * i].x) + __uint_as_float(cg[4 * i + 1].x)) + (__uint_as_float(cg[4 * i + 2].x) + __uint_as_float(cg[4 * i + 3].x));
        const float c2 = (__uint_as_float(cg[4 * i].z) + __uint_as_float(cg[4 * i + 1].z)) + (__uint_as_float(cg[4 * i + 2].z) + __uint_as_float(cg[4 * i + 3].z));
        const float h1 = rb(c1), m1 = rb(c1 - h1), l1 = c1 - h1 - m1, h2 = rb(c2), m2 = rb(c2 - h2), l2 = c2 - h2 - m2;
        const int tile = 4 * i + 2 * a + hh;
        if (tile < 6) *(LAS v4u*)(L + L_CF + tile * 512 + l31 * 16) = v4u{pk(h1, m1), pk(l1, h2), pk(m2, l2), 0u};
      }
    }
    await_cp(L, 1);
    {
      v4u st[16];
#pragma unroll
      for (int t = 0; t < 16; ++t) st[t] = ld128(rs, l31 * 16 + hh * 8192 + t * 512, pb + PN_ST + 2 * a * 8192);
      for (u32 spins = 0; spins < SPIN_CAP; ++spins) {
        bool ok = true;
#pragma unroll
        for (int t = 0; t < 16; ++t) ok &= (st[t].y == tag) & (st[t].w == tag);
        if (__all(ok)) break;
        RELOAD();
#pragma unroll
        for (int t = 0; t < 16; ++t) st[t] = ld128(rs, l31 * 16 + hh * 8192 + t * 512, pb + PN_ST + 2 * a * 8192);
      }
      float ms = 0.f;
#pragma unroll
      for (int t = 0; t < 16; ++t) ms += __uint_as_float(st[t].x);
      const float mean = ms * (1.f / 16.f);
      float q = 0.f;
#pragma unroll
      for (int t = 0; t < 16; ++t) { const float d = __uint_as_float(st[t].x) - mean; q += __uint_as_float(st[t].z) + 32.f * d * d; }
      const float den = rb(__builtin_amdgcn_sqrtf(rb(rb(q * (1.f / D)) + 1e-5f)));
      *(LAS v4u*)(L + L_KVF + a * 1024 + lane * 16) = v4u{pk(-mean, -mean), pk(-mean, den), pk(den, den), __float_as_uint(rcp(den * den))};
    }
    RELOAD();
    mark(L, H_PP, 2 + a, 5u);
    TS(6);
    {
      v4u wt[58];
      const u32 vd = (lane >> 2) * (D * 2) + (lane & 3) * 16, sd = a * 16 * (D * 2) + colg * 128;
      const Base3 wdst(L + a * 2048 + lane * 16);
      static_for<28, 86>([&](auto J) {
        wt[J.value - 28] = __builtin_amdgcn_raw_buffer_load_b128(rwd, NBV(vd + (J.value & 1) * 64), sd + pair_of(J.value >> 1) * 32 * (D * 2), 0); SB0(); });
      TS(7);
#define ST_WT(T0, T1, k) do { static_for<2 * (T0), 2 * (T1)>([&](auto J) { \
      *(LAS v4u*)wdst.template at<slot(P_WD + 4 * (J.value >> 1)) + (J.value & 1) * 1024>() = wt[J.value - 28]; }); \
      RELOAD(); mark(L, H_PP, 2 + a, k); RELOAD(); } while (0)
      await_cp(L, 4);
      ST_WT(14, 18, 6u);
      await_cp(L, 5);
      ST_WT(18, 26, 7u);
      await_cp(L, 6);
      ST_WT(26, 34, 8u);
      TS(8);
      await_cp(L, 7);
      ST_WT(34, 43, 9u);
      TS(9);
    }
    TS_DUMP();
    return;
  }

  if (w >= 6) {
    const int v = w & 1;
    __builtin_amdgcn_s_setprio(LPRIO);
    const u32 ldsb = (u32)(size_t)L;
    const u32 l1 = ldsb + v * 1024, l2 = ldsb + v * 2048;
    const u32 skv = (D + v * D + mem * 32) * 2, sq = v * 16 * (3 * D * 2) + mem * 64;
    const u32 so = 16 * v * 16 * (D * 2) + colg * 128, sd = v * 16 * (D * 2) + colg * 128;
    const u32 sg = pair0 * 64, sg2 = three ? sg + 128 : 1u << 30;
#define LD_KV(S0, S1, PC) static_for<S0, S1>([&](auto S) { gldsi<slot(P_KV + 2 * S.value), S.value * 16 * (3 * D * 2)>(rqkv, vq, skv, l1); PACE(PC); })
#define PACE(n) do { if ((n) > 0) __builtin_amdgcn_s_sleep(n); } while (0)
#define LD_Q(S0, S1, PC) static_for<S0, S1>([&](auto S) { gldsi<slot(P_Q + 2 * S.value), 2 * S.value * 16 * (3 * D * 2)>(rqkv, vq, sq, l1); PACE(PC); })
#define LD_DN(RS, SB, P0, T0, T1, PC)                                                                    \
  static_for<T0, T1>([&](auto T) {                                                                   \
    gldsi<slot((P0) + 4 * T.value), T.value * 16 * (D * 2)>(RS, vd, SB, l2);                         \
    gldsi<slot((P0) + 4 * T.value) + 1024, T.value * 16 * (D * 2) + 64>(RS, vd, SB, l2); PACE(PC);   \
  })
#define LD_GU(S0, S1, PC)                                                                                \
  static_for<S0, S1>([&](auto S) {                                                                   \
    gldsi<slot(P_GU + 6 * S.value), S.value * 16 * (F * 2)>(rgu, vg, sg, l1);                        \
    gldsi<slot(P_GU + 6 * S.value + 2), S.value * 16 * (F * 2) + 64>(rgu, vg, sg, l1);               \
    gldsi<slot(P_GU + 6 * S.value + 4), S.value * 16 * (F * 2)>(rgu, vg, sg2, l1); PACE(PC);         \
  })
    TS(1);
    VMCNT(0);
    SB0();
    int ln;
    asm volatile("v_mbcnt_lo_u32_b32 %0, -1, 0\n\tv_mbcnt_hi_u32_b32 %0, -1, %0" : "=v"(ln));
    const u32 vd = (ln >> 2) * (D * 2) + (ln & 3) * 16;
    const u32 vg = (ln >> 2) * (F * 2) + (((ln & 3) ^ (v << 1)) * 16);
    TS(2);
    await<4>(L, 3);
    LD_KV(KV1, 32, PKV);
    VMCNT(0);
    TS(11);
    bar_raw();
    v4u wt[48];
    const Base3 wdst1(L + v * 1024 + ln * 16);
#define GUN(s_, P_) (P_GU + ((s_) < 16 ? 6 * (s_) + 2 * (P_) : 96 + 32 * (P_) + 2 * ((s_) - 16)))
#define GUR(s_, P_) __builtin_amdgcn_raw_buffer_load_b128(rgu, NBV(vg), ((P_) == 2 ? sg2 : sg + (P_) * 64) + (s_) * 16 * (F * 2), 0)
#define GUW(s_, P_, r_) *(LAS v4u*)wdst1.template at<slot(GUN(s_, P_))>() = wt[r_]
#define LD_GU2(S0, S1, PC) static_for<S0, S1>([&](auto S) { constexpr int s_ = S.value; \
      gldsi<slot(GUN(s_, 0)), s_ * 16 * (F * 2)>(rgu, vg, sg, l1); SB0(); \
      wt[2 * (s_ - 16)] = GUR(s_, 1); wt[2 * (s_ - 16) + 1] = GUR(s_, 2); PACE(PC); SB0(); })
#define LD_WD(T0, T1) static_for<T0, T1>([&](auto T) { \
      gldsi<slot(P_WD + 4 * T.value), pair_of(T.value) * 32 * (D * 2)>(rwd, vd, sd, l2); \
      gldsi<slot(P_WD + 4 * T.value) + 1024, pair_of(T.value) * 32 * (D * 2) + 64>(rwd, vd, sd, l2); })
#define DONE(k) do { RELOAD(); mark(L, H_PP, v, k); RELOAD(); } while (0)
    await<4>(L, 2);
    gldsi<L_CF, 0>(v ? rwo : rqkv, (rank * 16 + (ln >> 2)) * (v ? D * 2 : 3 * D * 2) + (xcd & 1) * 512 + (ln & 3) * 128, 0u, l1);
    LD_Q(0, 16, 0);
    TS(3);
    bar_raw();
    VMCNT(0);
    bar_raw();
    gldsi<L_CF, 0>(rwd, v ? 1u << 30 : ((xcd >> 1) * 32 + rank + 128 * (ln >> 2)) * (D * 2) + (xcd & 1) * 512 + (ln & 3) * 128, 0u, l1);
    LD_DN(rwo, so, P_WO, 0, 16, 1);
    LD_GU(0, 6, PGU);
    VMCNT(18);
    TS(4);
    bar_raw();
    LD_GU(6, 12, PGU);
    await<4>(L, 4);
    static_for<0, 12>([&](auto J) { wt[32 + J.value] = GUR(12 + J.value / 3, J.value % 3); SB0(); });
    static_for<16, 20>([&](auto S) { constexpr int s_ = S.value;
      wt[28 + s_] = GUR(s_, 0); wt[2 * (s_ - 16)] = GUR(s_, 1); wt[2 * (s_ - 16) + 1] = GUR(s_, 2); SB0(); });
    VMCNT(24);
    TS(5);
    bar_raw();
    static_for<0, 12>([&](auto J) { GUW(12 + J.value / 3, J.value % 3, 32 + J.value); });
    static_for<16, 20>([&](auto S) { GUW(S.value, 0, 28 + S.value); });
    DONE(1);
    LD_GU2(20, 32, PG2);
    VMCNT(0);
    DONE(3);
    TS(6);
    await_cp(L, 1);
    {
      const u32 tdn = ((2 * rank + v) * 16 + (ln >> 2)) * (D * 2) + (xcd & 1) * 512 + (ln & 3) * 128;
      __builtin_amdgcn_s_waitcnt(0x0F70);
      __builtin_amdgcn_s_sleep(4);
      gldsi<L_T, 0>(rwd, tdn, 0u, l1);
      gldsi<L_T, 1024 * (D * 2)>(rwd, tdn, 0u, l1);
    }
    static_for<16, 32>([&](auto S) { GUW(S.value, 1, 2 * (S.value - 16)); });
    DONE(4);
    await_cp(L, 2);
    static_for<16, 32>([&](auto S) { GUW(S.value, 2, 2 * (S.value - 16) + 1); });
    DONE(5);
    TS(7);
    await_cp(L, 3);
    LD_WD(0, 10);
    await_cp(L, 4);
    LD_WD(10, 14);
    VMCNT(0);
    DONE(100);
    TS(8);
    TS_DUMP();
    return;
  }

  __builtin_amdgcn_s_setprio(2);
  const int s2 = w & 1, kh = w >> 1;
  const int strip2 = rowh * 2 + s2, ncol = colg * 64 + kh * 32;
  v4u aphi0, aphi1;
  u32 tag;
  {
    float xf[16];
#pragma unroll
    for (int i = 0; i < 8; ++i) { xf[2 * i] = lo16(r0[i >> 2][i & 3]); xf[2 * i + 1] = hi16(r0[i >> 2][i & 3]); }
    float sm = (((xf[0] + xf[1]) + (xf[2] + xf[3])) + ((xf[4] + xf[5]) + (xf[6] + xf[7]))) + (((xf[8] + xf[9]) + (xf[10] + xf[11])) + ((xf[12] + xf[13]) + (xf[14] + xf[15])));
    sm = dpp_add<DPP_X1>(sm); sm = dpp_add<DPP_X2>(sm); sm = dpp_add<DPP_HM>(sm);
    const float mw = sm * (1.f / 128.f);
    float q[4];
#pragma unroll
    for (int i = 0; i < 4; ++i) {
      const float d0 = xf[4 * i] - mw, d1 = xf[4 * i + 1] - mw, d2 = xf[4 * i + 2] - mw, d3 = xf[4 * i + 3] - mw;
      q[i] = (d0 * d0 + d1 * d1) + (d2 * d2 + d3 * d3);
    }
    float qq = (q[0] + q[1]) + (q[2] + q[3]);
    qq = dpp_add<DPP_X1>(qq); qq = dpp_add<DPP_X2>(qq); qq = dpp_add<DPP_HM>(qq);
    *(volatile LAS f32x2*)(L + L_KVF + (lane >> 3) * 32 + w * 8) = f32x2{mw, qq};
    sig(L, 1, w);
    await<4>(L, 1);
    const f32x4 pa = *(LAS f32x4*)(L + L_KVF + (lane >> 3) * 32), pc = *(LAS f32x4*)(L + L_KVF + (lane >> 3) * 32 + 16);
    const float mean = ((pa[0] + pa[2]) + (pc[0] + pc[2])) * 0.25f;
    const float f0 = pa[0] - mean, f1 = pa[2] - mean, f2 = pc[0] - mean, f3 = pc[2] - mean;
    const float m2 = ((pa[1] + pa[3]) + (pc[1] + pc[3])) + 128.f * ((f0 * f0 + f1 * f1) + (f2 * f2 + f3 * f3));
    const float m1 = rb(mean), r1 = rcp(rb(__builtin_amdgcn_sqrtf(rb(rb(m2 * (1.f / D)) + 1e-5f))));
    float xn[16];
#pragma unroll
    for (int i = 0; i < 8; ++i) {
      const u32 d = pk(xf[2 * i] - m1, xf[2 * i + 1] - m1);
      const u32 t = pk(lo16(d) * r1, hi16(d) * r1);
      xn[2 * i] = lo16(t); xn[2 * i + 1] = hi16(t);
    }
#pragma unroll
    for (int i = 0; i < 16; ++i) asm volatile("" : "+v"(xn[i]));
    tag = __builtin_amdgcn_readfirstlane(epoch) + 1u;
    TS(1);
    await<1>(L, 0);
    __builtin_amdgcn_s_sleep(PRE_MOD);
    TS(21);
    v4u g[8];
    {
      u32 todo = 0xFFu;
      for (u32 spins = 0; spins < SPIN_CAP && todo; ++spins) {
        RELOAD();
        static_for<0, 8>([&](auto I) {
          constexpr int i = I.value;
          if ((todo >> i) & 1u) g[i] = ld128(rs, lane * 16, OFF_MODR + panel * 32768 + (i & 3) * 8192 + (i >> 2) * 4096 + w * 1024);
        });
        todo = 0;
        static_for<0, 8>([&](auto I) { if (__builtin_amdgcn_ballot_w64((g[I.value].y != tag) | (g[I.value].w != tag))) todo |= 1u << I.value; });
      }
    }
    {
      u32 pr[2];
#pragma unroll
      for (int t = 0; t < 2; ++t)
        pr[t] = pk((__uint_as_float(g[4 * t].x) + __uint_as_float(g[4 * t + 1].x)) + (__uint_as_float(g[4 * t + 2].x) + __uint_as_float(g[4 * t + 3].x)),
                   (__uint_as_float(g[4 * t].z) + __uint_as_float(g[4 * t + 1].z)) + (__uint_as_float(g[4 * t + 2].z) + __uint_as_float(g[4 * t + 3].z)));
      *(LAS u32*)(L + L_T + w * TW + lane * 4) = pr[0];
      *(LAS u32*)(L + L_T + w * TW + 256 + lane * 4) = pk(1.f + lo16(pr[1]), 1.f + hi16(pr[1]));
    }
    v4u sav[2], cav[2];
#pragma unroll
    for (int i = 0; i < 2; ++i) {
      sav[i] = *(LAS v4u*)(L + L_T + w * TW + i * 128 + (lane & 7) * 16);
      cav[i] = *(LAS v4u*)(L + L_T + w * TW + 256 + i * 128 + (lane & 7) * 16);
    }
    sig(L, 3, w);
    TS(2);
    const int strip = mem >> 2, r = 8 * (mem & 3) + (lane >> 3);
#pragma unroll
    for (int i = 0; i < 2; ++i) {
      v4u o;
#pragma unroll
      for (int j = 0; j < 4; ++j) {
        const u32 sa = sav[i][j], sc = cav[i][j];
        const u32 t = pk(xn[8 * i + 2 * j] * lo16(sc), xn[8 * i + 2 * j + 1] * hi16(sc));
        o[j] = pk(lo16(t) + lo16(sa), hi16(t) + hi16(sa));
      }
      st128(o, rs, (4 * i + ((lane & 7) >> 1)) * 1024 + (r + 32 * (lane & 1)) * 16, pb + PN_H + strip * 32768 + w * 8192);
    }
    await<4>(L, 3);
    publish(rs, fw + FL_H + strip, (mem & 3) * 4 + w, lane);
  }
  TS(3);

  const int trq = (lane & 15) >> 2, trp = lane & 3, trb = (lane >> 4) & 1;
  const f32x16 zero16 = {0.f, 0.f, 0.f, 0.f, 0.f, 0.f, 0.f, 0.f, 0.f, 0.f, 0.f, 0.f, 0.f, 0.f, 0.f, 0.f};
  u32 phq[16];
  v4u wb[PD];
  {
    const Base3 wnat(L + (8 * hh + trq) * 64 + trb * 32 + trp * 8);
    waitn(rs, fw + FL_H + w, 0xFFFFu, lane);
    TS(4);
    v4u a[32];
    ldfrags<W0>(a, rs, lane * 16, pb + PN_H + w * 32768);
    bar_raw();
    {
      const int hs = (l31 >> 2) & 1;
      const LAS u16* wp = (const LAS u16*)(L + L_CF + 2048) + hs * 256 + 4 * hh * 16 + (l31 & 3) + 4 * (l31 >> 3);
      u32 e[8];
#pragma unroll
      for (int j = 0; j < 8; ++j) e[j] = wp[(8 * (j >> 2) + (j & 3)) * 16];
      const v4u av = {e[0] | (e[1] << 16), e[2] | (e[3] << 16), e[4] | (e[5] << 16), e[6] | (e[7] << 16)};
      const v4u z = {0, 0, 0, 0};
      aphi0 = hs == 0 ? av : z; aphi1 = hs == 1 ? av : z;
      asm volatile("" : "+v"(aphi0), "+v"(aphi1));
    }
    static_for<0, PD>([&](auto U) { wb[U.value] = ldw<256>(wnat.at<slot(P_KV + U.value)>()); });
    SB0();
    f32x16 kacc = zero16, vacc = zero16;
    static_for<0, 64>([&](auto U) {
      constexpr int u = U.value;
      const v4u cur = wb[u % PD];
      if constexpr (u + PD < 64) wb[u % PD] = ldw<256>(wnat.at<slot(P_KV + u + PD)>());
      if constexpr (u & 1) mfma_pin(a[u >> 1], cur, vacc); else mfma_pin(cur, a[u >> 1], kacc);
      if constexpr (!(u & 1) && (u >> 1) + W0 < 32) a[(u >> 1) + W0] = ldfrag<(u >> 1) + W0>(rs, lane * 16, pb + PN_H + w * 32768);
      if constexpr (u == 2 * (32 - W0)) sig(L, 2, w);
      SB0();
    });
    TS(5);
    f32x16 P = mfma(aphi0, pack8<0>(kacc), zero16);
    P = mfma(aphi1, pack8<8>(kacc), P);
    const v4u vb0 = pack8<0>(vacc), vb1 = pack8<8>(vacc);
    u32 phk[16];
    feat(P, phk);
    LAS unsigned char* T = L + L_T + w * TW;
#pragma unroll
    for (int c = 0; c < 4; ++c)
      *(LAS v4u*)(T + lane * 64 + ((c ^ ((lane >> 1) & 3)) * 16)) = v4u{phk[4 * c], phk[4 * c + 1], phk[4 * c + 2], phk[4 * c + 3]};
    const v4u ones = {0x3F803F80u, 0x3F803F80u, 0x3F803F80u, 0x3F803F80u};
    LAS unsigned char* tr = T + (4 * hh + trq) * 64 + (((2 * trb + (trp >> 1)) ^ ((2 * hh + (trq >> 1)) & 3)) * 16) + 8 * (trp & 1);
    const v4u t0 = ldw<512>(tr), t1 = ldw<512>(tr + 1024), t2 = ldw<512>(tr + 2048), t3 = ldw<512>(tr + 3072);
    f32x16 D0 = mfma(t0, trb ? ones : vb0, zero16);
    f32x16 D1 = mfma(t2, trb ? vb0 : ones, zero16);
    D0 = mfma(t1, trb ? ones : vb1, D0);
    D1 = mfma(t3, trb ? vb1 : ones, D1);
    {
      LAS unsigned char* pe = T + lane * 16;
      LAS unsigned char* pz = T + 1024 + (lane >> 4) * 16;
      LAS unsigned char* p0 = trb ? pz : pe;
      LAS unsigned char* p1 = trb ? pe : pz;
#pragma unroll
      for (int g = 0; g < 4; ++g) *(LAS f32x4*)(p0 + g * 1088) = f32x4{D0[4 * g], D0[4 * g + 1], D0[4 * g + 2], D0[4 * g + 3]};
#pragma unroll
      for (int g = 0; g < 4; ++g) *(LAS f32x4*)(p1 + g * 1088) = f32x4{D1[4 * g], D1[4 * g + 1], D1[4 * g + 2], D1[4 * g + 3]};
    }
    TS(6);
    bar();
    {
      f32x4 e[4], z[4];
#pragma unroll
      for (int v = 0; v < 4; ++v) {
        e[v] = *(LAS f32x4*)(L + L_T + v * TW + w * 1088 + lane * 16);
        z[v] = *(LAS f32x4*)(L + L_T + v * TW + w * 1088 + 1024 + (lane & 3) * 16);
      }
      SB0();
      const int idx = (mem * 4 + w) * 16 + panel;
      st128(__builtin_bit_cast(v4u, (e[0] + e[1]) + (e[2] + e[3])), rs, lane * 16, OFF_KVE + idx * 1024);
      st128(__builtin_bit_cast(v4u, (z[0] + z[1]) + (z[2] + z[3])), rs, lane < 4 ? lane * 16 : WS_BYTES, OFF_KVZ + idx * 64);
      publish(rs, FL_KV + mem * 2 + (w >> 1), (w & 1) * 16 + panel, lane);
      bar_raw();
      static_for<0, PD>([&](auto U) { wb[U.value] = ldw<256>(wnat.at<slot(P_Q + U.value)>()); });
    }
    TS(7);
    f32x16 qacc = zero16;
    static_for<0, 32>([&](auto U) {
      constexpr int u = U.value;
      const v4u cur = wb[u % PD];
      if constexpr (u + PD < 32) wb[u % PD] = ldw<256>(wnat.at<slot(P_Q + u + PD)>());
      mfma_pin(cur, a[u], qacc);
      SB0();
    });
    P = mfma(aphi0, pack8<0>(qacc), zero16);
    P = mfma(aphi1, pack8<8>(qacc), P);
    feat(P, phq);
#pragma unroll
    for (int i = 0; i < 16; ++i) asm volatile("" : "+v"(phq[i]));
  }
  v2u xres[4];
  {
    const u16* xr = p.x + (size_t)(panel * 128 + strip2 * 32 + l31) * D + ncol + 4 * hh;
#pragma unroll
    for (int i = 0; i < 4; ++i) xres[i] = *(const GAS v2u*)(xr + 8 * i);
  }
  TS(8);
  bar_raw();
  TS(9);
  {
    const bool on = (l31 >> 4) == hh;
    const v4u zv = {0, 0, 0, 0};
    f32x16 num = zero16, den = zero16;
    v4u ak[4], az[4];
#pragma unroll
    for (int s4 = 0; s4 < 4; ++s4) { az[s4] = *(LAS v4u*)(L + L_ZF + s4 * 32 + hh * 16); ak[s4] = *(LAS v4u*)(L + L_KVF + s4 * 512 + l31 * 16); }
    SB0();
#pragma unroll
    for (int s4 = 0; s4 < 4; ++s4) den = mfma(on ? az[s4] : zv, v4u{phq[4 * s4], phq[4 * s4 + 1], phq[4 * s4 + 2], phq[4 * s4 + 3]}, den);
#pragma unroll
    for (int s4 = 0; s4 < 4; ++s4) num = mfma(on ? ak[s4] : zv, v4u{phq[4 * s4], phq[4 * s4 + 1], phq[4 * s4 + 2], phq[4 * s4 + 3]}, num);
    const float d0 = rcp(rb(rb(den[0]) + 1e-6f)), d1 = rcp(rb(rb(den[8]) + 1e-6f));
    const v4u n0 = pack8<0>(num), n1 = pack8<8>(num);
    v4u o0, o1;
#pragma unroll
    for (int j = 0; j < 4; ++j) { o0[j] = pk(lo16(n0[j]) * d0, hi16(n0[j]) * d0); o1[j] = pk(lo16(n1[j]) * d1, hi16(n1[j]) * d1); }
    st128(o0, rs, lane * 16, pb + PN_O + w * 32768 + (2 * mem) * 1024);
    st128(o1, rs, lane * 16 + 1024, pb + PN_O + w * 32768 + (2 * mem) * 1024);
  }
#define WDN_BASES() const Base3 wown(L + (4 * hh + trq) * 64 + trb * 32 + trp * 8 + 3 * kh * 1024), woth(L + (4 * hh + trq) * 64 + trb * 32 + trp * 8 + (kh + 1) * 1024)
#define WDN(base, u) ldw<512>((((u) & 1) ? wown : woth).template at<slot((base) + 4 * ((u) >> 1))>())
  u32 x1p[8];
  {
    WDN_BASES();
    static_for<0, PD>([&](auto U) { wb[U.value] = WDN(P_WO, U.value); });
    publish(rs, fw + FL_O + w, mem, lane);
    TS(10);
    waitn(rs, fw + FL_O + strip2, 0xFFu << (kh * 8), lane);
    TS(11);
    v4u a[16];
    ldfrags<16>(a, rs, lane * 16, pb + PN_O + strip2 * 32768 + kh * 16384);
    sig(L, 4, w);
    SB0();
    f32x16 c0 = zero16, c1 = zero16;
    static_for<0, 32>([&](auto U) {
      constexpr int u = U.value;
      const v4u cur = wb[u % PD];
      if constexpr (u + PD < 32) wb[u % PD] = WDN(P_WO, u + PD);
      if constexpr (u & 1) mfma_pin(cur, a[u >> 1], c0); else mfma_pin(cur, a[u >> 1], c1);
      SB0();
    });
#pragma unroll
    for (int g = 0; g < 4; ++g) *(LAS f32x4*)(L + L_T + w * TW + g * 1024 + lane * 16) = f32x4{c1[4 * g], c1[4 * g + 1], c1[4 * g + 2], c1[4 * g + 3]};
    TS(12);
    bar();
    f32x4 o[4], ga[4], sc[4];
#pragma unroll
    for (int g = 0; g < 4; ++g) {
      o[g] = *(LAS f32x4*)(L + L_T + (w ^ 2) * TW + g * 1024 + lane * 16);
      ga[g] = *(LAS f32x4*)(L + L_VEC + (kh * 32 + 8 * g + 4 * hh) * 4);
      sc[g] = *(LAS f32x4*)(L + L_VEC + 256 + (kh * 32 + 8 * g + 4 * hh) * 4);
    }
    SB0();
    float x1[16], sp[4];
    u32 yp[8];
#pragma unroll
    for (int g = 0; g < 4; ++g) {
#pragma unroll
      for (int j = 0; j < 2; ++j) {
        const u32 y = pk(c0[4 * g + 2 * j] + o[g][2 * j], c0[4 * g + 2 * j + 1] + o[g][2 * j + 1]);
        const u32 t = pk(ga[g][2 * j] * lo16(y), ga[g][2 * j + 1] * hi16(y));
        const u32 r = pk(lo16(xres[g][j]) + lo16(t), hi16(xres[g][j]) + hi16(t));
        x1p[2 * g + j] = r;
        x1[4 * g + 2 * j] = lo16(r); x1[4 * g + 2 * j + 1] = hi16(r);
        yp[2 * g + j] = pk(lo16(r) * sc[g][2 * j], hi16(r) * sc[g][2 * j + 1]);
      }
    }
    const int tile = 2 * colg + kh;
    st128(v4u{yp[0], yp[1], yp[2], yp[3]}, rs, lane * 16, pb + PN_X1 + strip2 * 32768 + tile * 2048);
    st128(v4u{yp[4], yp[5], yp[6], yp[7]}, rs, lane * 16 + 1024, pb + PN_X1 + strip2 * 32768 + tile * 2048);
    SB0();
#pragma unroll
    for (int g = 0; g < 4; ++g) sp[g] = (x1[4 * g] + x1[4 * g + 1]) + (x1[4 * g + 2] + x1[4 * g + 3]);
    const float ss = (sp[0] + sp[1]) + (sp[2] + sp[3]);
    const float mt = fold32(ss, ss) * (1.f / 32.f);
    float q[4];
#pragma unroll
    for (int g = 0; g < 4; ++g) {
      const float d0 = x1[4 * g] - mt, d1 = x1[4 * g + 1] - mt, d2 = x1[4 * g + 2] - mt, d3 = x1[4 * g + 3] - mt;
      q[g] = (d0 * d0 + d1 * d1) + (d2 * d2 + d3 * d3);
    }
    const float qs = (q[0] + q[1]) + (q[2] + q[3]);
    const float qt = fold32(qs, qs);
    v4u sv = {__float_as_uint(mt), tag, __float_as_uint(qt), tag};
    asm volatile("" : "+v"(sv));
    publish(rs, fw + FL_X + strip2, tile, lane);
    st128(sv, rs, lane < 32 ? lane * 16 : WS_BYTES, pb + PN_ST + strip2 * 8192 + tile * 512);
  }
  asm volatile("" : "+v"(x1p[0]), "+v"(x1p[1]), "+v"(x1p[2]), "+v"(x1p[3]), "+v"(x1p[4]), "+v"(x1p[5]), "+v"(x1p[6]), "+v"(x1p[7]));
#define GU_S(u) ((u) < 96 ? (u) / 6 : 16 + (((u) - 96) % 32) / 2)
#define GU_I(u) ((u) < 96 ? (u) % 6 : 2 * (((u) - 96) / 32) + ((u) & 1))
#define WGU(u) ldw<512>((((u) & 1) ? wy : wx).template at<slot(P_GU + ((u) & ~1))>())
  {
    const Base3 wx(L + (4 * hh + trq) * 64 + trp * 8 + trb * (1024 + 32)), wy(L + (4 * hh + trq) * 64 + trp * 8 + (trb ? 1024 : 32));
    static_for<0, PD>([&](auto U) { wb[U.value] = WGU(U.value); });
    TS(13);
    waitn(rs, fw + FL_X + w, 0xFFFFu, lane);
    TS(14);
    constexpr int AW = 20;
    v4u a[AW];
    ldfrags<8>(a, rs, lane * 16, pb + PN_X1 + w * 32768);
    f32x16 acc[6];
#pragma unroll
    for (int i = 0; i < 6; ++i) acc[i] = zero16;
    float rr = 0.f;
    v4u bcor = {0, 0, 0, 0};
    v4u ho, cf0, cf1, pp;
    float t0 = 0.f, t1 = 0.f;
    const int go = pb + PN_G + (w * 96 + mem) * 1024;
    auto epi = [&](auto Ic, auto Kc) {
      constexpr int I = decltype(Ic)::value, k = decltype(Kc)::value, j = k >> 1;
      const f32x16& A = acc[I];
      if constexpr ((k & 1) == 0) { t0 = A[2 * j] * rr; t1 = A[2 * j + 1] * rr; asm volatile("" : "+v"(t0)); t0 *= A[8 + 2 * j]; }
      else {
        t1 *= A[9 + 2 * j];
        ho[j] = pk(t0, t1);
        if constexpr (j == 3) st128(ho, rs, (I < 4 || three) ? lane * 16 : WS_BYTES, go + ((I & 1) * 48 + (I >> 1) * 16) * 1024);
      }
    };
    static_for<0, 192>([&](auto Uc) {
      constexpr int u = decltype(Uc)::value, s = GU_S(u), i = GU_I(u), q = (u - 96) % 32;
      if constexpr (u == 6) TS(22);
      if constexpr (u == 60) TS(23);
      if constexpr (u == 96) TS(24);
      if constexpr (u == 128) TS(25);
      if constexpr (u == 160) TS(26);
      if constexpr (u == 56) mark(L, H_CP, w, 2u);
      if constexpr (u == 96 || u == 128 || u == 160) mark(L, H_CP, w, u / 32);
      if constexpr (u == 62 || u == 94 || u == 112) pp = peek(L, H_PP);
      if constexpr (u == 66) need_pp<false>(L, pp, 1u);
      if constexpr (u == 98) need_pp<false>(L, pp, 3u);
      if constexpr (u == 116) need_pp<true>(L, pp, 5u);
      const v4u cur = wb[u % PD];
      if constexpr (u + PD < 192) wb[u % PD] = WGU(u + PD);
      if constexpr (u >= 96 && q == 24) cf0 = *(LAS v4u*)(L + L_CF + i * 512 + l31 * 16);
      if constexpr (u >= 96 && q == 25) cf1 = *(LAS v4u*)(L + L_CF + i * 512 + l31 * 16);
      mfma_pin(cur, a[s % AW], acc[i]);
      if constexpr (u < 96 && i == 5 && s + AW < 32) a[s % AW] = ldfrag<s + AW>(rs, lane * 16, pb + PN_X1 + w * 32768);
      if constexpr (u < 2 * (AW - 8) && (u & 1)) a[8 + (u >> 1)] = ldfrag<8 + (u >> 1)>(rs, lane * 16, pb + PN_X1 + w * 32768);
      if constexpr (u == 2 * (AW - 8)) mark(L, H_CP, w, 1u);
      if constexpr (u == 117) bcor = *(LAS v4u*)(L + L_KVF + w * 512 + l31 * 16);
      if constexpr (u == 123) { rr = __uint_as_float(bcor.w); bcor = v4u{hh ? 0u : bcor.x, hh ? 0u : bcor.y, hh ? 0u : bcor.z, 0u}; }
      if constexpr (u >= 96 && q == 30) mfma_pin(cf0, bcor, acc[i]);
      if constexpr (u >= 96 && q == 31) mfma_pin(cf1, bcor, acc[i]);
      if constexpr (u >= 128 && q >= 1 && q < 17) epi(IC<(i & 6) - 2 + (q - 1) / 8>{}, IC<(q - 1) % 8>{});
      if constexpr (u == 164) publish(rs, fw + FL_G + w * 3, mem, lane);
      SB0();
    });
    mark(L, H_CP, w, 6u);
    TS(15);
    pp = peek(L, H_PP);
    static_for<0, 7>([&](auto Kc) { epi(IC<4>{}, Kc); });
    SB0();
    publish(rs, fw + FL_G + w * 3 + 1, mem, lane);
    SB0();
    static_for<7, 16>([&](auto Kc) { epi(IC<4 + Kc.value / 8>{}, IC<Kc.value % 8>{}); });
    need_pp<true>(L, pp, 6u);
    TS(16);
  }
  {
    WDN_BASES();
    static_for<0, PD>([&](auto U) { wb[U.value] = WDN(P_WD, U.value); });
    const int fi = fw + FL_G + strip2 * 3;
    u32 fl = ask(rs, fi, lane);
    publish(rs, fw + FL_G + w * 3 + 2, mem, lane);
    TS(17);
    poll(fl, rs, fi, 0xFFFFu, lane);
    TS(18);
    v4u a[43], pp;
    const int gi = pb + PN_G + (strip2 * 96 + kh * 48) * 1024;
    fl = ask(rs, fi + 1, lane);
    SB0();
    ldfrags<W0>(a, rs, lane * 16, gi);
    f32x16 c0 = zero16, c1 = zero16;
    static_for<0, 86>([&](auto U) {
      constexpr int u = U.value;
      if constexpr (u == 18) mark(L, H_CP, w, 7u);
      if constexpr (u == 26 || u == 42 || u == 58) pp = peek(L, H_PP);
      if constexpr (u == 30 || u == 46 || u == 62) need_pp<true>(L, pp, 7u + (u - 30) / 16);
      if constexpr (u == 2 * (16 - W0)) poll(fl, rs, fi + 1, 0xFFFFu, lane);
      if constexpr (u == 2 * (32 - W0) - 26) fl = ask(rs, fi + 2, lane);
      if constexpr (u == 2 * (32 - W0)) poll(fl, rs, fi + 2, 0x7FFu, lane);
      const v4u cur = wb[u % PD];
      if constexpr (u + PD < 86) wb[u % PD] = WDN(P_WD, u + PD);
      if constexpr (u & 1) mfma_pin(cur, a[u >> 1], c0); else mfma_pin(cur, a[u >> 1], c1);
      if constexpr (!(u & 1) && (u >> 1) + W0 < 43) a[(u >> 1) + W0] = ldfrag<(u >> 1) + W0>(rs, lane * 16, gi);
      SB0();
    });
#pragma unroll
    for (int g = 0; g < 4; ++g) *(LAS f32x4*)(L + L_T + w * TW + g * 1024 + lane * 16) = f32x4{c1[4 * g], c1[4 * g + 1], c1[4 * g + 2], c1[4 * g + 3]};
    f32x4 gb[4];
#pragma unroll
    for (int g = 0; g < 4; ++g) gb[g] = *(LAS f32x4*)(L + L_VEC + 512 + (kh * 32 + 8 * g + 4 * hh) * 4);
    TS(19);
    bar();
    f32x4 o[4];
#pragma unroll
    for (int g = 0; g < 4; ++g) o[g] = *(LAS f32x4*)(L + L_T + (w ^ 2) * TW + g * 1024 + lane * 16);
    SB0();
    u16* orow = p.out + (size_t)(panel * 128 + strip2 * 32 + l31) * D + ncol + 4 * hh;
#pragma unroll
    for (int g = 0; g < 4; ++g) {
      v2u r;
#pragma unroll
      for (int j = 0; j < 2; ++j) {
        r[j] = pk(lo16(x1p[2 * g + j]) + gb[g][2 * j] * (c0[4 * g + 2 * j] + o[g][2 * j]),
                  hi16(x1p[2 * g + j]) + gb[g][2 * j + 1] * (c0[4 * g + 2 * j + 1] + o[g][2 * j + 1]));
      }
      *(GAS v2u*)(orow + 8 * g) = r;
    }
  }
  if (b == 0 && tid == 0) __hip_atomic_store((GAS u32*)(ws + OFF_EPOCH), tag, RLX_AGENT);
}
}

extern "C" void kernel_launch(void* const* d_in, const int* in_sizes, int n_in, void* d_out, int out_size, void* d_ws,
                              size_t ws_size, hipStream_t stream) {
  using namespace mk;
  Params p;
  p.x = (const u16*)d_in[0]; p.wqkv = (const u16*)d_in[1]; p.wphi = (const u16*)d_in[2]; p.wo = (const u16*)d_in[3];
  p.wg = (const u16*)d_in[5]; p.wu = (const u16*)d_in[6]; p.wd = (const u16*)d_in[7]; p.c = (const u16*)d_in[8];
  p.wmod = (const u16*)d_in[9]; p.out = (u16*)d_out; p.ws = (unsigned char*)d_ws;
  (void)hipFuncSetAttribute((const void*)mega, hipFuncAttributeMaxDynamicSharedMemorySize, LDS_BYTES);
  mega<<<256, 512, LDS_BYTES, stream>>>(p);
}
```

```cpp
#define MK_LA 5
#define MK_LB 6
#define LCLK() __builtin_amdgcn_s_memrealtime()
#define LMUL 10
#include <hip/hip_runtime.h>

namespace mk {
typedef unsigned short u16;
typedef unsigned int u32;
typedef unsigned long long u64;
typedef u32 v4u __attribute__((ext_vector_type(4)));
typedef u32 v2u __attribute__((ext_vector_type(2)));
typedef float f32x2 __attribute__((ext_vector_type(2)));
typedef float f32x4 __attribute__((ext_vector_type(4)));
typedef float f32x16 __attribute__((ext_vector_type(16)));
typedef __bf16 bf16x2 __attribute__((ext_vector_type(2)));
typedef __bf16 bf16x8 __attribute__((ext_vector_type(8)));
typedef short s16x4 __attribute__((ext_vector_type(4)));
#define LAS __attribute__((address_space(3)))
#define GAS __attribute__((address_space(1)))
#define DI __device__ __forceinline__
#define RLX_AGENT __ATOMIC_RELAXED, __HIP_MEMORY_SCOPE_AGENT
#define SB0() __builtin_amdgcn_sched_barrier(0)
typedef __amdgpu_buffer_rsrc_t rsrc_t;

constexpr int D = 512, F = 1376;
constexpr int OFF_EPOCH = 0;
constexpr int OFF_MODG = 64 << 10;
constexpr int OFF_KVZ = 128 << 10;
constexpr int OFF_CG = 192 << 10;
constexpr int CG_PART = 2 * F * 16;
constexpr int OFF_ARR = 384 << 10;
constexpr int OFF_MODR = 512 << 10;
constexpr int OFF_KVE = 1 << 20;
constexpr int OFF_PAN = 2 << 20;
constexpr int PN_H = 0;
constexpr int PN_O = 128 << 10;
constexpr int PN_X1 = 256 << 10;
constexpr int PN_G = 384 << 10;
constexpr int PN_ST = 768 << 10;
constexpr int PN_FL = 800 << 10;
constexpr int FL_H = 0, FL_O = 4, FL_X = 8, FL_G = 12, FL_KV = 384;
constexpr int PSTR = (848 << 10) + 4224;
constexpr int WS_BYTES = 16 << 20;
constexpr int OFF_TS = 32 << 20;
constexpr int RING = 136;
constexpr int L_T = RING * 1024, TW = 4352;
constexpr int L_KVF = L_T + 4 * TW;
constexpr int L_ZF = L_KVF + 2048;
constexpr int L_VEC = L_ZF + 256;
constexpr int L_CF = L_VEC + 768;
constexpr int L_HINT = L_CF + 3072;
constexpr int LDS_BYTES = L_HINT + 128;
static_assert(LDS_BYTES <= 163840, "LDS");
constexpr int P_KV = 0, P_Q = 64, P_WO = 96, P_GU = 160, P_WD = 352;
constexpr int H_CP = 6, H_PP = 7;
constexpr int pair_of(int t) { return ((t & 15) < 11 ? 3 * (t & 15) : 2 * (t & 15) + 11) + (t >> 4); }
constexpr int slot(int n) { return (n % RING) * 1024; }
constexpr int AUX_SC1 = 16;
constexpr u32 SPIN_CAP = 1u << 16;
#define PRE_MOD 8
#define HI_DELAY 8
#define POLL_GAP 6
#define PRE_FL 4
#define KV1 32
#define PGU 4
#define PKV 0
#define GPUB 29
#define LPRIO 0
#define APRIO 1
#define TMOD 0
#define CW 1
#define DW 2
#define CSLP 3
#define PG2 2
#define TLBT 0
#define W0 12
constexpr int PD = 5;
constexpr int CODE_BYTES = 47000, CODE_KB = 48;

#define NBV(x) (x)
struct Params {
  const u16 *x, *wqkv, *wphi, *wo, *wg, *wu, *wd, *c, *wmod;
  u16* out;
  unsigned char* ws;
};

template <int V> struct IC { static constexpr int value = V; };
template <int I, int N, class Fn> DI void static_for(Fn&& f) {
  if constexpr (I < N) { f(IC<I>{}); static_for<I + 1, N>(f); }
}
DI float lo16(u32 v) { return __uint_as_float(v << 16); }
DI float hi16(u32 v) { return __uint_as_float(v & 0xffff0000u); }
DI u32 pk(float a, float b) { f32x2 v = {a, b}; return __builtin_bit_cast(u32, __builtin_convertvector(v, bf16x2)); }
DI float rb(float a) { return lo16(pk(a, 0.f)); }
DI float rcp(float a) { return __builtin_amdgcn_rcpf(a); }
DI f32x16 mfma(v4u a, v4u b, f32x16 c) {
  return __builtin_amdgcn_mfma_f32_32x32x16_bf16(__builtin_bit_cast(bf16x8, a), __builtin_bit_cast(bf16x8, b), c, 0, 0, 0);
}
DI void mfma_pin(v4u a, v4u b, f32x16& c) {
  c = mfma(a, b, c);
  asm volatile("" : "+v"(c));
}
template <int O> DI v4u pack8(const f32x16& x) {
  v4u r; r.x = pk(x[O], x[O + 1]); r.y = pk(x[O + 2], x[O + 3]); r.z = pk(x[O + 4], x[O + 5]); r.w = pk(x[O + 6], x[O + 7]); return r;
}
template <int T> DI v4u ldw(LAS unsigned char* p) {
  s16x4 a = __builtin_amdgcn_ds_read_tr16_b64_v4i16((LAS s16x4*)p);
  s16x4 b = __builtin_amdgcn_ds_read_tr16_b64_v4i16((LAS s16x4*)(p + T));
  return __builtin_bit_cast(v4u, __builtin_shufflevector(a, b, 0, 1, 2, 3, 4, 5, 6, 7));
}
struct Base3 {
  u32 b[3];
  DI explicit Base3(LAS unsigned char* p) {
    b[0] = (u32)(size_t)p; b[1] = b[0] + 65536u; b[2] = b[0] + 131072u;
    asm volatile("" : "+v"(b[0]), "+v"(b[1]), "+v"(b[2]));
  }
  template <int OFF> DI LAS unsigned char* at() const { return (LAS unsigned char*)(size_t)b[OFF >> 16] + (OFF & 65535); }
};
template <int LOFF, int SOFF> DI void gldsi(rsrc_t rs, u32 voff, u32 sbase, u32 lbase) {
  u32 t;
  asm volatile("s_add_u32 m0, %1, %2\n\ts_add_u32 %0, %3, %4\n\tbuffer_load_dwordx4 %5, %6, %0 offen lds"
               : "=&s"(t) : "s"(lbase), "i"(LOFF), "s"(sbase), "i"(SOFF), "v"(voff), "s"(rs) : "m0", "scc", "memory");
}
DI void bar() { asm volatile("s_waitcnt lgkmcnt(0)\n\ts_barrier" ::: "memory"); }
DI void bar_raw() { asm volatile("s_barrier" ::: "memory"); }
DI void drain() { asm volatile("s_waitcnt vmcnt(0)" ::: "memory"); }
#define VMCNT(n) asm volatile("s_waitcnt vmcnt(" #n ")" ::: "memory")
#define RELOAD() asm volatile("" ::: "memory")
DI v4u ld128(rsrc_t rs, int voff, int soff) { return __builtin_amdgcn_raw_buffer_load_b128(rs, voff, soff, AUX_SC1); }
DI v2u ld64(rsrc_t rs, int voff, int soff) { return __builtin_amdgcn_raw_buffer_load_b64(rs, voff, soff, AUX_SC1); }
DI void st128(v4u v, rsrc_t rs, int voff, int soff) { __builtin_amdgcn_raw_buffer_store_b128(v, rs, voff + soff, 0, AUX_SC1); }
DI void st64(v2u v, rsrc_t rs, int voff, int soff) { __builtin_amdgcn_raw_buffer_store_b64(v, rs, voff, soff, AUX_SC1); }
template <int S> DI v4u ldfrag(rsrc_t rs, int voff, int soff) { return ld128(rs, voff + (S & 3) * 1024, soff + (S >> 2) * 4096); }
template <int W, int N> DI void ldfrags(v4u (&a)[N], rsrc_t rs, int voff, int soff) {
  static_for<0, W>([&](auto S) { a[S.value] = ldfrag<S.value>(rs, voff, soff); SB0(); });
}
template <int CTRL> DI float dpp_add(float v) {
  return v + __uint_as_float(__builtin_amdgcn_update_dpp(0u, __float_as_uint(v), CTRL, 0xf, 0xf, false));
}
constexpr int DPP_X1 = 0xB1, DPP_X2 = 0x4E, DPP_HM = 0x141, DPP_ROR4 = 0x124, DPP_ROR8 = 0x128;
DI float fold32(float x, float y) {
  auto r = __builtin_amdgcn_permlane32_swap(__float_as_uint(x), __float_as_uint(y), false, false);
  return __uint_as_float(r[0]) + __uint_as_float(r[1]);
}
DI float fold16(float x, float y) {
  auto r = __builtin_amdgcn_permlane16_swap(__float_as_uint(x), __float_as_uint(y), false, false);
  return __uint_as_float(r[0]) + __uint_as_float(r[1]);
}
DI void red8q(const float (&a)[8], float& c0, float& c1) {
  const float b0 = fold32(a[0], a[4]), b1 = fold32(a[1], a[5]), b2 = fold32(a[2], a[6]), b3 = fold32(a[3], a[7]);
  c0 = fold16(b0, b2); c1 = fold16(b1, b3);
  c0 = dpp_add<DPP_ROR4>(c0); c1 = dpp_add<DPP_ROR4>(c1);
  c0 = dpp_add<DPP_ROR8>(c0); c1 = dpp_add<DPP_ROR8>(c1);
}
DI void red8(const float (&a)[8], float& c0, float& c1) {
  red8q(a, c0, c1);
  c0 = dpp_add<DPP_X1>(c0); c1 = dpp_add<DPP_X1>(c1);
  c0 = dpp_add<DPP_X2>(c0); c1 = dpp_add<DPP_X2>(c1);
}
DI void publish(rsrc_t rs, int word, int bit, int lane) {
  drain();
  (void)__builtin_amdgcn_raw_ptr_buffer_atomic_add_i32(1 << bit, rs, lane == 0 ? 0 : WS_BYTES, OFF_ARR + word * 128, 0);
}
DI u32 ask(rsrc_t rs, int word, int lane) { return __builtin_amdgcn_raw_ptr_buffer_atomic_add_i32(0, rs, lane == 0 ? 0 : WS_BYTES, OFF_ARR + word * 128, 0); }
DI void spin(rsrc_t rs, int word, u32 m, int lane) {
#pragma nounroll
  for (u32 spins = 0; spins < SPIN_CAP; ++spins)
    if ((__builtin_amdgcn_readfirstlane(ask(rs, word, lane)) & m) == m) break;
}
DI void poll(u32 f, rsrc_t rs, int word, u32 m, int lane) {
  if (__builtin_expect((__builtin_amdgcn_readfirstlane(f) & m) != m, 0)) spin(rs, word, m, lane);
  __builtin_amdgcn_fence(__ATOMIC_ACQUIRE, "wavefront");
  RELOAD();
}
DI void waitn(rsrc_t rs, int word, u32 m, int lane) {
  if (PRE_FL) __builtin_amdgcn_s_sleep(PRE_FL);
  spin(rs, word, m, lane);
  __builtin_amdgcn_fence(__ATOMIC_ACQUIRE, "wavefront");
  RELOAD();
}
DI void sig(LAS unsigned char* L, int i, int k) { *(volatile LAS u32*)(L + L_HINT + 16 * i + 4 * k) = 1u; }
template <int N> DI void await(LAS unsigned char* L, int i) {
#pragma nounroll
  for (u32 spins = 0; spins < (1u << 20); ++spins) {
    const v4u v = *(volatile LAS v4u*)(L + L_HINT + 16 * i);
    if (__builtin_amdgcn_readfirstlane(N == 1 ? v.x : (v.x & v.y & v.z & v.w))) break;
    __builtin_amdgcn_s_sleep(1);
  }
  RELOAD();
}
DI void mark(LAS unsigned char* L, int i, int k, u32 val) { *(volatile LAS u32*)(L + L_HINT + 16 * i + 4 * k) = val; }
DI v4u peek(LAS unsigned char* L, int i) { return *(volatile LAS v4u*)(L + L_HINT + 16 * i); }
DI u32 umin(u32 a, u32 b) { return a < b ? a : b; }
DI void await_cp(LAS unsigned char* L, u32 k) {
#pragma nounroll
  for (u32 spins = 0; spins < (1u << 20); ++spins) {
    const v4u v = peek(L, H_CP);
    if (__builtin_amdgcn_readfirstlane(umin(umin(v.x, v.y), umin(v.z, v.w))) >= k) break;
    __builtin_amdgcn_s_sleep(CSLP);
  }
  RELOAD();
}
template <bool AUX> DI u32 pp_min(v4u v) {
  u32 m = umin(v.x, v.y);
  if (AUX) m = umin(m, umin(v.z, v.w));
  return __builtin_amdgcn_readfirstlane(m);
}
template <bool AUX> DI void need_pp(LAS unsigned char* L, v4u v, u32 k) {
  if (__builtin_expect(pp_min<AUX>(v) < k, 0)) {
#pragma nounroll
    for (u32 spins = 0; spins < (1u << 20); ++spins)
      if (pp_min<AUX>(peek(L, H_PP)) >= k) break;
  }
  RELOAD();
}
DI void feat(const f32x16& P, u32 (&ph)[16]) {
#pragma clang fp contract(off)
  constexpr float L2E = 1.4426950408889634f;
  f32x2 p[8];
#pragma unroll
  for (int i = 0; i < 8; ++i) { const u32 t = pk(P[2 * i], P[2 * i + 1]); p[i] = f32x2{lo16(t), hi16(t)} * L2E; }
  float mx[4], mn[4];
#pragma unroll
  for (int i = 0; i < 4; ++i) {
    mx[i] = fmaxf(fmaxf(p[2 * i].x, p[2 * i].y), fmaxf(p[2 * i + 1].x, p[2 * i + 1].y));
    mn[i] = fminf(fminf(p[2 * i].x, p[2 * i].y), fminf(p[2 * i + 1].x, p[2 * i + 1].y));
  }
  const float mxl = fmaxf(fmaxf(mx[0], mx[1]), fmaxf(mx[2], mx[3])), mnl = fminf(fminf(mn[0], mn[1]), fminf(mn[2], mn[3]));
  f32x2 e1[8], e2[8];
#pragma unroll
  for (int i = 0; i < 8; ++i) { e1[i] = p[i] - mxl; e2[i] = mnl - p[i]; }
  SB0();
#pragma unroll
  for (int i = 0; i < 8; ++i) {
    e1[i] = f32x2{__builtin_amdgcn_exp2f(e1[i].x), __builtin_amdgcn_exp2f(e1[i].y)};
    e2[i] = f32x2{__builtin_amdgcn_exp2f(e2[i].x), __builtin_amdgcn_exp2f(e2[i].y)};
  }
  const f32x2 s1 = ((e1[0] + e1[1]) + (e1[2] + e1[3])) + ((e1[4] + e1[5]) + (e1[6] + e1[7]));
  const f32x2 s2 = ((e2[0] + e2[1]) + (e2[2] + e2[3])) + ((e2[4] + e2[5]) + (e2[6] + e2[7]));
  const float r1 = rcp(s1.x + s1.y), r2 = rcp(s2.x + s2.y);
#pragma unroll
  for (int i = 0; i < 8; ++i) { const f32x2 u = e1[i] * r1, v = e2[i] * r2; ph[i] = pk(u.x, u.y); ph[8 + i] = pk(v.x, v.y); }
}

#define TSCLK() __builtin_amdgcn_s_memrealtime()
#define TS(i) do { if ((i) == 0) { lk0 = __builtin_amdgcn_s_memrealtime(); __builtin_amdgcn_s_waitcnt(0xC07F); } if ((i) == MK_LA) { SB0(); lka = LCLK(); __builtin_amdgcn_s_waitcnt(0xC07F); SB0(); } if ((i) == MK_LB) { SB0(); lkb = LCLK(); __builtin_amdgcn_s_waitcnt(0xC07F); SB0(); } } while (0)
#define TS_DUMP() do {} while (0)
#define TS_INIT() u64 lk0 = 0, lka = 0, lkb = 0

__global__ void __launch_bounds__(512, 2) mega(Params p) {
  extern __shared__ __attribute__((aligned(16))) unsigned char smem[];
  LAS unsigned char* L = (LAS unsigned char*)smem;
  TS_INIT();
  TS(0);
  u64 pc0;
  asm volatile("s_getpc_b64 %0" : "=s"(pc0));
  const int tid = threadIdx.x, lane = tid & 63;
  int w = __builtin_amdgcn_readfirstlane(tid >> 6);
  const int b = blockIdx.x, xcd = b & 7, rank = b >> 3, panel = (xcd >> 1) * 4 + (rank >> 3), mem = (xcd & 1) * 8 + (rank & 7);
  const int hh = lane >> 5, l31 = lane & 31;
  unsigned char* ws = p.ws;
  const int rowh = mem & 1, colg = mem >> 1;
  const int pair0 = min(3 * mem, 2 * mem + 11);
  const bool three = mem < 11;
  const rsrc_t rs = __builtin_amdgcn_make_buffer_rsrc(ws, 0, WS_BYTES, 0x00020000);
  const int pb = OFF_PAN + panel * PSTR, fw = panel * 24;
  const int mcg = xcd * 8 + (rank >> 2), mkp = rank & 3;
  const rsrc_t rqkv = __builtin_amdgcn_make_buffer_rsrc((void*)p.wqkv, 0, D * 3 * D * 2, 0x00020000);
  const rsrc_t rwo = __builtin_amdgcn_make_buffer_rsrc((void*)p.wo, 0, D * D * 2, 0x00020000);
  const rsrc_t rgu = __builtin_amdgcn_make_buffer_rsrc((void*)((w & 1) ? p.wu : p.wg), 0, D * F * 2, 0x00020000);
  const rsrc_t rwd = __builtin_amdgcn_make_buffer_rsrc((void*)p.wd, 0, F * D * 2, 0x00020000);

  v4u r0[4];
  u32 cv0[4], epoch;
  u32 vq = (lane >> 2) * (3 * D * 2) + (lane & 3) * 16;
  asm volatile("" : "+v"(vq));
  {
    const u32 ld = (u32)(size_t)L + w * 1024;
    if (w <= 4) {
      const rsrc_t r1 = __builtin_amdgcn_make_buffer_rsrc((void*)(w == 4 ? p.wmod : p.x), 0, w == 4 ? D * 6 * D * 2 : 2048 * D * 2, 0x00020000);
      const int vo = w == 4 ? (128 * mkp + 4 * (lane >> 1)) * (6 * D * 2) + mcg * 32 + (lane & 1) * 16
                            : (panel * 128 + mem * 8 + (lane >> 3)) * (D * 2) + (lane & 7) * 16 + 256 * w;
#pragma unroll
      for (int i = 0; i < 4; ++i) r0[i] = __builtin_amdgcn_raw_buffer_load_b128(r1, vo, w == 4 ? i * (6 * D * 2) : i < 2 ? i * 128 : 1 << 30, 0);
      SB0();
    }
    {
      const rsrc_t r2 = __builtin_amdgcn_make_buffer_rsrc((void*)p.c, 0, w == 4 ? D * 2 : 0, 0x00020000);
      const v2u c2 = __builtin_amdgcn_raw_buffer_load_b64(r2, (128 * mkp + 4 * (lane >> 1)) * 2, 0, 0);
      cv0[0] = c2.x; cv0[1] = c2.y;
      epoch = __builtin_amdgcn_raw_buffer_load_b32(rs, 0, OFF_EPOCH, AUX_SC1);
      __builtin_amdgcn_raw_buffer_store_b32(0u, rs, (w == 4 && lane < 2) ? lane * 128 : WS_BYTES, OFF_ARR + b * 256, AUX_SC1);
      SB0();
    }
    if (w >= 5) {
      const rsrc_t rc = __builtin_amdgcn_make_buffer_rsrc((void*)pc0, 0, CODE_BYTES, 0x00020000);
      gldsi<slot(128), 0>(rc, lane < CODE_KB / 6 ? (u32)lane * 128u : 1u << 30, (u32)(((rank & 15) * 3 + w - 5) * (CODE_KB / 6) * 128), ld);
      const u32 tq = (rank * 16 + (lane >> 2)) * (3 * D * 2) + (xcd & 1) * 512 + (lane & 3) * 128;
      if (w != 5) gldsi<slot(128), 0>(rqkv, tq, (w - 5) * (D * 2), ld);
      else {
        gldsi<slot(128), 0>(rs, lane < 2 ? (u32)(pb + lane * (848 << 10)) : 1u << 30, 0u, ld);
        const rsrc_t rp = __builtin_amdgcn_make_buffer_rsrc((void*)p.wphi, 0, 32 * 256 * 2, 0x00020000);
        gldsi<L_CF + 2048, 0>(rp, lane * 16, mem * 1024, (u32)(size_t)L);
      }
    }
    if (tid < 32) *((LAS u32*)(L + L_HINT) + tid) = 0u;
    TS(27);
    bar();
    asm volatile("" : "+s"(w));
    if (w >= 6) {
      u32 lo = (u32)(size_t)L + (w & 1) * 1024, so = (D + (w & 1) * D + mem * 32) * 2;
#pragma nounroll
      for (int i = 0; i < KV1 / 2; ++i) {
        gldsi<0, 0>(rqkv, vq, so, lo);
        gldsi<2048, 16 * (3 * D * 2)>(rqkv, vq, so, lo);
        lo += 4096; so += 32 * (3 * D * 2);
      }
    }
    if (w == 4) {
      __builtin_amdgcn_s_setprio(3);
      TS(10);
      float sl[4], acc[8];
#pragma unroll
      for (int i = 0; i < 4; ++i) { const float c = (i & 1) ? hi16(cv0[i >> 1]) : lo16(cv0[i >> 1]); sl[i] = rb(c * rcp(1.f + __expf(-c))); }
      u32 tag = epoch;
      asm volatile("" : "+v"(tag));
      tag += 1u;
      TS(11);
#pragma unroll
      for (int j = 0; j < 8; ++j) acc[j] = 0.f;
#pragma unroll
      for (int i = 0; i < 4; ++i)
#pragma unroll
        for (int j = 0; j < 4; ++j) { acc[2 * j] += sl[i] * lo16(r0[i][j]); acc[2 * j + 1] += sl[i] * hi16(r0[i][j]); }
      float c0, c1;
      TS(12);
      red8q(acc, c0, c1);
      c0 = dpp_add<DPP_X2>(c0); c1 = dpp_add<DPP_X2>(c1);
      const int go = ((lane >> 1) & 7) * 32768 + (8 * (lane & 1) + 2 * (lane >> 4)) * 8;
      const v4u gv = {__float_as_uint(c0), tag, __float_as_uint(c1), tag};
      drain();
      st128(gv, rs, go, OFF_MODR + mkp * 8192 + mcg * 128);
      st128(gv, rs, go, OFF_MODR + 8 * 32768 + mkp * 8192 + mcg * 128);
      sig(L, 0, 0);
    }
    asm volatile("" : "+s"(w));
  }

  if (w == 4 || w == 5) {
    const int a = w & 1;
    u32 tag;
    tag = epoch;
    asm volatile("" : "+v"(tag));
    tag += 1u;
    drain();
    TS(1);
    bar();
    __builtin_amdgcn_s_setprio(APRIO);
    await<4>(L, 2);
    const int kp = xcd >> 1, cpair = (xcd & 1) * 24 + rank;
    const bool con = rank < ((xcd & 1) ? 19 : 24);
    const int col0 = a * F + cpair * 32;
    v4u wm[8];
#pragma unroll
    for (int i = 0; i < 8; ++i)
      wm[i] = __builtin_amdgcn_raw_buffer_load_b128(rgu, con ? (128 * kp + 2 * (lane >> 2)) * (F * 2) + cpair * 64 + (lane & 3) * 16 : 1 << 30, (32 * (i >> 1) + (i & 1)) * (F * 2), 0);
    SB0();
    if (a == 1) {
      v4u wn[8]; u32 cv[8];
      const int row0 = 128 * mkp + (lane >> 2);
#pragma unroll
      for (int i = 0; i < 8; ++i) cv[i] = p.c[row0 + 16 * i];
#pragma unroll
      for (int i = 0; i < 8; ++i) wn[i] = *(const GAS v4u*)(p.wmod + (size_t)(row0 + 16 * i) * (6 * D) + 1024 + mcg * 32 + (lane & 3) * 8);
      SB0();
      bar_raw();
      SB0();
      float acc[8];
#pragma unroll
      for (int j = 0; j < 8; ++j) acc[j] = 0.f;
#pragma unroll
      for (int i = 0; i < 8; ++i) {
        const float c = lo16(cv[i]), sl = rb(c * rcp(1.f + __expf(-c)));
#pragma unroll
        for (int j = 0; j < 4; ++j) { acc[2 * j] += sl * lo16(wn[i][j]); acc[2 * j + 1] += sl * hi16(wn[i][j]); }
      }
      float c0, c1;
      red8q(acc, c0, c1);
      st128(v4u{__float_as_uint(c0), tag, __float_as_uint(c1), tag}, rs, (lane & 12) == 0 ? (8 * (lane & 3) + 2 * (lane >> 4)) * 8 : WS_BYTES,
            OFF_MODG + mkp * 16384 + mcg * 256);
    } else {
      bar_raw();
    }
    bar_raw();
    TS(2);
    {
      const int idx0 = (mem * 4 + 2 * a) * 16;
      v4u e[32], zz[2];
      {
        u32 todo = 0xFFu;
        for (u32 spins = 0; spins < SPIN_CAP && todo; ++spins) {
          const u32 bad = ~__builtin_amdgcn_readfirstlane(ask(rs, FL_KV + mem * 2 + a, lane));
          __builtin_amdgcn_fence(__ATOMIC_ACQUIRE, "wavefront");
          RELOAD();
          static_for<0, 8>([&](auto G) {
            constexpr int g = G.value;
            if (((todo >> g) & 1u) && !(bad & (0x00030003u << (2 * g)))) {
              todo &= ~(1u << g);
              e[2 * g] = ld128(rs, lane * 16, OFF_KVE + (idx0 + 2 * g) * 1024);
              e[2 * g + 1] = ld128(rs, lane * 16, OFF_KVE + (idx0 + 2 * g + 1) * 1024);
              e[16 + 2 * g] = ld128(rs, lane * 16, OFF_KVE + (idx0 + 16 + 2 * g) * 1024);
              e[17 + 2 * g] = ld128(rs, lane * 16, OFF_KVE + (idx0 + 17 + 2 * g) * 1024);
            }
          });
        }
      }
      TS(3);
      zz[0] = ldfrag<0>(rs, lane * 16, OFF_KVZ + idx0 * 64);
      zz[1] = ldfrag<1>(rs, lane * 16, OFF_KVZ + idx0 * 64);
      SB0();
#pragma unroll
      for (int q = 0; q < 2; ++q) {
        f32x4 s0 = __builtin_bit_cast(f32x4, e[16 * q]), s1 = __builtin_bit_cast(f32x4, e[16 * q + 1]);
#pragma unroll
        for (int i = 2; i < 16; i += 2) { s0 += __builtin_bit_cast(f32x4, e[16 * q + i]); s1 += __builtin_bit_cast(f32x4, e[16 * q + i + 1]); }
        s0 += s1;
        *(LAS v2u*)(L + L_KVF + (2 * a + q) * 512 + l31 * 16 + hh * 8) = v2u{pk(s0[0], s0[1]), pk(s0[2], s0[3])};
      }
      f32x4 z;
#pragma unroll
      for (int j = 0; j < 4; ++j) {
        float t = fold32(__uint_as_float(zz[0][j]), __uint_as_float(zz[1][j]));
        t = fold16(t, t);
        t = dpp_add<DPP_ROR4>(t);
        z[j] = dpp_add<DPP_ROR8>(t);
      }
      if (l31 < 4) *(LAS v2u*)(L + L_ZF + (2 * a + hh) * 32 + ((lane & 1) ^ 1) * 16 + ((lane >> 1) & 1) * 8) = v2u{pk(z[0], z[1]), pk(z[2], z[3])};
    }
    TS(4);
    bar();
    u32 gsb[4], gsc[4];
    {
      const int vo = ((hh ? 2 : 3 * a) * D + colg * 64 + 2 * l31) * 8, ro = (D + 128 * kp + 2 * lane) * 8;
      v4u g[12];
      for (u32 spins = 0; spins < SPIN_CAP; ++spins) {
        RELOAD();
#pragma unroll
        for (int i = 0; i < 12; ++i) g[i] = ld128(rs, i < 4 ? vo : ro + (i >> 3) * (D * 8), OFF_MODG + (i & 3) * 16384);
        bool ok = true;
#pragma unroll
        for (int i = 0; i < 12; ++i) ok &= (g[i].y == tag) & (g[i].w == tag);
        if (__all(ok)) break;
      }
      u32 pr[3];
#pragma unroll
      for (int t = 0; t < 3; ++t)
        pr[t] = pk((__uint_as_float(g[4 * t].x) + __uint_as_float(g[4 * t + 1].x)) + (__uint_as_float(g[4 * t + 2].x) + __uint_as_float(g[4 * t + 3].x)),
                   (__uint_as_float(g[4 * t].z) + __uint_as_float(g[4 * t + 1].z)) + (__uint_as_float(g[4 * t + 2].z) + __uint_as_float(g[4 * t + 3].z)));
      const u32 v1 = pk(1.f + lo16(pr[0]), 1.f + hi16(pr[0])), vv = hh ? v1 : pr[0];
      *(LAS f32x2*)(L + L_VEC + (hh ? 1 : 2 * a) * 256 + l31 * 8) = f32x2{lo16(vv), hi16(vv)};
      const u32 sc = pk(1.f + lo16(pr[2]), 1.f + hi16(pr[2]));
#pragma unroll
      for (int i = 0; i < 4; ++i) {
        gsb[i] = __builtin_amdgcn_ds_bpermute((16 * i + (lane >> 2)) * 4, pr[1]);
        gsc[i] = __builtin_amdgcn_ds_bpermute((16 * i + (lane >> 2)) * 4, sc);
      }
    }
    if (con) {
      float a1[8], a2[8];
#pragma unroll
      for (int j = 0; j < 8; ++j) { a1[j] = 0.f; a2[j] = 0.f; }
#pragma unroll
      for (int i = 0; i < 8; ++i) {
        const float sb = (i & 1) ? hi16(gsb[i >> 1]) : lo16(gsb[i >> 1]), sc = (i & 1) ? hi16(gsc[i >> 1]) : lo16(gsc[i >> 1]);
#pragma unroll
        for (int j = 0; j < 4; ++j) {
          a1[2 * j] += sc * lo16(wm[i][j]); a1[2 * j + 1] += sc * hi16(wm[i][j]);
          a2[2 * j] += sb * lo16(wm[i][j]); a2[2 * j + 1] += sb * hi16(wm[i][j]);
        }
      }
      float c10, c11, c20, c21;
      red8q(a1, c10, c11); red8q(a2, c20, c21);
      const int k = (lane >> 2) & 3;
      const float val = k == 0 ? c10 : k == 1 ? c20 : k == 2 ? c11 : c21;
      st64(v2u{__float_as_uint(val), tag}, rs, (col0 + 8 * (lane & 3) + 2 * (lane >> 4)) * 16 + k * 8, OFF_CG + kp * CG_PART);
    }
    TS(5);
    bar();
    {
      const int ntile = three ? 6 : 4;
      v4u cg[8];
      int off[2];
#pragma unroll
      for (int i = 0; i < 2; ++i) {
        int tile = 4 * i + 2 * a + hh; tile = tile < ntile ? tile : 0;
        off[i] = (32 * pair0 + 16 * tile + (lane & 15) + ((lane >> 4) & 1) * F) * 16;
      }
      for (u32 spins = 0; spins < SPIN_CAP; ++spins) {
        RELOAD();
        bool ok = true;
#pragma unroll
        for (int i = 0; i < 8; ++i) cg[i] = ld128(rs, off[i >> 2], OFF_CG + (i & 3) * CG_PART);
#pragma unroll
        for (int i = 0; i < 8; ++i) ok &= (cg[i].y == tag) & (cg[i].w == tag);
        if (__all(ok)) break;
      }
#pragma unroll
      for (int i = 0; i < 2; ++i) {
        const float c1 = (__uint_as_float(cg[4 * i].x) + __uint_as_float(cg[4 * i + 1].x)) + (__uint_as_float(cg[4 * i + 2].x) + __uint_as_float(cg[4 * i + 3].x));
        const float c2 = (__uint_as_float(cg[4 * i].z) + __uint_as_float(cg[4 * i + 1].z)) + (__uint_as_float(cg[4 * i + 2].z) + __uint_as_float(cg[4 * i + 3].z));
        const float h1 = rb(c1), m1 = rb(c1 - h1), l1 = c1 - h1 - m1, h2 = rb(c2), m2 = rb(c2 - h2), l2 = c2 - h2 - m2;
        const int tile = 4 * i + 2 * a + hh;
        if (tile < 6) *(LAS v4u*)(L + L_CF + tile * 512 + l31 * 16) = v4u{pk(h1, m1), pk(l1, h2), pk(m2, l2), 0u};
      }
    }
    await_cp(L, 1);
    {
      v4u st[16];
#pragma unroll
      for (int t = 0; t < 16; ++t) st[t] = ld128(rs, l31 * 16 + hh * 8192 + t * 512, pb + PN_ST + 2 * a * 8192);
      for (u32 spins = 0; spins < SPIN_CAP; ++spins) {
        bool ok = true;
#pragma unroll
        for (int t = 0; t < 16; ++t) ok &= (st[t].y == tag) & (st[t].w == tag);
        if (__all(ok)) break;
        RELOAD();
#pragma unroll
        for (int t = 0; t < 16; ++t) st[t] = ld128(rs, l31 * 16 + hh * 8192 + t * 512, pb + PN_ST + 2 * a * 8192);
      }
      float ms = 0.f;
#pragma unroll
      for (int t = 0; t < 16; ++t) ms += __uint_as_float(st[t].x);
      const float mean = ms * (1.f / 16.f);
      float q = 0.f;
#pragma unroll
      for (int t = 0; t < 16; ++t) { const float d = __uint_as_float(st[t].x) - mean; q += __uint_as_float(st[t].z) + 32.f * d * d; }
      const float den = rb(__builtin_amdgcn_sqrtf(rb(rb(q * (1.f / D)) + 1e-5f)));
      *(LAS v4u*)(L + L_KVF + a * 1024 + lane * 16) = v4u{pk(-mean, -mean), pk(-mean, den), pk(den, den), __float_as_uint(rcp(den * den))};
    }
    RELOAD();
    mark(L, H_PP, 2 + a, 5u);
    TS(6);
    {
      v4u wt[58];
      const u32 vd = (lane >> 2) * (D * 2) + (lane & 3) * 16, sd = a * 16 * (D * 2) + colg * 128;
      const Base3 wdst(L + a * 2048 + lane * 16);
      static_for<28, 86>([&](auto J) {
        wt[J.value - 28] = __builtin_amdgcn_raw_buffer_load_b128(rwd, NBV(vd + (J.value & 1) * 64), sd + pair_of(J.value >> 1) * 32 * (D * 2), 0); SB0(); });
      TS(7);
#define ST_WT(T0, T1, k) do { static_for<2 * (T0), 2 * (T1)>([&](auto J) { \
      *(LAS v4u*)wdst.template at<slot(P_WD + 4 * (J.value >> 1)) + (J.value & 1) * 1024>() = wt[J.value - 28]; }); \
      RELOAD(); mark(L, H_PP, 2 + a, k); RELOAD(); } while (0)
      await_cp(L, 4);
      ST_WT(14, 18, 6u);
      await_cp(L, 5);
      ST_WT(18, 26, 7u);
      await_cp(L, 6);
      ST_WT(26, 34, 8u);
      TS(8);
      await_cp(L, 7);
      ST_WT(34, 43, 9u);
      TS(9);
    }
    TS_DUMP();
    return;
  }

  if (w >= 6) {
    const int v = w & 1;
    __builtin_amdgcn_s_setprio(LPRIO);
    const u32 ldsb = (u32)(size_t)L;
    const u32 l1 = ldsb + v * 1024, l2 = ldsb + v * 2048;
    const u32 skv = (D + v * D + mem * 32) * 2, sq = v * 16 * (3 * D * 2) + mem * 64;
    const u32 so = 16 * v * 16 * (D * 2) + colg * 128, sd = v * 16 * (D * 2) + colg * 128;
    const u32 sg = pair0 * 64, sg2 = three ? sg + 128 : 1u << 30;
#define LD_KV(S0, S1, PC) static_for<S0, S1>([&](auto S) { gldsi<slot(P_KV + 2 * S.value), S.value * 16 * (3 * D * 2)>(rqkv, vq, skv, l1); PACE(PC); })
#define PACE(n) do { if ((n) > 0) __builtin_amdgcn_s_sleep(n); } while (0)
#define LD_Q(S0, S1, PC) static_for<S0, S1>([&](auto S) { gldsi<slot(P_Q + 2 * S.value), 2 * S.value * 16 * (3 * D * 2)>(rqkv, vq, sq, l1); PACE(PC); })
#define LD_DN(RS, SB, P0, T0, T1, PC)                                                                    \
  static_for<T0, T1>([&](auto T) {                                                                   \
    gldsi<slot((P0) + 4 * T.value), T.value * 16 * (D * 2)>(RS, vd, SB, l2);                         \
    gldsi<slot((P0) + 4 * T.value) + 1024, T.value * 16 * (D * 2) + 64>(RS, vd, SB, l2); PACE(PC);   \
  })
#define LD_GU(S0, S1, PC)                                                                                \
  static_for<S0, S1>([&](auto S) {                                                                   \
    gldsi<slot(P_GU + 6 * S.value), S.value * 16 * (F * 2)>(rgu, vg, sg, l1);                        \
    gldsi<slot(P_GU + 6 * S.value + 2), S.value * 16 * (F * 2) + 64>(rgu, vg, sg, l1);               \
    gldsi<slot(P_GU + 6 * S.value + 4), S.value * 16 * (F * 2)>(rgu, vg, sg2, l1); PACE(PC);         \
  })
    TS(1);
    VMCNT(0);
    SB0();
    int ln;
    asm volatile("v_mbcnt_lo_u32_b32 %0, -1, 0\n\tv_mbcnt_hi_u32_b32 %0, -1, %0" : "=v"(ln));
    const u32 vd = (ln >> 2) * (D * 2) + (ln & 3) * 16;
    const u32 vg = (ln >> 2) * (F * 2) + (((ln & 3) ^ (v << 1)) * 16);
    TS(2);
    await<4>(L, 3);
    LD_KV(KV1, 32, PKV);
    VMCNT(0);
    TS(11);
    bar_raw();
    v4u wt[48];
    const Base3 wdst1(L + v * 1024 + ln * 16);
#define GUN(s_, P_) (P_GU + ((s_) < 16 ? 6 * (s_) + 2 * (P_) : 96 + 32 * (P_) + 2 * ((s_) - 16)))
#define GUR(s_, P_) __builtin_amdgcn_raw_buffer_load_b128(rgu, NBV(vg), ((P_) == 2 ? sg2 : sg + (P_) * 64) + (s_) * 16 * (F * 2), 0)
#define GUW(s_, P_, r_) *(LAS v4u*)wdst1.template at<slot(GUN(s_, P_))>() = wt[r_]
#define LD_GU2(S0, S1, PC) static_for<S0, S1>([&](auto S) { constexpr int s_ = S.value; \
      gldsi<slot(GUN(s_, 0)), s_ * 16 * (F * 2)>(rgu, vg, sg, l1); SB0(); \
      wt[2 * (s_ - 16)] = GUR(s_, 1); wt[2 * (s_ - 16) + 1] = GUR(s_, 2); PACE(PC); SB0(); })
#define LD_WD(T0, T1) static_for<T0, T1>([&](auto T) { \
      gldsi<slot(P_WD + 4 * T.value), pair_of(T.value) * 32 * (D * 2)>(rwd, vd, sd, l2); \
      gldsi<slot(P_WD + 4 * T.value) + 1024, pair_of(T.value) * 32 * (D * 2) + 64>(rwd, vd, sd, l2); })
#define DONE(k) do { RELOAD(); mark(L, H_PP, v, k); RELOAD(); } while (0)
    await<4>(L, 2);
    gldsi<L_CF, 0>(v ? rwo : rqkv, (rank * 16 + (ln >> 2)) * (v ? D * 2 : 3 * D * 2) + (xcd & 1) * 512 + (ln & 3) * 128, 0u, l1);
    LD_Q(0, 16, 0);
    TS(3);
    bar_raw();
    VMCNT(0);
    bar_raw();
    gldsi<L_CF, 0>(rwd, v ? 1u << 30 : ((xcd >> 1) * 32 + rank + 128 * (ln >> 2)) * (D * 2) + (xcd & 1) * 512 + (ln & 3) * 128, 0u, l1);
    LD_DN(rwo, so, P_WO, 0, 16, 1);
    LD_GU(0, 6, PGU);
    VMCNT(18);
    TS(4);
    bar_raw();
    LD_GU(6, 12, PGU);
    await<4>(L, 4);
    static_for<0, 12>([&](auto J) { wt[32 + J.value] = GUR(12 + J.value / 3, J.value % 3); SB0(); });
    static_for<16, 20>([&](auto S) { constexpr int s_ = S.value;
      wt[28 + s_] = GUR(s_, 0); wt[2 * (s_ - 16)] = GUR(s_, 1); wt[2 * (s_ - 16) + 1] = GUR(s_, 2); SB0(); });
    VMCNT(24);
    TS(5);
    bar_raw();
    static_for<0, 12>([&](auto J) { GUW(12 + J.value / 3, J.value % 3, 32 + J.value); });
    static_for<16, 20>([&](auto S) { GUW(S.value, 0, 28 + S.value); });
    DONE(1);
    LD_GU2(20, 32, PG2);
    VMCNT(0);
    DONE(3);
    TS(6);
    await_cp(L, 1);
    {
      const u32 tdn = ((2 * rank + v) * 16 + (ln >> 2)) * (D * 2) + (xcd & 1) * 512 + (ln & 3) * 128;
      __builtin_amdgcn_s_waitcnt(0x0F70);
      __builtin_amdgcn_s_sleep(4);
      gldsi<L_T, 0>(rwd, tdn, 0u, l1);
      gldsi<L_T, 1024 * (D * 2)>(rwd, tdn, 0u, l1);
    }
    static_for<16, 32>([&](auto S) { GUW(S.value, 1, 2 * (S.value - 16)); });
    DONE(4);
    await_cp(L, 2);
    static_for<16, 32>([&](auto S) { GUW(S.value, 2, 2 * (S.value - 16) + 1); });
    DONE(5);
    TS(7);
    await_cp(L, 3);
    LD_WD(0, 10);
    await_cp(L, 4);
    LD_WD(10, 14);
    VMCNT(0);
    DONE(100);
    TS(8);
    TS_DUMP();
    return;
  }

  __builtin_amdgcn_s_setprio(2);
  const int s2 = w & 1, kh = w >> 1;
  const int strip2 = rowh * 2 + s2, ncol = colg * 64 + kh * 32;
  v4u aphi0, aphi1;
  u32 tag;
  {
    float xf[16];
#pragma unroll
    for (int i = 0; i < 8; ++i) { xf[2 * i] = lo16(r0[i >> 2][i & 3]); xf[2 * i + 1] = hi16(r0[i >> 2][i & 3]); }
    float sm = (((xf[0] + xf[1]) + (xf[2] + xf[3])) + ((xf[4] + xf[5]) + (xf[6] + xf[7]))) + (((xf[8] + xf[9]) + (xf[10] + xf[11])) + ((xf[12] + xf[13]) + (xf[14] + xf[15])));
    sm = dpp_add<DPP_X1>(sm); sm = dpp_add<DPP_X2>(sm); sm = dpp_add<DPP_HM>(sm);
    const float mw = sm * (1.f / 128.f);
    float q[4];
#pragma unroll
    for (int i = 0; i < 4; ++i) {
      const float d0 = xf[4 * i] - mw, d1 = xf[4 * i + 1] - mw, d2 = xf[4 * i + 2] - mw, d3 = xf[4 * i + 3] - mw;
      q[i] = (d0 * d0 + d1 * d1) + (d2 * d2 + d3 * d3);
    }
    float qq = (q[0] + q[1]) + (q[2] + q[3]);
    qq = dpp_add<DPP_X1>(qq); qq = dpp_add<DPP_X2>(qq); qq = dpp_add<DPP_HM>(qq);
    *(volatile LAS f32x2*)(L + L_KVF + (lane >> 3) * 32 + w * 8) = f32x2{mw, qq};
    sig(L, 1, w);
    await<4>(L, 1);
    const f32x4 pa = *(LAS f32x4*)(L + L_KVF + (lane >> 3) * 32), pc = *(LAS f32x4*)(L + L_KVF + (lane >> 3) * 32 + 16);
    const float mean = ((pa[0] + pa[2]) + (pc[0] + pc[2])) * 0.25f;
    const float f0 = pa[0] - mean, f1 = pa[2] - mean, f2 = pc[0] - mean, f3 = pc[2] - mean;
    const float m2 = ((pa[1] + pa[3]) + (pc[1] + pc[3])) + 128.f * ((f0 * f0 + f1 * f1) + (f2 * f2 + f3 * f3));
    const float m1 = rb(mean), r1 = rcp(rb(__builtin_amdgcn_sqrtf(rb(rb(m2 * (1.f / D)) + 1e-5f))));
    float xn[16];
#pragma unroll
    for (int i = 0; i < 8; ++i) {
      const u32 d = pk(xf[2 * i] - m1, xf[2 * i + 1] - m1);
      const u32 t = pk(lo16(d) * r1, hi16(d) * r1);
      xn[2 * i] = lo16(t); xn[2 * i + 1] = hi16(t);
    }
#pragma unroll
    for (int i = 0; i < 16; ++i) asm volatile("" : "+v"(xn[i]));
    tag = __builtin_amdgcn_readfirstlane(epoch) + 1u;
    TS(1);
    await<1>(L, 0);
    __builtin_amdgcn_s_sleep(PRE_MOD);
    TS(21);
    v4u g[8];
    {
      u32 todo = 0xFFu;
      for (u32 spins = 0; spins < SPIN_CAP && todo; ++spins) {
        RELOAD();
        static_for<0, 8>([&](auto I) {
          constexpr int i = I.value;
          if ((todo >> i) & 1u) g[i] = ld128(rs, lane * 16, OFF_MODR + panel * 32768 + (i & 3) * 8192 + (i >> 2) * 4096 + w * 1024);
        });
        todo = 0;
        static_for<0, 8>([&](auto I) { if (__builtin_amdgcn_ballot_w64((g[I.value].y != tag) | (g[I.value].w != tag))) todo |= 1u << I.value; });
      }
    }
    {
      u32 pr[2];
#pragma unroll
      for (int t = 0; t < 2; ++t)
        pr[t] = pk((__uint_as_float(g[4 * t].x) + __uint_as_float(g[4 * t + 1].x)) + (__uint_as_float(g[4 * t + 2].x) + __uint_as_float(g[4 * t + 3].x)),
                   (__uint_as_float(g[4 * t].z) + __uint_as_float(g[4 * t + 1].z)) + (__uint_as_float(g[4 * t + 2].z) + __uint_as_float(g[4 * t + 3].z)));
      *(LAS u32*)(L + L_T + w * TW + lane * 4) = pr[0];
      *(LAS u32*)(L + L_T + w * TW + 256 + lane * 4) = pk(1.f + lo16(pr[1]), 1.f + hi16(pr[1]));
    }
    v4u sav[2], cav[2];
#pragma unroll
    for (int i = 0; i < 2; ++i) {
      sav[i] = *(LAS v4u*)(L + L_T + w * TW + i * 128 + (lane & 7) * 16);
      cav[i] = *(LAS v4u*)(L + L_T + w * TW + 256 + i * 128 + (lane & 7) * 16);
    }
    sig(L, 3, w);
    TS(2);
    const int strip = mem >> 2, r = 8 * (mem & 3) + (lane >> 3);
#pragma unroll
    for (int i = 0; i < 2; ++i) {
      v4u o;
#pragma unroll
      for (int j = 0; j < 4; ++j) {
        const u32 sa = sav[i][j], sc = cav[i][j];
        const u32 t = pk(xn[8 * i + 2 * j] * lo16(sc), xn[8 * i + 2 * j + 1] * hi16(sc));
        o[j] = pk(lo16(t) + lo16(sa), hi16(t) + hi16(sa));
      }
      st128(o, rs, (4 * i + ((lane & 7) >> 1)) * 1024 + (r + 32 * (lane & 1)) * 16, pb + PN_H + strip * 32768 + w * 8192);
    }
    await<4>(L, 3);
    publish(rs, fw + FL_H + strip, (mem & 3) * 4 + w, lane);
  }
  TS(3);

  const int trq = (lane & 15) >> 2, trp = lane & 3, trb = (lane >> 4) & 1;
  const f32x16 zero16 = {0.f, 0.f, 0.f, 0.f, 0.f, 0.f, 0.f, 0.f, 0.f, 0.f, 0.f, 0.f, 0.f, 0.f, 0.f, 0.f};
  u32 phq[16];
  v4u wb[PD];
  {
    const Base3 wnat(L + (8 * hh + trq) * 64 + trb * 32 + trp * 8);
    waitn(rs, fw + FL_H + w, 0xFFFFu, lane);
    TS(4);
    v4u a[32];
    ldfrags<W0>(a, rs, lane * 16, pb + PN_H + w * 32768);
    bar_raw();
    {
      const int hs = (l31 >> 2) & 1;
      const LAS u16* wp = (const LAS u16*)(L + L_CF + 2048) + hs * 256 + 4 * hh * 16 + (l31 & 3) + 4 * (l31 >> 3);
      u32 e[8];
#pragma unroll
      for (int j = 0; j < 8; ++j) e[j] = wp[(8 * (j >> 2) + (j & 3)) * 16];
      const v4u av = {e[0] | (e[1] << 16), e[2] | (e[3] << 16), e[4] | (e[5] << 16), e[6] | (e[7] << 16)};
      const v4u z = {0, 0, 0, 0};
      aphi0 = hs == 0 ? av : z; aphi1 = hs == 1 ? av : z;
      asm volatile("" : "+v"(aphi0), "+v"(aphi1));
    }
    static_for<0, PD>([&](auto U) { wb[U.value] = ldw<256>(wnat.at<slot(P_KV + U.value)>()); });
    SB0();
    f32x16 kacc = zero16, vacc = zero16;
    static_for<0, 64>([&](auto U) {
      constexpr int u = U.value;
      const v4u cur = wb[u % PD];
      if constexpr (u + PD < 64) wb[u % PD] = ldw<256>(wnat.at<slot(P_KV + u + PD)>());
      if constexpr (u & 1) mfma_pin(a[u >> 1], cur, vacc); else mfma_pin(cur, a[u >> 1], kacc);
      if constexpr (!(u & 1) && (u >> 1) + W0 < 32) a[(u >> 1) + W0] = ldfrag<(u >> 1) + W0>(rs, lane * 16, pb + PN_H + w * 32768);
      if constexpr (u == 2 * (32 - W0)) sig(L, 2, w);
      SB0();
    });
    TS(5);
    f32x16 P = mfma(aphi0, pack8<0>(kacc), zero16);
    P = mfma(aphi1, pack8<8>(kacc), P);
    const v4u vb0 = pack8<0>(vacc), vb1 = pack8<8>(vacc);
    u32 phk[16];
    feat(P, phk);
    LAS unsigned char* T = L + L_T + w * TW;
#pragma unroll
    for (int c = 0; c < 4; ++c)
      *(LAS v4u*)(T + lane * 64 + ((c ^ ((lane >> 1) & 3)) * 16)) = v4u{phk[4 * c], phk[4 * c + 1], phk[4 * c + 2], phk[4 * c + 3]};
    const v4u ones = {0x3F803F80u, 0x3F803F80u, 0x3F803F80u, 0x3F803F80u};
    LAS unsigned char* tr = T + (4 * hh + trq) * 64 + (((2 * trb + (trp >> 1)) ^ ((2 * hh + (trq >> 1)) & 3)) * 16) + 8 * (trp & 1);
    const v4u t0 = ldw<512>(tr), t1 = ldw<512>(tr + 1024), t2 = ldw<512>(tr + 2048), t3 = ldw<512>(tr + 3072);
    f32x16 D0 = mfma(t0, trb ? ones : vb0, zero16);
    f32x16 D1 = mfma(t2, trb ? vb0 : ones, zero16);
    D0 = mfma(t1, trb ? ones : vb1, D0);
    D1 = mfma(t3, trb ? vb1 : ones, D1);
    {
      LAS unsigned char* pe = T + lane * 16;
      LAS unsigned char* pz = T + 1024 + (lane >> 4) * 16;
      LAS unsigned char* p0 = trb ? pz : pe;
      LAS unsigned char* p1 = trb ? pe : pz;
#pragma unroll
      for (int g = 0; g < 4; ++g) *(LAS f32x4*)(p0 + g * 1088) = f32x4{D0[4 * g], D0[4 * g + 1], D0[4 * g + 2], D0[4 * g + 3]};
#pragma unroll
      for (int g = 0; g < 4; ++g) *(LAS f32x4*)(p1 + g * 1088) = f32x4{D1[4 * g], D1[4 * g + 1], D1[4 * g + 2], D1[4 * g + 3]};
    }
    TS(6);
    bar();
    {
      f32x4 e[4], z[4];
#pragma unroll
      for (int v = 0; v < 4; ++v) {
        e[v] = *(LAS f32x4*)(L + L_T + v * TW + w * 1088 + lane * 16);
        z[v] = *(LAS f32x4*)(L + L_T + v * TW + w * 1088 + 1024 + (lane & 3) * 16);
      }
      SB0();
      const int idx = (mem * 4 + w) * 16 + panel;
      st128(__builtin_bit_cast(v4u, (e[0] + e[1]) + (e[2] + e[3])), rs, lane * 16, OFF_KVE + idx * 1024);
      st128(__builtin_bit_cast(v4u, (z[0] + z[1]) + (z[2] + z[3])), rs, lane < 4 ? lane * 16 : WS_BYTES, OFF_KVZ + idx * 64);
      publish(rs, FL_KV + mem * 2 + (w >> 1), (w & 1) * 16 + panel, lane);
      bar_raw();
      static_for<0, PD>([&](auto U) { wb[U.value] = ldw<256>(wnat.at<slot(P_Q + U.value)>()); });
    }
    TS(7);
    f32x16 qacc = zero16;
    static_for<0, 32>([&](auto U) {
      constexpr int u = U.value;
      const v4u cur = wb[u % PD];
      if constexpr (u + PD < 32) wb[u % PD] = ldw<256>(wnat.at<slot(P_Q + u + PD)>());
      mfma_pin(cur, a[u], qacc);
      SB0();
    });
    P = mfma(aphi0, pack8<0>(qacc), zero16);
    P = mfma(aphi1, pack8<8>(qacc), P);
    feat(P, phq);
#pragma unroll
    for (int i = 0; i < 16; ++i) asm volatile("" : "+v"(phq[i]));
  }
  v2u xres[4];
  {
    const u16* xr = p.x + (size_t)(panel * 128 + strip2 * 32 + l31) * D + ncol + 4 * hh;
#pragma unroll
    for (int i = 0; i < 4; ++i) xres[i] = *(const GAS v2u*)(xr + 8 * i);
  }
  TS(8);
  bar_raw();
  TS(9);
  {
    const bool on = (l31 >> 4) == hh;
    const v4u zv = {0, 0, 0, 0};
    f32x16 num = zero16, den = zero16;
    v4u ak[4], az[4];
#pragma unroll
    for (int s4 = 0; s4 < 4; ++s4) { az[s4] = *(LAS v4u*)(L + L_ZF + s4 * 32 + hh * 16); ak[s4] = *(LAS v4u*)(L + L_KVF + s4 * 512 + l31 * 16); }
    SB0();
#pragma unroll
    for (int s4 = 0; s4 < 4; ++s4) den = mfma(on ? az[s4] : zv, v4u{phq[4 * s4], phq[4 * s4 + 1], phq[4 * s4 + 2], phq[4 * s4 + 3]}, den);
#pragma unroll
    for (int s4 = 0; s4 < 4; ++s4) num = mfma(on ? ak[s4] : zv, v4u{phq[4 * s4], phq[4 * s4 + 1], phq[4 * s4 + 2], phq[4 * s4 + 3]}, num);
    const float d0 = rcp(rb(rb(den[0]) + 1e-6f)), d1 = rcp(rb(rb(den[8]) + 1e-6f));
    const v4u n0 = pack8<0>(num), n1 = pack8<8>(num);
    v4u o0, o1;
#pragma unroll
    for (int j = 0; j < 4; ++j) { o0[j] = pk(lo16(n0[j]) * d0, hi16(n0[j]) * d0); o1[j] = pk(lo16(n1[j]) * d1, hi16(n1[j]) * d1); }
    st128(o0, rs, lane * 16, pb + PN_O + w * 32768 + (2 * mem) * 1024);
    st128(o1, rs, lane * 16 + 1024, pb + PN_O + w * 32768 + (2 * mem) * 1024);
  }
#define WDN_BASES() const Base3 wown(L + (4 * hh + trq) * 64 + trb * 32 + trp * 8 + 3 * kh * 1024), woth(L + (4 * hh + trq) * 64 + trb * 32 + trp * 8 + (kh + 1) * 1024)
#define WDN(base, u) ldw<512>((((u) & 1) ? wown : woth).template at<slot((base) + 4 * ((u) >> 1))>())
  u32 x1p[8];
  {
    WDN_BASES();
    static_for<0, PD>([&](auto U) { wb[U.value] = WDN(P_WO, U.value); });
    publish(rs, fw + FL_O + w, mem, lane);
    TS(10);
    waitn(rs, fw + FL_O + strip2, 0xFFu << (kh * 8), lane);
    TS(11);
    v4u a[16];
    ldfrags<16>(a, rs, lane * 16, pb + PN_O + strip2 * 32768 + kh * 16384);
    sig(L, 4, w);
    SB0();
    f32x16 c0 = zero16, c1 = zero16;
    static_for<0, 32>([&](auto U) {
      constexpr int u = U.value;
      const v4u cur = wb[u % PD];
      if constexpr (u + PD < 32) wb[u % PD] = WDN(P_WO, u + PD);
      if constexpr (u & 1) mfma_pin(cur, a[u >> 1], c0); else mfma_pin(cur, a[u >> 1], c1);
      SB0();
    });
#pragma unroll
    for (int g = 0; g < 4; ++g) *(LAS f32x4*)(L + L_T + w * TW + g * 1024 + lane * 16) = f32x4{c1[4 * g], c1[4 * g + 1], c1[4 * g + 2], c1[4 * g + 3]};
    TS(12);
    bar();
    f32x4 o[4], ga[4], sc[4];
#pragma unroll
    for (int g = 0; g < 4; ++g) {
      o[g] = *(LAS f32x4*)(L + L_T + (w ^ 2) * TW + g * 1024 + lane * 16);
      ga[g] = *(LAS f32x4*)(L + L_VEC + (kh * 32 + 8 * g + 4 * hh) * 4);
      sc[g] = *(LAS f32x4*)(L + L_VEC + 256 + (kh * 32 + 8 * g + 4 * hh) * 4);
    }
    SB0();
    float x1[16], sp[4];
    u32 yp[8];
#pragma unroll
    for (int g = 0; g < 4; ++g) {
#pragma unroll
      for (int j = 0; j < 2; ++j) {
        const u32 y = pk(c0[4 * g + 2 * j] + o[g][2 * j], c0[4 * g + 2 * j + 1] + o[g][2 * j + 1]);
        const u32 t = pk(ga[g][2 * j] * lo16(y), ga[g][2 * j + 1] * hi16(y));
        const u32 r = pk(lo16(xres[g][j]) + lo16(t), hi16(xres[g][j]) + hi16(t));
        x1p[2 * g + j] = r;
        x1[4 * g + 2 * j] = lo16(r); x1[4 * g + 2 * j + 1] = hi16(r);
        yp[2 * g + j] = pk(lo16(r) * sc[g][2 * j], hi16(r) * sc[g][2 * j + 1]);
      }
    }
    const int tile = 2 * colg + kh;
    st128(v4u{yp[0], yp[1], yp[2], yp[3]}, rs, lane * 16, pb + PN_X1 + strip2 * 32768 + tile * 2048);
    st128(v4u{yp[4], yp[5], yp[6], yp[7]}, rs, lane * 16 + 1024, pb + PN_X1 + strip2 * 32768 + tile * 2048);
    SB0();
#pragma unroll
    for (int g = 0; g < 4; ++g) sp[g] = (x1[4 * g] + x1[4 * g + 1]) + (x1[4 * g + 2] + x1[4 * g + 3]);
    const float ss = (sp[0] + sp[1]) + (sp[2] + sp[3]);
    const float mt = fold32(ss, ss) * (1.f / 32.f);
    float q[4];
#pragma unroll
    for (int g = 0; g < 4; ++g) {
      const float d0 = x1[4 * g] - mt, d1 = x1[4 * g + 1] - mt, d2 = x1[4 * g + 2] - mt, d3 = x1[4 * g + 3] - mt;
      q[g] = (d0 * d0 + d1 * d1) + (d2 * d2 + d3 * d3);
    }
    const float qs = (q[0] + q[1]) + (q[2] + q[3]);
    const float qt = fold32(qs, qs);
    v4u sv = {__float_as_uint(mt), tag, __float_as_uint(qt), tag};
    asm volatile("" : "+v"(sv));
    publish(rs, fw + FL_X + strip2, tile, lane);
    st128(sv, rs, lane < 32 ? lane * 16 : WS_BYTES, pb + PN_ST + strip2 * 8192 + tile * 512);
  }
  asm volatile("" : "+v"(x1p[0]), "+v"(x1p[1]), "+v"(x1p[2]), "+v"(x1p[3]), "+v"(x1p[4]), "+v"(x1p[5]), "+v"(x1p[6]), "+v"(x1p[7]));
#define GU_S(u) ((u) < 96 ? (u) / 6 : 16 + (((u) - 96) % 32) / 2)
#define GU_I(u) ((u) < 96 ? (u) % 6 : 2 * (((u) - 96) / 32) + ((u) & 1))
#define WGU(u) ldw<512>((((u) & 1) ? wy : wx).template at<slot(P_GU + ((u) & ~1))>())
  {
    const Base3 wx(L + (4 * hh + trq) * 64 + trp * 8 + trb * (1024 + 32)), wy(L + (4 * hh + trq) * 64 + trp * 8 + (trb ? 1024 : 32));
    static_for<0, PD>([&](auto U) { wb[U.value] = WGU(U.value); });
    TS(13);
    waitn(rs, fw + FL_X + w, 0xFFFFu, lane);
    TS(14);
    constexpr int AW = 20;
    v4u a[AW];
    ldfrags<8>(a, rs, lane * 16, pb + PN_X1 + w * 32768);
    f32x16 acc[6];
#pragma unroll
    for (int i = 0; i < 6; ++i) acc[i] = zero16;
    float rr = 0.f;
    v4u bcor = {0, 0, 0, 0};
    v4u ho, cf0, cf1, pp;
    float t0 = 0.f, t1 = 0.f;
    const int go = pb + PN_G + (w * 96 + mem) * 1024;
    auto epi = [&](auto Ic, auto Kc) {
      constexpr int I = decltype(Ic)::value, k = decltype(Kc)::value, j = k >> 1;
      const f32x16& A = acc[I];
      if constexpr ((k & 1) == 0) { t0 = A[2 * j] * rr; t1 = A[2 * j + 1] * rr; asm volatile("" : "+v"(t0)); t0 *= A[8 + 2 * j]; }
      else {
        t1 *= A[9 + 2 * j];
        ho[j] = pk(t0, t1);
        if constexpr (j == 3) st128(ho, rs, (I < 4 || three) ? lane * 16 : WS_BYTES, go + ((I & 1) * 48 + (I >> 1) * 16) * 1024);
      }
    };
    static_for<0, 192>([&](auto Uc) {
      constexpr int u = decltype(Uc)::value, s = GU_S(u), i = GU_I(u), q = (u - 96) % 32;
      if constexpr (u == 6) TS(22);
      if constexpr (u == 60) TS(23);
      if constexpr (u == 96) TS(24);
      if constexpr (u == 128) TS(25);
      if constexpr (u == 160) TS(26);
      if constexpr (u == 56) mark(L, H_CP, w, 2u);
      if constexpr (u == 96 || u == 128 || u == 160) mark(L, H_CP, w, u / 32);
      if constexpr (u == 62 || u == 94 || u == 112) pp = peek(L, H_PP);
      if constexpr (u == 66) need_pp<false>(L, pp, 1u);
      if constexpr (u == 98) need_pp<false>(L, pp, 3u);
      if constexpr (u == 116) need_pp<true>(L, pp, 5u);
      const v4u cur = wb[u % PD];
      if constexpr (u + PD < 192) wb[u % PD] = WGU(u + PD);
      if constexpr (u >= 96 && q == 24) cf0 = *(LAS v4u*)(L + L_CF + i * 512 + l31 * 16);
      if constexpr (u >= 96 && q == 25) cf1 = *(LAS v4u*)(L + L_CF + i * 512 + l31 * 16);
      mfma_pin(cur, a[s % AW], acc[i]);
      if constexpr (u < 96 && i == 5 && s + AW < 32) a[s % AW] = ldfrag<s + AW>(rs, lane * 16, pb + PN_X1 + w * 32768);
      if constexpr (u < 2 * (AW - 8) && (u & 1)) a[8 + (u >> 1)] = ldfrag<8 + (u >> 1)>(rs, lane * 16, pb + PN_X1 + w * 32768);
      if constexpr (u == 2 * (AW - 8)) mark(L, H_CP, w, 1u);
      if constexpr (u == 117) bcor = *(LAS v4u*)(L + L_KVF + w * 512 + l31 * 16);
      if constexpr (u == 123) { rr = __uint_as_float(bcor.w); bcor = v4u{hh ? 0u : bcor.x, hh ? 0u : bcor.y, hh ? 0u : bcor.z, 0u}; }
      if constexpr (u >= 96 && q == 30) mfma_pin(cf0, bcor, acc[i]);
      if constexpr (u >= 96 && q == 31) mfma_pin(cf1, bcor, acc[i]);
      if constexpr (u >= 128 && q >= 1 && q < 17) epi(IC<(i & 6) - 2 + (q - 1) / 8>{}, IC<(q - 1) % 8>{});
      if constexpr (u == 164) publish(rs, fw + FL_G + w * 3, mem, lane);
      SB0();
    });
    mark(L, H_CP, w, 6u);
    TS(15);
    pp = peek(L, H_PP);
    static_for<0, 7>([&](auto Kc) { epi(IC<4>{}, Kc); });
    SB0();
    publish(rs, fw + FL_G + w * 3 + 1, mem, lane);
    SB0();
    static_for<7, 16>([&](auto Kc) { epi(IC<4 + Kc.value / 8>{}, IC<Kc.value % 8>{}); });
    need_pp<true>(L, pp, 6u);
    TS(16);
  }
  {
    WDN_BASES();
    static_for<0, PD>([&](auto U) { wb[U.value] = WDN(P_WD, U.value); });
    const int fi = fw + FL_G + strip2 * 3;
    u32 fl = ask(rs, fi, lane);
    publish(rs, fw + FL_G + w * 3 + 2, mem, lane);
    TS(17);
    poll(fl, rs, fi, 0xFFFFu, lane);
    TS(18);
    v4u a[43], pp;
    const int gi = pb + PN_G + (strip2 * 96 + kh * 48) * 1024;
    fl = ask(rs, fi + 1, lane);
    SB0();
    ldfrags<W0>(a, rs, lane * 16, gi);
    f32x16 c0 = zero16, c1 = zero16;
    static_for<0, 86>([&](auto U) {
      constexpr int u = U.value;
      if constexpr (u == 18) mark(L, H_CP, w, 7u);
      if constexpr (u == 26 || u == 42 || u == 58) pp = peek(L, H_PP);
      if constexpr (u == 30 || u == 46 || u == 62) need_pp<true>(L, pp, 7u + (u - 30) / 16);
      if constexpr (u == 2 * (16 - W0)) poll(fl, rs, fi + 1, 0xFFFFu, lane);
      if constexpr (u == 2 * (32 - W0) - 26) fl = ask(rs, fi + 2, lane);
      if constexpr (u == 2 * (32 - W0)) poll(fl, rs, fi + 2, 0x7FFu, lane);
      const v4u cur = wb[u % PD];
      if constexpr (u + PD < 86) wb[u % PD] = WDN(P_WD, u + PD);
      if constexpr (u & 1) mfma_pin(cur, a[u >> 1], c0); else mfma_pin(cur, a[u >> 1], c1);
      if constexpr (!(u & 1) && (u >> 1) + W0 < 43) a[(u >> 1) + W0] = ldfrag<(u >> 1) + W0>(rs, lane * 16, gi);
      SB0();
    });
#pragma unroll
    for (int g = 0; g < 4; ++g) *(LAS f32x4*)(L + L_T + w * TW + g * 1024 + lane * 16) = f32x4{c1[4 * g], c1[4 * g + 1], c1[4 * g + 2], c1[4 * g + 3]};
    f32x4 gb[4];
#pragma unroll
    for (int g = 0; g < 4; ++g) gb[g] = *(LAS f32x4*)(L + L_VEC + 512 + (kh * 32 + 8 * g + 4 * hh) * 4);
    TS(19);
    bar();
    f32x4 o[4];
#pragma unroll
    for (int g = 0; g < 4; ++g) o[g] = *(LAS f32x4*)(L + L_T + (w ^ 2) * TW + g * 1024 + lane * 16);
    SB0();
    u16* orow = p.out + (size_t)(panel * 128 + strip2 * 32 + l31) * D + ncol + 4 * hh;
#pragma unroll
    for (int g = 0; g < 4; ++g) {
      v2u r;
#pragma unroll
      for (int j = 0; j < 2; ++j) {
        r[j] = pk(lo16(x1p[2 * g + j]) + gb[g][2 * j] * (c0[4 * g + 2 * j] + o[g][2 * j]),
                  hi16(x1p[2 * g + j]) + gb[g][2 * j + 1] * (c0[4 * g + 2 * j + 1] + o[g][2 * j + 1]));
      }
      *(GAS v2u*)(orow + 8 * g) = r;
    }
  }
  if (b == 0 && tid == 0) __hip_atomic_store((GAS u32*)(ws + OFF_EPOCH), tag, RLX_AGENT);
  if (b == 77 && w == 0) {
    const u64 goal = lk0 + 5000 + LMUL * (lkb - lka);
    while (__builtin_amdgcn_s_memrealtime() < goal) __builtin_amdgcn_s_sleep(1);
  }
}
}

extern "C" void kernel_launch(void* const* d_in, const int* in_sizes, int n_in, void* d_out, int out_size, void* d_ws,
                              size_t ws_size, hipStream_t stream) {
  using namespace mk;
  Params p;
  p.x = (const u16*)d_in[0]; p.wqkv = (const u16*)d_in[1]; p.wphi = (const u16*)d_in[2]; p.wo = (const u16*)d_in[3];
  p.wg = (const u16*)d_in[5]; p.wu = (const u16*)d_in[6]; p.wd = (const u16*)d_in[7]; p.c = (const u16*)d_in[8];
  p.wmod = (const u16*)d_in[9]; p.out = (u16*)d_out; p.ws = (unsigned char*)d_ws;
  (void)hipFuncSetAttribute((const void*)mega, hipFuncAttributeMaxDynamicSharedMemorySize, LDS_BYTES);
  mega<<<256, 512, LDS_BYTES, stream>>>(p);
}
```

```cpp
#include <hip/hip_runtime.h>

namespace mk {
typedef unsigned short u16;
typedef unsigned int u32;
typedef unsigned long long u64;
typedef u32 v4u __attribute__((ext_vector_type(4)));
typedef u32 v2u __attribute__((ext_vector_type(2)));
typedef float f32x2 __attribute__((ext_vector_type(2)));
typedef float f32x4 __attribute__((ext_vector_type(4)));
typedef float f32x16 __attribute__((ext_vector_type(16)));
typedef __bf16 bf16x2 __attribute__((ext_vector_type(2)));
typedef __bf16 bf16x8 __attribute__((ext_vector_type(8)));
typedef short s16x4 __attribute__((ext_vector_type(4)));
#define LAS __attribute__((address_space(3)))
#define GAS __attribute__((address_space(1)))
#define DI __device__ __forceinline__
#define RLX_AGENT __ATOMIC_RELAXED, __HIP_MEMORY_SCOPE_AGENT
#define SB0() __builtin_amdgcn_sched_barrier(0)
typedef __amdgpu_buffer_rsrc_t rsrc_t;

constexpr int D = 512, F = 1376;
constexpr int OFF_EPOCH = 0;
constexpr int OFF_MODG = 64 << 10;
constexpr int OFF_KVZ = 128 << 10;
constexpr int OFF_CG = 192 << 10;
constexpr int CG_PART = 2 * F * 16;
constexpr int OFF_ARR = 384 << 10;
constexpr int OFF_MODR = 512 << 10;
constexpr int OFF_KVE = 1 << 20;
constexpr int OFF_PAN = 2 << 20;
constexpr int PN_H = 0;
constexpr int PN_O = 128 << 10;
constexpr int PN_X1 = 256 << 10;
constexpr int PN_G = 384 << 10;
constexpr int PN_ST = 768 << 10;
constexpr int PN_FL = 800 << 10;
constexpr int FL_H = 0, FL_O = 4, FL_X = 8, FL_G = 12, FL_KV = 384;
constexpr int PSTR = (848 << 10) + 4224;
constexpr int WS_BYTES = 16 << 20;
constexpr int OFF_TS = 32 << 20;
constexpr int RING = 136;
constexpr int L_T = RING * 1024, TW = 4352;
constexpr int L_KVF = L_T + 4 * TW;
constexpr int L_ZF = L_KVF + 2048;
constexpr int L_VEC = L_ZF + 256;
constexpr int L_CF = L_VEC + 768;
constexpr int L_HINT = L_CF + 3072;
constexpr int LDS_BYTES = L_HINT + 256;
static_assert(LDS_BYTES <= 163840, "LDS");
constexpr int P_KV = 0, P_Q = 64, P_WO = 96, P_GU = 160, P_WD = 352;
constexpr int H_CP = 6, H_PP = 7, H_C0 = 8, H_P0 = 9;
constexpr int pair_of(int t) { return ((t & 15) < 11 ? 3 * (t & 15) : 2 * (t & 15) + 11) + (t >> 4); }
constexpr int slot(int n) { return (n % RING) * 1024; }
constexpr int AUX_SC1 = 16;
constexpr u32 SPIN_CAP = 1u << 16;
#define PRE_MOD 8
#define HI_DELAY 8
#define POLL_GAP 6
#define PRE_FL 4
#define KV1 32
#define PGU 4
#define PKV 0
#define GPUB 27
#define LPRIO 0
#define APRIO 3
#define TMOD 0
#define CW 1
#define DW 2
#define CSLP 3
#define PG2 2
#define TLBT 0
#define LITE 1
#define EQ 1
#define PQE 2
#define GASK 165
#define PUB2 6
#define W0 12
#define PDV 5
#define WPC 0
#define WPACE() do { if (WPC) { SB0(); __builtin_amdgcn_s_sleep(WPC); SB0(); } } while (0)
constexpr int PD = PDV;
constexpr int CODE_BYTES = 47000, CODE_KB = 48;

#define NBV(x) (x)
struct Params {
  const u16 *x, *wqkv, *wphi, *wo, *wg, *wu, *wd, *c, *wmod;
  u16* out;
  unsigned char* ws;
};

template <int V> struct IC { static constexpr int value = V; };
template <int I, int N, class Fn> DI void static_for(Fn&& f) {
  if constexpr (I < N) { f(IC<I>{}); static_for<I + 1, N>(f); }
}
DI float lo16(u32 v) { return __uint_as_float(v << 16); }
DI float hi16(u32 v) { return __uint_as_float(v & 0xffff0000u); }
DI u32 pk(float a, float b) { f32x2 v = {a, b}; return __builtin_bit_cast(u32, __builtin_convertvector(v, bf16x2)); }
DI float rb(float a) { return lo16(pk(a, 0.f)); }
DI float rcp(float a) { return __builtin_amdgcn_rcpf(a); }
DI f32x16 mfma(v4u a, v4u b, f32x16 c) {
  return __builtin_amdgcn_mfma_f32_32x32x16_bf16(__builtin_bit_cast(bf16x8, a), __builtin_bit_cast(bf16x8, b), c, 0, 0, 0);
}
DI void mfma_pin(v4u a, v4u b, f32x16& c) {
  c = mfma(a, b, c);
  asm volatile("" : "+v"(c));
}
template <int O> DI v4u pack8(const f32x16& x) {
  v4u r; r.x = pk(x[O], x[O + 1]); r.y = pk(x[O + 2], x[O + 3]); r.z = pk(x[O + 4], x[O + 5]); r.w = pk(x[O + 6], x[O + 7]); return r;
}
template <int T> DI v4u ldw(LAS unsigned char* p) {
  s16x4 a = __builtin_amdgcn_ds_read_tr16_b64_v4i16((LAS s16x4*)p);
  s16x4 b = __builtin_amdgcn_ds_read_tr16_b64_v4i16((LAS s16x4*)(p + T));
  return __builtin_bit_cast(v4u, __builtin_shufflevector(a, b, 0, 1, 2, 3, 4, 5, 6, 7));
}
struct Base3 {
  u32 b[3];
  DI explicit Base3(LAS unsigned char* p) {
    b[0] = (u32)(size_t)p; b[1] = b[0] + 65536u; b[2] = b[0] + 131072u;
    asm volatile("" : "+v"(b[0]), "+v"(b[1]), "+v"(b[2]));
  }
  template <int OFF> DI LAS unsigned char* at() const { return (LAS unsigned char*)(size_t)b[OFF >> 16] + (OFF & 65535); }
};
template <int LOFF, int SOFF> DI void gldsi(rsrc_t rs, u32 voff, u32 sbase, u32 lbase) {
  u32 t;
  asm volatile("s_add_u32 m0, %1, %2\n\ts_add_u32 %0, %3, %4\n\tbuffer_load_dwordx4 %5, %6, %0 offen lds"
               : "=&s"(t) : "s"(lbase), "i"(LOFF), "s"(sbase), "i"(SOFF), "v"(voff), "s"(rs) : "m0", "scc", "memory");
}
DI void bar() { asm volatile("s_waitcnt lgkmcnt(0)\n\ts_barrier" ::: "memory"); }
DI void bar_raw() { asm volatile("s_barrier" ::: "memory"); }
DI void drain() { asm volatile("s_waitcnt vmcnt(0)" ::: "memory"); }
#define VMCNT(n) asm volatile("s_waitcnt vmcnt(" #n ")" ::: "memory")
#define RELOAD() asm volatile("" ::: "memory")
DI v4u ld128(rsrc_t rs, int voff, int soff) { return __builtin_amdgcn_raw_buffer_load_b128(rs, voff, soff, AUX_SC1); }
DI v2u ld64(rsrc_t rs, int voff, int soff) { return __builtin_amdgcn_raw_buffer_load_b64(rs, voff, soff, AUX_SC1); }
DI void st128(v4u v, rsrc_t rs, int voff, int soff) { __builtin_amdgcn_raw_buffer_store_b128(v, rs, voff + soff, 0, AUX_SC1); }
DI void st64(v2u v, rsrc_t rs, int voff, int soff) { __builtin_amdgcn_raw_buffer_store_b64(v, rs, voff, soff, AUX_SC1); }
template <int S> DI v4u ldfrag(rsrc_t rs, int voff, int soff) { return ld128(rs, voff + (S & 3) * 1024, soff + (S >> 2) * 4096); }
template <int W, int N> DI void ldfrags(v4u (&a)[N], rsrc_t rs, int voff, int soff) {
  static_for<0, W>([&](auto S) { a[S.value] = ldfrag<S.value>(rs, voff, soff); SB0(); });
}
template <int CTRL> DI float dpp_add(float v) {
  return v + __uint_as_float(__builtin_amdgcn_update_dpp(0u, __float_as_uint(v), CTRL, 0xf, 0xf, false));
}
constexpr int DPP_X1 = 0xB1, DPP_X2 = 0x4E, DPP_HM = 0x141, DPP_ROR4 = 0x124, DPP_ROR8 = 0x128;
DI float fold32(float x, float y) {
  auto r = __builtin_amdgcn_permlane32_swap(__float_as_uint(x), __float_as_uint(y), false, false);
  return __uint_as_float(r[0]) + __uint_as_float(r[1]);
}
DI float fold16(float x, float y) {
  auto r = __builtin_amdgcn_permlane16_swap(__float_as_uint(x), __float_as_uint(y), false, false);
  return __uint_as_float(r[0]) + __uint_as_float(r[1]);
}
DI void red8q(const float (&a)[8], float& c0, float& c1) {
  const float b0 = fold32(a[0], a[4]), b1 = fold32(a[1], a[5]), b2 = fold32(a[2], a[6]), b3 = fold32(a[3], a[7]);
  c0 = fold16(b0, b2); c1 = fold16(b1, b3);
  c0 = dpp_add<DPP_ROR4>(c0); c1 = dpp_add<DPP_ROR4>(c1);
  c0 = dpp_add<DPP_ROR8>(c0); c1 = dpp_add<DPP_ROR8>(c1);
}
DI void red8(const float (&a)[8], float& c0, float& c1) {
  red8q(a, c0, c1);
  c0 = dpp_add<DPP_X1>(c0); c1 = dpp_add<DPP_X1>(c1);
  c0 = dpp_add<DPP_X2>(c0); c1 = dpp_add<DPP_X2>(c1);
}
DI void publish(rsrc_t rs, int word, int bit, int lane) {
  drain();
  (void)__builtin_amdgcn_raw_ptr_buffer_atomic_add_i32(1 << bit, rs, lane == 0 ? 0 : WS_BYTES, OFF_ARR + word * 128, 0);
}
DI u32 ask(rsrc_t rs, int word, int lane) { return __builtin_amdgcn_raw_ptr_buffer_atomic_add_i32(0, rs, lane == 0 ? 0 : WS_BYTES, OFF_ARR + word * 128, 0); }
DI void spin(rsrc_t rs, int word, u32 m, int lane) {
#pragma nounroll
  for (u32 spins = 0; spins < SPIN_CAP; ++spins)
    if ((__builtin_amdgcn_readfirstlane(ask(rs, word, lane)) & m) == m) break;
}
DI void poll(u32 f, rsrc_t rs, int word, u32 m, int lane) {
  if (__builtin_expect((__builtin_amdgcn_readfirstlane(f) & m) != m, 0)) spin(rs, word, m, lane);
  __builtin_amdgcn_fence(__ATOMIC_ACQUIRE, "wavefront");
  RELOAD();
}
DI void waitn(rsrc_t rs, int word, u32 m, int lane) {
  if (PRE_FL) __builtin_amdgcn_s_sleep(PRE_FL);
  spin(rs, word, m, lane);
  __builtin_amdgcn_fence(__ATOMIC_ACQUIRE, "wavefront");
  RELOAD();
}
DI void sig(LAS unsigned char* L, int i, int k) { *(volatile LAS u32*)(L + L_HINT + 16 * i + 4 * k) = 1u; }
template <int N> DI void await(LAS unsigned char* L, int i) {
#pragma nounroll
  for (u32 spins = 0; spins < (1u << 20); ++spins) {
    const v4u v = *(volatile LAS v4u*)(L + L_HINT + 16 * i);
    if (__builtin_amdgcn_readfirstlane(N == 1 ? v.x : (v.x & v.y & v.z & v.w))) break;
    __builtin_amdgcn_s_sleep(1);
  }
  RELOAD();
}
DI void mark(LAS unsigned char* L, int i, int k, u32 val) { *(volatile LAS u32*)(L + L_HINT + 16 * i + 4 * k) = val; }
DI v4u peek(LAS unsigned char* L, int i) { return *(volatile LAS v4u*)(L + L_HINT + 16 * i); }
DI u32 umin(u32 a, u32 b) { return a < b ? a : b; }
DI void await_cp(LAS unsigned char* L, u32 k) {
#pragma nounroll
  for (u32 spins = 0; spins < (1u << 20); ++spins) {
    const v4u v = peek(L, H_CP);
    if (__builtin_amdgcn_readfirstlane(umin(umin(v.x, v.y), umin(v.z, v.w))) >= k) break;
    __builtin_amdgcn_s_sleep(CSLP);
  }
  RELOAD();
}
template <int SLP> DI void reach(LAS unsigned char* L, int i, u32 k0, u32 k1, u32 k2, u32 k3) {
#pragma nounroll
  for (u32 spins = 0; spins < (1u << 20); ++spins) {
    const v4u v = peek(L, i);
    if (__builtin_amdgcn_readfirstlane((v.x >= k0) & (v.y >= k1) & (v.z >= k2) & (v.w >= k3))) break;
    if (SLP) __builtin_amdgcn_s_sleep(SLP);
  }
  RELOAD();
}
DI void reach1(LAS unsigned char* L, int i, int k, u32 val) {
#pragma nounroll
  for (u32 spins = 0; spins < (1u << 20); ++spins)
    if (__builtin_amdgcn_readfirstlane(*(volatile LAS u32*)(L + L_HINT + 16 * i + 4 * k)) >= val) break;
  RELOAD();
}
#define MARK(i, k, val) do { RELOAD(); mark(L, i, k, val); RELOAD(); } while (0)
template <bool AUX> DI u32 pp_min(v4u v) {
  u32 m = umin(v.x, v.y);
  if (AUX) m = umin(m, umin(v.z, v.w));
  return __builtin_amdgcn_readfirstlane(m);
}
template <bool AUX> DI void need_pp(LAS unsigned char* L, v4u v, u32 k) {
  if (__builtin_expect(pp_min<AUX>(v) < k, 0)) {
#pragma nounroll
    for (u32 spins = 0; spins < (1u << 20); ++spins)
      if (pp_min<AUX>(peek(L, H_PP)) >= k) break;
  }
  RELOAD();
}
DI void feat(const f32x16& P, u32 (&ph)[16]) {
#pragma clang fp contract(off)
  constexpr float L2E = 1.4426950408889634f;
  f32x2 p[8];
#pragma unroll
  for (int i = 0; i < 8; ++i) { const u32 t = pk(P[2 * i], P[2 * i + 1]); p[i] = f32x2{lo16(t), hi16(t)} * L2E; }
  float mx[4], mn[4];
#pragma unroll
  for (int i = 0; i < 4; ++i) {
    mx[i] = fmaxf(fmaxf(p[2 * i].x, p[2 * i].y), fmaxf(p[2 * i + 1].x, p[2 * i + 1].y));
    mn[i] = fminf(fminf(p[2 * i].x, p[2 * i].y), fminf(p[2 * i + 1].x, p[2 * i + 1].y));
  }
  const float mxl = fmaxf(fmaxf(mx[0], mx[1]), fmaxf(mx[2], mx[3])), mnl = fminf(fminf(mn[0], mn[1]), fminf(mn[2], mn[3]));
  f32x2 e1[8], e2[8];
#pragma unroll
  for (int i = 0; i < 8; ++i) { e1[i] = p[i] - mxl; e2[i] = mnl - p[i]; }
  SB0();
#pragma unroll
  for (int i = 0; i < 8; ++i) {
    e1[i] = f32x2{__builtin_amdgcn_exp2f(e1[i].x), __builtin_amdgcn_exp2f(e1[i].y)};
    e2[i] = f32x2{__builtin_amdgcn_exp2f(e2[i].x), __builtin_amdgcn_exp2f(e2[i].y)};
  }
  const f32x2 s1 = ((e1[0] + e1[1]) + (e1[2] + e1[3])) + ((e1[4] + e1[5]) + (e1[6] + e1[7]));
  const f32x2 s2 = ((e2[0] + e2[1]) + (e2[2] + e2[3])) + ((e2[4] + e2[5]) + (e2[6] + e2[7]));
  const float r1 = rcp(s1.x + s1.y), r2 = rcp(s2.x + s2.y);
#pragma unroll
  for (int i = 0; i < 8; ++i) { const f32x2 u = e1[i] * r1, v = e2[i] * r2; ph[i] = pk(u.x, u.y); ph[8 + i] = pk(v.x, v.y); }
}

#define TSCLK() __builtin_amdgcn_s_memrealtime()
#define TS(i) do {} while (0)
#define TS_DUMP() do {} while (0)
#define TS_INIT() do {} while (0)

__global__ void __launch_bounds__(512, 2) mega(Params p) {
  extern __shared__ __attribute__((aligned(16))) unsigned char smem[];
  LAS unsigned char* L = (LAS unsigned char*)smem;
  TS_INIT();
  TS(0);
  u64 pc0;
  asm volatile("s_getpc_b64 %0" : "=s"(pc0));
  const int tid = threadIdx.x, lane = tid & 63;
  int w = __builtin_amdgcn_readfirstlane(tid >> 6);
  const int b = blockIdx.x, xcd = b & 7, rank = b >> 3, panel = (xcd >> 1) * 4 + (rank >> 3), mem = (xcd & 1) * 8 + (rank & 7);
  const int hh = lane >> 5, l31 = lane & 31;
  unsigned char* ws = p.ws;
  const int rowh = mem & 1, colg = mem >> 1;
  const int pair0 = min(3 * mem, 2 * mem + 11);
  const bool three = mem < 11;
  const rsrc_t rs = __builtin_amdgcn_make_buffer_rsrc(ws, 0, WS_BYTES, 0x00020000);
  const int pb = OFF_PAN + panel * PSTR, fw = panel * 24;
  const int mcg = xcd * 8 + (rank >> 2), mkp = rank & 3;
  const rsrc_t rqkv = __builtin_amdgcn_make_buffer_rsrc((void*)p.wqkv, 0, D * 3 * D * 2, 0x00020000);
  const rsrc_t rwo = __builtin_amdgcn_make_buffer_rsrc((void*)p.wo, 0, D * D * 2, 0x00020000);
  const rsrc_t rgu = __builtin_amdgcn_make_buffer_rsrc((void*)((w & 1) ? p.wu : p.wg), 0, D * F * 2, 0x00020000);
  const rsrc_t rwd = __builtin_amdgcn_make_buffer_rsrc((void*)p.wd, 0, F * D * 2, 0x00020000);

  v4u r0[4];
  u32 cv0[4], epoch;
  u32 vq = (lane >> 2) * (3 * D * 2) + (lane & 3) * 16;
  asm volatile("" : "+v"(vq));
  {
    const u32 ld = (u32)(size_t)L + w * 1024;
    if (w <= 4) {
      const rsrc_t r1 = __builtin_amdgcn_make_buffer_rsrc((void*)(w == 4 ? p.wmod : p.x), 0, w == 4 ? D * 6 * D * 2 : 2048 * D * 2, 0x00020000);
      const int vo = w == 4 ? (128 * mkp + 4 * (lane >> 1)) * (6 * D * 2) + mcg * 32 + (lane & 1) * 16
                            : (panel * 128 + mem * 8 + (lane >> 3)) * (D * 2) + (lane & 7) * 16 + 256 * w;
#pragma unroll
      for (int i = 0; i < 4; ++i) r0[i] = __builtin_amdgcn_raw_buffer_load_b128(r1, vo, w == 4 ? i * (6 * D * 2) : i < 2 ? i * 128 : 1 << 30, 0);
      SB0();
    }
    {
      const rsrc_t r2 = __builtin_amdgcn_make_buffer_rsrc((void*)p.c, 0, w == 4 ? D * 2 : 0, 0x00020000);
      const v2u c2 = __builtin_amdgcn_raw_buffer_load_b64(r2, (128 * mkp + 4 * (lane >> 1)) * 2, 0, 0);
      cv0[0] = c2.x; cv0[1] = c2.y;
      epoch = __builtin_amdgcn_raw_buffer_load_b32(rs, 0, OFF_EPOCH, AUX_SC1);
      __builtin_amdgcn_raw_buffer_store_b32(0u, rs, (w == 4 && lane < 2) ? lane * 128 : WS_BYTES, OFF_ARR + b * 256, AUX_SC1);
      SB0();
    }
    if (w >= 5) {
      const rsrc_t rc = __builtin_amdgcn_make_buffer_rsrc((void*)pc0, 0, CODE_BYTES, 0x00020000);
      gldsi<slot(128), 0>(rc, lane < CODE_KB / 6 ? (u32)lane * 128u : 1u << 30, (u32)(((rank & 15) * 3 + w - 5) * (CODE_KB / 6) * 128), ld);
      const u32 tq = (rank * 16 + (lane >> 2)) * (3 * D * 2) + (xcd & 1) * 512 + (lane & 3) * 128;
      if (EQ || w != 5) gldsi<slot(128), 0>(rqkv, tq, (w - 5) * (D * 2), ld);
      if (w == 5) {
        gldsi<slot(128), 0>(rs, lane < 2 ? (u32)(pb + lane * (848 << 10)) : 1u << 30, 0u, ld);
        const rsrc_t rp = __builtin_amdgcn_make_buffer_rsrc((void*)p.wphi, 0, 32 * 256 * 2, 0x00020000);
        gldsi<L_CF + 2048, 0>(rp, lane * 16, mem * 1024, (u32)(size_t)L);
      }
    }
    if (tid < 64) *((LAS u32*)(L + L_HINT) + tid) = 0u;
    TS(27);
    bar();
    asm volatile("" : "+s"(w));
    if (w >= 6) {
      u32 lo = (u32)(size_t)L + (w & 1) * 1024, so = (D + (w & 1) * D + mem * 32) * 2;
#pragma nounroll
      for (int i = 0; i < KV1 / 2; ++i) {
        gldsi<0, 0>(rqkv, vq, so, lo);
        gldsi<2048, 16 * (3 * D * 2)>(rqkv, vq, so, lo);
        lo += 4096; so += 32 * (3 * D * 2);
      }
    }
    if (w == 4) {
      __builtin_amdgcn_s_setprio(3);
      TS(10);
      float sl[4], acc[8];
#pragma unroll
      for (int i = 0; i < 4; ++i) { const float c = (i & 1) ? hi16(cv0[i >> 1]) : lo16(cv0[i >> 1]); sl[i] = rb(c * rcp(1.f + __expf(-c))); }
      u32 tag = epoch;
      asm volatile("" : "+v"(tag));
      tag += 1u;
      TS(11);
#pragma unroll
      for (int j = 0; j < 8; ++j) acc[j] = 0.f;
#pragma unroll
      for (int i = 0; i < 4; ++i)
#pragma unroll
        for (int j = 0; j < 4; ++j) { acc[2 * j] += sl[i] * lo16(r0[i][j]); acc[2 * j + 1] += sl[i] * hi16(r0[i][j]); }
      float c0, c1;
      TS(12);
      red8q(acc, c0, c1);
      c0 = dpp_add<DPP_X2>(c0); c1 = dpp_add<DPP_X2>(c1);
      const int go = ((lane >> 1) & 7) * 32768 + (8 * (lane & 1) + 2 * (lane >> 4)) * 8;
      const v4u gv = {__float_as_uint(c0), tag, __float_as_uint(c1), tag};
      drain();
      st128(gv, rs, go, OFF_MODR + mkp * 8192 + mcg * 128);
      st128(gv, rs, go, OFF_MODR + 8 * 32768 + mkp * 8192 + mcg * 128);
      sig(L, 0, 0);
    }
    asm volatile("" : "+s"(w));
  }

  if (w == 4 || w == 5) {
    const int a = w & 1;
    u32 tag;
    tag = epoch;
    asm volatile("" : "+v"(tag));
    tag += 1u;
    drain();
    TS(1);
    bar();
    __builtin_amdgcn_s_setprio(APRIO);
    await<4>(L, 2);
    const int kp = xcd >> 1, cpair = (xcd & 1) * 24 + rank;
    const bool con = rank < ((xcd & 1) ? 19 : 24);
    const int col0 = a * F + cpair * 32;
    v4u wm[8];
#pragma unroll
    for (int i = 0; i < 8; ++i)
      wm[i] = __builtin_amdgcn_raw_buffer_load_b128(rgu, con ? (128 * kp + 2 * (lane >> 2)) * (F * 2) + cpair * 64 + (lane & 3) * 16 : 1 << 30, (32 * (i >> 1) + (i & 1)) * (F * 2), 0);
    SB0();
    if (a == 1) {
      v4u wn[8]; u32 cv[8];
      const int row0 = 128 * mkp + (lane >> 2);
#pragma unroll
      for (int i = 0; i < 8; ++i) cv[i] = p.c[row0 + 16 * i];
#pragma unroll
      for (int i = 0; i < 8; ++i) wn[i] = *(const GAS v4u*)(p.wmod + (size_t)(row0 + 16 * i) * (6 * D) + 1024 + mcg * 32 + (lane & 3) * 8);
      SB0();
      float acc[8];
#pragma unroll
      for (int j = 0; j < 8; ++j) acc[j] = 0.f;
#pragma unroll
      for (int i = 0; i < 8; ++i) {
        const float c = lo16(cv[i]), sl = rb(c * rcp(1.f + __expf(-c)));
#pragma unroll
        for (int j = 0; j < 4; ++j) { acc[2 * j] += sl * lo16(wn[i][j]); acc[2 * j + 1] += sl * hi16(wn[i][j]); }
      }
      float c0, c1;
      red8q(acc, c0, c1);
      st128(v4u{__float_as_uint(c0), tag, __float_as_uint(c1), tag}, rs, (lane & 12) == 0 ? (8 * (lane & 3) + 2 * (lane >> 4)) * 8 : WS_BYTES,
            OFF_MODG + mkp * 16384 + mcg * 256);
    }
    reach<1>(L, H_C0, 2u, 2u, 2u, 2u);
    TS(2);
    {
      const int idx0 = (mem * 4 + 2 * a) * 16;
      v4u e[32], zz[2];
      {
        u32 todo = 0xFFu;
        for (u32 spins = 0; spins < SPIN_CAP && todo; ++spins) {
          const u32 bad = ~__builtin_amdgcn_readfirstlane(ask(rs, FL_KV + mem * 2 + a, lane));
          __builtin_amdgcn_fence(__ATOMIC_ACQUIRE, "wavefront");
          RELOAD();
          static_for<0, 8>([&](auto G) {
            constexpr int g = G.value;
            if (((todo >> g) & 1u) && !(bad & (0x00030003u << (2 * g)))) {
              todo &= ~(1u << g);
              e[2 * g] = ld128(rs, lane * 16, OFF_KVE + (idx0 + 2 * g) * 1024);
              e[2 * g + 1] = ld128(rs, lane * 16, OFF_KVE + (idx0 + 2 * g + 1) * 1024);
              e[16 + 2 * g] = ld128(rs, lane * 16, OFF_KVE + (idx0 + 16 + 2 * g) * 1024);
              e[17 + 2 * g] = ld128(rs, lane * 16, OFF_KVE + (idx0 + 17 + 2 * g) * 1024);
            }
          });
        }
      }
      TS(3);
      zz[0] = ldfrag<0>(rs, lane * 16, OFF_KVZ + idx0 * 64);
      zz[1] = ldfrag<1>(rs, lane * 16, OFF_KVZ + idx0 * 64);
      SB0();
#pragma unroll
      for (int q = 0; q < 2; ++q) {
        f32x4 s0 = __builtin_bit_cast(f32x4, e[16 * q]), s1 = __builtin_bit_cast(f32x4, e[16 * q + 1]);
#pragma unroll
        for (int i = 2; i < 16; i += 2) { s0 += __builtin_bit_cast(f32x4, e[16 * q + i]); s1 += __builtin_bit_cast(f32x4, e[16 * q + i + 1]); }
        s0 += s1;
        *(LAS v2u*)(L + L_KVF + (2 * a + q) * 512 + l31 * 16 + hh * 8) = v2u{pk(s0[0], s0[1]), pk(s0[2], s0[3])};
      }
      f32x4 z;
#pragma unroll
      for (int j = 0; j < 4; ++j) {
        float t = fold32(__uint_as_float(zz[0][j]), __uint_as_float(zz[1][j]));
        t = fold16(t, t);
        t = dpp_add<DPP_ROR4>(t);
        z[j] = dpp_add<DPP_ROR8>(t);
      }
      if (l31 < 4) *(LAS v2u*)(L + L_ZF + (2 * a + hh) * 32 + ((lane & 1) ^ 1) * 16 + ((lane >> 1) & 1) * 8) = v2u{pk(z[0], z[1]), pk(z[2], z[3])};
    }
    MARK(H_P0, 2 + a, 1u);
    __builtin_amdgcn_s_setprio(1);
    TS(4);
    u32 gsb[4], gsc[4];
    {
      const int vo = ((hh ? 2 : 3 * a) * D + colg * 64 + 2 * l31) * 8, ro = (D + 128 * kp + 2 * lane) * 8;
      v4u g[12];
      for (u32 spins = 0; spins < SPIN_CAP; ++spins) {
        RELOAD();
#pragma unroll
        for (int i = 0; i < 12; ++i) g[i] = ld128(rs, i < 4 ? vo : ro + (i >> 3) * (D * 8), OFF_MODG + (i & 3) * 16384);
        bool ok = true;
#pragma unroll
        for (int i = 0; i < 12; ++i) ok &= (g[i].y == tag) & (g[i].w == tag);
        if (__all(ok)) break;
      }
      u32 pr[3];
#pragma unroll
      for (int t = 0; t < 3; ++t)
        pr[t] = pk((__uint_as_float(g[4 * t].x) + __uint_as_float(g[4 * t + 1].x)) + (__uint_as_float(g[4 * t + 2].x) + __uint_as_float(g[4 * t + 3].x)),
                   (__uint_as_float(g[4 * t].z) + __uint_as_float(g[4 * t + 1].z)) + (__uint_as_float(g[4 * t + 2].z) + __uint_as_float(g[4 * t + 3].z)));
      const u32 v1 = pk(1.f + lo16(pr[0]), 1.f + hi16(pr[0])), vv = hh ? v1 : pr[0];
      *(LAS f32x2*)(L + L_VEC + (hh ? 1 : 2 * a) * 256 + l31 * 8) = f32x2{lo16(vv), hi16(vv)};
      const u32 sc = pk(1.f + lo16(pr[2]), 1.f + hi16(pr[2]));
#pragma unroll
      for (int i = 0; i < 4; ++i) {
        gsb[i] = __builtin_amdgcn_ds_bpermute((16 * i + (lane >> 2)) * 4, pr[1]);
        gsc[i] = __builtin_amdgcn_ds_bpermute((16 * i + (lane >> 2)) * 4, sc);
      }
    }
    MARK(H_P0, 2 + a, 2u);
    if (con) {
      float a1[8], a2[8];
#pragma unroll
      for (int j = 0; j < 8; ++j) { a1[j] = 0.f; a2[j] = 0.f; }
#pragma unroll
      for (int i = 0; i < 8; ++i) {
        const float sb = (i & 1) ? hi16(gsb[i >> 1]) : lo16(gsb[i >> 1]), sc = (i & 1) ? hi16(gsc[i >> 1]) : lo16(gsc[i >> 1]);
#pragma unroll
        for (int j = 0; j < 4; ++j) {
          a1[2 * j] += sc * lo16(wm[i][j]); a1[2 * j + 1] += sc * hi16(wm[i][j]);
          a2[2 * j] += sb * lo16(wm[i][j]); a2[2 * j + 1] += sb * hi16(wm[i][j]);
        }
      }
      float c10, c11, c20, c21;
      red8q(a1, c10, c11); red8q(a2, c20, c21);
      const int k = (lane >> 2) & 3;
      const float val = k == 0 ? c10 : k == 1 ? c20 : k == 2 ? c11 : c21;
      st64(v2u{__float_as_uint(val), tag}, rs, (col0 + 8 * (lane & 3) + 2 * (lane >> 4)) * 16 + k * 8, OFF_CG + kp * CG_PART);
    }
    TS(5);
    reach<2>(L, H_C0, 4u, 4u, 4u, 4u);
    {
      const int ntile = three ? 6 : 4;
      v4u cg[8];
      int off[2];
#pragma unroll
      for (int i = 0; i < 2; ++i) {
        int tile = 4 * i + 2 * a + hh; tile = tile < ntile ? tile : 0;
        off[i] = (32 * pair0 + 16 * tile + (lane & 15) + ((lane >> 4) & 1) * F) * 16;
      }
      for (u32 spins = 0; spins < SPIN_CAP; ++spins) {
        RELOAD();
        bool ok = true;
#pragma unroll
        for (int i = 0; i < 8; ++i) cg[i] = ld128(rs, off[i >> 2], OFF_CG + (i & 3) * CG_PART);
#pragma unroll
        for (int i = 0; i < 8; ++i) ok &= (cg[i].y == tag) & (cg[i].w == tag);
        if (__all(ok)) break;
      }
#pragma unroll
      for (int i = 0; i < 2; ++i) {
        const float c1 = (__uint_as_float(cg[4 * i].x) + __uint_as_float(cg[4 * i + 1].x)) + (__uint_as_float(cg[4 * i + 2].x) + __uint_as_float(cg[4 * i + 3].x));
        const float c2 = (__uint_as_float(cg[4 * i].z) + __uint_as_float(cg[4 * i + 1].z)) + (__uint_as_float(cg[4 * i + 2].z) + __uint_as_float(cg[4 * i + 3].z));
        const float h1 = rb(c1), m1 = rb(c1 - h1), l1 = c1 - h1 - m1, h2 = rb(c2), m2 = rb(c2 - h2), l2 = c2 - h2 - m2;
        const int tile = 4 * i + 2 * a + hh;
        if (tile < 6) *(LAS v4u*)(L + L_CF + tile * 512 + l31 * 16) = v4u{pk(h1, m1), pk(l1, h2), pk(m2, l2), 0u};
      }
    }
    await_cp(L, 1);
    {
      v4u st[16];
#pragma unroll
      for (int t = 0; t < 16; ++t) st[t] = ld128(rs, l31 * 16 + hh * 8192 + t * 512, pb + PN_ST + 2 * a * 8192);
      for (u32 spins = 0; spins < SPIN_CAP; ++spins) {
        bool ok = true;
#pragma unroll
        for (int t = 0; t < 16; ++t) ok &= (st[t].y == tag) & (st[t].w == tag);
        if (__all(ok)) break;
        RELOAD();
#pragma unroll
        for (int t = 0; t < 16; ++t) st[t] = ld128(rs, l31 * 16 + hh * 8192 + t * 512, pb + PN_ST + 2 * a * 8192);
      }
      float ms = 0.f;
#pragma unroll
      for (int t = 0; t < 16; ++t) ms += __uint_as_float(st[t].x);
      const float mean = ms * (1.f / 16.f);
      float q = 0.f;
#pragma unroll
      for (int t = 0; t < 16; ++t) { const float d = __uint_as_float(st[t].x) - mean; q += __uint_as_float(st[t].z) + 32.f * d * d; }
      const float den = rb(__builtin_amdgcn_sqrtf(rb(rb(q * (1.f / D)) + 1e-5f)));
      *(LAS v4u*)(L + L_KVF + a * 1024 + lane * 16) = v4u{pk(-mean, -mean), pk(-mean, den), pk(den, den), __float_as_uint(rcp(den * den))};
    }
    RELOAD();
    mark(L, H_PP, 2 + a, 5u);
    TS(6);
    {
      v4u wt[58];
      const u32 vd = (lane >> 2) * (D * 2) + (lane & 3) * 16, sd = a * 16 * (D * 2) + colg * 128;
      const Base3 wdst(L + a * 2048 + lane * 16);
      static_for<28, 86>([&](auto J) {
        wt[J.value - 28] = __builtin_amdgcn_raw_buffer_load_b128(rwd, NBV(vd + (J.value & 1) * 64), sd + pair_of(J.value >> 1) * 32 * (D * 2), 0); SB0(); });
      TS(7);
#define ST_WT(T0, T1, k) do { static_for<2 * (T0), 2 * (T1)>([&](auto J) { \
      *(LAS v4u*)wdst.template at<slot(P_WD + 4 * (J.value >> 1)) + (J.value & 1) * 1024>() = wt[J.value - 28]; WPACE(); }); \
      RELOAD(); mark(L, H_PP, 2 + a, k); RELOAD(); } while (0)
      await_cp(L, 4);
      ST_WT(14, 18, 6u);
      await_cp(L, 5);
      ST_WT(18, 26, 7u);
      await_cp(L, 6);
      ST_WT(26, 34, 8u);
      TS(8);
      await_cp(L, 7);
      ST_WT(34, 43, 9u);
      TS(9);
    }
    TS_DUMP();
    return;
  }

  if (w >= 6) {
    const int v = w & 1;
    __builtin_amdgcn_s_setprio(LPRIO);
    const u32 ldsb = (u32)(size_t)L;
    const u32 l1 = ldsb + v * 1024, l2 = ldsb + v * 2048;
    const u32 skv = (D + v * D + mem * 32) * 2, sq = v * 16 * (3 * D * 2) + mem * 64;
    const u32 so = 16 * v * 16 * (D * 2) + colg * 128, sd = v * 16 * (D * 2) + colg * 128;
    const u32 sg = pair0 * 64, sg2 = three ? sg + 128 : 1u << 30;
#define LD_KV(S0, S1, PC) static_for<S0, S1>([&](auto S) { gldsi<slot(P_KV + 2 * S.value), S.value * 16 * (3 * D * 2)>(rqkv, vq, skv, l1); PACE(PC); })
#define PACE(n) do { if ((n) > 0) __builtin_amdgcn_s_sleep(n); } while (0)
#define LD_Q(S0, S1, PC) static_for<S0, S1>([&](auto S) { gldsi<slot(P_Q + 2 * S.value), 2 * S.value * 16 * (3 * D * 2)>(rqkv, vq, sq, l1); PACE(PC); })
#define LD_DN(RS, SB, P0, T0, T1, PC)                                                                    \
  static_for<T0, T1>([&](auto T) {                                                                   \
    gldsi<slot((P0) + 4 * T.value), T.value * 16 * (D * 2)>(RS, vd, SB, l2);                         \
    gldsi<slot((P0) + 4 * T.value) + 1024, T.value * 16 * (D * 2) + 64>(RS, vd, SB, l2); PACE(PC);   \
  })
#define LD_GU(S0, S1, PC)                                                                                \
  static_for<S0, S1>([&](auto S) {                                                                   \
    gldsi<slot(P_GU + 6 * S.value), S.value * 16 * (F * 2)>(rgu, vg, sg, l1);                        \
    gldsi<slot(P_GU + 6 * S.value + 2), S.value * 16 * (F * 2) + 64>(rgu, vg, sg, l1);               \
    gldsi<slot(P_GU + 6 * S.value + 4), S.value * 16 * (F * 2)>(rgu, vg, sg2, l1); PACE(PC);         \
  })
    TS(1);
    VMCNT(0);
    SB0();
    int ln;
    asm volatile("v_mbcnt_lo_u32_b32 %0, -1, 0\n\tv_mbcnt_hi_u32_b32 %0, -1, %0" : "=v"(ln));
    const u32 vd = (ln >> 2) * (D * 2) + (ln & 3) * 16;
    const u32 vg = (ln >> 2) * (F * 2) + (((ln & 3) ^ (v << 1)) * 16);
    TS(2);
    LD_Q(0, 16, PQE);
    TS(11);
    bar_raw();
    v4u wt[48];
    const Base3 wdst1(L + v * 1024 + ln * 16);
#define GUN(s_, P_) (P_GU + ((s_) < 16 ? 6 * (s_) + 2 * (P_) : 96 + 32 * (P_) + 2 * ((s_) - 16)))
#define GUR(s_, P_) __builtin_amdgcn_raw_buffer_load_b128(rgu, NBV(vg), ((P_) == 2 ? sg2 : sg + (P_) * 64) + (s_) * 16 * (F * 2), 0)
#define GUW(s_, P_, r_) *(LAS v4u*)wdst1.template at<slot(GUN(s_, P_))>() = wt[r_]
#define LD_GU2(S0, S1, PC) static_for<S0, S1>([&](auto S) { constexpr int s_ = S.value; \
      gldsi<slot(GUN(s_, 0)), s_ * 16 * (F * 2)>(rgu, vg, sg, l1); SB0(); \
      wt[2 * (s_ - 16)] = GUR(s_, 1); wt[2 * (s_ - 16) + 1] = GUR(s_, 2); PACE(PC); SB0(); })
#define LD_WD(T0, T1) static_for<T0, T1>([&](auto T) { \
      gldsi<slot(P_WD + 4 * T.value), pair_of(T.value) * 32 * (D * 2)>(rwd, vd, sd, l2); \
      gldsi<slot(P_WD + 4 * T.value) + 1024, pair_of(T.value) * 32 * (D * 2) + 64>(rwd, vd, sd, l2); })
#define DONE(k) do { RELOAD(); mark(L, H_PP, v, k); RELOAD(); } while (0)
    await<4>(L, 2);
    VMCNT(0);
    MARK(H_P0, v, 1u);
    gldsi<L_CF, 0>(rwo, ln < 32 ? ((2 * rank + v) * 8 + (ln >> 2)) * (D * 2) + (xcd & 1) * 512 + (ln & 3) * 128 : 1u << 30, 0u, l1);
    LD_DN(rwo, so, P_WO, 0, 8, 0);
    TS(3);
    VMCNT(0);
    MARK(H_P0, v, 1u);
    reach<1>(L, H_C0, 2u, 2u, 2u, 2u);
    gldsi<L_CF, 0>(rwd, v ? 1u << 30 : ((xcd >> 1) * 32 + rank + 128 * (ln >> 2)) * (D * 2) + (xcd & 1) * 512 + (ln & 3) * 128, 0u, l1);
    LD_DN(rwo, so, P_WO, (EQ ? 8 : 0), 16, 1);
    LD_GU(0, 6, PGU);
    VMCNT(18);
    MARK(H_P0, v, 2u);
    TS(4);
    reach<2>(L, H_C0, 3u, 3u, 3u, 3u);
    reach<2>(L, H_P0, 0u, 0u, 1u, 1u);
    LD_GU(6, 12, PGU);
    await<4>(L, 4);
    static_for<0, 12>([&](auto J) { wt[32 + J.value] = GUR(12 + J.value / 3, J.value % 3); SB0(); });
    static_for<16, 20>([&](auto S) { constexpr int s_ = S.value;
      wt[28 + s_] = GUR(s_, 0); wt[2 * (s_ - 16)] = GUR(s_, 1); wt[2 * (s_ - 16) + 1] = GUR(s_, 2); SB0(); });
    VMCNT(24);
    MARK(H_P0, v, 3u);
    TS(5);
    reach<1>(L, H_C0, 4u, 4u, 4u, 4u);
    static_for<0, 12>([&](auto J) { GUW(12 + J.value / 3, J.value % 3, 32 + J.value); });
    static_for<16, 20>([&](auto S) { GUW(S.value, 0, 28 + S.value); });
    DONE(1);
    LD_GU2(20, 32, PG2);
    VMCNT(0);
    DONE(3);
    TS(6);
    await_cp(L, 1);
    {
      const u32 tdn = ((2 * rank + v) * 16 + (ln >> 2)) * (D * 2) + (xcd & 1) * 512 + (ln & 3) * 128;
      __builtin_amdgcn_s_waitcnt(0x0F70);
      __builtin_amdgcn_s_sleep(4);
      gldsi<L_T, 0>(rwd, tdn, 0u, l1);
      gldsi<L_T, 1024 * (D * 2)>(rwd, tdn, 0u, l1);
    }
    static_for<16, 32>([&](auto S) { GUW(S.value, 1, 2 * (S.value - 16)); WPACE(); });
    DONE(4);
    await_cp(L, 2);
    static_for<16, 32>([&](auto S) { GUW(S.value, 2, 2 * (S.value - 16) + 1); WPACE(); });
    DONE(5);
    TS(7);
    await_cp(L, 3);
    LD_WD(0, 10);
    await_cp(L, 4);
    LD_WD(10, 14);
    VMCNT(0);
    DONE(100);
    TS(8);
    TS_DUMP();
    return;
  }

  __builtin_amdgcn_s_setprio(2);
  const int s2 = w & 1, kh = w >> 1;
  const int strip2 = rowh * 2 + s2, ncol = colg * 64 + kh * 32;
  v4u aphi0, aphi1;
  u32 tag;
  {
    float xf[16];
#pragma unroll
    for (int i = 0; i < 8; ++i) { xf[2 * i] = lo16(r0[i >> 2][i & 3]); xf[2 * i + 1] = hi16(r0[i >> 2][i & 3]); }
    float sm = (((xf[0] + xf[1]) + (xf[2] + xf[3])) + ((xf[4] + xf[5]) + (xf[6] + xf[7]))) + (((xf[8] + xf[9]) + (xf[10] + xf[11])) + ((xf[12] + xf[13]) + (xf[14] + xf[15])));
    sm = dpp_add<DPP_X1>(sm); sm = dpp_add<DPP_X2>(sm); sm = dpp_add<DPP_HM>(sm);
    const float mw = sm * (1.f / 128.f);
    float q[4];
#pragma unroll
    for (int i = 0; i < 4; ++i) {
      const float d0 = xf[4 * i] - mw, d1 = xf[4 * i + 1] - mw, d2 = xf[4 * i + 2] - mw, d3 = xf[4 * i + 3] - mw;
      q[i] = (d0 * d0 + d1 * d1) + (d2 * d2 + d3 * d3);
    }
    float qq = (q[0] + q[1]) + (q[2] + q[3]);
    qq = dpp_add<DPP_X1>(qq); qq = dpp_add<DPP_X2>(qq); qq = dpp_add<DPP_HM>(qq);
    *(volatile LAS f32x2*)(L + L_KVF + (lane >> 3) * 32 + w * 8) = f32x2{mw, qq};
    sig(L, 1, w);
    await<4>(L, 1);
    const f32x4 pa = *(LAS f32x4*)(L + L_KVF + (lane >> 3) * 32), pc = *(LAS f32x4*)(L + L_KVF + (lane >> 3) * 32 + 16);
    const float mean = ((pa[0] + pa[2]) + (pc[0] + pc[2])) * 0.25f;
    const float f0 = pa[0] - mean, f1 = pa[2] - mean, f2 = pc[0] - mean, f3 = pc[2] - mean;
    const float m2 = ((pa[1] + pa[3]) + (pc[1] + pc[3])) + 128.f * ((f0 * f0 + f1 * f1) + (f2 * f2 + f3 * f3));
    const float m1 = rb(mean), r1 = rcp(rb(__builtin_amdgcn_sqrtf(rb(rb(m2 * (1.f / D)) + 1e-5f))));
    float xn[16];
#pragma unroll
    for (int i = 0; i < 8; ++i) {
      const u32 d = pk(xf[2 * i] - m1, xf[2 * i + 1] - m1);
      const u32 t = pk(lo16(d) * r1, hi16(d) * r1);
      xn[2 * i] = lo16(t); xn[2 * i + 1] = hi16(t);
    }
#pragma unroll
    for (int i = 0; i < 16; ++i) asm volatile("" : "+v"(xn[i]));
    tag = __builtin_amdgcn_readfirstlane(epoch) + 1u;
    TS(1);
    await<1>(L, 0);
    __builtin_amdgcn_s_sleep(PRE_MOD);
    TS(21);
    v4u g[8];
    {
      u32 todo = 0xFFu;
      for (u32 spins = 0; spins < SPIN_CAP && todo; ++spins) {
        RELOAD();
        static_for<0, 8>([&](auto I) {
          constexpr int i = I.value;
          if ((todo >> i) & 1u) g[i] = ld128(rs, lane * 16, OFF_MODR + panel * 32768 + (i & 3) * 8192 + (i >> 2) * 4096 + w * 1024);
        });
        todo = 0;
        static_for<0, 8>([&](auto I) { if (__builtin_amdgcn_ballot_w64((g[I.value].y != tag) | (g[I.value].w != tag))) todo |= 1u << I.value; });
      }
    }
    {
      u32 pr[2];
#pragma unroll
      for (int t = 0; t < 2; ++t)
        pr[t] = pk((__uint_as_float(g[4 * t].x) + __uint_as_float(g[4 * t + 1].x)) + (__uint_as_float(g[4 * t + 2].x) + __uint_as_float(g[4 * t + 3].x)),
                   (__uint_as_float(g[4 * t].z) + __uint_as_float(g[4 * t + 1].z)) + (__uint_as_float(g[4 * t + 2].z) + __uint_as_float(g[4 * t + 3].z)));
      *(LAS u32*)(L + L_T + w * TW + lane * 4) = pr[0];
      *(LAS u32*)(L + L_T + w * TW + 256 + lane * 4) = pk(1.f + lo16(pr[1]), 1.f + hi16(pr[1]));
    }
    v4u sav[2], cav[2];
#pragma unroll
    for (int i = 0; i < 2; ++i) {
      sav[i] = *(LAS v4u*)(L + L_T + w * TW + i * 128 + (lane & 7) * 16);
      cav[i] = *(LAS v4u*)(L + L_T + w * TW + 256 + i * 128 + (lane & 7) * 16);
    }
    sig(L, 3, w);
    TS(2);
    const int strip = mem >> 2, r = 8 * (mem & 3) + (lane >> 3);
#pragma unroll
    for (int i = 0; i < 2; ++i) {
      v4u o;
#pragma unroll
      for (int j = 0; j < 4; ++j) {
        const u32 sa = sav[i][j], sc = cav[i][j];
        const u32 t = pk(xn[8 * i + 2 * j] * lo16(sc), xn[8 * i + 2 * j + 1] * hi16(sc));
        o[j] = pk(lo16(t) + lo16(sa), hi16(t) + hi16(sa));
      }
      st128(o, rs, (4 * i + ((lane & 7) >> 1)) * 1024 + (r + 32 * (lane & 1)) * 16, pb + PN_H + strip * 32768 + w * 8192);
    }
    await<4>(L, 3);
    publish(rs, fw + FL_H + strip, (mem & 3) * 4 + w, lane);
  }
  TS(3);

  const int trq = (lane & 15) >> 2, trp = lane & 3, trb = (lane >> 4) & 1;
  const f32x16 zero16 = {0.f, 0.f, 0.f, 0.f, 0.f, 0.f, 0.f, 0.f, 0.f, 0.f, 0.f, 0.f, 0.f, 0.f, 0.f, 0.f};
  u32 phq[16];
  v4u wb[PD];
  {
    const Base3 wnat(L + (8 * hh + trq) * 64 + trb * 32 + trp * 8);
    waitn(rs, fw + FL_H + w, 0xFFFFu, lane);
    TS(4);
    v4u a[32];
    ldfrags<W0>(a, rs, lane * 16, pb + PN_H + w * 32768);
    bar_raw();
    {
      const int hs = (l31 >> 2) & 1;
      const LAS u16* wp = (const LAS u16*)(L + L_CF + 2048) + hs * 256 + 4 * hh * 16 + (l31 & 3) + 4 * (l31 >> 3);
      u32 e[8];
#pragma unroll
      for (int j = 0; j < 8; ++j) e[j] = wp[(8 * (j >> 2) + (j & 3)) * 16];
      const v4u av = {e[0] | (e[1] << 16), e[2] | (e[3] << 16), e[4] | (e[5] << 16), e[6] | (e[7] << 16)};
      const v4u z = {0, 0, 0, 0};
      aphi0 = hs == 0 ? av : z; aphi1 = hs == 1 ? av : z;
      asm volatile("" : "+v"(aphi0), "+v"(aphi1));
    }
    static_for<0, PD>([&](auto U) { wb[U.value] = ldw<256>(wnat.at<slot(P_KV + U.value)>()); });
    SB0();
    f32x16 kacc = zero16, vacc = zero16;
    static_for<0, 64>([&](auto U) {
      constexpr int u = U.value;
      const v4u cur = wb[u % PD];
      if constexpr (u + PD < 64) wb[u % PD] = ldw<256>(wnat.at<slot(P_KV + u + PD)>());
      if constexpr (u & 1) mfma_pin(a[u >> 1], cur, vacc); else mfma_pin(cur, a[u >> 1], kacc);
      if constexpr (!(u & 1) && (u >> 1) + W0 < 32) a[(u >> 1) + W0] = ldfrag<(u >> 1) + W0>(rs, lane * 16, pb + PN_H + w * 32768);
      if constexpr (u == 2 * (32 - W0)) sig(L, 2, w);
      SB0();
    });
    TS(5);
    f32x16 P = mfma(aphi0, pack8<0>(kacc), zero16);
    P = mfma(aphi1, pack8<8>(kacc), P);
    const v4u vb0 = pack8<0>(vacc), vb1 = pack8<8>(vacc);
    u32 phk[16];
    feat(P, phk);
    LAS unsigned char* T = L + L_T + w * TW;
#pragma unroll
    for (int c = 0; c < 4; ++c)
      *(LAS v4u*)(T + lane * 64 + ((c ^ ((lane >> 1) & 3)) * 16)) = v4u{phk[4 * c], phk[4 * c + 1], phk[4 * c + 2], phk[4 * c + 3]};
    const v4u ones = {0x3F803F80u, 0x3F803F80u, 0x3F803F80u, 0x3F803F80u};
    LAS unsigned char* tr = T + (4 * hh + trq) * 64 + (((2 * trb + (trp >> 1)) ^ ((2 * hh + (trq >> 1)) & 3)) * 16) + 8 * (trp & 1);
    const v4u t0 = ldw<512>(tr), t1 = ldw<512>(tr + 1024), t2 = ldw<512>(tr + 2048), t3 = ldw<512>(tr + 3072);
    f32x16 D0 = mfma(t0, trb ? ones : vb0, zero16);
    f32x16 D1 = mfma(t2, trb ? vb0 : ones, zero16);
    D0 = mfma(t1, trb ? ones : vb1, D0);
    D1 = mfma(t3, trb ? vb1 : ones, D1);
    {
      LAS unsigned char* pe = T + lane * 16;
      LAS unsigned char* pz = T + 1024 + (lane >> 4) * 16;
      LAS unsigned char* p0 = trb ? pz : pe;
      LAS unsigned char* p1 = trb ? pe : pz;
#pragma unroll
      for (int g = 0; g < 4; ++g) *(LAS f32x4*)(p0 + g * 1088) = f32x4{D0[4 * g], D0[4 * g + 1], D0[4 * g + 2], D0[4 * g + 3]};
#pragma unroll
      for (int g = 0; g < 4; ++g) *(LAS f32x4*)(p1 + g * 1088) = f32x4{D1[4 * g], D1[4 * g + 1], D1[4 * g + 2], D1[4 * g + 3]};
    }
    TS(6);
    MARK(H_C0, w, 1u);
    reach<0>(L, H_C0, 1u, 1u, 1u, 1u);
    {
      f32x4 e[4], z[4];
#pragma unroll
      for (int v = 0; v < 4; ++v) {
        e[v] = *(LAS f32x4*)(L + L_T + v * TW + w * 1088 + lane * 16);
        z[v] = *(LAS f32x4*)(L + L_T + v * TW + w * 1088 + 1024 + (lane & 3) * 16);
      }
      SB0();
      const int idx = (mem * 4 + w) * 16 + panel;
      st128(__builtin_bit_cast(v4u, (e[0] + e[1]) + (e[2] + e[3])), rs, lane * 16, OFF_KVE + idx * 1024);
      st128(__builtin_bit_cast(v4u, (z[0] + z[1]) + (z[2] + z[3])), rs, lane < 4 ? lane * 16 : WS_BYTES, OFF_KVZ + idx * 64);
      publish(rs, FL_KV + mem * 2 + (w >> 1), (w & 1) * 16 + panel, lane);
      MARK(H_C0, w, 2u);
      reach<1>(L, H_P0, 1u, 1u, 0u, 0u);
      static_for<0, PD>([&](auto U) { wb[U.value] = ldw<256>(wnat.at<slot(P_Q + U.value)>()); });
    }
    TS(7);
    f32x16 qacc = zero16;
    static_for<0, 32>([&](auto U) {
      constexpr int u = U.value;
      const v4u cur = wb[u % PD];
      if constexpr (u + PD < 32) wb[u % PD] = ldw<256>(wnat.at<slot(P_Q + u + PD)>());
      mfma_pin(cur, a[u], qacc);
      SB0();
    });
    MARK(H_C0, w, 3u);
    P = mfma(aphi0, pack8<0>(qacc), zero16);
    P = mfma(aphi1, pack8<8>(qacc), P);
    feat(P, phq);
#pragma unroll
    for (int i = 0; i < 16; ++i) asm volatile("" : "+v"(phq[i]));
  }
  v2u xres[4];
  {
    const u16* xr = p.x + (size_t)(panel * 128 + strip2 * 32 + l31) * D + ncol + 4 * hh;
#pragma unroll
    for (int i = 0; i < 4; ++i) xres[i] = *(const GAS v2u*)(xr + 8 * i);
  }
  TS(8);
  reach<1>(L, H_P0, 0u, 0u, 1u, 1u);
  TS(9);
  {
    const bool on = (l31 >> 4) == hh;
    const v4u zv = {0, 0, 0, 0};
    f32x16 num = zero16, den = zero16;
    v4u ak[4], az[4];
#pragma unroll
    for (int s4 = 0; s4 < 4; ++s4) { az[s4] = *(LAS v4u*)(L + L_ZF + s4 * 32 + hh * 16); ak[s4] = *(LAS v4u*)(L + L_KVF + s4 * 512 + l31 * 16); }
    SB0();
#pragma unroll
    for (int s4 = 0; s4 < 4; ++s4) den = mfma(on ? az[s4] : zv, v4u{phq[4 * s4], phq[4 * s4 + 1], phq[4 * s4 + 2], phq[4 * s4 + 3]}, den);
#pragma unroll
    for (int s4 = 0; s4 < 4; ++s4) num = mfma(on ? ak[s4] : zv, v4u{phq[4 * s4], phq[4 * s4 + 1], phq[4 * s4 + 2], phq[4 * s4 + 3]}, num);
    const float d0 = rcp(rb(rb(den[0]) + 1e-6f)), d1 = rcp(rb(rb(den[8]) + 1e-6f));
    const v4u n0 = pack8<0>(num), n1 = pack8<8>(num);
    v4u o0, o1;
#pragma unroll
    for (int j = 0; j < 4; ++j) { o0[j] = pk(lo16(n0[j]) * d0, hi16(n0[j]) * d0); o1[j] = pk(lo16(n1[j]) * d1, hi16(n1[j]) * d1); }
    st128(o0, rs, lane * 16, pb + PN_O + w * 32768 + (2 * mem) * 1024);
    st128(o1, rs, lane * 16 + 1024, pb + PN_O + w * 32768 + (2 * mem) * 1024);
  }
#define WDN_BASES() const Base3 wown(L + (4 * hh + trq) * 64 + trb * 32 + trp * 8 + 3 * kh * 1024), woth(L + (4 * hh + trq) * 64 + trb * 32 + trp * 8 + (kh + 1) * 1024)
#define WDN(base, u) ldw<512>((((u) & 1) ? wown : woth).template at<slot((base) + 4 * ((u) >> 1))>())
  u32 x1p[8];
  {
    WDN_BASES();
    publish(rs, fw + FL_O + w, mem, lane);
    reach<0>(L, H_P0, 2u, 2u, 0u, 0u);
    static_for<0, PD>([&](auto U) { wb[U.value] = WDN(P_WO, U.value); });
    TS(10);
    waitn(rs, fw + FL_O + strip2, 0xFFu << (kh * 8), lane);
    TS(11);
    v4u a[16];
    ldfrags<16>(a, rs, lane * 16, pb + PN_O + strip2 * 32768 + kh * 16384);
    sig(L, 4, w);
    reach<0>(L, H_P0, 0u, 0u, 2u, 2u);
    f32x4 ga[4], sc[4];
#pragma unroll
    for (int g = 0; g < 4; ++g) {
      ga[g] = *(LAS f32x4*)(L + L_VEC + (kh * 32 + 8 * g + 4 * hh) * 4);
      sc[g] = *(LAS f32x4*)(L + L_VEC + 256 + (kh * 32 + 8 * g + 4 * hh) * 4);
    }
    SB0();
    f32x16 c0 = zero16, c1 = zero16;
    static_for<0, 32>([&](auto U) {
      constexpr int u = U.value;
      const v4u cur = wb[u % PD];
      if constexpr (u + PD < 32) wb[u % PD] = WDN(P_WO, u + PD);
      if constexpr (u & 1) mfma_pin(cur, a[u >> 1], c0); else mfma_pin(cur, a[u >> 1], c1);
      SB0();
    });
#pragma unroll
    for (int g = 0; g < 4; ++g) *(LAS f32x4*)(L + L_T + w * TW + g * 1024 + lane * 16) = f32x4{c1[4 * g], c1[4 * g + 1], c1[4 * g + 2], c1[4 * g + 3]};
    TS(12);
    MARK(H_C0, w, 4u);
    reach1(L, H_C0, w ^ 2, 4u);
    f32x4 o[4];
#pragma unroll
    for (int g = 0; g < 4; ++g) o[g] = *(LAS f32x4*)(L + L_T + (w ^ 2) * TW + g * 1024 + lane * 16);
    SB0();
    float x1[16], sp[4];
    u32 yp[8];
#pragma unroll
    for (int g = 0; g < 4; ++g) {
#pragma unroll
      for (int j = 0; j < 2; ++j) {
        const u32 r = pk(lo16(xres[g][j]) + ga[g][2 * j] * (c0[4 * g + 2 * j] + o[g][2 * j]),
                         hi16(xres[g][j]) + ga[g][2 * j + 1] * (c0[4 * g + 2 * j + 1] + o[g][2 * j + 1]));
        x1p[2 * g + j] = r;
        x1[4 * g + 2 * j] = lo16(r); x1[4 * g + 2 * j + 1] = hi16(r);
        yp[2 * g + j] = pk(lo16(r) * sc[g][2 * j], hi16(r) * sc[g][2 * j + 1]);
      }
    }
    const int tile = 2 * colg + kh;
    st128(v4u{yp[0], yp[1], yp[2], yp[3]}, rs, lane * 16, pb + PN_X1 + strip2 * 32768 + tile * 2048);
    st128(v4u{yp[4], yp[5], yp[6], yp[7]}, rs, lane * 16 + 1024, pb + PN_X1 + strip2 * 32768 + tile * 2048);
    SB0();
#pragma unroll
    for (int g = 0; g < 4; ++g) sp[g] = (x1[4 * g] + x1[4 * g + 1]) + (x1[4 * g + 2] + x1[4 * g + 3]);
    const float ss = (sp[0] + sp[1]) + (sp[2] + sp[3]);
    const float mt = fold32(ss, ss) * (1.f / 32.f);
    float q[4];
#pragma unroll
    for (int g = 0; g < 4; ++g) {
      const float d0 = x1[4 * g] - mt, d1 = x1[4 * g + 1] - mt, d2 = x1[4 * g + 2] - mt, d3 = x1[4 * g + 3] - mt;
      q[g] = (d0 * d0 + d1 * d1) + (d2 * d2 + d3 * d3);
    }
    const float qs = (q[0] + q[1]) + (q[2] + q[3]);
    const float qt = fold32(qs, qs);
    v4u sv = {__float_as_uint(mt), tag, __float_as_uint(qt), tag};
    asm volatile("" : "+v"(sv));
    publish(rs, fw + FL_X + strip2, tile, lane);
    st128(sv, rs, lane < 32 ? lane * 16 : WS_BYTES, pb + PN_ST + strip2 * 8192 + tile * 512);
  }
  asm volatile("" : "+v"(x1p[0]), "+v"(x1p[1]), "+v"(x1p[2]), "+v"(x1p[3]), "+v"(x1p[4]), "+v"(x1p[5]), "+v"(x1p[6]), "+v"(x1p[7]));
#define GU_S(u) ((u) < 96 ? (u) / 6 : 16 + (((u) - 96) % 32) / 2)
#define GU_I(u) ((u) < 96 ? (u) % 6 : 2 * (((u) - 96) / 32) + ((u) & 1))
#define WGU(u) ldw<512>((((u) & 1) ? wy : wx).template at<slot(P_GU + ((u) & ~1))>())
  {
    const Base3 wx(L + (4 * hh + trq) * 64 + trp * 8 + trb * (1024 + 32)), wy(L + (4 * hh + trq) * 64 + trp * 8 + (trb ? 1024 : 32));
    reach<0>(L, H_P0, 3u, 3u, 0u, 0u);
    static_for<0, PD>([&](auto U) { wb[U.value] = WGU(U.value); });
    TS(13);
    waitn(rs, fw + FL_X + w, 0xFFFFu, lane);
    TS(14);
    constexpr int AW = 20;
    v4u a[AW];
    ldfrags<8>(a, rs, lane * 16, pb + PN_X1 + w * 32768);
    f32x16 acc[6];
#pragma unroll
    for (int i = 0; i < 6; ++i) acc[i] = zero16;
    float rr = 0.f;
    v4u bcor = {0, 0, 0, 0};
    v4u ho, cf0, cf1, pp;
    float t0 = 0.f, t1 = 0.f;
    const int go = pb + PN_G + (w * 96 + mem) * 1024, fi = fw + FL_G + strip2 * 3, gi = pb + PN_G + (strip2 * 96 + kh * 48) * 1024;
    u32 fl = 0;
    auto epi = [&](auto Ic, auto Kc) {
      constexpr int I = decltype(Ic)::value, k = decltype(Kc)::value, j = k >> 1;
      const f32x16& A = acc[I];
      if constexpr ((k & 1) == 0) { t0 = A[2 * j] * rr; t1 = A[2 * j + 1] * rr; asm volatile("" : "+v"(t0)); t0 *= A[8 + 2 * j]; }
      else {
        t1 *= A[9 + 2 * j];
        ho[j] = pk(t0, t1);
        if constexpr (j == 3) st128(ho, rs, (I < 4 || three) ? lane * 16 : WS_BYTES, go + ((I & 1) * 48 + (I >> 1) * 16) * 1024);
      }
    };
    static_for<0, 192>([&](auto Uc) {
      constexpr int u = decltype(Uc)::value, s = GU_S(u), i = GU_I(u), q = (u - 96) % 32;
      if constexpr (u == 6) TS(22);
      if constexpr (u == 60) TS(23);
      if constexpr (u == 96) TS(24);
      if constexpr (u == 128) TS(25);
      if constexpr (u == 160) TS(26);
      if constexpr (u == 56) mark(L, H_CP, w, 2u);
      if constexpr (u == 96 || u == 128 || u == 160) mark(L, H_CP, w, u / 32);
      if constexpr (u == 60 || u == 92 || u == 112) pp = peek(L, H_PP);
      if constexpr (u == 64) need_pp<false>(L, pp, 1u);
      if constexpr (u == 96) need_pp<false>(L, pp, 3u);
      if constexpr (u == 116) need_pp<true>(L, pp, 5u);
      const v4u cur = wb[u % PD];
      if constexpr (u + PD < 192) wb[u % PD] = WGU(u + PD);
      if constexpr (u >= 96 && q == 24) cf0 = *(LAS v4u*)(L + L_CF + i * 512 + l31 * 16);
      if constexpr (u >= 96 && q == 25) cf1 = *(LAS v4u*)(L + L_CF + i * 512 + l31 * 16);
      mfma_pin(cur, a[s % AW], acc[i]);
      if constexpr (u < 96 && i == 5 && s + AW < 32) a[s % AW] = ldfrag<s + AW>(rs, lane * 16, pb + PN_X1 + w * 32768);
      if constexpr (u < 2 * (AW - 8) && (u & 1)) a[8 + (u >> 1)] = ldfrag<8 + (u >> 1)>(rs, lane * 16, pb + PN_X1 + w * 32768);
      if constexpr (u == 2 * (AW - 8)) mark(L, H_CP, w, 1u);
      if constexpr (u == 117) bcor = *(LAS v4u*)(L + L_KVF + w * 512 + l31 * 16);
      if constexpr (u == 123) { rr = __uint_as_float(bcor.w); bcor = v4u{hh ? 0u : bcor.x, hh ? 0u : bcor.y, hh ? 0u : bcor.z, 0u}; }
      if constexpr (u >= 96 && q == 30) mfma_pin(cf0, bcor, acc[i]);
      if constexpr (u >= 96 && q == 31) mfma_pin(cf1, bcor, acc[i]);
      if constexpr (u >= 128 && q >= 1 && q < 9) {
        epi(IC<(i & 6) - 2 + (q - 1) / 4>{}, IC<2 * ((q - 1) % 4)>{});
        epi(IC<(i & 6) - 2 + (q - 1) / 4>{}, IC<2 * ((q - 1) % 4) + 1>{});
      }
      if constexpr (u >= 128 && q == GPUB) publish(rs, fw + FL_G + w * 3 + (i >> 1) - 1, mem, lane);
      if constexpr (u == GASK) fl = ask(rs, fi, lane);
      if constexpr (u == 184) pp = peek(L, H_PP);
      SB0();
    });
    mark(L, H_CP, w, 6u);
    TS(15);
    v4u g[43];
    poll(fl, rs, fi, 0xFFFFu, lane);
    ldfrags<W0>(g, rs, lane * 16, gi);
    TS(16);
    need_pp<true>(L, pp, 6u);
    WDN_BASES();
    static_for<0, PD>([&](auto U) { wb[U.value] = WDN(P_WD, U.value); });
    static_for<0, 16>([&](auto Kc) { epi(IC<4 + Kc.value / 8>{}, IC<Kc.value % 8>{}); });
    SB0();
    fl = ask(rs, fi + 1, lane);
    SB0();
    TS(18);
    f32x16 c0 = zero16, c1 = zero16;
    static_for<0, 86>([&](auto U) {
      constexpr int u = U.value;
      if constexpr (u == 18) mark(L, H_CP, w, 7u);
      if constexpr (u == PUB2) {
        asm volatile("s_waitcnt vmcnt(%0)" ::"i"(1 + PUB2 / 2) : "memory");
        (void)__builtin_amdgcn_raw_ptr_buffer_atomic_add_i32(1 << mem, rs, lane == 0 ? 0 : WS_BYTES, OFF_ARR + (fw + FL_G + w * 3 + 2) * 128, 0);
      }
      if constexpr (u == 24 || u == 40 || u == 56) pp = peek(L, H_PP);
      if constexpr (u == 28 || u == 44 || u == 60) need_pp<true>(L, pp, 7u + (u - 28) / 16);
      if constexpr (u == 2 * (16 - W0)) poll(fl, rs, fi + 1, 0xFFFFu, lane);
      if constexpr (u == 2 * (32 - W0) - 26) fl = ask(rs, fi + 2, lane);
      if constexpr (u == 2 * (32 - W0)) poll(fl, rs, fi + 2, 0x7FFu, lane);
      const v4u cur = wb[u % PD];
      if constexpr (u + PD < 86) wb[u % PD] = WDN(P_WD, u + PD);
      if constexpr (u & 1) mfma_pin(cur, g[u >> 1], c0); else mfma_pin(cur, g[u >> 1], c1);
      if constexpr (!(u & 1) && (u >> 1) + W0 < 43) g[(u >> 1) + W0] = ldfrag<(u >> 1) + W0>(rs, lane * 16, gi);
      SB0();
    });
#pragma unroll
    for (int g = 0; g < 4; ++g) *(LAS f32x4*)(L + L_T + w * TW + g * 1024 + lane * 16) = f32x4{c1[4 * g], c1[4 * g + 1], c1[4 * g + 2], c1[4 * g + 3]};
    f32x4 gb[4];
#pragma unroll
    for (int g = 0; g < 4; ++g) gb[g] = *(LAS f32x4*)(L + L_VEC + 512 + (kh * 32 + 8 * g + 4 * hh) * 4);
    TS(19);
    MARK(H_C0, w, 5u);
    reach1(L, H_C0, w ^ 2, 5u);
    f32x4 o[4];
#pragma unroll
    for (int g = 0; g < 4; ++g) o[g] = *(LAS f32x4*)(L + L_T + (w ^ 2) * TW + g * 1024 + lane * 16);
    SB0();
    u16* orow = p.out + (size_t)(panel * 128 + strip2 * 32 + l31) * D + ncol + 4 * hh;
#pragma unroll
    for (int g = 0; g < 4; ++g) {
      v2u r;
#pragma unroll
      for (int j = 0; j < 2; ++j) {
        r[j] = pk(lo16(x1p[2 * g + j]) + gb[g][2 * j] * (c0[4 * g + 2 * j] + o[g][2 * j]),
                  hi16(x1p[2 * g + j]) + gb[g][2 * j + 1] * (c0[4 * g + 2 * j + 1] + o[g][2 * j + 1]));
      }
      *(GAS v2u*)(orow + 8 * g) = r;
    }
  }
  if (b == 0 && tid == 0) __hip_atomic_store((GAS u32*)(ws + OFF_EPOCH), tag, RLX_AGENT);
}
}

extern "C" void kernel_launch(void* const* d_in, const int* in_sizes, int n_in, void* d_out, int out_size, void* d_ws,
                              size_t ws_size, hipStream_t stream) {
  using namespace mk;
  Params p;
  p.x = (const u16*)d_in[0]; p.wqkv = (const u16*)d_in[1]; p.wphi = (const u16*)d_in[2]; p.wo = (const u16*)d_in[3];
  p.wg = (const u16*)d_in[5]; p.wu = (const u16*)d_in[6]; p.wd = (const u16*)d_in[7]; p.c = (const u16*)d_in[8];
  p.wmod = (const u16*)d_in[9]; p.out = (u16*)d_out; p.ws = (unsigned char*)d_ws;
  (void)hipFuncSetAttribute((const void*)mega, hipFuncAttributeMaxDynamicSharedMemorySize, LDS_BYTES);
  mega<<<256, 512, LDS_BYTES, stream>>>(p);
}
```

```cpp
#define OAUX 2
#include <hip/hip_runtime.h>

namespace mk {
typedef unsigned short u16;
typedef unsigned int u32;
typedef unsigned long long u64;
typedef u32 v4u __attribute__((ext_vector_type(4)));
typedef u32 v2u __attribute__((ext_vector_type(2)));
typedef float f32x2 __attribute__((ext_vector_type(2)));
typedef float f32x4 __attribute__((ext_vector_type(4)));
typedef float f32x16 __attribute__((ext_vector_type(16)));
typedef __bf16 bf16x2 __attribute__((ext_vector_type(2)));
typedef __bf16 bf16x8 __attribute__((ext_vector_type(8)));
typedef short s16x4 __attribute__((ext_vector_type(4)));
#define LAS __attribute__((address_space(3)))
#define GAS __attribute__((address_space(1)))
#define DI __device__ __forceinline__
#define RLX_AGENT __ATOMIC_RELAXED, __HIP_MEMORY_SCOPE_AGENT
#define SB0() __builtin_amdgcn_sched_barrier(0)
typedef __amdgpu_buffer_rsrc_t rsrc_t;

constexpr int D = 512, F = 1376;
constexpr int OFF_EPOCH = 0;
constexpr int OFF_MODG = 64 << 10;
constexpr int OFF_KVZ = 128 << 10;
constexpr int OFF_CG = 192 << 10;
constexpr int CG_PART = 2 * F * 16;
constexpr int OFF_ARR = 384 << 10;
constexpr int OFF_MODR = 512 << 10;
constexpr int OFF_KVE = 1 << 20;
constexpr int OFF_PAN = 2 << 20;
constexpr int PN_H = 0;
constexpr int PN_O = 128 << 10;
constexpr int PN_X1 = 256 << 10;
constexpr int PN_G = 384 << 10;
constexpr int PN_ST = 768 << 10;
constexpr int PN_FL = 800 << 10;
constexpr int FL_H = 0, FL_O = 4, FL_X = 8, FL_G = 12, FL_KV = 384;
constexpr int PSTR = (848 << 10) + 4224;
constexpr int WS_BYTES = 16 << 20;
constexpr int OFF_TS = 32 << 20;
constexpr int RING = 136;
constexpr int L_T = RING * 1024, TW = 4352;
constexpr int L_KVF = L_T + 4 * TW;
constexpr int L_ZF = L_KVF + 2048;
constexpr int L_VEC = L_ZF + 256;
constexpr int L_CF = L_VEC + 768;
constexpr int L_HINT = L_CF + 3072;
constexpr int LDS_BYTES = L_HINT + 256;
static_assert(LDS_BYTES <= 163840, "LDS");
constexpr int P_KV = 0, P_Q = 64, P_WO = 96, P_GU = 160, P_WD = 352;
constexpr int H_CP = 6, H_PP = 7, H_C0 = 8, H_P0 = 9;
constexpr int pair_of(int t) { return ((t & 15) < 11 ? 3 * (t & 15) : 2 * (t & 15) + 11) + (t >> 4); }
constexpr int slot(int n) { return (n % RING) * 1024; }
constexpr int AUX_SC1 = 16;
constexpr u32 SPIN_CAP = 1u << 16;
#define PRE_MOD 8
#define HI_DELAY 8
#define POLL_GAP 6
#define PRE_FL 4
#define KV1 32
#define PGU 4
#define PKV 0
#define GPUB 26
#define LPRIO 0
#define APRIO 3
#define TMOD 0
#define CW 1
#define DW 2
#define CSLP 3
#define PG2 2
#define TLBT 0
#define LITE 1
#define EQ 1
#define PQE 2
#define GASK 158
#define PUB2 24
#define WG0 16
#define GU0 12
#define GLA 16
#define GA2 18
#define GU2 50
#define W0 12
#define PDV 5
#define WPC 0
#define WPACE() do { if (WPC) { SB0(); __builtin_amdgcn_s_sleep(WPC); SB0(); } } while (0)
constexpr int PD = PDV;
constexpr int CODE_BYTES = 47000, CODE_KB = 48;

#define NBV(x) (x)
struct Params {
  const u16 *x, *wqkv, *wphi, *wo, *wg, *wu, *wd, *c, *wmod;
  u16* out;
  unsigned char* ws;
};

template <int V> struct IC { static constexpr int value = V; };
template <int I, int N, class Fn> DI void static_for(Fn&& f) {
  if constexpr (I < N) { f(IC<I>{}); static_for<I + 1, N>(f); }
}
DI float lo16(u32 v) { return __uint_as_float(v << 16); }
DI float hi16(u32 v) { return __uint_as_float(v & 0xffff0000u); }
DI u32 pk(float a, float b) { f32x2 v = {a, b}; return __builtin_bit_cast(u32, __builtin_convertvector(v, bf16x2)); }
DI float rb(float a) { return lo16(pk(a, 0.f)); }
DI float rcp(float a) { return __builtin_amdgcn_rcpf(a); }
DI f32x16 mfma(v4u a, v4u b, f32x16 c) {
  return __builtin_amdgcn_mfma_f32_32x32x16_bf16(__builtin_bit_cast(bf16x8, a), __builtin_bit_cast(bf16x8, b), c, 0, 0, 0);
}
DI void mfma_pin(v4u a, v4u b, f32x16& c) {
  c = mfma(a, b, c);
  asm volatile("" : "+v"(c));
}
template <int O> DI v4u pack8(const f32x16& x) {
  v4u r; r.x = pk(x[O], x[O + 1]); r.y = pk(x[O + 2], x[O + 3]); r.z = pk(x[O + 4], x[O + 5]); r.w = pk(x[O + 6], x[O + 7]); return r;
}
template <int T> DI v4u ldw(LAS unsigned char* p) {
  s16x4 a = __builtin_amdgcn_ds_read_tr16_b64_v4i16((LAS s16x4*)p);
  s16x4 b = __builtin_amdgcn_ds_read_tr16_b64_v4i16((LAS s16x4*)(p + T));
  return __builtin_bit_cast(v4u, __builtin_shufflevector(a, b, 0, 1, 2, 3, 4, 5, 6, 7));
}
struct Base3 {
  u32 b[3];
  DI explicit Base3(LAS unsigned char* p) {
    b[0] = (u32)(size_t)p; b[1] = b[0] + 65536u; b[2] = b[0] + 131072u;
    asm volatile("" : "+v"(b[0]), "+v"(b[1]), "+v"(b[2]));
  }
  template <int OFF> DI LAS unsigned char* at() const { return (LAS unsigned char*)(size_t)b[OFF >> 16] + (OFF & 65535); }
};
template <int LOFF, int SOFF> DI void gldsi(rsrc_t rs, u32 voff, u32 sbase, u32 lbase) {
  u32 t;
  asm volatile("s_add_u32 m0, %1, %2\n\ts_add_u32 %0, %3, %4\n\tbuffer_load_dwordx4 %5, %6, %0 offen lds"
               : "=&s"(t) : "s"(lbase), "i"(LOFF), "s"(sbase), "i"(SOFF), "v"(voff), "s"(rs) : "m0", "scc", "memory");
}
DI void bar() { asm volatile("s_waitcnt lgkmcnt(0)\n\ts_barrier" ::: "memory"); }
DI void bar_raw() { asm volatile("s_barrier" ::: "memory"); }
DI void drain() { asm volatile("s_waitcnt vmcnt(0)" ::: "memory"); }
#define VMCNT(n) asm volatile("s_waitcnt vmcnt(" #n ")" ::: "memory")
#define RELOAD() asm volatile("" ::: "memory")
DI v4u ld128(rsrc_t rs, int voff, int soff) { return __builtin_amdgcn_raw_buffer_load_b128(rs, voff, soff, AUX_SC1); }
DI v2u ld64(rsrc_t rs, int voff, int soff) { return __builtin_amdgcn_raw_buffer_load_b64(rs, voff, soff, AUX_SC1); }
DI void st128(v4u v, rsrc_t rs, int voff, int soff) { __builtin_amdgcn_raw_buffer_store_b128(v, rs, voff + soff, 0, AUX_SC1); }
DI void st64(v2u v, rsrc_t rs, int voff, int soff) { __builtin_amdgcn_raw_buffer_store_b64(v, rs, voff, soff, AUX_SC1); }
template <int S> DI v4u ldfrag(rsrc_t rs, int voff, int soff) { return ld128(rs, voff + (S & 3) * 1024, soff + (S >> 2) * 4096); }
template <int W, int N> DI void ldfrags(v4u (&a)[N], rsrc_t rs, int voff, int soff) {
  static_for<0, W>([&](auto S) { a[S.value] = ldfrag<S.value>(rs, voff, soff); SB0(); });
}
template <int CTRL> DI float dpp_add(float v) {
  return v + __uint_as_float(__builtin_amdgcn_update_dpp(0u, __float_as_uint(v), CTRL, 0xf, 0xf, false));
}
constexpr int DPP_X1 = 0xB1, DPP_X2 = 0x4E, DPP_HM = 0x141, DPP_ROR4 = 0x124, DPP_ROR8 = 0x128;
DI float fold32(float x, float y) {
  auto r = __builtin_amdgcn_permlane32_swap(__float_as_uint(x), __float_as_uint(y), false, false);
  return __uint_as_float(r[0]) + __uint_as_float(r[1]);
}
DI float fold16(float x, float y) {
  auto r = __builtin_amdgcn_permlane16_swap(__float_as_uint(x), __float_as_uint(y), false, false);
  return __uint_as_float(r[0]) + __uint_as_float(r[1]);
}
DI void red8q(const float (&a)[8], float& c0, float& c1) {
  const float b0 = fold32(a[0], a[4]), b1 = fold32(a[1], a[5]), b2 = fold32(a[2], a[6]), b3 = fold32(a[3], a[7]);
  c0 = fold16(b0, b2); c1 = fold16(b1, b3);
  c0 = dpp_add<DPP_ROR4>(c0); c1 = dpp_add<DPP_ROR4>(c1);
  c0 = dpp_add<DPP_ROR8>(c0); c1 = dpp_add<DPP_ROR8>(c1);
}
DI void red8(const float (&a)[8], float& c0, float& c1) {
  red8q(a, c0, c1);
  c0 = dpp_add<DPP_X1>(c0); c1 = dpp_add<DPP_X1>(c1);
  c0 = dpp_add<DPP_X2>(c0); c1 = dpp_add<DPP_X2>(c1);
}
DI void publish(rsrc_t rs, int word, int bit, int lane) {
  drain();
  (void)__builtin_amdgcn_raw_ptr_buffer_atomic_add_i32(1 << bit, rs, lane == 0 ? 0 : WS_BYTES, OFF_ARR + word * 128, 0);
}
DI u32 ask(rsrc_t rs, int word, int lane) { return __builtin_amdgcn_raw_ptr_buffer_atomic_add_i32(0, rs, lane == 0 ? 0 : WS_BYTES, OFF_ARR + word * 128, 0); }
DI void spin(rsrc_t rs, int word, u32 m, int lane) {
#pragma nounroll
  for (u32 spins = 0; spins < SPIN_CAP; ++spins)
    if ((__builtin_amdgcn_readfirstlane(ask(rs, word, lane)) & m) == m) break;
}
DI void poll(u32 f, rsrc_t rs, int word, u32 m, int lane) {
  if (__builtin_expect((__builtin_amdgcn_readfirstlane(f) & m) != m, 0)) spin(rs, word, m, lane);
  __builtin_amdgcn_fence(__ATOMIC_ACQUIRE, "wavefront");
  RELOAD();
}
DI void waitn(rsrc_t rs, int word, u32 m, int lane) {
  if (PRE_FL) __builtin_amdgcn_s_sleep(PRE_FL);
  spin(rs, word, m, lane);
  __builtin_amdgcn_fence(__ATOMIC_ACQUIRE, "wavefront");
  RELOAD();
}
DI void sig(LAS unsigned char* L, int i, int k) { *(volatile LAS u32*)(L + L_HINT + 16 * i + 4 * k) = 1u; }
template <int N> DI void await(LAS unsigned char* L, int i) {
#pragma nounroll
  for (u32 spins = 0; spins < (1u << 20); ++spins) {
    const v4u v = *(volatile LAS v4u*)(L + L_HINT + 16 * i);
    if (__builtin_amdgcn_readfirstlane(N == 1 ? v.x : (v.x & v.y & v.z & v.w))) break;
    __builtin_amdgcn_s_sleep(1);
  }
  RELOAD();
}
DI void mark(LAS unsigned char* L, int i, int k, u32 val) { *(volatile LAS u32*)(L + L_HINT + 16 * i + 4 * k) = val; }
DI v4u peek(LAS unsigned char* L, int i) { return *(volatile LAS v4u*)(L + L_HINT + 16 * i); }
DI u32 umin(u32 a, u32 b) { return a < b ? a : b; }
DI void await_cp(LAS unsigned char* L, u32 k) {
#pragma nounroll
  for (u32 spins = 0; spins < (1u << 20); ++spins) {
    const v4u v = peek(L, H_CP);
    if (__builtin_amdgcn_readfirstlane(umin(umin(v.x, v.y), umin(v.z, v.w))) >= k) break;
    __builtin_amdgcn_s_sleep(CSLP);
  }
  RELOAD();
}
template <int SLP> DI void reach(LAS unsigned char* L, int i, u32 k0, u32 k1, u32 k2, u32 k3) {
#pragma nounroll
  for (u32 spins = 0; spins < (1u << 20); ++spins) {
    const v4u v = peek(L, i);
    if (__builtin_amdgcn_readfirstlane((v.x >= k0) & (v.y >= k1) & (v.z >= k2) & (v.w >= k3))) break;
    if (SLP) __builtin_amdgcn_s_sleep(SLP);
  }
  RELOAD();
}
DI void reach1(LAS unsigned char* L, int i, int k, u32 val) {
#pragma nounroll
  for (u32 spins = 0; spins < (1u << 20); ++spins)
    if (__builtin_amdgcn_readfirstlane(*(volatile LAS u32*)(L + L_HINT + 16 * i + 4 * k)) >= val) break;
  RELOAD();
}
#define MARK(i, k, val) do { RELOAD(); mark(L, i, k, val); RELOAD(); } while (0)
template <bool AUX> DI u32 pp_min(v4u v) {
  u32 m = umin(v.x, v.y);
  if (AUX) m = umin(m, umin(v.z, v.w));
  return __builtin_amdgcn_readfirstlane(m);
}
template <bool AUX> DI void need_pp(LAS unsigned char* L, v4u v, u32 k) {
  if (__builtin_expect(pp_min<AUX>(v) < k, 0)) {
#pragma nounroll
    for (u32 spins = 0; spins < (1u << 20); ++spins)
      if (pp_min<AUX>(peek(L, H_PP)) >= k) break;
  }
  RELOAD();
}
DI void feat(const f32x16& P, u32 (&ph)[16]) {
#pragma clang fp contract(off)
  constexpr float L2E = 1.4426950408889634f;
  f32x2 p[8];
#pragma unroll
  for (int i = 0; i < 8; ++i) { const u32 t = pk(P[2 * i], P[2 * i + 1]); p[i] = f32x2{lo16(t), hi16(t)} * L2E; }
  float mx[4], mn[4];
#pragma unroll
  for (int i = 0; i < 4; ++i) {
    mx[i] = fmaxf(fmaxf(p[2 * i].x, p[2 * i].y), fmaxf(p[2 * i + 1].x, p[2 * i + 1].y));
    mn[i] = fminf(fminf(p[2 * i].x, p[2 * i].y), fminf(p[2 * i + 1].x, p[2 * i + 1].y));
  }
  const float mxl = fmaxf(fmaxf(mx[0], mx[1]), fmaxf(mx[2], mx[3])), mnl = fminf(fminf(mn[0], mn[1]), fminf(mn[2], mn[3]));
  f32x2 e1[8], e2[8];
#pragma unroll
  for (int i = 0; i < 8; ++i) { e1[i] = p[i] - mxl; e2[i] = mnl - p[i]; }
  SB0();
#pragma unroll
  for (int i = 0; i < 8; ++i) {
    e1[i] = f32x2{__builtin_amdgcn_exp2f(e1[i].x), __builtin_amdgcn_exp2f(e1[i].y)};
    e2[i] = f32x2{__builtin_amdgcn_exp2f(e2[i].x), __builtin_amdgcn_exp2f(e2[i].y)};
  }
  const f32x2 s1 = ((e1[0] + e1[1]) + (e1[2] + e1[3])) + ((e1[4] + e1[5]) + (e1[6] + e1[7]));
  const f32x2 s2 = ((e2[0] + e2[1]) + (e2[2] + e2[3])) + ((e2[4] + e2[5]) + (e2[6] + e2[7]));
  const float r1 = rcp(s1.x + s1.y), r2 = rcp(s2.x + s2.y);
#pragma unroll
  for (int i = 0; i < 8; ++i) { const f32x2 u = e1[i] * r1, v = e2[i] * r2; ph[i] = pk(u.x, u.y); ph[8 + i] = pk(v.x, v.y); }
}

#define TSCLK() __builtin_amdgcn_s_memrealtime()
#define TS(i) do {} while (0)
#define TS_DUMP() do {} while (0)
#define TS_INIT() do {} while (0)

__global__ void __launch_bounds__(512, 2) mega(Params p) {
  extern __shared__ __attribute__((aligned(16))) unsigned char smem[];
  LAS unsigned char* L = (LAS unsigned char*)smem;
  TS_INIT();
  TS(0);
  u64 pc0;
  asm volatile("s_getpc_b64 %0" : "=s"(pc0));
  const int tid = threadIdx.x, lane = tid & 63;
  int w = __builtin_amdgcn_readfirstlane(tid >> 6);
  const int b = blockIdx.x, xcd = b & 7, rank = b >> 3, panel = (xcd >> 1) * 4 + (rank >> 3), mem = (xcd & 1) * 8 + (rank & 7);
  const int hh = lane >> 5, l31 = lane & 31;
  unsigned char* ws = p.ws;
  const int rowh = mem & 1, colg = mem >> 1;
  const int pair0 = min(3 * mem, 2 * mem + 11);
  const bool three = mem < 11;
  const rsrc_t rs = __builtin_amdgcn_make_buffer_rsrc(ws, 0, WS_BYTES, 0x00020000);
  const int pb = OFF_PAN + panel * PSTR, fw = panel * 24;
  const int mcg = xcd * 8 + (rank >> 2), mkp = rank & 3;
  const rsrc_t rqkv = __builtin_amdgcn_make_buffer_rsrc((void*)p.wqkv, 0, D * 3 * D * 2, 0x00020000);
  const rsrc_t rwo = __builtin_amdgcn_make_buffer_rsrc((void*)p.wo, 0, D * D * 2, 0x00020000);
  const rsrc_t rgu = __builtin_amdgcn_make_buffer_rsrc((void*)((w & 1) ? p.wu : p.wg), 0, D * F * 2, 0x00020000);
  const rsrc_t rwd = __builtin_amdgcn_make_buffer_rsrc((void*)p.wd, 0, F * D * 2, 0x00020000);

  v4u r0[4];
  u32 cv0[4], epoch;
  u32 vq = (lane >> 2) * (3 * D * 2) + (lane & 3) * 16;
  asm volatile("" : "+v"(vq));
  {
    const u32 ld = (u32)(size_t)L + w * 1024;
    if (w <= 4) {
      const rsrc_t r1 = __builtin_amdgcn_make_buffer_rsrc((void*)(w == 4 ? p.wmod : p.x), 0, w == 4 ? D * 6 * D * 2 : 2048 * D * 2, 0x00020000);
      const int vo = w == 4 ? (128 * mkp + 4 * (lane >> 1)) * (6 * D * 2) + mcg * 32 + (lane & 1) * 16
                            : (panel * 128 + mem * 8 + (lane >> 3)) * (D * 2) + (lane & 7) * 16 + 256 * w;
#pragma unroll
      for (int i = 0; i < 4; ++i) r0[i] = __builtin_amdgcn_raw_buffer_load_b128(r1, vo, w == 4 ? i * (6 * D * 2) : i < 2 ? i * 128 : 1 << 30, 0);
      SB0();
    }
    {
      const rsrc_t r2 = __builtin_amdgcn_make_buffer_rsrc((void*)p.c, 0, w == 4 ? D * 2 : 0, 0x00020000);
      const v2u c2 = __builtin_amdgcn_raw_buffer_load_b64(r2, (128 * mkp + 4 * (lane >> 1)) * 2, 0, 0);
      cv0[0] = c2.x; cv0[1] = c2.y;
      epoch = __builtin_amdgcn_raw_buffer_load_b32(rs, 0, OFF_EPOCH, AUX_SC1);
      __builtin_amdgcn_raw_buffer_store_b32(0u, rs, (w == 4 && lane < 2) ? lane * 128 : WS_BYTES, OFF_ARR + b * 256, AUX_SC1);
      SB0();
    }
    if (w >= 5) {
      const rsrc_t rc = __builtin_amdgcn_make_buffer_rsrc((void*)pc0, 0, CODE_BYTES, 0x00020000);
      gldsi<slot(128), 0>(rc, lane < CODE_KB / 6 ? (u32)lane * 128u : 1u << 30, (u32)(((rank & 15) * 3 + w - 5) * (CODE_KB / 6) * 128), ld);
      const u32 tq = (rank * 16 + (lane >> 2)) * (3 * D * 2) + (xcd & 1) * 512 + (lane & 3) * 128;
      if (EQ || w != 5) gldsi<slot(128), 0>(rqkv, tq, (w - 5) * (D * 2), ld);
      if (w == 5) {
        gldsi<slot(128), 0>(rs, lane < 2 ? (u32)(pb + lane * (848 << 10)) : 1u << 30, 0u, ld);
        const rsrc_t rp = __builtin_amdgcn_make_buffer_rsrc((void*)p.wphi, 0, 32 * 256 * 2, 0x00020000);
        gldsi<L_CF + 2048, 0>(rp, lane * 16, mem * 1024, (u32)(size_t)L);
      }
    }
    if (tid < 64) *((LAS u32*)(L + L_HINT) + tid) = 0u;
    TS(27);
    bar();
    asm volatile("" : "+s"(w));
    if (w >= 6) {
      u32 lo = (u32)(size_t)L + (w & 1) * 1024, so = (D + (w & 1) * D + mem * 32) * 2;
#pragma nounroll
      for (int i = 0; i < KV1 / 2; ++i) {
        gldsi<0, 0>(rqkv, vq, so, lo);
        gldsi<2048, 16 * (3 * D * 2)>(rqkv, vq, so, lo);
        lo += 4096; so += 32 * (3 * D * 2);
      }
    }
    if (w == 4) {
      __builtin_amdgcn_s_setprio(3);
      TS(10);
      float sl[4], acc[8];
#pragma unroll
      for (int i = 0; i < 4; ++i) { const float c = (i & 1) ? hi16(cv0[i >> 1]) : lo16(cv0[i >> 1]); sl[i] = rb(c * rcp(1.f + __expf(-c))); }
      u32 tag = epoch;
      asm volatile("" : "+v"(tag));
      tag += 1u;
      TS(11);
#pragma unroll
      for (int j = 0; j < 8; ++j) acc[j] = 0.f;
#pragma unroll
      for (int i = 0; i < 4; ++i)
#pragma unroll
        for (int j = 0; j < 4; ++j) { acc[2 * j] += sl[i] * lo16(r0[i][j]); acc[2 * j + 1] += sl[i] * hi16(r0[i][j]); }
      float c0, c1;
      TS(12);
      red8q(acc, c0, c1);
      c0 = dpp_add<DPP_X2>(c0); c1 = dpp_add<DPP_X2>(c1);
      const int go = ((lane >> 1) & 7) * 32768 + (8 * (lane & 1) + 2 * (lane >> 4)) * 8;
      const v4u gv = {__float_as_uint(c0), tag, __float_as_uint(c1), tag};
      drain();
      st128(gv, rs, go, OFF_MODR + mkp * 8192 + mcg * 128);
      st128(gv, rs, go, OFF_MODR + 8 * 32768 + mkp * 8192 + mcg * 128);
      sig(L, 0, 0);
    }
    asm volatile("" : "+s"(w));
  }

  if (w == 4 || w == 5) {
    const int a = w & 1;
    u32 tag;
    tag = epoch;
    asm volatile("" : "+v"(tag));
    tag += 1u;
    drain();
    TS(1);
    bar();
    __builtin_amdgcn_s_setprio(APRIO);
    await<4>(L, 2);
    const int kp = xcd >> 1, cpair = (xcd & 1) * 24 + rank;
    const bool con = rank < ((xcd & 1) ? 19 : 24);
    const int col0 = a * F + cpair * 32;
    v4u wm[8];
#pragma unroll
    for (int i = 0; i < 8; ++i)
      wm[i] = __builtin_amdgcn_raw_buffer_load_b128(rgu, con ? (128 * kp + 2 * (lane >> 2)) * (F * 2) + cpair * 64 + (lane & 3) * 16 : 1 << 30, (32 * (i >> 1) + (i & 1)) * (F * 2), 0);
    SB0();
    if (a == 1) {
      v4u wn[8]; u32 cv[8];
      const int row0 = 128 * mkp + (lane >> 2);
#pragma unroll
      for (int i = 0; i < 8; ++i) cv[i] = p.c[row0 + 16 * i];
#pragma unroll
      for (int i = 0; i < 8; ++i) wn[i] = *(const GAS v4u*)(p.wmod + (size_t)(row0 + 16 * i) * (6 * D) + 1024 + mcg * 32 + (lane & 3) * 8);
      SB0();
      float acc[8];
#pragma unroll
      for (int j = 0; j < 8; ++j) acc[j] = 0.f;
#pragma unroll
      for (int i = 0; i < 8; ++i) {
        const float c = lo16(cv[i]), sl = rb(c * rcp(1.f + __expf(-c)));
#pragma unroll
        for (int j = 0; j < 4; ++j) { acc[2 * j] += sl * lo16(wn[i][j]); acc[2 * j + 1] += sl * hi16(wn[i][j]); }
      }
      float c0, c1;
      red8q(acc, c0, c1);
      st128(v4u{__float_as_uint(c0), tag, __float_as_uint(c1), tag}, rs, (lane & 12) == 0 ? (8 * (lane & 3) + 2 * (lane >> 4)) * 8 : WS_BYTES,
            OFF_MODG + mkp * 16384 + mcg * 256);
    }
    reach<1>(L, H_C0, 2u, 2u, 2u, 2u);
    TS(2);
    {
      const int idx0 = (mem * 4 + 2 * a) * 16;
      v4u e[32], zz[2];
      {
        u32 todo = 0xFFu;
        for (u32 spins = 0; spins < SPIN_CAP && todo; ++spins) {
          const u32 bad = ~__builtin_amdgcn_readfirstlane(ask(rs, FL_KV + mem * 2 + a, lane));
          __builtin_amdgcn_fence(__ATOMIC_ACQUIRE, "wavefront");
          RELOAD();
          static_for<0, 8>([&](auto G) {
            constexpr int g = G.value;
            if (((todo >> g) & 1u) && !(bad & (0x00030003u << (2 * g)))) {
              todo &= ~(1u << g);
              e[2 * g] = ld128(rs, lane * 16, OFF_KVE + (idx0 + 2 * g) * 1024);
              e[2 * g + 1] = ld128(rs, lane * 16, OFF_KVE + (idx0 + 2 * g + 1) * 1024);
              e[16 + 2 * g] = ld128(rs, lane * 16, OFF_KVE + (idx0 + 16 + 2 * g) * 1024);
              e[17 + 2 * g] = ld128(rs, lane * 16, OFF_KVE + (idx0 + 17 + 2 * g) * 1024);
            }
          });
        }
      }
      TS(3);
      zz[0] = ldfrag<0>(rs, lane * 16, OFF_KVZ + idx0 * 64);
      zz[1] = ldfrag<1>(rs, lane * 16, OFF_KVZ + idx0 * 64);
      SB0();
#pragma unroll
      for (int q = 0; q < 2; ++q) {
        f32x4 s0 = __builtin_bit_cast(f32x4, e[16 * q]), s1 = __builtin_bit_cast(f32x4, e[16 * q + 1]);
#pragma unroll
        for (int i = 2; i < 16; i += 2) { s0 += __builtin_bit_cast(f32x4, e[16 * q + i]); s1 += __builtin_bit_cast(f32x4, e[16 * q + i + 1]); }
        s0 += s1;
        *(LAS v2u*)(L + L_KVF + (2 * a + q) * 512 + l31 * 16 + hh * 8) = v2u{pk(s0[0], s0[1]), pk(s0[2], s0[3])};
      }
      f32x4 z;
#pragma unroll
      for (int j = 0; j < 4; ++j) {
        float t = fold32(__uint_as_float(zz[0][j]), __uint_as_float(zz[1][j]));
        t = fold16(t, t);
        t = dpp_add<DPP_ROR4>(t);
        z[j] = dpp_add<DPP_ROR8>(t);
      }
      if (l31 < 4) *(LAS v2u*)(L + L_ZF + (2 * a + hh) * 32 + ((lane & 1) ^ 1) * 16 + ((lane >> 1) & 1) * 8) = v2u{pk(z[0], z[1]), pk(z[2], z[3])};
    }
    MARK(H_P0, 2 + a, 1u);
    __builtin_amdgcn_s_setprio(1);
    TS(4);
    u32 gsb[4], gsc[4];
    {
      const int vo = ((hh ? 2 : 3 * a) * D + colg * 64 + 2 * l31) * 8, ro = (D + 128 * kp + 2 * lane) * 8;
      v4u g[12];
      for (u32 spins = 0; spins < SPIN_CAP; ++spins) {
        RELOAD();
#pragma unroll
        for (int i = 0; i < 12; ++i) g[i] = ld128(rs, i < 4 ? vo : ro + (i >> 3) * (D * 8), OFF_MODG + (i & 3) * 16384);
        bool ok = true;
#pragma unroll
        for (int i = 0; i < 12; ++i) ok &= (g[i].y == tag) & (g[i].w == tag);
        if (__all(ok)) break;
      }
      u32 pr[3];
#pragma unroll
      for (int t = 0; t < 3; ++t)
        pr[t] = pk((__uint_as_float(g[4 * t].x) + __uint_as_float(g[4 * t + 1].x)) + (__uint_as_float(g[4 * t + 2].x) + __uint_as_float(g[4 * t + 3].x)),
                   (__uint_as_float(g[4 * t].z) + __uint_as_float(g[4 * t + 1].z)) + (__uint_as_float(g[4 * t + 2].z) + __uint_as_float(g[4 * t + 3].z)));
      const u32 v1 = pk(1.f + lo16(pr[0]), 1.f + hi16(pr[0])), vv = hh ? v1 : pr[0];
      *(LAS f32x2*)(L + L_VEC + (hh ? 1 : 2 * a) * 256 + l31 * 8) = f32x2{lo16(vv), hi16(vv)};
      const u32 sc = pk(1.f + lo16(pr[2]), 1.f + hi16(pr[2]));
#pragma unroll
      for (int i = 0; i < 4; ++i) {
        gsb[i] = __builtin_amdgcn_ds_bpermute((16 * i + (lane >> 2)) * 4, pr[1]);
        gsc[i] = __builtin_amdgcn_ds_bpermute((16 * i + (lane >> 2)) * 4, sc);
      }
    }
    MARK(H_P0, 2 + a, 2u);
    if (con) {
      float a1[8], a2[8];
#pragma unroll
      for (int j = 0; j < 8; ++j) { a1[j] = 0.f; a2[j] = 0.f; }
#pragma unroll
      for (int i = 0; i < 8; ++i) {
        const float sb = (i & 1) ? hi16(gsb[i >> 1]) : lo16(gsb[i >> 1]), sc = (i & 1) ? hi16(gsc[i >> 1]) : lo16(gsc[i >> 1]);
#pragma unroll
        for (int j = 0; j < 4; ++j) {
          a1[2 * j] += sc * lo16(wm[i][j]); a1[2 * j + 1] += sc * hi16(wm[i][j]);
          a2[2 * j] += sb * lo16(wm[i][j]); a2[2 * j + 1] += sb * hi16(wm[i][j]);
        }
      }
      float c10, c11, c20, c21;
      red8q(a1, c10, c11); red8q(a2, c20, c21);
      const int k = (lane >> 2) & 3;
      const float val = k == 0 ? c10 : k == 1 ? c20 : k == 2 ? c11 : c21;
      st64(v2u{__float_as_uint(val), tag}, rs, (col0 + 8 * (lane & 3) + 2 * (lane >> 4)) * 16 + k * 8, OFF_CG + kp * CG_PART);
    }
    TS(5);
    reach<2>(L, H_C0, 4u, 4u, 4u, 4u);
    {
      const int ntile = three ? 6 : 4;
      v4u cg[8];
      int off[2];
#pragma unroll
      for (int i = 0; i < 2; ++i) {
        int tile = 4 * i + 2 * a + hh; tile = tile < ntile ? tile : 0;
        off[i] = (32 * pair0 + 16 * tile + (lane & 15) + ((lane >> 4) & 1) * F) * 16;
      }
      for (u32 spins = 0; spins < SPIN_CAP; ++spins) {
        RELOAD();
        bool ok = true;
#pragma unroll
        for (int i = 0; i < 8; ++i) cg[i] = ld128(rs, off[i >> 2], OFF_CG + (i & 3) * CG_PART);
#pragma unroll
        for (int i = 0; i < 8; ++i) ok &= (cg[i].y == tag) & (cg[i].w == tag);
        if (__all(ok)) break;
      }
#pragma unroll
      for (int i = 0; i < 2; ++i) {
        const float c1 = (__uint_as_float(cg[4 * i].x) + __uint_as_float(cg[4 * i + 1].x)) + (__uint_as_float(cg[4 * i + 2].x) + __uint_as_float(cg[4 * i + 3].x));
        const float c2 = (__uint_as_float(cg[4 * i].z) + __uint_as_float(cg[4 * i + 1].z)) + (__uint_as_float(cg[4 * i + 2].z) + __uint_as_float(cg[4 * i + 3].z));
        const float h1 = rb(c1), m1 = rb(c1 - h1), l1 = c1 - h1 - m1, h2 = rb(c2), m2 = rb(c2 - h2), l2 = c2 - h2 - m2;
        const int tile = 4 * i + 2 * a + hh;
        if (tile < 6) *(LAS v4u*)(L + L_CF + tile * 512 + l31 * 16) = v4u{pk(h1, m1), pk(l1, h2), pk(m2, l2), 0u};
      }
    }
    reach<1>(L, 5, a < 1, a < 1, a, a);
    {
      v4u st[16];
#pragma unroll
      for (int t = 0; t < 16; ++t) st[t] = ld128(rs, l31 * 16 + hh * 8192 + t * 512, pb + PN_ST + 2 * a * 8192);
      for (u32 spins = 0; spins < SPIN_CAP; ++spins) {
        bool ok = true;
#pragma unroll
        for (int t = 0; t < 16; ++t) ok &= (st[t].y == tag) & (st[t].w == tag);
        if (__all(ok)) break;
        RELOAD();
#pragma unroll
        for (int t = 0; t < 16; ++t) st[t] = ld128(rs, l31 * 16 + hh * 8192 + t * 512, pb + PN_ST + 2 * a * 8192);
      }
      float ms = 0.f;
#pragma unroll
      for (int t = 0; t < 16; ++t) ms += __uint_as_float(st[t].x);
      const float mean = ms * (1.f / 16.f);
      float q = 0.f;
#pragma unroll
      for (int t = 0; t < 16; ++t) { const float d = __uint_as_float(st[t].x) - mean; q += __uint_as_float(st[t].z) + 32.f * d * d; }
      const float den = rb(__builtin_amdgcn_sqrtf(rb(rb(q * (1.f / D)) + 1e-5f)));
      *(LAS v4u*)(L + L_KVF + a * 1024 + lane * 16) = v4u{pk(-mean, -mean), pk(-mean, den), pk(den, den), __float_as_uint(rcp(den * den))};
    }
    RELOAD();
    mark(L, H_PP, 2 + a, 5u);
    TS(6);
    await_cp(L, 2);
    {
      v4u wt[58];
      const u32 vd = (lane >> 2) * (D * 2) + (lane & 3) * 16, sd = a * 16 * (D * 2) + colg * 128;
      const Base3 wdst(L + a * 2048 + lane * 16);
      static_for<28, 86>([&](auto J) {
        wt[J.value - 28] = __builtin_amdgcn_raw_buffer_load_b128(rwd, NBV(vd + (J.value & 1) * 64), sd + pair_of(J.value >> 1) * 32 * (D * 2), 0); SB0(); });
      TS(7);
#define ST_WT(T0, T1, k) do { static_for<2 * (T0), 2 * (T1)>([&](auto J) { \
      *(LAS v4u*)wdst.template at<slot(P_WD + 4 * (J.value >> 1)) + (J.value & 1) * 1024>() = wt[J.value - 28]; WPACE(); }); \
      RELOAD(); mark(L, H_PP, 2 + a, k); RELOAD(); } while (0)
      await_cp(L, 4);
      ST_WT(14, 18, 6u);
      await_cp(L, 5);
      ST_WT(18, 26, 7u);
      await_cp(L, 6);
      ST_WT(26, 34, 8u);
      TS(8);
      await_cp(L, 7);
      ST_WT(34, 43, 9u);
      TS(9);
    }
    TS_DUMP();
    return;
  }

  if (w >= 6) {
    const int v = w & 1;
    __builtin_amdgcn_s_setprio(LPRIO);
    const u32 ldsb = (u32)(size_t)L;
    const u32 l1 = ldsb + v * 1024, l2 = ldsb + v * 2048;
    const u32 skv = (D + v * D + mem * 32) * 2, sq = v * 16 * (3 * D * 2) + mem * 64;
    const u32 so = 16 * v * 16 * (D * 2) + colg * 128, sd = v * 16 * (D * 2) + colg * 128;
    const u32 sg = pair0 * 64, sg2 = three ? sg + 128 : 1u << 30;
#define LD_KV(S0, S1, PC) static_for<S0, S1>([&](auto S) { gldsi<slot(P_KV + 2 * S.value), S.value * 16 * (3 * D * 2)>(rqkv, vq, skv, l1); PACE(PC); })
#define PACE(n) do { if ((n) > 0) __builtin_amdgcn_s_sleep(n); } while (0)
#define LD_Q(S0, S1, PC) static_for<S0, S1>([&](auto S) { gldsi<slot(P_Q + 2 * S.value), 2 * S.value * 16 * (3 * D * 2)>(rqkv, vq, sq, l1); PACE(PC); })
#define LD_DN(RS, SB, P0, T0, T1, PC)                                                                    \
  static_for<T0, T1>([&](auto T) {                                                                   \
    gldsi<slot((P0) + 4 * T.value), T.value * 16 * (D * 2)>(RS, vd, SB, l2);                         \
    gldsi<slot((P0) + 4 * T.value) + 1024, T.value * 16 * (D * 2) + 64>(RS, vd, SB, l2); PACE(PC);   \
  })
#define LD_GU(S0, S1, PC)                                                                                \
  static_for<S0, S1>([&](auto S) {                                                                   \
    gldsi<slot(P_GU + 6 * S.value), S.value * 16 * (F * 2)>(rgu, vg, sg, l1);                        \
    gldsi<slot(P_GU + 6 * S.value + 2), S.value * 16 * (F * 2) + 64>(rgu, vg, sg, l1);               \
    gldsi<slot(P_GU + 6 * S.value + 4), S.value * 16 * (F * 2)>(rgu, vg, sg2, l1); PACE(PC);         \
  })
    TS(1);
    VMCNT(0);
    SB0();
    int ln;
    asm volatile("v_mbcnt_lo_u32_b32 %0, -1, 0\n\tv_mbcnt_hi_u32_b32 %0, -1, %0" : "=v"(ln));
    const u32 vd = (ln >> 2) * (D * 2) + (ln & 3) * 16;
    const u32 vg = (ln >> 2) * (F * 2) + (((ln & 3) ^ (v << 1)) * 16);
    TS(2);
    LD_Q(0, 16, PQE);
    TS(11);
    bar_raw();
    v4u wt[36];
    const Base3 wdst1(L + v * 1024 + ln * 16);
#define GUN(s_, P_) (P_GU + ((s_) < 12 ? 6 * (s_) + 2 * (P_) : 72 + 40 * (P_) + 2 * ((s_) - 12)))
#define GUR(s_, P_) __builtin_amdgcn_raw_buffer_load_b128(rgu, NBV(vg), ((P_) == 2 ? sg2 : sg + (P_) * 64) + (s_) * 16 * (F * 2), 0)
#define GUW(s_, P_, r_) *(LAS v4u*)wdst1.template at<slot(GUN(s_, P_))>() = wt[r_]
#define LD_GU2(S0, S1, PC) static_for<S0, S1>([&](auto S) { constexpr int s_ = S.value; \
      gldsi<slot(GUN(s_, 0)), s_ * 16 * (F * 2)>(rgu, vg, sg, l1); SB0(); \
      if constexpr (s_ < 24) gldsi<slot(GUN(s_, 1)), s_ * 16 * (F * 2) + 64>(rgu, vg, sg, l1); \
      else wt[20 + s_ - 24] = GUR(s_, 1); \
      SB0(); wt[s_ - 12] = GUR(s_, 2); PACE(PC); SB0(); })
#define LD_WD(T0, T1) static_for<T0, T1>([&](auto T) { \
      gldsi<slot(P_WD + 4 * T.value), pair_of(T.value) * 32 * (D * 2)>(rwd, vd, sd, l2); \
      gldsi<slot(P_WD + 4 * T.value) + 1024, pair_of(T.value) * 32 * (D * 2) + 64>(rwd, vd, sd, l2); })
#define DONE(k) do { RELOAD(); mark(L, H_PP, v, k); RELOAD(); } while (0)
    await<4>(L, 2);
    VMCNT(0);
    MARK(H_P0, v, 1u);
    gldsi<L_CF, 0>(rwo, ln < 32 ? ((2 * rank + v) * 8 + (ln >> 2)) * (D * 2) + (xcd & 1) * 512 + (ln & 3) * 128 : 1u << 30, 0u, l1);
    LD_DN(rwo, so, P_WO, 0, 8, 0);
    TS(3);
    VMCNT(0);
    MARK(H_P0, v, 1u);
    reach<1>(L, H_C0, 2u, 2u, 2u, 2u);
    gldsi<L_CF, 0>(rwd, v ? 1u << 30 : ((xcd >> 1) * 32 + rank + 128 * (ln >> 2)) * (D * 2) + (xcd & 1) * 512 + (ln & 3) * 128, 0u, l1);
    LD_DN(rwo, so, P_WO, (EQ ? 8 : 0), 16, 1);
    LD_GU(0, 6, PGU);
    VMCNT(18);
    MARK(H_P0, v, 2u);
    TS(4);
    reach<2>(L, H_C0, 3u, 3u, 3u, 3u);
    reach<2>(L, H_P0, 0u, 0u, 1u, 1u);
    LD_GU(6, 12, PGU);
    await<4>(L, 4);
    static_for<12, 20>([&](auto S) { constexpr int s_ = S.value;
      wt[20 + s_ - 12] = GUR(s_, 0); wt[28 + s_ - 12] = GUR(s_, 1); wt[s_ - 12] = GUR(s_, 2); SB0(); });
    VMCNT(24);
    MARK(H_P0, v, 3u);
    TS(5);
    reach<1>(L, H_C0, 4u, 4u, 4u, 4u);
    static_for<12, 20>([&](auto S) { GUW(S.value, 0, 20 + S.value - 12); GUW(S.value, 1, 28 + S.value - 12); });
    LD_GU2(20, 32, PG2);
    VMCNT(0);
    DONE(3);
    TS(6);
    await_cp(L, 1);
    {
      const u32 tdn = ((2 * rank + v) * 16 + (ln >> 2)) * (D * 2) + (xcd & 1) * 512 + (ln & 3) * 128;
      __builtin_amdgcn_s_waitcnt(0x0F70);
      gldsi<L_T, 0>(rwd, tdn, 0u, l1);
      gldsi<L_T, 1024 * (D * 2)>(rwd, tdn, 0u, l1);
    }
    static_for<24, 32>([&](auto S) { GUW(S.value, 1, 20 + S.value - 24); });
    DONE(4);
    await_cp(L, 2);
    static_for<12, 32>([&](auto S) { GUW(S.value, 2, S.value - 12); });
    DONE(5);
    TS(7);
    await_cp(L, 3);
    LD_WD(0, 10);
    await_cp(L, 4);
    LD_WD(10, 14);
    VMCNT(0);
    DONE(100);
    TS(8);
    TS_DUMP();
    return;
  }

  __builtin_amdgcn_s_setprio(2);
  const int s2 = w & 1, kh = w >> 1;
  const int strip2 = rowh * 2 + s2, ncol = colg * 64 + kh * 32;
  v4u aphi0, aphi1;
  u32 tag;
  {
    float xf[16];
#pragma unroll
    for (int i = 0; i < 8; ++i) { xf[2 * i] = lo16(r0[i >> 2][i & 3]); xf[2 * i + 1] = hi16(r0[i >> 2][i & 3]); }
    float sm = (((xf[0] + xf[1]) + (xf[2] + xf[3])) + ((xf[4] + xf[5]) + (xf[6] + xf[7]))) + (((xf[8] + xf[9]) + (xf[10] + xf[11])) + ((xf[12] + xf[13]) + (xf[14] + xf[15])));
    sm = dpp_add<DPP_X1>(sm); sm = dpp_add<DPP_X2>(sm); sm = dpp_add<DPP_HM>(sm);
    const float mw = sm * (1.f / 128.f);
    float q[4];
#pragma unroll
    for (int i = 0; i < 4; ++i) {
      const float d0 = xf[4 * i] - mw, d1 = xf[4 * i + 1] - mw, d2 = xf[4 * i + 2] - mw, d3 = xf[4 * i + 3] - mw;
      q[i] = (d0 * d0 + d1 * d1) + (d2 * d2 + d3 * d3);
    }
    float qq = (q[0] + q[1]) + (q[2] + q[3]);
    qq = dpp_add<DPP_X1>(qq); qq = dpp_add<DPP_X2>(qq); qq = dpp_add<DPP_HM>(qq);
    *(volatile LAS f32x2*)(L + L_KVF + (lane >> 3) * 32 + w * 8) = f32x2{mw, qq};
    sig(L, 1, w);
    await<4>(L, 1);
    const f32x4 pa = *(LAS f32x4*)(L + L_KVF + (lane >> 3) * 32), pc = *(LAS f32x4*)(L + L_KVF + (lane >> 3) * 32 + 16);
    const float mean = ((pa[0] + pa[2]) + (pc[0] + pc[2])) * 0.25f;
    const float f0 = pa[0] - mean, f1 = pa[2] - mean, f2 = pc[0] - mean, f3 = pc[2] - mean;
    const float m2 = ((pa[1] + pa[3]) + (pc[1] + pc[3])) + 128.f * ((f0 * f0 + f1 * f1) + (f2 * f2 + f3 * f3));
    const float m1 = rb(mean), r1 = rcp(rb(__builtin_amdgcn_sqrtf(rb(rb(m2 * (1.f / D)) + 1e-5f))));
    float xn[16];
#pragma unroll
    for (int i = 0; i < 8; ++i) {
      const u32 d = pk(xf[2 * i] - m1, xf[2 * i + 1] - m1);
      const u32 t = pk(lo16(d) * r1, hi16(d) * r1);
      xn[2 * i] = lo16(t); xn[2 * i + 1] = hi16(t);
    }
#pragma unroll
    for (int i = 0; i < 16; ++i) asm volatile("" : "+v"(xn[i]));
    tag = __builtin_amdgcn_readfirstlane(epoch) + 1u;
    TS(1);
    await<1>(L, 0);
    __builtin_amdgcn_s_sleep(PRE_MOD);
    TS(21);
    v4u g[8];
    {
      u32 todo = 0xFFu;
      for (u32 spins = 0; spins < SPIN_CAP && todo; ++spins) {
        RELOAD();
        static_for<0, 8>([&](auto I) {
          constexpr int i = I.value;
          if ((todo >> i) & 1u) g[i] = ld128(rs, lane * 16, OFF_MODR + panel * 32768 + (i & 3) * 8192 + (i >> 2) * 4096 + w * 1024);
        });
        todo = 0;
        static_for<0, 8>([&](auto I) { if (__builtin_amdgcn_ballot_w64((g[I.value].y != tag) | (g[I.value].w != tag))) todo |= 1u << I.value; });
      }
    }
    {
      u32 pr[2];
#pragma unroll
      for (int t = 0; t < 2; ++t)
        pr[t] = pk((__uint_as_float(g[4 * t].x) + __uint_as_float(g[4 * t + 1].x)) + (__uint_as_float(g[4 * t + 2].x) + __uint_as_float(g[4 * t + 3].x)),
                   (__uint_as_float(g[4 * t].z) + __uint_as_float(g[4 * t + 1].z)) + (__uint_as_float(g[4 * t + 2].z) + __uint_as_float(g[4 * t + 3].z)));
      *(LAS u32*)(L + L_T + w * TW + lane * 4) = pr[0];
      *(LAS u32*)(L + L_T + w * TW + 256 + lane * 4) = pk(1.f + lo16(pr[1]), 1.f + hi16(pr[1]));
    }
    v4u sav[2], cav[2];
#pragma unroll
    for (int i = 0; i < 2; ++i) {
      sav[i] = *(LAS v4u*)(L + L_T + w * TW + i * 128 + (lane & 7) * 16);
      cav[i] = *(LAS v4u*)(L + L_T + w * TW + 256 + i * 128 + (lane & 7) * 16);
    }
    sig(L, 3, w);
    TS(2);
    const int strip = mem >> 2, r = 8 * (mem & 3) + (lane >> 3);
#pragma unroll
    for (int i = 0; i < 2; ++i) {
      v4u o;
#pragma unroll
      for (int j = 0; j < 4; ++j) {
        const u32 sa = sav[i][j], sc = cav[i][j];
        const u32 t = pk(xn[8 * i + 2 * j] * lo16(sc), xn[8 * i + 2 * j + 1] * hi16(sc));
        o[j] = pk(lo16(t) + lo16(sa), hi16(t) + hi16(sa));
      }
      st128(o, rs, (4 * i + ((lane & 7) >> 1)) * 1024 + (r + 32 * (lane & 1)) * 16, pb + PN_H + strip * 32768 + w * 8192);
    }
    await<4>(L, 3);
    publish(rs, fw + FL_H + strip, (mem & 3) * 4 + w, lane);
  }
  TS(3);

  const int trq = (lane & 15) >> 2, trp = lane & 3, trb = (lane >> 4) & 1;
  const f32x16 zero16 = {0.f, 0.f, 0.f, 0.f, 0.f, 0.f, 0.f, 0.f, 0.f, 0.f, 0.f, 0.f, 0.f, 0.f, 0.f, 0.f};
  u32 phq[16];
  v4u wb[PD];
  {
    const Base3 wnat(L + (8 * hh + trq) * 64 + trb * 32 + trp * 8);
    waitn(rs, fw + FL_H + w, 0xFFFFu, lane);
    TS(4);
    v4u a[32];
    ldfrags<W0>(a, rs, lane * 16, pb + PN_H + w * 32768);
    bar_raw();
    {
      const int hs = (l31 >> 2) & 1;
      const LAS u16* wp = (const LAS u16*)(L + L_CF + 2048) + hs * 256 + 4 * hh * 16 + (l31 & 3) + 4 * (l31 >> 3);
      u32 e[8];
#pragma unroll
      for (int j = 0; j < 8; ++j) e[j] = wp[(8 * (j >> 2) + (j & 3)) * 16];
      const v4u av = {e[0] | (e[1] << 16), e[2] | (e[3] << 16), e[4] | (e[5] << 16), e[6] | (e[7] << 16)};
      const v4u z = {0, 0, 0, 0};
      aphi0 = hs == 0 ? av : z; aphi1 = hs == 1 ? av : z;
      asm volatile("" : "+v"(aphi0), "+v"(aphi1));
    }
    static_for<0, PD>([&](auto U) { wb[U.value] = ldw<256>(wnat.at<slot(P_KV + U.value)>()); });
    SB0();
    f32x16 kacc = zero16, vacc = zero16;
    static_for<0, 64>([&](auto U) {
      constexpr int u = U.value;
      const v4u cur = wb[u % PD];
      if constexpr (u + PD < 64) wb[u % PD] = ldw<256>(wnat.at<slot(P_KV + u + PD)>());
      if constexpr (u & 1) mfma_pin(a[u >> 1], cur, vacc); else mfma_pin(cur, a[u >> 1], kacc);
      if constexpr (!(u & 1) && (u >> 1) + W0 < 32) a[(u >> 1) + W0] = ldfrag<(u >> 1) + W0>(rs, lane * 16, pb + PN_H + w * 32768);
      if constexpr (u == 2 * (32 - W0)) sig(L, 2, w);
      SB0();
    });
    TS(5);
    f32x16 P = mfma(aphi0, pack8<0>(kacc), zero16);
    P = mfma(aphi1, pack8<8>(kacc), P);
    const v4u vb0 = pack8<0>(vacc), vb1 = pack8<8>(vacc);
    u32 phk[16];
    feat(P, phk);
    LAS unsigned char* T = L + L_T + w * TW;
#pragma unroll
    for (int c = 0; c < 4; ++c)
      *(LAS v4u*)(T + lane * 64 + ((c ^ ((lane >> 1) & 3)) * 16)) = v4u{phk[4 * c], phk[4 * c + 1], phk[4 * c + 2], phk[4 * c + 3]};
    const v4u ones = {0x3F803F80u, 0x3F803F80u, 0x3F803F80u, 0x3F803F80u};
    LAS unsigned char* tr = T + (4 * hh + trq) * 64 + (((2 * trb + (trp >> 1)) ^ ((2 * hh + (trq >> 1)) & 3)) * 16) + 8 * (trp & 1);
    const v4u t0 = ldw<512>(tr), t1 = ldw<512>(tr + 1024), t2 = ldw<512>(tr + 2048), t3 = ldw<512>(tr + 3072);
    f32x16 D0 = mfma(t0, trb ? ones : vb0, zero16);
    f32x16 D1 = mfma(t2, trb ? vb0 : ones, zero16);
    D0 = mfma(t1, trb ? ones : vb1, D0);
    D1 = mfma(t3, trb ? vb1 : ones, D1);
    {
      LAS unsigned char* pe = T + lane * 16;
      LAS unsigned char* pz = T + 1024 + (lane >> 4) * 16;
      LAS unsigned char* p0 = trb ? pz : pe;
      LAS unsigned char* p1 = trb ? pe : pz;
#pragma unroll
      for (int g = 0; g < 4; ++g) *(LAS f32x4*)(p0 + g * 1088) = f32x4{D0[4 * g], D0[4 * g + 1], D0[4 * g + 2], D0[4 * g + 3]};
#pragma unroll
      for (int g = 0; g < 4; ++g) *(LAS f32x4*)(p1 + g * 1088) = f32x4{D1[4 * g], D1[4 * g + 1], D1[4 * g + 2], D1[4 * g + 3]};
    }
    TS(6);
    MARK(H_C0, w, 1u);
    reach<0>(L, H_C0, 1u, 1u, 1u, 1u);
    {
      f32x4 e[4], z[4];
#pragma unroll
      for (int v = 0; v < 4; ++v) {
        e[v] = *(LAS f32x4*)(L + L_T + v * TW + w * 1088 + lane * 16);
        z[v] = *(LAS f32x4*)(L + L_T + v * TW + w * 1088 + 1024 + (lane & 3) * 16);
      }
      SB0();
      const int idx = (mem * 4 + w) * 16 + panel;
      st128(__builtin_bit_cast(v4u, (e[0] + e[1]) + (e[2] + e[3])), rs, lane * 16, OFF_KVE + idx * 1024);
      st128(__builtin_bit_cast(v4u, (z[0] + z[1]) + (z[2] + z[3])), rs, lane < 4 ? lane * 16 : WS_BYTES, OFF_KVZ + idx * 64);
      publish(rs, FL_KV + mem * 2 + (w >> 1), (w & 1) * 16 + panel, lane);
      MARK(H_C0, w, 2u);
      reach<1>(L, H_P0, 1u, 1u, 0u, 0u);
      static_for<0, PD>([&](auto U) { wb[U.value] = ldw<256>(wnat.at<slot(P_Q + U.value)>()); });
    }
    TS(7);
    f32x16 qacc = zero16;
    static_for<0, 32>([&](auto U) {
      constexpr int u = U.value;
      const v4u cur = wb[u % PD];
      if constexpr (u + PD < 32) wb[u % PD] = ldw<256>(wnat.at<slot(P_Q + u + PD)>());
      mfma_pin(cur, a[u], qacc);
      SB0();
    });
    MARK(H_C0, w, 3u);
    P = mfma(aphi0, pack8<0>(qacc), zero16);
    P = mfma(aphi1, pack8<8>(qacc), P);
    feat(P, phq);
#pragma unroll
    for (int i = 0; i < 16; ++i) asm volatile("" : "+v"(phq[i]));
  }
  v2u xres[4];
  {
    const u16* xr = p.x + (size_t)(panel * 128 + strip2 * 32 + l31) * D + ncol + 4 * hh;
#pragma unroll
    for (int i = 0; i < 4; ++i) xres[i] = *(const GAS v2u*)(xr + 8 * i);
  }
  TS(8);
  reach<1>(L, H_P0, 0u, 0u, 1u, 1u);
  TS(9);
  {
    const bool on = (l31 >> 4) == hh;
    const v4u zv = {0, 0, 0, 0};
    f32x16 num = zero16, den = zero16;
    v4u ak[4], az[4];
#pragma unroll
    for (int s4 = 0; s4 < 4; ++s4) { az[s4] = *(LAS v4u*)(L + L_ZF + s4 * 32 + hh * 16); ak[s4] = *(LAS v4u*)(L + L_KVF + s4 * 512 + l31 * 16); }
    SB0();
#pragma unroll
    for (int s4 = 0; s4 < 4; ++s4) den = mfma(on ? az[s4] : zv, v4u{phq[4 * s4], phq[4 * s4 + 1], phq[4 * s4 + 2], phq[4 * s4 + 3]}, den);
#pragma unroll
    for (int s4 = 0; s4 < 4; ++s4) num = mfma(on ? ak[s4] : zv, v4u{phq[4 * s4], phq[4 * s4 + 1], phq[4 * s4 + 2], phq[4 * s4 + 3]}, num);
    const float d0 = rcp(rb(rb(den[0]) + 1e-6f)), d1 = rcp(rb(rb(den[8]) + 1e-6f));
    const v4u n0 = pack8<0>(num), n1 = pack8<8>(num);
    v4u o0, o1;
#pragma unroll
    for (int j = 0; j < 4; ++j) { o0[j] = pk(lo16(n0[j]) * d0, hi16(n0[j]) * d0); o1[j] = pk(lo16(n1[j]) * d1, hi16(n1[j]) * d1); }
    st128(o0, rs, lane * 16, pb + PN_O + w * 32768 + (2 * mem) * 1024);
    st128(o1, rs, lane * 16 + 1024, pb + PN_O + w * 32768 + (2 * mem) * 1024);
  }
#define WDN_BASES() const Base3 wown(L + (4 * hh + trq) * 64 + trb * 32 + trp * 8 + 3 * kh * 1024), woth(L + (4 * hh + trq) * 64 + trb * 32 + trp * 8 + (kh + 1) * 1024)
#define WDN(base, u) ldw<512>((((u) & 1) ? wown : woth).template at<slot((base) + 4 * ((u) >> 1))>())
  u32 x1p[8];
  {
    WDN_BASES();
    publish(rs, fw + FL_O + w, mem, lane);
    reach<0>(L, H_P0, 2u, 2u, 0u, 0u);
    static_for<0, PD>([&](auto U) { wb[U.value] = WDN(P_WO, U.value); });
    TS(10);
    waitn(rs, fw + FL_O + strip2, 0xFFu << (kh * 8), lane);
    TS(11);
    v4u a[16];
    ldfrags<16>(a, rs, lane * 16, pb + PN_O + strip2 * 32768 + kh * 16384);
    sig(L, 4, w);
    reach<0>(L, H_P0, 0u, 0u, 2u, 2u);
    f32x4 ga[4], sc[4];
#pragma unroll
    for (int g = 0; g < 4; ++g) {
      ga[g] = *(LAS f32x4*)(L + L_VEC + (kh * 32 + 8 * g + 4 * hh) * 4);
      sc[g] = *(LAS f32x4*)(L + L_VEC + 256 + (kh * 32 + 8 * g + 4 * hh) * 4);
    }
    SB0();
    f32x16 c0 = zero16, c1 = zero16;
    static_for<0, 32>([&](auto U) {
      constexpr int u = U.value;
      const v4u cur = wb[u % PD];
      if constexpr (u + PD < 32) wb[u % PD] = WDN(P_WO, u + PD);
      if constexpr (u & 1) mfma_pin(cur, a[u >> 1], c0); else mfma_pin(cur, a[u >> 1], c1);
      SB0();
    });
#pragma unroll
    for (int g = 0; g < 4; ++g) *(LAS f32x4*)(L + L_T + w * TW + g * 1024 + lane * 16) = f32x4{c1[4 * g], c1[4 * g + 1], c1[4 * g + 2], c1[4 * g + 3]};
    TS(12);
    MARK(H_C0, w, 4u);
    reach1(L, H_C0, w ^ 2, 4u);
    f32x4 o[4];
#pragma unroll
    for (int g = 0; g < 4; ++g) o[g] = *(LAS f32x4*)(L + L_T + (w ^ 2) * TW + g * 1024 + lane * 16);
    SB0();
    float x1[16], sp[4];
    u32 yp[8];
#pragma unroll
    for (int g = 0; g < 4; ++g) {
#pragma unroll
      for (int j = 0; j < 2; ++j) {
        const u32 r = pk(lo16(xres[g][j]) + ga[g][2 * j] * (c0[4 * g + 2 * j] + o[g][2 * j]),
                         hi16(xres[g][j]) + ga[g][2 * j + 1] * (c0[4 * g + 2 * j + 1] + o[g][2 * j + 1]));
        x1p[2 * g + j] = r;
        x1[4 * g + 2 * j] = lo16(r); x1[4 * g + 2 * j + 1] = hi16(r);
        yp[2 * g + j] = pk(lo16(r) * sc[g][2 * j], hi16(r) * sc[g][2 * j + 1]);
      }
    }
    const int tile = 2 * colg + kh;
    st128(v4u{yp[0], yp[1], yp[2], yp[3]}, rs, lane * 16, pb + PN_X1 + strip2 * 32768 + tile * 2048);
    st128(v4u{yp[4], yp[5], yp[6], yp[7]}, rs, lane * 16 + 1024, pb + PN_X1 + strip2 * 32768 + tile * 2048);
    SB0();
#pragma unroll
    for (int g = 0; g < 4; ++g) sp[g] = (x1[4 * g] + x1[4 * g + 1]) + (x1[4 * g + 2] + x1[4 * g + 3]);
    const float ss = (sp[0] + sp[1]) + (sp[2] + sp[3]);
    const float mt = fold32(ss, ss) * (1.f / 32.f);
    float q[4];
#pragma unroll
    for (int g = 0; g < 4; ++g) {
      const float d0 = x1[4 * g] - mt, d1 = x1[4 * g + 1] - mt, d2 = x1[4 * g + 2] - mt, d3 = x1[4 * g + 3] - mt;
      q[g] = (d0 * d0 + d1 * d1) + (d2 * d2 + d3 * d3);
    }
    const float qs = (q[0] + q[1]) + (q[2] + q[3]);
    const float qt = fold32(qs, qs);
    v4u sv = {__float_as_uint(mt), tag, __float_as_uint(qt), tag};
    asm volatile("" : "+v"(sv));
    publish(rs, fw + FL_X + strip2, tile, lane);
    st128(sv, rs, lane < 32 ? lane * 16 : WS_BYTES, pb + PN_ST + strip2 * 8192 + tile * 512);
  }
  asm volatile("" : "+v"(x1p[0]), "+v"(x1p[1]), "+v"(x1p[2]), "+v"(x1p[3]), "+v"(x1p[4]), "+v"(x1p[5]), "+v"(x1p[6]), "+v"(x1p[7]));
#define GU_S(u) ((u) < 72 ? (u) / 6 : 12 + (((u) - 72) % 40) / 2)
#define GU_I(u) ((u) < 72 ? (u) % 6 : 2 * (((u) - 72) / 40) + ((u) & 1))
#define WGU(u) ldw<512>((((u) & 1) ? wy : wx).template at<slot(P_GU + ((u) & ~1))>())
  {
    const Base3 wx(L + (4 * hh + trq) * 64 + trp * 8 + trb * (1024 + 32)), wy(L + (4 * hh + trq) * 64 + trp * 8 + (trb ? 1024 : 32));
    reach<0>(L, H_P0, 3u, 3u, 0u, 0u);
    static_for<0, PD>([&](auto U) { wb[U.value] = WGU(U.value); });
    TS(13);
    waitn(rs, fw + FL_X + w, 0xFFFFu, lane);
    TS(14);
    constexpr int AW = 20;
    v4u a[AW];
    ldfrags<8>(a, rs, lane * 16, pb + PN_X1 + w * 32768);
    sig(L, 5, w);
    f32x16 acc[6];
#pragma unroll
    for (int i = 0; i < 6; ++i) acc[i] = zero16;
    float rr = 0.f;
    v4u bcor = {0, 0, 0, 0};
    v4u ho, cf0, cf1, pp;
    float t0 = 0.f, t1 = 0.f;
    const int go = pb + PN_G + (w * 96 + mem) * 1024, fi = fw + FL_G + strip2 * 3, gi = pb + PN_G + (strip2 * 96 + kh * 48) * 1024;
    u32 fl = 0;
    int gv = 0;
    bool early = false;
    v4u g[43];
    auto post = [&](int P) { (void)__builtin_amdgcn_raw_ptr_buffer_atomic_add_i32(1 << mem, rs, lane == 0 ? 0 : WS_BYTES, OFF_ARR + (fw + FL_G + w * 3 + P) * 128, 0); };
    auto epi = [&](auto Ic, auto Kc) {
      constexpr int I = decltype(Ic)::value, k = decltype(Kc)::value, j = k >> 1;
      const f32x16& A = acc[I];
      if constexpr ((k & 1) == 0) { t0 = A[2 * j] * rr; t1 = A[2 * j + 1] * rr; asm volatile("" : "+v"(t0)); t0 *= A[8 + 2 * j]; }
      else {
        t1 *= A[9 + 2 * j];
        ho[j] = pk(t0, t1);
        if constexpr (j == 3) st128(ho, rs, (I < 4 || three) ? lane * 16 : WS_BYTES, go + ((I & 1) * 48 + (I >> 1) * 16) * 1024);
      }
    };
    static_for<0, 192>([&](auto Uc) {
      constexpr int u = decltype(Uc)::value, s = GU_S(u), i = GU_I(u), q = (u + 8) % 40;
      if constexpr (u == 6) TS(22);
      if constexpr (u == 60) TS(23);
      if constexpr (u == 96) TS(24);
      if constexpr (u == 128) TS(25);
      if constexpr (u == 160) TS(26);
      if constexpr (u == 56) mark(L, H_CP, w, 2u);
      if constexpr (u == 96 || u == 128 || u == 160) mark(L, H_CP, w, u / 32);
      if constexpr (u == 60 || u == 96 || u == 140) pp = peek(L, H_PP);
      if constexpr (u == 64) need_pp<false>(L, pp, 3u);
      if constexpr (u == 100) need_pp<true>(L, pp, 4u);
      if constexpr (u == 144) need_pp<false>(L, pp, 5u);
      const v4u cur = wb[u % PD];
      if constexpr (u + PD < 192) wb[u % PD] = WGU(u + PD);
      if constexpr (u >= 72 && q == 32) cf0 = *(LAS v4u*)(L + L_CF + i * 512 + l31 * 16);
      if constexpr (u >= 72 && q == 33) cf1 = *(LAS v4u*)(L + L_CF + i * 512 + l31 * 16);
      mfma_pin(cur, a[s % AW], acc[i]);
      if constexpr (u < 72 && i == 5) a[s % AW] = ldfrag<s + AW>(rs, lane * 16, pb + PN_X1 + w * 32768);
      if constexpr (u < 2 * (AW - 8) && !(u & 1)) a[8 + (u >> 1)] = ldfrag<8 + (u >> 1)>(rs, lane * 16, pb + PN_X1 + w * 32768);
      if constexpr (u == 24) mark(L, H_CP, w, 1u);
      if constexpr (u == 101) bcor = *(LAS v4u*)(L + L_KVF + w * 512 + l31 * 16);
      if constexpr (u == 107) { rr = __uint_as_float(bcor.w); bcor = v4u{hh ? 0u : bcor.x, hh ? 0u : bcor.y, hh ? 0u : bcor.z, 0u}; }
      if constexpr (u >= 72 && q == 38) mfma_pin(cf0, bcor, acc[i]);
      if constexpr (u >= 72 && q == 39) mfma_pin(cf1, bcor, acc[i]);
      if constexpr (u >= 112 && q >= 1 && q < 9) {
        epi(IC<(i & 6) - 2 + (q - 1) / 4>{}, IC<2 * ((q - 1) % 4)>{});
        epi(IC<(i & 6) - 2 + (q - 1) / 4>{}, IC<2 * ((q - 1) % 4) + 1>{});
      }
      if constexpr (u == 112 + GPUB) publish(rs, fw + FL_G + w * 3, mem, lane);
      if constexpr (u == GASK) fl = ask(rs, fi, lane);
      if constexpr (u == 176) {
        early = (__builtin_amdgcn_readfirstlane(fl) & 0xFFFFu) == 0xFFFFu;
        __builtin_amdgcn_fence(__ATOMIC_ACQUIRE, "wavefront");
        RELOAD();
        gv = early ? lane * 16 : WS_BYTES;
      }
      if constexpr (u == 180) { VMCNT(4); post(1); }
      if constexpr (u >= 176) g[u - 176] = ldfrag<u - 176>(rs, gv, gi);
      if constexpr (u == 184) pp = peek(L, H_PP);
      SB0();
    });
    mark(L, H_CP, w, 6u);
    TS(15);
    need_pp<true>(L, pp, 6u);
    WDN_BASES();
    static_for<0, PD>([&](auto U) { wb[U.value] = WDN(P_WD, U.value); });
    if (!early) {
      spin(rs, fi, 0xFFFFu, lane);
      __builtin_amdgcn_fence(__ATOMIC_ACQUIRE, "wavefront");
      RELOAD();
      static_for<0, 16>([&](auto T) { g[T.value] = ld128(rs, lane * 16, gi + T.value * 1024); });
    }
    SB0();
    fl = ask(rs, fi + 1, lane);
    SB0();
    TS(18);
    f32x16 c0 = zero16, c1 = zero16;
    static_for<0, 86>([&](auto U) {
      constexpr int u = U.value;
      if constexpr (u == 18) mark(L, H_CP, w, 7u);
#define GL(t) ((t) < 32 ? GU0 + (t) - 16 : GU2 + (t) - 32)
      if constexpr (u == PUB2) {
        asm volatile("s_waitcnt vmcnt(%0)" ::"i"(PUB2 - GU0 < 16 ? PUB2 - GU0 : 16) : "memory");
        post(2);
      }
      if constexpr (u == 24 || u == 40 || u == 56) pp = peek(L, H_PP);
      if constexpr (u == 28 || u == 44 || u == 60) need_pp<true>(L, pp, 7u + (u - 28) / 16);
      if constexpr (u == GL(16)) poll(fl, rs, fi + 1, 0xFFFFu, lane);
      if constexpr (u == GL(32) - GA2) fl = ask(rs, fi + 2, lane);
      if constexpr (u == GL(32)) poll(fl, rs, fi + 2, 0x7FFu, lane);
      const v4u cur = wb[u % PD];
      if constexpr (u + PD < 86) wb[u % PD] = WDN(P_WD, u + PD);
      if constexpr (u & 1) mfma_pin(cur, g[u >> 1], c0); else mfma_pin(cur, g[u >> 1], c1);
      if constexpr (u >= 1 && u < 9) {
        epi(IC<4 + (u - 1) / 4>{}, IC<2 * ((u - 1) % 4)>{});
        epi(IC<4 + (u - 1) / 4>{}, IC<2 * ((u - 1) % 4) + 1>{});
      }
      static_for<16, 43>([&](auto T) { if constexpr (GL(T.value) == u) g[T.value] = ldfrag<T.value>(rs, lane * 16, gi); });
      SB0();
    });
#pragma unroll
    for (int g = 0; g < 4; ++g) *(LAS f32x4*)(L + L_T + w * TW + g * 1024 + lane * 16) = f32x4{c1[4 * g], c1[4 * g + 1], c1[4 * g + 2], c1[4 * g + 3]};
    f32x4 gb[4];
#pragma unroll
    for (int g = 0; g < 4; ++g) gb[g] = *(LAS f32x4*)(L + L_VEC + 512 + (kh * 32 + 8 * g + 4 * hh) * 4);
    TS(19);
    MARK(H_C0, w, 5u);
    reach1(L, H_C0, w ^ 2, 5u);
    f32x4 o[4];
#pragma unroll
    for (int g = 0; g < 4; ++g) o[g] = *(LAS f32x4*)(L + L_T + (w ^ 2) * TW + g * 1024 + lane * 16);
    SB0();
    const rsrc_t ro = __builtin_amdgcn_make_buffer_rsrc((void*)p.out, 0, 2048 * D * 2, 0x00020000);
    v2u r[4];
#pragma unroll
    for (int g = 0; g < 4; ++g)
#pragma unroll
      for (int j = 0; j < 2; ++j)
        r[g][j] = pk(lo16(x1p[2 * g + j]) + gb[g][2 * j] * (c0[4 * g + 2 * j] + o[g][2 * j]),
                     hi16(x1p[2 * g + j]) + gb[g][2 * j + 1] * (c0[4 * g + 2 * j + 1] + o[g][2 * j + 1]));
    const int oo = ((panel * 128 + strip2 * 32 + l31) * D + ncol + 8 * hh) * 2;
#pragma unroll
    for (int g = 0; g < 4; g += 2) {
      const auto x = __builtin_amdgcn_permlane32_swap(r[g][0], r[g + 1][0], false, false);
      const auto y = __builtin_amdgcn_permlane32_swap(r[g][1], r[g + 1][1], false, false);
      __builtin_amdgcn_raw_buffer_store_b128(v4u{x[0], y[0], x[1], y[1]}, ro, oo + g * 16, 0, OAUX);
    }
  }
  if (b == 0 && tid == 0) __hip_atomic_store((GAS u32*)(ws + OFF_EPOCH), tag, RLX_AGENT);
}
}

extern "C" void kernel_launch(void* const* d_in, const int* in_sizes, int n_in, void* d_out, int out_size, void* d_ws,
                              size_t ws_size, hipStream_t stream) {
  using namespace mk;
  Params p;
  p.x = (const u16*)d_in[0]; p.wqkv = (const u16*)d_in[1]; p.wphi = (const u16*)d_in[2]; p.wo = (const u16*)d_in[3];
  p.wg = (const u16*)d_in[5]; p.wu = (const u16*)d_in[6]; p.wd = (const u16*)d_in[7]; p.c = (const u16*)d_in[8];
  p.wmod = (const u16*)d_in[9]; p.out = (u16*)d_out; p.ws = (unsigned char*)d_ws;
  (void)hipFuncSetAttribute((const void*)mega, hipFuncAttributeMaxDynamicSharedMemorySize, LDS_BYTES);
  mega<<<256, 512, LDS_BYTES, stream>>>(p);
}
```

```cpp
#define MK_LA 15
#define MK_LB 19
#define LCLK() __builtin_amdgcn_s_memrealtime()
#define LMUL 10
#define OAUX 2
#include <hip/hip_runtime.h>

namespace mk {
typedef unsigned short u16;
typedef unsigned int u32;
typedef unsigned long long u64;
typedef u32 v4u __attribute__((ext_vector_type(4)));
typedef u32 v2u __attribute__((ext_vector_type(2)));
typedef float f32x2 __attribute__((ext_vector_type(2)));
typedef float f32x4 __attribute__((ext_vector_type(4)));
typedef float f32x16 __attribute__((ext_vector_type(16)));
typedef __bf16 bf16x2 __attribute__((ext_vector_type(2)));
typedef __bf16 bf16x8 __attribute__((ext_vector_type(8)));
typedef short s16x4 __attribute__((ext_vector_type(4)));
#define LAS __attribute__((address_space(3)))
#define GAS __attribute__((address_space(1)))
#define DI __device__ __forceinline__
#define RLX_AGENT __ATOMIC_RELAXED, __HIP_MEMORY_SCOPE_AGENT
#define SB0() __builtin_amdgcn_sched_barrier(0)
typedef __amdgpu_buffer_rsrc_t rsrc_t;

constexpr int D = 512, F = 1376;
constexpr int OFF_EPOCH = 0;
constexpr int OFF_MODG = 64 << 10;
constexpr int OFF_KVZ = 128 << 10;
constexpr int OFF_CG = 192 << 10;
constexpr int CG_PART = 2 * F * 16;
constexpr int OFF_ARR = 384 << 10;
constexpr int OFF_MODR = 512 << 10;
constexpr int OFF_KVE = 1 << 20;
constexpr int OFF_PAN = 2 << 20;
constexpr int PN_H = 0;
constexpr int PN_O = 128 << 10;
constexpr int PN_X1 = 256 << 10;
constexpr int PN_G = 384 << 10;
constexpr int PN_ST = 768 << 10;
constexpr int PN_FL = 800 << 10;
constexpr int FL_H = 0, FL_O = 4, FL_X = 8, FL_G = 12, FL_KV = 384;
constexpr int PSTR = (848 << 10) + 4224;
constexpr int WS_BYTES = 16 << 20;
constexpr int OFF_TS = 32 << 20;
constexpr int RING = 136;
constexpr int L_T = RING * 1024, TW = 4352;
constexpr int L_KVF = L_T + 4 * TW;
constexpr int L_ZF = L_KVF + 2048;
constexpr int L_VEC = L_ZF + 256;
constexpr int L_CF = L_VEC + 768;
constexpr int L_HINT = L_CF + 3072;
constexpr int LDS_BYTES = L_HINT + 256;
static_assert(LDS_BYTES <= 163840, "LDS");
constexpr int P_KV = 0, P_Q = 64, P_WO = 96, P_GU = 160, P_WD = 352;
constexpr int H_CP = 6, H_PP = 7, H_C0 = 8, H_P0 = 9;
constexpr int pair_of(int t) { return ((t & 15) < 11 ? 3 * (t & 15) : 2 * (t & 15) + 11) + (t >> 4); }
constexpr int slot(int n) { return (n % RING) * 1024; }
constexpr int AUX_SC1 = 16;
constexpr u32 SPIN_CAP = 1u << 16;
#define PRE_MOD 20
#define HI_DELAY 8
#define POLL_GAP 6
#define PRE_FL 4
#define KV1 32
#define PGU 4
#define PKV 0
#define GPUB 26
#define LPRIO 0
#define APRIO 3
#define TMOD 0
#define CW 3
#define DW 0
#define CSLP 3
#define PG2 2
#define TLBT 0
#define LITE 1
#define EQ 1
#define PQE 2
#define GASK 158
#define PUB2 24
#define WG0 16
#define GU0 12
#define GLA 16
#define GA2 18
#define GU2 50
#define W1N 8
#define W2N 6
#define PG3 4
#define QT 1
#define W0 12
#define PDV 5
#define WPC 0
#define WPACE() do { if (WPC) { SB0(); __builtin_amdgcn_s_sleep(WPC); SB0(); } } while (0)
constexpr int PD = PDV;
constexpr int CODE_BYTES = 47000, CODE_KB = 48;

#define NBV(x) (x)
struct Params {
  const u16 *x, *wqkv, *wphi, *wo, *wg, *wu, *wd, *c, *wmod;
  u16* out;
  unsigned char* ws;
};

template <int V> struct IC { static constexpr int value = V; };
template <int I, int N, class Fn> DI void static_for(Fn&& f) {
  if constexpr (I < N) { f(IC<I>{}); static_for<I + 1, N>(f); }
}
DI float lo16(u32 v) { return __uint_as_float(v << 16); }
DI float hi16(u32 v) { return __uint_as_float(v & 0xffff0000u); }
DI u32 pk(float a, float b) { f32x2 v = {a, b}; return __builtin_bit_cast(u32, __builtin_convertvector(v, bf16x2)); }
DI float rb(float a) { return lo16(pk(a, 0.f)); }
DI float rcp(float a) { return __builtin_amdgcn_rcpf(a); }
DI f32x16 mfma(v4u a, v4u b, f32x16 c) {
  return __builtin_amdgcn_mfma_f32_32x32x16_bf16(__builtin_bit_cast(bf16x8, a), __builtin_bit_cast(bf16x8, b), c, 0, 0, 0);
}
DI void mfma_pin(v4u a, v4u b, f32x16& c) {
  c = mfma(a, b, c);
  asm volatile("" : "+v"(c));
}
template <int O> DI v4u pack8(const f32x16& x) {
  v4u r; r.x = pk(x[O], x[O + 1]); r.y = pk(x[O + 2], x[O + 3]); r.z = pk(x[O + 4], x[O + 5]); r.w = pk(x[O + 6], x[O + 7]); return r;
}
template <int T> DI v4u ldw(LAS unsigned char* p) {
  s16x4 a = __builtin_amdgcn_ds_read_tr16_b64_v4i16((LAS s16x4*)p);
  s16x4 b = __builtin_amdgcn_ds_read_tr16_b64_v4i16((LAS s16x4*)(p + T));
  return __builtin_bit_cast(v4u, __builtin_shufflevector(a, b, 0, 1, 2, 3, 4, 5, 6, 7));
}
struct Base3 {
  u32 b[3];
  DI explicit Base3(LAS unsigned char* p) {
    b[0] = (u32)(size_t)p; b[1] = b[0] + 65536u; b[2] = b[0] + 131072u;
    asm volatile("" : "+v"(b[0]), "+v"(b[1]), "+v"(b[2]));
  }
  template <int OFF> DI LAS unsigned char* at() const { return (LAS unsigned char*)(size_t)b[OFF >> 16] + (OFF & 65535); }
};
template <int LOFF, int SOFF> DI void gldsi(rsrc_t rs, u32 voff, u32 sbase, u32 lbase) {
  u32 t;
  asm volatile("s_add_u32 m0, %1, %2\n\ts_add_u32 %0, %3, %4\n\tbuffer_load_dwordx4 %5, %6, %0 offen lds"
               : "=&s"(t) : "s"(lbase), "i"(LOFF), "s"(sbase), "i"(SOFF), "v"(voff), "s"(rs) : "m0", "scc", "memory");
}
DI void bar() { asm volatile("s_waitcnt lgkmcnt(0)\n\ts_barrier" ::: "memory"); }
DI void bar_raw() { asm volatile("s_barrier" ::: "memory"); }
DI void drain() { asm volatile("s_waitcnt vmcnt(0)" ::: "memory"); }
#define VMCNT(n) asm volatile("s_waitcnt vmcnt(" #n ")" ::: "memory")
#define RELOAD() asm volatile("" ::: "memory")
DI v4u ld128(rsrc_t rs, int voff, int soff) { return __builtin_amdgcn_raw_buffer_load_b128(rs, voff, soff, AUX_SC1); }
DI v2u ld64(rsrc_t rs, int voff, int soff) { return __builtin_amdgcn_raw_buffer_load_b64(rs, voff, soff, AUX_SC1); }
DI void st128(v4u v, rsrc_t rs, int voff, int soff) { __builtin_amdgcn_raw_buffer_store_b128(v, rs, voff + soff, 0, AUX_SC1); }
DI void st64(v2u v, rsrc_t rs, int voff, int soff) { __builtin_amdgcn_raw_buffer_store_b64(v, rs, voff, soff, AUX_SC1); }
template <int S> DI v4u ldfrag(rsrc_t rs, int voff, int soff) { return ld128(rs, voff + (S & 3) * 1024, soff + (S >> 2) * 4096); }
template <int W, int N> DI void ldfrags(v4u (&a)[N], rsrc_t rs, int voff, int soff) {
  static_for<0, W>([&](auto S) { a[S.value] = ldfrag<S.value>(rs, voff, soff); SB0(); });
}
template <int CTRL> DI float dpp_add(float v) {
  return v + __uint_as_float(__builtin_amdgcn_update_dpp(0u, __float_as_uint(v), CTRL, 0xf, 0xf, false));
}
constexpr int DPP_X1 = 0xB1, DPP_X2 = 0x4E, DPP_HM = 0x141, DPP_ROR4 = 0x124, DPP_ROR8 = 0x128;
DI float fold32(float x, float y) {
  auto r = __builtin_amdgcn_permlane32_swap(__float_as_uint(x), __float_as_uint(y), false, false);
  return __uint_as_float(r[0]) + __uint_as_float(r[1]);
}
DI float fold16(float x, float y) {
  auto r = __builtin_amdgcn_permlane16_swap(__float_as_uint(x), __float_as_uint(y), false, false);
  return __uint_as_float(r[0]) + __uint_as_float(r[1]);
}
DI void red8q(const float (&a)[8], float& c0, float& c1) {
  const float b0 = fold32(a[0], a[4]), b1 = fold32(a[1], a[5]), b2 = fold32(a[2], a[6]), b3 = fold32(a[3], a[7]);
  c0 = fold16(b0, b2); c1 = fold16(b1, b3);
  c0 = dpp_add<DPP_ROR4>(c0); c1 = dpp_add<DPP_ROR4>(c1);
  c0 = dpp_add<DPP_ROR8>(c0); c1 = dpp_add<DPP_ROR8>(c1);
}
DI void red8(const float (&a)[8], float& c0, float& c1) {
  red8q(a, c0, c1);
  c0 = dpp_add<DPP_X1>(c0); c1 = dpp_add<DPP_X1>(c1);
  c0 = dpp_add<DPP_X2>(c0); c1 = dpp_add<DPP_X2>(c1);
}
DI void publish(rsrc_t rs, int word, int bit, int lane) {
  drain();
  (void)__builtin_amdgcn_raw_ptr_buffer_atomic_add_i32(1 << bit, rs, lane == 0 ? 0 : WS_BYTES, OFF_ARR + word * 128, 0);
}
DI u32 ask(rsrc_t rs, int word, int lane) { return __builtin_amdgcn_raw_ptr_buffer_atomic_add_i32(0, rs, lane == 0 ? 0 : WS_BYTES, OFF_ARR + word * 128, 0); }
DI void spin(rsrc_t rs, int word, u32 m, int lane) {
#pragma nounroll
  for (u32 spins = 0; spins < SPIN_CAP; ++spins)
    if ((__builtin_amdgcn_readfirstlane(ask(rs, word, lane)) & m) == m) break;
}
DI void poll(u32 f, rsrc_t rs, int word, u32 m, int lane) {
  if (__builtin_expect((__builtin_amdgcn_readfirstlane(f) & m) != m, 0)) spin(rs, word, m, lane);
  __builtin_amdgcn_fence(__ATOMIC_ACQUIRE, "wavefront");
  RELOAD();
}
DI void waitn(rsrc_t rs, int word, u32 m, int lane) {
  if (PRE_FL) __builtin_amdgcn_s_sleep(PRE_FL);
  spin(rs, word, m, lane);
  __builtin_amdgcn_fence(__ATOMIC_ACQUIRE, "wavefront");
  RELOAD();
}
DI void sig(LAS unsigned char* L, int i, int k) { *(volatile LAS u32*)(L + L_HINT + 16 * i + 4 * k) = 1u; }
template <int N> DI void await(LAS unsigned char* L, int i) {
#pragma nounroll
  for (u32 spins = 0; spins < (1u << 20); ++spins) {
    const v4u v = *(volatile LAS v4u*)(L + L_HINT + 16 * i);
    if (__builtin_amdgcn_readfirstlane(N == 1 ? v.x : (v.x & v.y & v.z & v.w))) break;
    __builtin_amdgcn_s_sleep(1);
  }
  RELOAD();
}
DI void mark(LAS unsigned char* L, int i, int k, u32 val) { *(volatile LAS u32*)(L + L_HINT + 16 * i + 4 * k) = val; }
DI v4u peek(LAS unsigned char* L, int i) { return *(volatile LAS v4u*)(L + L_HINT + 16 * i); }
DI u32 umin(u32 a, u32 b) { return a < b ? a : b; }
DI void await_cp(LAS unsigned char* L, u32 k) {
#pragma nounroll
  for (u32 spins = 0; spins < (1u << 20); ++spins) {
    const v4u v = peek(L, H_CP);
    if (__builtin_amdgcn_readfirstlane(umin(umin(v.x, v.y), umin(v.z, v.w))) >= k) break;
    __builtin_amdgcn_s_sleep(CSLP);
  }
  RELOAD();
}
template <int SLP> DI void reach(LAS unsigned char* L, int i, u32 k0, u32 k1, u32 k2, u32 k3) {
#pragma nounroll
  for (u32 spins = 0; spins < (1u << 20); ++spins) {
    const v4u v = peek(L, i);
    if (__builtin_amdgcn_readfirstlane((v.x >= k0) & (v.y >= k1) & (v.z >= k2) & (v.w >= k3))) break;
    if (SLP) __builtin_amdgcn_s_sleep(SLP);
  }
  RELOAD();
}
DI void reach1(LAS unsigned char* L, int i, int k, u32 val) {
#pragma nounroll
  for (u32 spins = 0; spins < (1u << 20); ++spins)
    if (__builtin_amdgcn_readfirstlane(*(volatile LAS u32*)(L + L_HINT + 16 * i + 4 * k)) >= val) break;
  RELOAD();
}
#define MARK(i, k, val) do { RELOAD(); mark(L, i, k, val); RELOAD(); } while (0)
template <bool AUX> DI u32 pp_min(v4u v) {
  u32 m = umin(v.x, v.y);
  if (AUX) m = umin(m, umin(v.z, v.w));
  return __builtin_amdgcn_readfirstlane(m);
}
template <bool AUX> DI void need_pp(LAS unsigned char* L, v4u v, u32 k) {
  if (__builtin_expect(pp_min<AUX>(v) < k, 0)) {
#pragma nounroll
    for (u32 spins = 0; spins < (1u << 20); ++spins)
      if (pp_min<AUX>(peek(L, H_PP)) >= k) break;
  }
  RELOAD();
}
DI void feat(const f32x16& P, u32 (&ph)[16]) {
#pragma clang fp contract(off)
  constexpr float L2E = 1.4426950408889634f;
  f32x2 p[8];
#pragma unroll
  for (int i = 0; i < 8; ++i) { const u32 t = pk(P[2 * i], P[2 * i + 1]); p[i] = f32x2{lo16(t), hi16(t)} * L2E; }
  float mx[4], mn[4];
#pragma unroll
  for (int i = 0; i < 4; ++i) {
    mx[i] = fmaxf(fmaxf(p[2 * i].x, p[2 * i].y), fmaxf(p[2 * i + 1].x, p[2 * i + 1].y));
    mn[i] = fminf(fminf(p[2 * i].x, p[2 * i].y), fminf(p[2 * i + 1].x, p[2 * i + 1].y));
  }
  const float mxl = fmaxf(fmaxf(mx[0], mx[1]), fmaxf(mx[2], mx[3])), mnl = fminf(fminf(mn[0], mn[1]), fminf(mn[2], mn[3]));
  f32x2 e1[8], e2[8];
#pragma unroll
  for (int i = 0; i < 8; ++i) { e1[i] = p[i] - mxl; e2[i] = mnl - p[i]; }
  SB0();
#pragma unroll
  for (int i = 0; i < 8; ++i) {
    e1[i] = f32x2{__builtin_amdgcn_exp2f(e1[i].x), __builtin_amdgcn_exp2f(e1[i].y)};
    e2[i] = f32x2{__builtin_amdgcn_exp2f(e2[i].x), __builtin_amdgcn_exp2f(e2[i].y)};
  }
  const f32x2 s1 = ((e1[0] + e1[1]) + (e1[2] + e1[3])) + ((e1[4] + e1[5]) + (e1[6] + e1[7]));
  const f32x2 s2 = ((e2[0] + e2[1]) + (e2[2] + e2[3])) + ((e2[4] + e2[5]) + (e2[6] + e2[7]));
  const float r1 = rcp(s1.x + s1.y), r2 = rcp(s2.x + s2.y);
#pragma unroll
  for (int i = 0; i < 8; ++i) { const f32x2 u = e1[i] * r1, v = e2[i] * r2; ph[i] = pk(u.x, u.y); ph[8 + i] = pk(v.x, v.y); }
}

#define TSCLK() __builtin_amdgcn_s_memrealtime()
#define TS(i) do { if ((i) == 0) { lk0 = __builtin_amdgcn_s_memrealtime(); __builtin_amdgcn_s_waitcnt(0xC07F); } if ((i) == MK_LA) { SB0(); lka = LCLK(); __builtin_amdgcn_s_waitcnt(0xC07F); SB0(); } if ((i) == MK_LB) { SB0(); lkb = LCLK(); __builtin_amdgcn_s_waitcnt(0xC07F); SB0(); } } while (0)
#define TS_DUMP() do {} while (0)
#define TS_INIT() u64 lk0 = 0, lka = 0, lkb = 0

__global__ void __launch_bounds__(512, 2) mega(Params p) {
  extern __shared__ __attribute__((aligned(16))) unsigned char smem[];
  LAS unsigned char* L = (LAS unsigned char*)smem;
  TS_INIT();
  TS(0);
  u64 pc0;
  asm volatile("s_getpc_b64 %0" : "=s"(pc0));
  const int tid = threadIdx.x, lane = tid & 63;
  int w = __builtin_amdgcn_readfirstlane(tid >> 6);
  const int b = blockIdx.x, xcd = b & 7, rank = b >> 3, panel = (xcd >> 1) * 4 + (rank >> 3), mem = (xcd & 1) * 8 + (rank & 7);
  const int hh = lane >> 5, l31 = lane & 31;
  unsigned char* ws = p.ws;
  const int rowh = mem & 1, colg = mem >> 1;
  const int pair0 = min(3 * mem, 2 * mem + 11);
  const bool three = mem < 11;
  const rsrc_t rs = __builtin_amdgcn_make_buffer_rsrc(ws, 0, WS_BYTES, 0x00020000);
  const int pb = OFF_PAN + panel * PSTR, fw = panel * 24;
  const int mcg = xcd * 8 + (rank >> 2), mkp = rank & 3;
  const rsrc_t rqkv = __builtin_amdgcn_make_buffer_rsrc((void*)p.wqkv, 0, D * 3 * D * 2, 0x00020000);
  const rsrc_t rwo = __builtin_amdgcn_make_buffer_rsrc((void*)p.wo, 0, D * D * 2, 0x00020000);
  const rsrc_t rgu = __builtin_amdgcn_make_buffer_rsrc((void*)((w & 1) ? p.wu : p.wg), 0, D * F * 2, 0x00020000);
  const rsrc_t rwd = __builtin_amdgcn_make_buffer_rsrc((void*)p.wd, 0, F * D * 2, 0x00020000);

  v4u r0[4];
  u32 cv0[4], epoch;
  u32 vq = (lane >> 2) * (3 * D * 2) + (lane & 3) * 16;
  asm volatile("" : "+v"(vq));
  {
    const u32 ld = (u32)(size_t)L + w * 1024;
    if (w <= 4) {
      const rsrc_t r1 = __builtin_amdgcn_make_buffer_rsrc((void*)(w == 4 ? p.wmod : p.x), 0, w == 4 ? D * 6 * D * 2 : 2048 * D * 2, 0x00020000);
      const int vo = w == 4 ? (128 * mkp + 4 * (lane >> 1)) * (6 * D * 2) + mcg * 32 + (lane & 1) * 16
                            : (panel * 128 + mem * 8 + (lane >> 3)) * (D * 2) + (lane & 7) * 16 + 256 * w;
#pragma unroll
      for (int i = 0; i < 4; ++i) r0[i] = __builtin_amdgcn_raw_buffer_load_b128(r1, vo, w == 4 ? i * (6 * D * 2) : i < 2 ? i * 128 : 1 << 30, 0);
      SB0();
    }
    {
      const rsrc_t r2 = __builtin_amdgcn_make_buffer_rsrc((void*)p.c, 0, w == 4 ? D * 2 : 0, 0x00020000);
      const v2u c2 = __builtin_amdgcn_raw_buffer_load_b64(r2, (128 * mkp + 4 * (lane >> 1)) * 2, 0, 0);
      cv0[0] = c2.x; cv0[1] = c2.y;
      epoch = __builtin_amdgcn_raw_buffer_load_b32(rs, 0, OFF_EPOCH, AUX_SC1);
      __builtin_amdgcn_raw_buffer_store_b32(0u, rs, (w == 4 && lane < 2) ? lane * 128 : WS_BYTES, OFF_ARR + b * 256, AUX_SC1);
      SB0();
    }
    if (w >= 5) {
      const rsrc_t rc = __builtin_amdgcn_make_buffer_rsrc((void*)pc0, 0, CODE_BYTES, 0x00020000);
      gldsi<slot(128), 0>(rc, lane < CODE_KB / 6 ? (u32)lane * 128u : 1u << 30, (u32)(((rank & 15) * 3 + w - 5) * (CODE_KB / 6) * 128), ld);
      const u32 tq = (rank * 16 + (lane >> 2)) * (3 * D * 2) + (xcd & 1) * 512 + (lane & 3) * 128;
      if (EQ || w != 5) gldsi<slot(128), 0>(rqkv, tq, (w - 5) * (D * 2), ld);
      if (w == 5) {
        gldsi<slot(128), 0>(rs, lane < 2 ? (u32)(pb + lane * (848 << 10)) : 1u << 30, 0u, ld);
        const rsrc_t rp = __builtin_amdgcn_make_buffer_rsrc((void*)p.wphi, 0, 32 * 256 * 2, 0x00020000);
        gldsi<L_CF + 2048, 0>(rp, lane * 16, mem * 1024, (u32)(size_t)L);
      }
    }
    if (tid < 64) *((LAS u32*)(L + L_HINT) + tid) = 0u;
    TS(27);
    bar();
    asm volatile("" : "+s"(w));
    if (w >= 6) {
      u32 lo = (u32)(size_t)L + (w & 1) * 1024, so = (D + (w & 1) * D + mem * 32) * 2;
#pragma nounroll
      for (int i = 0; i < KV1 / 2; ++i) {
        gldsi<0, 0>(rqkv, vq, so, lo);
        gldsi<2048, 16 * (3 * D * 2)>(rqkv, vq, so, lo);
        lo += 4096; so += 32 * (3 * D * 2);
      }
    }
    if (w == 4) {
      __builtin_amdgcn_s_setprio(3);
      TS(10);
      float sl[4], acc[8];
#pragma unroll
      for (int i = 0; i < 4; ++i) { const float c = (i & 1) ? hi16(cv0[i >> 1]) : lo16(cv0[i >> 1]); sl[i] = rb(c * rcp(1.f + __expf(-c))); }
      u32 tag = epoch;
      asm volatile("" : "+v"(tag));
      tag += 1u;
      TS(11);
#pragma unroll
      for (int j = 0; j < 8; ++j) acc[j] = 0.f;
#pragma unroll
      for (int i = 0; i < 4; ++i)
#pragma unroll
        for (int j = 0; j < 4; ++j) { acc[2 * j] += sl[i] * lo16(r0[i][j]); acc[2 * j + 1] += sl[i] * hi16(r0[i][j]); }
      float c0, c1;
      TS(12);
      red8q(acc, c0, c1);
      c0 = dpp_add<DPP_X2>(c0); c1 = dpp_add<DPP_X2>(c1);
      const int go = ((lane >> 1) & 7) * 32768 + (8 * (lane & 1) + 2 * (lane >> 4)) * 8;
      const v4u gv = {__float_as_uint(c0), tag, __float_as_uint(c1), tag};
      drain();
      st128(gv, rs, go, OFF_MODR + mkp * 8192 + mcg * 128);
      st128(gv, rs, go, OFF_MODR + 8 * 32768 + mkp * 8192 + mcg * 128);
      sig(L, 0, 0);
    }
    asm volatile("" : "+s"(w));
  }

  if (w == 4 || w == 5) {
    const int a = w & 1;
    u32 tag;
    tag = epoch;
    asm volatile("" : "+v"(tag));
    tag += 1u;
    drain();
    TS(1);
    bar();
    __builtin_amdgcn_s_setprio(APRIO);
    await<4>(L, 2);
    const int kp = xcd >> 1, cpair = (xcd & 1) * 24 + rank;
    const bool con = rank < ((xcd & 1) ? 19 : 24);
    const int col0 = a * F + cpair * 32;
    v4u wm[8];
    if (a == 1) {
      v4u wn[8]; u32 cv[8];
      const int row0 = 128 * mkp + (lane >> 2);
#pragma unroll
      for (int i = 0; i < 8; ++i) cv[i] = p.c[row0 + 16 * i];
#pragma unroll
      for (int i = 0; i < 8; ++i) wn[i] = *(const GAS v4u*)(p.wmod + (size_t)(row0 + 16 * i) * (6 * D) + 1024 + mcg * 32 + (lane & 3) * 8);
      SB0();
      float acc[8];
#pragma unroll
      for (int j = 0; j < 8; ++j) acc[j] = 0.f;
#pragma unroll
      for (int i = 0; i < 8; ++i) {
        const float c = lo16(cv[i]), sl = rb(c * rcp(1.f + __expf(-c)));
#pragma unroll
        for (int j = 0; j < 4; ++j) { acc[2 * j] += sl * lo16(wn[i][j]); acc[2 * j + 1] += sl * hi16(wn[i][j]); }
      }
      float c0, c1;
      red8q(acc, c0, c1);
      st128(v4u{__float_as_uint(c0), tag, __float_as_uint(c1), tag}, rs, (lane & 12) == 0 ? (8 * (lane & 3) + 2 * (lane >> 4)) * 8 : WS_BYTES,
            OFF_MODG + mkp * 16384 + mcg * 256);
    }
    reach<1>(L, H_C0, 2u, 2u, 2u, 2u);
    TS(2);
    {
      const int idx0 = (mem * 4 + 2 * a) * 16;
      v4u e[32], zz[2];
      {
        u32 todo = 0xFFu;
        for (u32 spins = 0; spins < SPIN_CAP && todo; ++spins) {
          const u32 bad = ~__builtin_amdgcn_readfirstlane(ask(rs, FL_KV + mem * 2 + a, lane));
          __builtin_amdgcn_fence(__ATOMIC_ACQUIRE, "wavefront");
          RELOAD();
          static_for<0, 8>([&](auto G) {
            constexpr int g = G.value;
            if (((todo >> g) & 1u) && !(bad & (0x00030003u << (2 * g)))) {
              todo &= ~(1u << g);
              e[2 * g] = ld128(rs, lane * 16, OFF_KVE + (idx0 + 2 * g) * 1024);
              e[2 * g + 1] = ld128(rs, lane * 16, OFF_KVE + (idx0 + 2 * g + 1) * 1024);
              e[16 + 2 * g] = ld128(rs, lane * 16, OFF_KVE + (idx0 + 16 + 2 * g) * 1024);
              e[17 + 2 * g] = ld128(rs, lane * 16, OFF_KVE + (idx0 + 17 + 2 * g) * 1024);
            }
          });
        }
      }
      TS(3);
      zz[0] = ldfrag<0>(rs, lane * 16, OFF_KVZ + idx0 * 64);
      zz[1] = ldfrag<1>(rs, lane * 16, OFF_KVZ + idx0 * 64);
      SB0();
#pragma unroll
      for (int q = 0; q < 2; ++q) {
        f32x4 s0 = __builtin_bit_cast(f32x4, e[16 * q]), s1 = __builtin_bit_cast(f32x4, e[16 * q + 1]);
#pragma unroll
        for (int i = 2; i < 16; i += 2) { s0 += __builtin_bit_cast(f32x4, e[16 * q + i]); s1 += __builtin_bit_cast(f32x4, e[16 * q + i + 1]); }
        s0 += s1;
        *(LAS v2u*)(L + L_KVF + (2 * a + q) * 512 + l31 * 16 + hh * 8) = v2u{pk(s0[0], s0[1]), pk(s0[2], s0[3])};
      }
      f32x4 z;
#pragma unroll
      for (int j = 0; j < 4; ++j) {
        float t = fold32(__uint_as_float(zz[0][j]), __uint_as_float(zz[1][j]));
        t = fold16(t, t);
        t = dpp_add<DPP_ROR4>(t);
        z[j] = dpp_add<DPP_ROR8>(t);
      }
      if (l31 < 4) *(LAS v2u*)(L + L_ZF + (2 * a + hh) * 32 + ((lane & 1) ^ 1) * 16 + ((lane >> 1) & 1) * 8) = v2u{pk(z[0], z[1]), pk(z[2], z[3])};
    }
    MARK(H_P0, 2 + a, 1u);
    __builtin_amdgcn_s_setprio(1);
    TS(4);
#pragma unroll
    for (int i = 0; i < 8; ++i)
      wm[i] = __builtin_amdgcn_raw_buffer_load_b128(rgu, con ? (128 * kp + 2 * (lane >> 2)) * (F * 2) + cpair * 64 + (lane & 3) * 16 : 1 << 30, (32 * (i >> 1) + (i & 1)) * (F * 2), 0);
    SB0();
    u32 gsb[4], gsc[4];
    {
      const int vo = ((hh ? 2 : 3 * a) * D + colg * 64 + 2 * l31) * 8, ro = (D + 128 * kp + 2 * lane) * 8;
      v4u g[12];
      for (u32 spins = 0; spins < SPIN_CAP; ++spins) {
        RELOAD();
#pragma unroll
        for (int i = 0; i < 12; ++i) g[i] = ld128(rs, i < 4 ? vo : ro + (i >> 3) * (D * 8), OFF_MODG + (i & 3) * 16384);
        bool ok = true;
#pragma unroll
        for (int i = 0; i < 12; ++i) ok &= (g[i].y == tag) & (g[i].w == tag);
        if (__all(ok)) break;
      }
      u32 pr[3];
#pragma unroll
      for (int t = 0; t < 3; ++t)
        pr[t] = pk((__uint_as_float(g[4 * t].x) + __uint_as_float(g[4 * t + 1].x)) + (__uint_as_float(g[4 * t + 2].x) + __uint_as_float(g[4 * t + 3].x)),
                   (__uint_as_float(g[4 * t].z) + __uint_as_float(g[4 * t + 1].z)) + (__uint_as_float(g[4 * t + 2].z) + __uint_as_float(g[4 * t + 3].z)));
      const u32 v1 = pk(1.f + lo16(pr[0]), 1.f + hi16(pr[0])), vv = hh ? v1 : pr[0];
      *(LAS f32x2*)(L + L_VEC + (hh ? 1 : 2 * a) * 256 + l31 * 8) = f32x2{lo16(vv), hi16(vv)};
      const u32 sc = pk(1.f + lo16(pr[2]), 1.f + hi16(pr[2]));
#pragma unroll
      for (int i = 0; i < 4; ++i) {
        gsb[i] = __builtin_amdgcn_ds_bpermute((16 * i + (lane >> 2)) * 4, pr[1]);
        gsc[i] = __builtin_amdgcn_ds_bpermute((16 * i + (lane >> 2)) * 4, sc);
      }
    }
    MARK(H_P0, 2 + a, 2u);
    if (con) {
      float a1[8], a2[8];
#pragma unroll
      for (int j = 0; j < 8; ++j) { a1[j] = 0.f; a2[j] = 0.f; }
#pragma unroll
      for (int i = 0; i < 8; ++i) {
        const float sb = (i & 1) ? hi16(gsb[i >> 1]) : lo16(gsb[i >> 1]), sc = (i & 1) ? hi16(gsc[i >> 1]) : lo16(gsc[i >> 1]);
#pragma unroll
        for (int j = 0; j < 4; ++j) {
          a1[2 * j] += sc * lo16(wm[i][j]); a1[2 * j + 1] += sc * hi16(wm[i][j]);
          a2[2 * j] += sb * lo16(wm[i][j]); a2[2 * j + 1] += sb * hi16(wm[i][j]);
        }
      }
      float c10, c11, c20, c21;
      red8q(a1, c10, c11); red8q(a2, c20, c21);
      const int k = (lane >> 2) & 3;
      const float val = k == 0 ? c10 : k == 1 ? c20 : k == 2 ? c11 : c21;
      st64(v2u{__float_as_uint(val), tag}, rs, (col0 + 8 * (lane & 3) + 2 * (lane >> 4)) * 16 + k * 8, OFF_CG + kp * CG_PART);
    }
    TS(5);
    reach<2>(L, H_C0, 4u, 4u, 4u, 4u);
    {
      const int ntile = three ? 6 : 4;
      v4u cg[8];
      int off[2];
#pragma unroll
      for (int i = 0; i < 2; ++i) {
        int tile = 4 * i + 2 * a + hh; tile = tile < ntile ? tile : 0;
        off[i] = (32 * pair0 + 16 * tile + (lane & 15) + ((lane >> 4) & 1) * F) * 16;
      }
      for (u32 spins = 0; spins < SPIN_CAP; ++spins) {
        RELOAD();
        bool ok = true;
#pragma unroll
        for (int i = 0; i < 8; ++i) cg[i] = ld128(rs, off[i >> 2], OFF_CG + (i & 3) * CG_PART);
#pragma unroll
        for (int i = 0; i < 8; ++i) ok &= (cg[i].y == tag) & (cg[i].w == tag);
        if (__all(ok)) break;
      }
#pragma unroll
      for (int i = 0; i < 2; ++i) {
        const float c1 = (__uint_as_float(cg[4 * i].x) + __uint_as_float(cg[4 * i + 1].x)) + (__uint_as_float(cg[4 * i + 2].x) + __uint_as_float(cg[4 * i + 3].x));
        const float c2 = (__uint_as_float(cg[4 * i].z) + __uint_as_float(cg[4 * i + 1].z)) + (__uint_as_float(cg[4 * i + 2].z) + __uint_as_float(cg[4 * i + 3].z));
        const float h1 = rb(c1), m1 = rb(c1 - h1), l1 = c1 - h1 - m1, h2 = rb(c2), m2 = rb(c2 - h2), l2 = c2 - h2 - m2;
        const int tile = 4 * i + 2 * a + hh;
        if (tile < 6) *(LAS v4u*)(L + L_CF + tile * 512 + l31 * 16) = v4u{pk(h1, m1), pk(l1, h2), pk(m2, l2), 0u};
      }
    }
    reach<1>(L, 5, a < 1, a < 1, a, a);
    {
      v4u st[16];
#pragma unroll
      for (int t = 0; t < 16; ++t) st[t] = ld128(rs, l31 * 16 + hh * 8192 + t * 512, pb + PN_ST + 2 * a * 8192);
      for (u32 spins = 0; spins < SPIN_CAP; ++spins) {
        bool ok = true;
#pragma unroll
        for (int t = 0; t < 16; ++t) ok &= (st[t].y == tag) & (st[t].w == tag);
        if (__all(ok)) break;
        RELOAD();
#pragma unroll
        for (int t = 0; t < 16; ++t) st[t] = ld128(rs, l31 * 16 + hh * 8192 + t * 512, pb + PN_ST + 2 * a * 8192);
      }
      float ms = 0.f;
#pragma unroll
      for (int t = 0; t < 16; ++t) ms += __uint_as_float(st[t].x);
      const float mean = ms * (1.f / 16.f);
      float q = 0.f;
#pragma unroll
      for (int t = 0; t < 16; ++t) { const float d = __uint_as_float(st[t].x) - mean; q += __uint_as_float(st[t].z) + 32.f * d * d; }
      const float den = rb(__builtin_amdgcn_sqrtf(rb(rb(q * (1.f / D)) + 1e-5f)));
      *(LAS v4u*)(L + L_KVF + a * 1024 + lane * 16) = v4u{pk(-mean, -mean), pk(-mean, den), pk(den, den), __float_as_uint(rcp(den * den))};
    }
    RELOAD();
    mark(L, H_PP, 2 + a, 5u);
    TS(6);
    await_cp(L, 2);
    {
      v4u wt[58];
      const u32 vd = (lane >> 2) * (D * 2) + (lane & 3) * 16, sd = a * 16 * (D * 2) + colg * 128;
      const Base3 wdst(L + a * 2048 + lane * 16);
      static_for<28, 86>([&](auto J) {
        wt[J.value - 28] = __builtin_amdgcn_raw_buffer_load_b128(rwd, NBV(vd + (J.value & 1) * 64), sd + pair_of(J.value >> 1) * 32 * (D * 2), 0); SB0(); });
      TS(7);
#define ST_WT(T0, T1, k) do { static_for<2 * (T0), 2 * (T1)>([&](auto J) { \
      *(LAS v4u*)wdst.template at<slot(P_WD + 4 * (J.value >> 1)) + (J.value & 1) * 1024>() = wt[J.value - 28]; WPACE(); }); \
      RELOAD(); mark(L, H_PP, 2 + a, k); RELOAD(); } while (0)
      await_cp(L, 4);
      ST_WT(14, 18, 6u);
      await_cp(L, 5);
      ST_WT(18, 26, 7u);
      await_cp(L, 6);
      ST_WT(26, 34, 8u);
      TS(8);
      await_cp(L, 7);
      ST_WT(34, 43, 9u);
      TS(9);
    }
    TS_DUMP();
    return;
  }

  if (w >= 6) {
    const int v = w & 1;
    __builtin_amdgcn_s_setprio(LPRIO);
    const u32 ldsb = (u32)(size_t)L;
    const u32 l1 = ldsb + v * 1024, l2 = ldsb + v * 2048;
    const u32 skv = (D + v * D + mem * 32) * 2, sq = v * 16 * (3 * D * 2) + mem * 64;
    const u32 so = 16 * v * 16 * (D * 2) + colg * 128, sd = v * 16 * (D * 2) + colg * 128;
    const u32 sg = pair0 * 64, sg2 = three ? sg + 128 : 1u << 30;
#define LD_KV(S0, S1, PC) static_for<S0, S1>([&](auto S) { gldsi<slot(P_KV + 2 * S.value), S.value * 16 * (3 * D * 2)>(rqkv, vq, skv, l1); PACE(PC); })
#define PACE(n) do { if ((n) > 0) __builtin_amdgcn_s_sleep(n); } while (0)
#define LD_Q(S0, S1, PC) static_for<S0, S1>([&](auto S) { gldsi<slot(P_Q + 2 * S.value), 2 * S.value * 16 * (3 * D * 2)>(rqkv, vq, sq, l1); PACE(PC); })
#define LD_DN(RS, SB, P0, T0, T1, PC)                                                                    \
  static_for<T0, T1>([&](auto T) {                                                                   \
    gldsi<slot((P0) + 4 * T.value), T.value * 16 * (D * 2)>(RS, vd, SB, l2);                         \
    gldsi<slot((P0) + 4 * T.value) + 1024, T.value * 16 * (D * 2) + 64>(RS, vd, SB, l2); PACE(PC);   \
  })
#define LD_GU(S0, S1, PC)                                                                                \
  static_for<S0, S1>([&](auto S) {                                                                   \
    gldsi<slot(P_GU + 6 * S.value), S.value * 16 * (F * 2)>(rgu, vg, sg, l1);                        \
    gldsi<slot(P_GU + 6 * S.value + 2), S.value * 16 * (F * 2) + 64>(rgu, vg, sg, l1);               \
    gldsi<slot(P_GU + 6 * S.value + 4), S.value * 16 * (F * 2)>(rgu, vg, sg2, l1); PACE(PC);         \
  })
    TS(1);
    VMCNT(0);
    SB0();
    int ln;
    asm volatile("v_mbcnt_lo_u32_b32 %0, -1, 0\n\tv_mbcnt_hi_u32_b32 %0, -1, %0" : "=v"(ln));
    const u32 vd = (ln >> 2) * (D * 2) + (ln & 3) * 16;
    const u32 vg = (ln >> 2) * (F * 2) + (((ln & 3) ^ (v << 1)) * 16);
    TS(2);
    LD_Q(0, 16, PQE);
    TS(11);
    bar_raw();
    v4u wt[36];
    const Base3 wdst1(L + v * 1024 + ln * 16);
#define GUN(s_, P_) (P_GU + ((s_) < 12 ? 6 * (s_) + 2 * (P_) : 72 + 40 * (P_) + 2 * ((s_) - 12)))
#define GUR(s_, P_) __builtin_amdgcn_raw_buffer_load_b128(rgu, NBV(vg), ((P_) == 2 ? sg2 : sg + (P_) * 64) + (s_) * 16 * (F * 2), 0)
#define GUW(s_, P_, r_) *(LAS v4u*)wdst1.template at<slot(GUN(s_, P_))>() = wt[r_]
#define LD_GU2(S0, S1, PC) static_for<S0, S1>([&](auto S) { constexpr int s_ = S.value; \
      gldsi<slot(GUN(s_, 0)), s_ * 16 * (F * 2)>(rgu, vg, sg, l1); SB0(); \
      if constexpr (s_ < 24) gldsi<slot(GUN(s_, 1)), s_ * 16 * (F * 2) + 64>(rgu, vg, sg, l1); \
      else wt[20 + s_ - 24] = GUR(s_, 1); \
      SB0(); wt[s_ - 12] = GUR(s_, 2); PACE(PC); SB0(); })
#define LD_WD(T0, T1) static_for<T0, T1>([&](auto T) { \
      gldsi<slot(P_WD + 4 * T.value), pair_of(T.value) * 32 * (D * 2)>(rwd, vd, sd, l2); \
      gldsi<slot(P_WD + 4 * T.value) + 1024, pair_of(T.value) * 32 * (D * 2) + 64>(rwd, vd, sd, l2); })
#define DONE(k) do { RELOAD(); mark(L, H_PP, v, k); RELOAD(); } while (0)
    await<4>(L, 2);
    VMCNT(0);
    MARK(H_P0, v, 1u);
    gldsi<L_CF, 0>(rwo, ln < 32 ? ((2 * rank + v) * 8 + (ln >> 2)) * (D * 2) + (xcd & 1) * 512 + (ln & 3) * 128 : 1u << 30, 0u, l1);
    gldsi<L_CF, 0>(rgu, (ln & 15) < 13 ? ((xcd >> 1) * 128 + 4 * rank + (ln >> 4)) * (F * 2) + (xcd & 1) * 1536 + (ln & 15) * 128 : 1u << 30, 0u, l1);
    LD_DN(rwo, so, P_WO, 0, W1N, 0);
    TS(3);
    VMCNT(0);
    MARK(H_P0, v, 1u);
    reach<1>(L, H_C0, 2u, 2u, 2u, 2u);
    LD_DN(rwo, so, P_WO, W1N, 16, 1);
    LD_GU(0, W2N, PGU);
    asm volatile("s_waitcnt vmcnt(%0)" ::"i"(3 * W2N) : "memory");
    MARK(H_P0, v, 2u);
    TS(4);
    reach<2>(L, H_C0, 3u, 3u, 3u, 3u);
    reach<2>(L, H_P0, 0u, 0u, 1u, 1u);
    LD_GU(W2N, 12, PG3);
    await<4>(L, 4);
    static_for<12, 20>([&](auto S) { constexpr int s_ = S.value;
      wt[20 + s_ - 12] = GUR(s_, 0); wt[28 + s_ - 12] = GUR(s_, 1); wt[s_ - 12] = GUR(s_, 2); SB0(); });
    VMCNT(24);
    MARK(H_P0, v, 3u);
    TS(5);
    reach<1>(L, H_C0, 4u, 4u, 4u, 4u);
    static_for<12, 20>([&](auto S) { GUW(S.value, 0, 20 + S.value - 12); GUW(S.value, 1, 28 + S.value - 12); });
    LD_GU2(20, 32, PG2);
    VMCNT(0);
    DONE(3);
    TS(6);
    await_cp(L, 1);
    {
      const u32 tdn = ((2 * rank + v) * 16 + (ln >> 2)) * (D * 2) + (xcd & 1) * 512 + (ln & 3) * 128;
      __builtin_amdgcn_s_waitcnt(0x0F70);
      gldsi<L_T, 0>(rwd, tdn, 0u, l1);
      gldsi<L_T, 1024 * (D * 2)>(rwd, tdn, 0u, l1);
    }
    static_for<24, 32>([&](auto S) { GUW(S.value, 1, 20 + S.value - 24); });
    DONE(4);
    await_cp(L, 2);
    static_for<12, 32>([&](auto S) { GUW(S.value, 2, S.value - 12); });
    DONE(5);
    TS(7);
    await_cp(L, 3);
    LD_WD(0, 10);
    await_cp(L, 4);
    LD_WD(10, 14);
    VMCNT(0);
    DONE(100);
    TS(8);
    TS_DUMP();
    return;
  }

  __builtin_amdgcn_s_setprio(2);
  const int s2 = w & 1, kh = w >> 1;
  const int strip2 = rowh * 2 + s2, ncol = colg * 64 + kh * 32;
  v4u aphi0, aphi1;
  u32 tag;
  {
    float xf[16];
#pragma unroll
    for (int i = 0; i < 8; ++i) { xf[2 * i] = lo16(r0[i >> 2][i & 3]); xf[2 * i + 1] = hi16(r0[i >> 2][i & 3]); }
    float sm = (((xf[0] + xf[1]) + (xf[2] + xf[3])) + ((xf[4] + xf[5]) + (xf[6] + xf[7]))) + (((xf[8] + xf[9]) + (xf[10] + xf[11])) + ((xf[12] + xf[13]) + (xf[14] + xf[15])));
    sm = dpp_add<DPP_X1>(sm); sm = dpp_add<DPP_X2>(sm); sm = dpp_add<DPP_HM>(sm);
    const float mw = sm * (1.f / 128.f);
    float q[4];
#pragma unroll
    for (int i = 0; i < 4; ++i) {
      const float d0 = xf[4 * i] - mw, d1 = xf[4 * i + 1] - mw, d2 = xf[4 * i + 2] - mw, d3 = xf[4 * i + 3] - mw;
      q[i] = (d0 * d0 + d1 * d1) + (d2 * d2 + d3 * d3);
    }
    float qq = (q[0] + q[1]) + (q[2] + q[3]);
    qq = dpp_add<DPP_X1>(qq); qq = dpp_add<DPP_X2>(qq); qq = dpp_add<DPP_HM>(qq);
    *(volatile LAS f32x2*)(L + L_KVF + (lane >> 3) * 32 + w * 8) = f32x2{mw, qq};
    sig(L, 1, w);
    await<4>(L, 1);
    const f32x4 pa = *(LAS f32x4*)(L + L_KVF + (lane >> 3) * 32), pc = *(LAS f32x4*)(L + L_KVF + (lane >> 3) * 32 + 16);
    const float mean = ((pa[0] + pa[2]) + (pc[0] + pc[2])) * 0.25f;
    const float f0 = pa[0] - mean, f1 = pa[2] - mean, f2 = pc[0] - mean, f3 = pc[2] - mean;
    const float m2 = ((pa[1] + pa[3]) + (pc[1] + pc[3])) + 128.f * ((f0 * f0 + f1 * f1) + (f2 * f2 + f3 * f3));
    const float m1 = rb(mean), r1 = rcp(rb(__builtin_amdgcn_sqrtf(rb(rb(m2 * (1.f / D)) + 1e-5f))));
    float xn[16];
#pragma unroll
    for (int i = 0; i < 8; ++i) {
      const u32 d = pk(xf[2 * i] - m1, xf[2 * i + 1] - m1);
      const u32 t = pk(lo16(d) * r1, hi16(d) * r1);
      xn[2 * i] = lo16(t); xn[2 * i + 1] = hi16(t);
    }
#pragma unroll
    for (int i = 0; i < 16; ++i) asm volatile("" : "+v"(xn[i]));
    tag = __builtin_amdgcn_readfirstlane(epoch) + 1u;
    TS(1);
    await<1>(L, 0);
    __builtin_amdgcn_s_sleep(PRE_MOD);
    TS(21);
    v4u g[8];
    {
      u32 todo = 0xFFu;
      for (u32 spins = 0; spins < SPIN_CAP && todo; ++spins) {
        RELOAD();
        static_for<0, 8>([&](auto I) {
          constexpr int i = I.value;
          if ((todo >> i) & 1u) g[i] = ld128(rs, lane * 16, OFF_MODR + panel * 32768 + (i & 3) * 8192 + (i >> 2) * 4096 + w * 1024);
        });
        todo = 0;
        static_for<0, 8>([&](auto I) { if (__builtin_amdgcn_ballot_w64((g[I.value].y != tag) | (g[I.value].w != tag))) todo |= 1u << I.value; });
      }
    }
    {
      u32 pr[2];
#pragma unroll
      for (int t = 0; t < 2; ++t)
        pr[t] = pk((__uint_as_float(g[4 * t].x) + __uint_as_float(g[4 * t + 1].x)) + (__uint_as_float(g[4 * t + 2].x) + __uint_as_float(g[4 * t + 3].x)),
                   (__uint_as_float(g[4 * t].z) + __uint_as_float(g[4 * t + 1].z)) + (__uint_as_float(g[4 * t + 2].z) + __uint_as_float(g[4 * t + 3].z)));
      *(LAS u32*)(L + L_T + w * TW + lane * 4) = pr[0];
      *(LAS u32*)(L + L_T + w * TW + 256 + lane * 4) = pk(1.f + lo16(pr[1]), 1.f + hi16(pr[1]));
    }
    v4u sav[2], cav[2];
#pragma unroll
    for (int i = 0; i < 2; ++i) {
      sav[i] = *(LAS v4u*)(L + L_T + w * TW + i * 128 + (lane & 7) * 16);
      cav[i] = *(LAS v4u*)(L + L_T + w * TW + 256 + i * 128 + (lane & 7) * 16);
    }
    sig(L, 3, w);
    TS(2);
    const int strip = mem >> 2, r = 8 * (mem & 3) + (lane >> 3);
#pragma unroll
    for (int i = 0; i < 2; ++i) {
      v4u o;
#pragma unroll
      for (int j = 0; j < 4; ++j) {
        const u32 sa = sav[i][j], sc = cav[i][j];
        const u32 t = pk(xn[8 * i + 2 * j] * lo16(sc), xn[8 * i + 2 * j + 1] * hi16(sc));
        o[j] = pk(lo16(t) + lo16(sa), hi16(t) + hi16(sa));
      }
      st128(o, rs, (4 * i + ((lane & 7) >> 1)) * 1024 + (r + 32 * (lane & 1)) * 16, pb + PN_H + strip * 32768 + w * 8192);
    }
    await<4>(L, 3);
    publish(rs, fw + FL_H + strip, (mem & 3) * 4 + w, lane);
  }
  TS(3);

  const int trq = (lane & 15) >> 2, trp = lane & 3, trb = (lane >> 4) & 1;
  const f32x16 zero16 = {0.f, 0.f, 0.f, 0.f, 0.f, 0.f, 0.f, 0.f, 0.f, 0.f, 0.f, 0.f, 0.f, 0.f, 0.f, 0.f};
  u32 phq[16];
  v4u wb[PD];
  {
    const Base3 wnat(L + (8 * hh + trq) * 64 + trb * 32 + trp * 8);
    waitn(rs, fw + FL_H + w, 0xFFFFu, lane);
    TS(4);
    v4u a[32];
    ldfrags<W0>(a, rs, lane * 16, pb + PN_H + w * 32768);
    bar_raw();
    {
      const int hs = (l31 >> 2) & 1;
      const LAS u16* wp = (const LAS u16*)(L + L_CF + 2048) + hs * 256 + 4 * hh * 16 + (l31 & 3) + 4 * (l31 >> 3);
      u32 e[8];
#pragma unroll
      for (int j = 0; j < 8; ++j) e[j] = wp[(8 * (j >> 2) + (j & 3)) * 16];
      const v4u av = {e[0] | (e[1] << 16), e[2] | (e[3] << 16), e[4] | (e[5] << 16), e[6] | (e[7] << 16)};
      const v4u z = {0, 0, 0, 0};
      aphi0 = hs == 0 ? av : z; aphi1 = hs == 1 ? av : z;
      asm volatile("" : "+v"(aphi0), "+v"(aphi1));
    }
    static_for<0, PD>([&](auto U) { wb[U.value] = ldw<256>(wnat.at<slot(P_KV + U.value)>()); });
    SB0();
    f32x16 kacc = zero16, vacc = zero16;
    static_for<0, 64>([&](auto U) {
      constexpr int u = U.value;
      const v4u cur = wb[u % PD];
      if constexpr (u + PD < 64) wb[u % PD] = ldw<256>(wnat.at<slot(P_KV + u + PD)>());
      if constexpr (u & 1) mfma_pin(a[u >> 1], cur, vacc); else mfma_pin(cur, a[u >> 1], kacc);
      if constexpr (!(u & 1) && (u >> 1) + W0 < 32) a[(u >> 1) + W0] = ldfrag<(u >> 1) + W0>(rs, lane * 16, pb + PN_H + w * 32768);
      if constexpr (u == 2 * (32 - W0)) sig(L, 2, w);
      SB0();
    });
    TS(5);
    f32x16 P = mfma(aphi0, pack8<0>(kacc), zero16);
    P = mfma(aphi1, pack8<8>(kacc), P);
    const v4u vb0 = pack8<0>(vacc), vb1 = pack8<8>(vacc);
    u32 phk[16];
    feat(P, phk);
    LAS unsigned char* T = L + L_T + w * TW;
#pragma unroll
    for (int c = 0; c < 4; ++c)
      *(LAS v4u*)(T + lane * 64 + ((c ^ ((lane >> 1) & 3)) * 16)) = v4u{phk[4 * c], phk[4 * c + 1], phk[4 * c + 2], phk[4 * c + 3]};
    const v4u ones = {0x3F803F80u, 0x3F803F80u, 0x3F803F80u, 0x3F803F80u};
    LAS unsigned char* tr = T + (4 * hh + trq) * 64 + (((2 * trb + (trp >> 1)) ^ ((2 * hh + (trq >> 1)) & 3)) * 16) + 8 * (trp & 1);
    const v4u t0 = ldw<512>(tr), t1 = ldw<512>(tr + 1024), t2 = ldw<512>(tr + 2048), t3 = ldw<512>(tr + 3072);
    f32x16 D0 = mfma(t0, trb ? ones : vb0, zero16);
    f32x16 D1 = mfma(t2, trb ? vb0 : ones, zero16);
    D0 = mfma(t1, trb ? ones : vb1, D0);
    D1 = mfma(t3, trb ? vb1 : ones, D1);
    {
      LAS unsigned char* pe = T + lane * 16;
      LAS unsigned char* pz = T + 1024 + (lane >> 4) * 16;
      LAS unsigned char* p0 = trb ? pz : pe;
      LAS unsigned char* p1 = trb ? pe : pz;
#pragma unroll
      for (int g = 0; g < 4; ++g) *(LAS f32x4*)(p0 + g * 1088) = f32x4{D0[4 * g], D0[4 * g + 1], D0[4 * g + 2], D0[4 * g + 3]};
#pragma unroll
      for (int g = 0; g < 4; ++g) *(LAS f32x4*)(p1 + g * 1088) = f32x4{D1[4 * g], D1[4 * g + 1], D1[4 * g + 2], D1[4 * g + 3]};
    }
    TS(6);
    MARK(H_C0, w, 1u);
    reach<0>(L, H_C0, 1u, 1u, 1u, 1u);
    {
      f32x4 e[4], z[4];
#pragma unroll
      for (int v = 0; v < 4; ++v) {
        e[v] = *(LAS f32x4*)(L + L_T + v * TW + w * 1088 + lane * 16);
        z[v] = *(LAS f32x4*)(L + L_T + v * TW + w * 1088 + 1024 + (lane & 3) * 16);
      }
      SB0();
      const int idx = (mem * 4 + w) * 16 + panel;
      st128(__builtin_bit_cast(v4u, (e[0] + e[1]) + (e[2] + e[3])), rs, lane * 16, OFF_KVE + idx * 1024);
      st128(__builtin_bit_cast(v4u, (z[0] + z[1]) + (z[2] + z[3])), rs, lane < 4 ? lane * 16 : WS_BYTES, OFF_KVZ + idx * 64);
      publish(rs, FL_KV + mem * 2 + (w >> 1), (w & 1) * 16 + panel, lane);
      MARK(H_C0, w, 2u);
      reach<1>(L, H_P0, 1u, 1u, 0u, 0u);
      static_for<0, PD>([&](auto U) { wb[U.value] = ldw<256>(wnat.at<slot(P_Q + U.value)>()); });
    }
    TS(7);
    f32x16 qacc = zero16;
    static_for<0, 32>([&](auto U) {
      constexpr int u = U.value;
      const v4u cur = wb[u % PD];
      if constexpr (u + PD < 32) wb[u % PD] = ldw<256>(wnat.at<slot(P_Q + u + PD)>());
      mfma_pin(cur, a[u], qacc);
      SB0();
    });
    MARK(H_C0, w, 3u);
    P = mfma(aphi0, pack8<0>(qacc), zero16);
    P = mfma(aphi1, pack8<8>(qacc), P);
    feat(P, phq);
#pragma unroll
    for (int i = 0; i < 16; ++i) asm volatile("" : "+v"(phq[i]));
  }
  v2u xres[4];
  {
    const u16* xr = p.x + (size_t)(panel * 128 + strip2 * 32 + l31) * D + ncol + 4 * hh;
#pragma unroll
    for (int i = 0; i < 4; ++i) xres[i] = *(const GAS v2u*)(xr + 8 * i);
  }
  TS(8);
  reach<1>(L, H_P0, 0u, 0u, 1u, 1u);
  TS(9);
  {
    const bool on = (l31 >> 4) == hh;
    const v4u zv = {0, 0, 0, 0};
    f32x16 num = zero16, den = zero16;
    v4u ak[4], az[4];
#pragma unroll
    for (int s4 = 0; s4 < 4; ++s4) { az[s4] = *(LAS v4u*)(L + L_ZF + s4 * 32 + hh * 16); ak[s4] = *(LAS v4u*)(L + L_KVF + s4 * 512 + l31 * 16); }
    SB0();
#pragma unroll
    for (int s4 = 0; s4 < 4; ++s4) den = mfma(on ? az[s4] : zv, v4u{phq[4 * s4], phq[4 * s4 + 1], phq[4 * s4 + 2], phq[4 * s4 + 3]}, den);
#pragma unroll
    for (int s4 = 0; s4 < 4; ++s4) num = mfma(on ? ak[s4] : zv, v4u{phq[4 * s4], phq[4 * s4 + 1], phq[4 * s4 + 2], phq[4 * s4 + 3]}, num);
    const float d0 = rcp(rb(rb(den[0]) + 1e-6f)), d1 = rcp(rb(rb(den[8]) + 1e-6f));
    const v4u n0 = pack8<0>(num), n1 = pack8<8>(num);
    v4u o0, o1;
#pragma unroll
    for (int j = 0; j < 4; ++j) { o0[j] = pk(lo16(n0[j]) * d0, hi16(n0[j]) * d0); o1[j] = pk(lo16(n1[j]) * d1, hi16(n1[j]) * d1); }
    st128(o0, rs, lane * 16, pb + PN_O + w * 32768 + (2 * mem) * 1024);
    st128(o1, rs, lane * 16 + 1024, pb + PN_O + w * 32768 + (2 * mem) * 1024);
  }
#define WDN_BASES() const Base3 wown(L + (4 * hh + trq) * 64 + trb * 32 + trp * 8 + 3 * kh * 1024), woth(L + (4 * hh + trq) * 64 + trb * 32 + trp * 8 + (kh + 1) * 1024)
#define WDN(base, u) ldw<512>((((u) & 1) ? wown : woth).template at<slot((base) + 4 * ((u) >> 1))>())
  u32 x1p[8];
  {
    WDN_BASES();
    publish(rs, fw + FL_O + w, mem, lane);
    reach<0>(L, H_P0, 2u, 2u, 0u, 0u);
    static_for<0, PD>([&](auto U) { wb[U.value] = WDN(P_WO, U.value); });
    TS(10);
    waitn(rs, fw + FL_O + strip2, 0xFFu << (kh * 8), lane);
    TS(11);
    v4u a[16];
    ldfrags<16>(a, rs, lane * 16, pb + PN_O + strip2 * 32768 + kh * 16384);
    sig(L, 4, w);
    reach<0>(L, H_P0, 0u, 0u, 2u, 2u);
    f32x4 ga[4], sc[4];
#pragma unroll
    for (int g = 0; g < 4; ++g) {
      ga[g] = *(LAS f32x4*)(L + L_VEC + (kh * 32 + 8 * g + 4 * hh) * 4);
      sc[g] = *(LAS f32x4*)(L + L_VEC + 256 + (kh * 32 + 8 * g + 4 * hh) * 4);
    }
    SB0();
    f32x16 c0 = zero16, c1 = zero16;
    static_for<0, 32>([&](auto U) {
      constexpr int u = U.value;
      const v4u cur = wb[u % PD];
      if constexpr (u + PD < 32) wb[u % PD] = WDN(P_WO, u + PD);
      if constexpr (u & 1) mfma_pin(cur, a[u >> 1], c0); else mfma_pin(cur, a[u >> 1], c1);
      SB0();
    });
#pragma unroll
    for (int g = 0; g < 4; ++g) *(LAS f32x4*)(L + L_T + w * TW + g * 1024 + lane * 16) = f32x4{c1[4 * g], c1[4 * g + 1], c1[4 * g + 2], c1[4 * g + 3]};
    TS(12);
    MARK(H_C0, w, 4u);
    reach1(L, H_C0, w ^ 2, 4u);
    f32x4 o[4];
#pragma unroll
    for (int g = 0; g < 4; ++g) o[g] = *(LAS f32x4*)(L + L_T + (w ^ 2) * TW + g * 1024 + lane * 16);
    SB0();
    float x1[16], sp[4];
    u32 yp[8];
#pragma unroll
    for (int g = 0; g < 4; ++g) {
#pragma unroll
      for (int j = 0; j < 2; ++j) {
        const u32 r = pk(lo16(xres[g][j]) + ga[g][2 * j] * (c0[4 * g + 2 * j] + o[g][2 * j]),
                         hi16(xres[g][j]) + ga[g][2 * j + 1] * (c0[4 * g + 2 * j + 1] + o[g][2 * j + 1]));
        x1p[2 * g + j] = r;
        x1[4 * g + 2 * j] = lo16(r); x1[4 * g + 2 * j + 1] = hi16(r);
        yp[2 * g + j] = pk(lo16(r) * sc[g][2 * j], hi16(r) * sc[g][2 * j + 1]);
      }
    }
    const int tile = 2 * colg + kh;
    st128(v4u{yp[0], yp[1], yp[2], yp[3]}, rs, lane * 16, pb + PN_X1 + strip2 * 32768 + tile * 2048);
    st128(v4u{yp[4], yp[5], yp[6], yp[7]}, rs, lane * 16 + 1024, pb + PN_X1 + strip2 * 32768 + tile * 2048);
    SB0();
#pragma unroll
    for (int g = 0; g < 4; ++g) sp[g] = (x1[4 * g] + x1[4 * g + 1]) + (x1[4 * g + 2] + x1[4 * g + 3]);
    const float ss = (sp[0] + sp[1]) + (sp[2] + sp[3]);
    const float mt = fold32(ss, ss) * (1.f / 32.f);
    float q[4];
#pragma unroll
    for (int g = 0; g < 4; ++g) {
      const float d0 = x1[4 * g] - mt, d1 = x1[4 * g + 1] - mt, d2 = x1[4 * g + 2] - mt, d3 = x1[4 * g + 3] - mt;
      q[g] = (d0 * d0 + d1 * d1) + (d2 * d2 + d3 * d3);
    }
    const float qs = (q[0] + q[1]) + (q[2] + q[3]);
    const float qt = fold32(qs, qs);
    v4u sv = {__float_as_uint(mt), tag, __float_as_uint(qt), tag};
    asm volatile("" : "+v"(sv));
    publish(rs, fw + FL_X + strip2, tile, lane);
    st128(sv, rs, lane < 32 ? lane * 16 : WS_BYTES, pb + PN_ST + strip2 * 8192 + tile * 512);
  }
  asm volatile("" : "+v"(x1p[0]), "+v"(x1p[1]), "+v"(x1p[2]), "+v"(x1p[3]), "+v"(x1p[4]), "+v"(x1p[5]), "+v"(x1p[6]), "+v"(x1p[7]));
#define GU_S(u) ((u) < 72 ? (u) / 6 : 12 + (((u) - 72) % 40) / 2)
#define GU_I(u) ((u) < 72 ? (u) % 6 : 2 * (((u) - 72) / 40) + ((u) & 1))
#define WGU(u) ldw<512>((((u) & 1) ? wy : wx).template at<slot(P_GU + ((u) & ~1))>())
  {
    const Base3 wx(L + (4 * hh + trq) * 64 + trp * 8 + trb * (1024 + 32)), wy(L + (4 * hh + trq) * 64 + trp * 8 + (trb ? 1024 : 32));
    reach<0>(L, H_P0, 3u, 3u, 0u, 0u);
    static_for<0, PD>([&](auto U) { wb[U.value] = WGU(U.value); });
    TS(13);
    waitn(rs, fw + FL_X + w, 0xFFFFu, lane);
    TS(14);
    constexpr int AW = 20;
    v4u a[AW];
    ldfrags<8>(a, rs, lane * 16, pb + PN_X1 + w * 32768);
    sig(L, 5, w);
    f32x16 acc[6];
#pragma unroll
    for (int i = 0; i < 6; ++i) acc[i] = zero16;
    float rr = 0.f;
    v4u bcor = {0, 0, 0, 0};
    v4u ho, cf0, cf1, pp;
    float t0 = 0.f, t1 = 0.f;
    const int go = pb + PN_G + (w * 96 + mem) * 1024, fi = fw + FL_G + strip2 * 3, gi = pb + PN_G + (strip2 * 96 + kh * 48) * 1024;
    u32 fl = 0;
    int gv = 0;
    bool early = false;
    v4u g[43];
    auto post = [&](int P) { (void)__builtin_amdgcn_raw_ptr_buffer_atomic_add_i32(1 << mem, rs, lane == 0 ? 0 : WS_BYTES, OFF_ARR + (fw + FL_G + w * 3 + P) * 128, 0); };
    auto epi = [&](auto Ic, auto Kc) {
      constexpr int I = decltype(Ic)::value, k = decltype(Kc)::value, j = k >> 1;
      const f32x16& A = acc[I];
      if constexpr ((k & 1) == 0) { t0 = A[2 * j] * rr; t1 = A[2 * j + 1] * rr; asm volatile("" : "+v"(t0)); t0 *= A[8 + 2 * j]; }
      else {
        t1 *= A[9 + 2 * j];
        ho[j] = pk(t0, t1);
        if constexpr (j == 3) st128(ho, rs, (I < 4 || three) ? lane * 16 : WS_BYTES, go + ((I & 1) * 48 + (I >> 1) * 16) * 1024);
      }
    };
    static_for<0, 192>([&](auto Uc) {
      constexpr int u = decltype(Uc)::value, s = GU_S(u), i = GU_I(u), q = (u + 8) % 40;
      if constexpr (u == 6) TS(22);
      if constexpr (u == 60) TS(23);
      if constexpr (u == 96) TS(24);
      if constexpr (u == 128) TS(25);
      if constexpr (u == 160) TS(26);
      if constexpr (u == 56) mark(L, H_CP, w, 2u);
      if constexpr (u == 96 || u == 128 || u == 160) mark(L, H_CP, w, u / 32);
      if constexpr (u == 60 || u == 96 || u == 140) pp = peek(L, H_PP);
      if constexpr (u == 64) need_pp<false>(L, pp, 3u);
      if constexpr (u == 100) need_pp<true>(L, pp, 4u);
      if constexpr (u == 144) need_pp<false>(L, pp, 5u);
      const v4u cur = wb[u % PD];
      if constexpr (u + PD < 192) wb[u % PD] = WGU(u + PD);
      if constexpr (u >= 72 && q == 32) cf0 = *(LAS v4u*)(L + L_CF + i * 512 + l31 * 16);
      if constexpr (u >= 72 && q == 33) cf1 = *(LAS v4u*)(L + L_CF + i * 512 + l31 * 16);
      mfma_pin(cur, a[s % AW], acc[i]);
      if constexpr (u < 72 && i == 5) a[s % AW] = ldfrag<s + AW>(rs, lane * 16, pb + PN_X1 + w * 32768);
      if constexpr (u < 2 * (AW - 8) && !(u & 1)) a[8 + (u >> 1)] = ldfrag<8 + (u >> 1)>(rs, lane * 16, pb + PN_X1 + w * 32768);
      if constexpr (u == 24) mark(L, H_CP, w, 1u);
      if constexpr (u == 101) bcor = *(LAS v4u*)(L + L_KVF + w * 512 + l31 * 16);
      if constexpr (u == 107) { rr = __uint_as_float(bcor.w); bcor = v4u{hh ? 0u : bcor.x, hh ? 0u : bcor.y, hh ? 0u : bcor.z, 0u}; }
      if constexpr (u >= 72 && q == 38) mfma_pin(cf0, bcor, acc[i]);
      if constexpr (u >= 72 && q == 39) mfma_pin(cf1, bcor, acc[i]);
      if constexpr (u >= 112 && q >= 1 && q < 9) {
        epi(IC<(i & 6) - 2 + (q - 1) / 4>{}, IC<2 * ((q - 1) % 4)>{});
        epi(IC<(i & 6) - 2 + (q - 1) / 4>{}, IC<2 * ((q - 1) % 4) + 1>{});
      }
      if constexpr (u == 112 + GPUB) publish(rs, fw + FL_G + w * 3, mem, lane);
      if constexpr (u == GASK) fl = ask(rs, fi, lane);
      if constexpr (u == 176) {
        early = (__builtin_amdgcn_readfirstlane(fl) & 0xFFFFu) == 0xFFFFu;
        __builtin_amdgcn_fence(__ATOMIC_ACQUIRE, "wavefront");
        RELOAD();
        gv = early ? lane * 16 : WS_BYTES;
      }
      if constexpr (u == 180) { VMCNT(4); post(1); }
      if constexpr (u >= 176) g[u - 176] = ldfrag<u - 176>(rs, gv, gi);
      if constexpr (u == 184) pp = peek(L, H_PP);
      SB0();
    });
    mark(L, H_CP, w, 6u);
    TS(15);
    need_pp<true>(L, pp, 6u);
    WDN_BASES();
    static_for<0, PD>([&](auto U) { wb[U.value] = WDN(P_WD, U.value); });
    if (!early) {
      spin(rs, fi, 0xFFFFu, lane);
      __builtin_amdgcn_fence(__ATOMIC_ACQUIRE, "wavefront");
      RELOAD();
      static_for<0, 16>([&](auto T) { g[T.value] = ld128(rs, lane * 16, gi + T.value * 1024); });
    }
    SB0();
    fl = ask(rs, fi + 1, lane);
    SB0();
    TS(18);
    f32x16 c0 = zero16, c1 = zero16;
    static_for<0, 86>([&](auto U) {
      constexpr int u = U.value;
      if constexpr (u == 18) mark(L, H_CP, w, 7u);
#define GL(t) ((t) < 32 ? GU0 + (t) - 16 : GU2 + (t) - 32)
      if constexpr (u == PUB2) {
        asm volatile("s_waitcnt vmcnt(%0)" ::"i"(PUB2 - GU0 < 16 ? PUB2 - GU0 : 16) : "memory");
        post(2);
      }
      if constexpr (u == 24 || u == 40 || u == 56) pp = peek(L, H_PP);
      if constexpr (u == 28 || u == 44 || u == 60) need_pp<true>(L, pp, 7u + (u - 28) / 16);
      if constexpr (u == GL(16)) poll(fl, rs, fi + 1, 0xFFFFu, lane);
      if constexpr (u == GL(32) - GA2) fl = ask(rs, fi + 2, lane);
      if constexpr (u == GL(32)) poll(fl, rs, fi + 2, 0x7FFu, lane);
      const v4u cur = wb[u % PD];
      if constexpr (u + PD < 86) wb[u % PD] = WDN(P_WD, u + PD);
      if constexpr (u & 1) mfma_pin(cur, g[u >> 1], c0); else mfma_pin(cur, g[u >> 1], c1);
      if constexpr (u >= 1 && u < 9) {
        epi(IC<4 + (u - 1) / 4>{}, IC<2 * ((u - 1) % 4)>{});
        epi(IC<4 + (u - 1) / 4>{}, IC<2 * ((u - 1) % 4) + 1>{});
      }
      static_for<16, 43>([&](auto T) { if constexpr (GL(T.value) == u) g[T.value] = ldfrag<T.value>(rs, lane * 16, gi); });
      SB0();
    });
#pragma unroll
    for (int g = 0; g < 4; ++g) *(LAS f32x4*)(L + L_T + w * TW + g * 1024 + lane * 16) = f32x4{c1[4 * g], c1[4 * g + 1], c1[4 * g + 2], c1[4 * g + 3]};
    f32x4 gb[4];
#pragma unroll
    for (int g = 0; g < 4; ++g) gb[g] = *(LAS f32x4*)(L + L_VEC + 512 + (kh * 32 + 8 * g + 4 * hh) * 4);
    TS(19);
    MARK(H_C0, w, 5u);
    reach1(L, H_C0, w ^ 2, 5u);
    f32x4 o[4];
#pragma unroll
    for (int g = 0; g < 4; ++g) o[g] = *(LAS f32x4*)(L + L_T + (w ^ 2) * TW + g * 1024 + lane * 16);
    SB0();
    const rsrc_t ro = __builtin_amdgcn_make_buffer_rsrc((void*)p.out, 0, 2048 * D * 2, 0x00020000);
    v2u r[4];
#pragma unroll
    for (int g = 0; g < 4; ++g)
#pragma unroll
      for (int j = 0; j < 2; ++j)
        r[g][j] = pk(lo16(x1p[2 * g + j]) + gb[g][2 * j] * (c0[4 * g + 2 * j] + o[g][2 * j]),
                     hi16(x1p[2 * g + j]) + gb[g][2 * j + 1] * (c0[4 * g + 2 * j + 1] + o[g][2 * j + 1]));
    const int oo = ((panel * 128 + strip2 * 32 + l31) * D + ncol + 8 * hh) * 2;
#pragma unroll
    for (int g = 0; g < 4; g += 2) {
      const auto x = __builtin_amdgcn_permlane32_swap(r[g][0], r[g + 1][0], false, false);
      const auto y = __builtin_amdgcn_permlane32_swap(r[g][1], r[g + 1][1], false, false);
      __builtin_amdgcn_raw_buffer_store_b128(v4u{x[0], y[0], x[1], y[1]}, ro, oo + g * 16, 0, OAUX);
    }
  }
  if (b == 0 && tid == 0) __hip_atomic_store((GAS u32*)(ws + OFF_EPOCH), tag, RLX_AGENT);
  if (b == 77 && w == 0) {
    const u64 goal = lk0 + 5000 + LMUL * (lkb - lka);
    while (__builtin_amdgcn_s_memrealtime() < goal) __builtin_amdgcn_s_sleep(1);
  }
}
}

extern "C" void kernel_launch(void* const* d_in, const int* in_sizes, int n_in, void* d_out, int out_size, void* d_ws,
                              size_t ws_size, hipStream_t stream) {
  using namespace mk;
  Params p;
  p.x = (const u16*)d_in[0]; p.wqkv = (const u16*)d_in[1]; p.wphi = (const u16*)d_in[2]; p.wo = (const u16*)d_in[3];
  p.wg = (const u16*)d_in[5]; p.wu = (const u16*)d_in[6]; p.wd = (const u16*)d_in[7]; p.c = (const u16*)d_in[8];
  p.wmod = (const u16*)d_in[9]; p.out = (u16*)d_out; p.ws = (unsigned char*)d_ws;
  (void)hipFuncSetAttribute((const void*)mega, hipFuncAttributeMaxDynamicSharedMemorySize, LDS_BYTES);
  mega<<<256, 512, LDS_BYTES, stream>>>(p);
}
```

```cpp
#define OAUX 2
#include <hip/hip_runtime.h>

namespace mk {
typedef unsigned short u16;
typedef unsigned int u32;
typedef unsigned long long u64;
typedef u32 v4u __attribute__((ext_vector_type(4)));
typedef u32 v2u __attribute__((ext_vector_type(2)));
typedef float f32x2 __attribute__((ext_vector_type(2)));
typedef float f32x4 __attribute__((ext_vector_type(4)));
typedef float f32x16 __attribute__((ext_vector_type(16)));
typedef __bf16 bf16x2 __attribute__((ext_vector_type(2)));
typedef __bf16 bf16x8 __attribute__((ext_vector_type(8)));
typedef short s16x4 __attribute__((ext_vector_type(4)));
#define LAS __attribute__((address_space(3)))
#define GAS __attribute__((address_space(1)))
#define DI __device__ __forceinline__
#define RLX_AGENT __ATOMIC_RELAXED, __HIP_MEMORY_SCOPE_AGENT
#define SB0() __builtin_amdgcn_sched_barrier(0)
typedef __amdgpu_buffer_rsrc_t rsrc_t;

constexpr int D = 512, F = 1376;
constexpr int OFF_EPOCH = 0;
constexpr int OFF_MODG = 64 << 10;
constexpr int OFF_KVZ = 128 << 10;
constexpr int OFF_CG = 192 << 10;
constexpr int CG_PART = 2 * F * 16;
constexpr int OFF_ARR = 384 << 10;
constexpr int OFF_MODR = 512 << 10;
constexpr int OFF_KVE = 1 << 20;
constexpr int OFF_PAN = 2 << 20;
constexpr int PN_H = 0;
constexpr int PN_O = 128 << 10;
constexpr int PN_X1 = 256 << 10;
constexpr int PN_G = 384 << 10;
constexpr int PN_ST = 768 << 10;
constexpr int PN_FL = 800 << 10;
constexpr int FL_H = 0, FL_O = 4, FL_X = 8, FL_G = 12, FL_KV = 384;
constexpr int PSTR = (848 << 10) + 4224;
constexpr int WS_BYTES = 16 << 20;
constexpr int OFF_TS = 32 << 20;
constexpr int RING = 136;
constexpr int L_T = RING * 1024, TW = 4352;
constexpr int L_KVF = L_T + 4 * TW;
constexpr int L_ZF = L_KVF + 2048;
constexpr int L_VEC = L_ZF + 256;
constexpr int L_CF = L_VEC + 768;
constexpr int L_HINT = L_CF + 3072;
constexpr int LDS_BYTES = L_HINT + 256;
static_assert(LDS_BYTES <= 163840, "LDS");
constexpr int P_KV = 0, P_Q = 64, P_WO = 96, P_GU = 160, P_WD = 352;
constexpr int H_CP = 6, H_PP = 7, H_C0 = 8, H_P0 = 9;
constexpr int pair_of(int t) { return ((t & 15) < 11 ? 3 * (t & 15) : 2 * (t & 15) + 11) + (t >> 4); }
constexpr int slot(int n) { return (n % RING) * 1024; }
constexpr int kv_piece(int u) { return u < 45 ? (u % 3 < 2 ? 2 * (2 + 2 * (u / 3) + u % 3 - 2) : 2 * (u / 3) + 1) : u == 45 ? 60 : u == 46 ? 62 : 2 * (u - 32) + 1; }
constexpr int AUX_SC1 = 16;
constexpr u32 SPIN_CAP = 1u << 16;
#define PRE_MOD 20
#define HI_DELAY 8
#define POLL_GAP 6
#define PRE_FL 4
#define KV1 32
#define PGU 4
#define PKV 0
#define GPUB 26
#define LPRIO 0
#define APRIO 3
#define TMOD 0
#define CW 3
#define DW 0
#define CSLP 3
#define PG2 2
#define TLBT 0
#define LITE 1
#define EQ 1
#define PQE 2
#define GASK 158
#define PUB2 24
#define WG0 16
#define GU0 12
#define GLA 16
#define GA2 18
#define GU2 50
#define W1N 8
#define W2N 6
#define PG3 4
#define QT 1
#define W0 12
#define PDV 5
#define WPC 0
#define WPACE() do { if (WPC) { SB0(); __builtin_amdgcn_s_sleep(WPC); SB0(); } } while (0)
constexpr int PD = PDV;
constexpr int CODE_BYTES = 47000, CODE_KB = 48;

#define NBV(x) (x)
struct Params {
  const u16 *x, *wqkv, *wphi, *wo, *wg, *wu, *wd, *c, *wmod;
  u16* out;
  unsigned char* ws;
};

template <int V> struct IC { static constexpr int value = V; };
template <int I, int N, class Fn> DI void static_for(Fn&& f) {
  if constexpr (I < N) { f(IC<I>{}); static_for<I + 1, N>(f); }
}
DI float lo16(u32 v) { return __uint_as_float(v << 16); }
DI float hi16(u32 v) { return __uint_as_float(v & 0xffff0000u); }
DI u32 pk(float a, float b) { f32x2 v = {a, b}; return __builtin_bit_cast(u32, __builtin_convertvector(v, bf16x2)); }
DI float rb(float a) { return lo16(pk(a, 0.f)); }
DI float rcp(float a) { return __builtin_amdgcn_rcpf(a); }
DI f32x16 mfma(v4u a, v4u b, f32x16 c) {
  return __builtin_amdgcn_mfma_f32_32x32x16_bf16(__builtin_bit_cast(bf16x8, a), __builtin_bit_cast(bf16x8, b), c, 0, 0, 0);
}
DI void mfma_pin(v4u a, v4u b, f32x16& c) {
  c = mfma(a, b, c);
  asm volatile("" : "+v"(c));
}
template <int O> DI v4u pack8(const f32x16& x) {
  v4u r; r.x = pk(x[O], x[O + 1]); r.y = pk(x[O + 2], x[O + 3]); r.z = pk(x[O + 4], x[O + 5]); r.w = pk(x[O + 6], x[O + 7]); return r;
}
template <int T> DI v4u ldw(LAS unsigned char* p) {
  s16x4 a = __builtin_amdgcn_ds_read_tr16_b64_v4i16((LAS s16x4*)p);
  s16x4 b = __builtin_amdgcn_ds_read_tr16_b64_v4i16((LAS s16x4*)(p + T));
  return __builtin_bit_cast(v4u, __builtin_shufflevector(a, b, 0, 1, 2, 3, 4, 5, 6, 7));
}
struct Base3 {
  u32 b[3];
  DI explicit Base3(LAS unsigned char* p) {
    b[0] = (u32)(size_t)p; b[1] = b[0] + 65536u; b[2] = b[0] + 131072u;
    asm volatile("" : "+v"(b[0]), "+v"(b[1]), "+v"(b[2]));
  }
  template <int OFF> DI LAS unsigned char* at() const { return (LAS unsigned char*)(size_t)b[OFF >> 16] + (OFF & 65535); }
};
template <int LOFF, int SOFF> DI void gldsi(rsrc_t rs, u32 voff, u32 sbase, u32 lbase) {
  u32 t;
  asm volatile("s_add_u32 m0, %1, %2\n\ts_add_u32 %0, %3, %4\n\tbuffer_load_dwordx4 %5, %6, %0 offen lds"
               : "=&s"(t) : "s"(lbase), "i"(LOFF), "s"(sbase), "i"(SOFF), "v"(voff), "s"(rs) : "m0", "scc", "memory");
}
DI void bar() { asm volatile("s_waitcnt lgkmcnt(0)\n\ts_barrier" ::: "memory"); }
DI void bar_raw() { asm volatile("s_barrier" ::: "memory"); }
DI void drain() { asm volatile("s_waitcnt vmcnt(0)" ::: "memory"); }
#define VMCNT(n) asm volatile("s_waitcnt vmcnt(" #n ")" ::: "memory")
#define RELOAD() asm volatile("" ::: "memory")
DI v4u ld128(rsrc_t rs, int voff, int soff) { return __builtin_amdgcn_raw_buffer_load_b128(rs, voff, soff, AUX_SC1); }
DI v2u ld64(rsrc_t rs, int voff, int soff) { return __builtin_amdgcn_raw_buffer_load_b64(rs, voff, soff, AUX_SC1); }
DI void st128(v4u v, rsrc_t rs, int voff, int soff) { __builtin_amdgcn_raw_buffer_store_b128(v, rs, voff + soff, 0, AUX_SC1); }
DI void st64(v2u v, rsrc_t rs, int voff, int soff) { __builtin_amdgcn_raw_buffer_store_b64(v, rs, voff, soff, AUX_SC1); }
template <int S> DI v4u ldfrag(rsrc_t rs, int voff, int soff) { return ld128(rs, voff + (S & 3) * 1024, soff + (S >> 2) * 4096); }
template <int W, int N> DI void ldfrags(v4u (&a)[N], rsrc_t rs, int voff, int soff) {
  static_for<0, W>([&](auto S) { a[S.value] = ldfrag<S.value>(rs, voff, soff); SB0(); });
}
template <int CTRL> DI float dpp_add(float v) {
  return v + __uint_as_float(__builtin_amdgcn_update_dpp(0u, __float_as_uint(v), CTRL, 0xf, 0xf, false));
}
constexpr int DPP_X1 = 0xB1, DPP_X2 = 0x4E, DPP_HM = 0x141, DPP_ROR4 = 0x124, DPP_ROR8 = 0x128;
DI float fold32(float x, float y) {
  auto r = __builtin_amdgcn_permlane32_swap(__float_as_uint(x), __float_as_uint(y), false, false);
  return __uint_as_float(r[0]) + __uint_as_float(r[1]);
}
DI float fold16(float x, float y) {
  auto r = __builtin_amdgcn_permlane16_swap(__float_as_uint(x), __float_as_uint(y), false, false);
  return __uint_as_float(r[0]) + __uint_as_float(r[1]);
}
DI void red8q(const float (&a)[8], float& c0, float& c1) {
  const float b0 = fold32(a[0], a[4]), b1 = fold32(a[1], a[5]), b2 = fold32(a[2], a[6]), b3 = fold32(a[3], a[7]);
  c0 = fold16(b0, b2); c1 = fold16(b1, b3);
  c0 = dpp_add<DPP_ROR4>(c0); c1 = dpp_add<DPP_ROR4>(c1);
  c0 = dpp_add<DPP_ROR8>(c0); c1 = dpp_add<DPP_ROR8>(c1);
}
DI void red8(const float (&a)[8], float& c0, float& c1) {
  red8q(a, c0, c1);
  c0 = dpp_add<DPP_X1>(c0); c1 = dpp_add<DPP_X1>(c1);
  c0 = dpp_add<DPP_X2>(c0); c1 = dpp_add<DPP_X2>(c1);
}
DI void publish(rsrc_t rs, int word, int bit, int lane) {
  drain();
  (void)__builtin_amdgcn_raw_ptr_buffer_atomic_add_i32(1 << bit, rs, lane == 0 ? 0 : WS_BYTES, OFF_ARR + word * 128, 0);
}
DI u32 ask(rsrc_t rs, int word, int lane) { return __builtin_amdgcn_raw_ptr_buffer_atomic_add_i32(0, rs, lane == 0 ? 0 : WS_BYTES, OFF_ARR + word * 128, 0); }
DI void spin(rsrc_t rs, int word, u32 m, int lane) {
#pragma nounroll
  for (u32 spins = 0; spins < SPIN_CAP; ++spins)
    if ((__builtin_amdgcn_readfirstlane(ask(rs, word, lane)) & m) == m) break;
}
DI void poll(u32 f, rsrc_t rs, int word, u32 m, int lane) {
  if (__builtin_expect((__builtin_amdgcn_readfirstlane(f) & m) != m, 0)) spin(rs, word, m, lane);
  __builtin_amdgcn_fence(__ATOMIC_ACQUIRE, "wavefront");
  RELOAD();
}
DI void waitn(rsrc_t rs, int word, u32 m, int lane) {
  if (PRE_FL) __builtin_amdgcn_s_sleep(PRE_FL);
  spin(rs, word, m, lane);
  __builtin_amdgcn_fence(__ATOMIC_ACQUIRE, "wavefront");
  RELOAD();
}
DI void sig(LAS unsigned char* L, int i, int k) { *(volatile LAS u32*)(L + L_HINT + 16 * i + 4 * k) = 1u; }
template <int N> DI void await(LAS unsigned char* L, int i) {
#pragma nounroll
  for (u32 spins = 0; spins < (1u << 20); ++spins) {
    const v4u v = *(volatile LAS v4u*)(L + L_HINT + 16 * i);
    if (__builtin_amdgcn_readfirstlane(N == 1 ? v.x : (v.x & v.y & v.z & v.w))) break;
    __builtin_amdgcn_s_sleep(1);
  }
  RELOAD();
}
DI void mark(LAS unsigned char* L, int i, int k, u32 val) { *(volatile LAS u32*)(L + L_HINT + 16 * i + 4 * k) = val; }
DI v4u peek(LAS unsigned char* L, int i) { return *(volatile LAS v4u*)(L + L_HINT + 16 * i); }
DI u32 umin(u32 a, u32 b) { return a < b ? a : b; }
DI void await_cp(LAS unsigned char* L, u32 k) {
#pragma nounroll
  for (u32 spins = 0; spins < (1u << 20); ++spins) {
    const v4u v = peek(L, H_CP);
    if (__builtin_amdgcn_readfirstlane(umin(umin(v.x, v.y), umin(v.z, v.w))) >= k) break;
    __builtin_amdgcn_s_sleep(CSLP);
  }
  RELOAD();
}
template <int SLP> DI void reach(LAS unsigned char* L, int i, u32 k0, u32 k1, u32 k2, u32 k3) {
#pragma nounroll
  for (u32 spins = 0; spins < (1u << 20); ++spins) {
    const v4u v = peek(L, i);
    if (__builtin_amdgcn_readfirstlane((v.x >= k0) & (v.y >= k1) & (v.z >= k2) & (v.w >= k3))) break;
    if (SLP) __builtin_amdgcn_s_sleep(SLP);
  }
  RELOAD();
}
DI void reach1(LAS unsigned char* L, int i, int k, u32 val) {
#pragma nounroll
  for (u32 spins = 0; spins < (1u << 20); ++spins)
    if (__builtin_amdgcn_readfirstlane(*(volatile LAS u32*)(L + L_HINT + 16 * i + 4 * k)) >= val) break;
  RELOAD();
}
#define MARK(i, k, val) do { RELOAD(); mark(L, i, k, val); RELOAD(); } while (0)
template <bool AUX> DI u32 pp_min(v4u v) {
  u32 m = umin(v.x, v.y);
  if (AUX) m = umin(m, umin(v.z, v.w));
  return __builtin_amdgcn_readfirstlane(m);
}
template <bool AUX> DI void need_pp(LAS unsigned char* L, v4u v, u32 k) {
  if (__builtin_expect(pp_min<AUX>(v) < k, 0)) {
#pragma nounroll
    for (u32 spins = 0; spins < (1u << 20); ++spins)
      if (pp_min<AUX>(peek(L, H_PP)) >= k) break;
  }
  RELOAD();
}
template <class Fn> DI void feat(const f32x16& P, u32 (&ph)[16], Fn&& fill) {
#pragma clang fp contract(off)
  constexpr float L2E = 1.4426950408889634f;
  f32x2 p[8];
  float mx[4], mn[4];
  static_for<0, 4>([&](auto I) {
    constexpr int i = I.value;
    const u32 t0 = pk(P[4 * i], P[4 * i + 1]), t1 = pk(P[4 * i + 2], P[4 * i + 3]);
    p[2 * i] = f32x2{lo16(t0), hi16(t0)} * L2E; p[2 * i + 1] = f32x2{lo16(t1), hi16(t1)} * L2E;
    mx[i] = fmaxf(fmaxf(p[2 * i].x, p[2 * i].y), fmaxf(p[2 * i + 1].x, p[2 * i + 1].y));
    mn[i] = fminf(fminf(p[2 * i].x, p[2 * i].y), fminf(p[2 * i + 1].x, p[2 * i + 1].y));
    fill(IC<i>{});
  });
  const float mxl = fmaxf(fmaxf(mx[0], mx[1]), fmaxf(mx[2], mx[3])), mnl = fminf(fminf(mn[0], mn[1]), fminf(mn[2], mn[3]));
  f32x2 e1[8], e2[8];
#pragma unroll
  for (int i = 0; i < 8; ++i) { e1[i] = p[i] - mxl; e2[i] = mnl - p[i]; }
  SB0();
  static_for<0, 8>([&](auto I) {
    constexpr int i = I.value;
    fill(IC<4 + i>{});
    e1[i] = f32x2{__builtin_amdgcn_exp2f(e1[i].x), __builtin_amdgcn_exp2f(e1[i].y)};
    e2[i] = f32x2{__builtin_amdgcn_exp2f(e2[i].x), __builtin_amdgcn_exp2f(e2[i].y)};
  });
  fill(IC<12>{});
  const f32x2 s1 = ((e1[0] + e1[1]) + (e1[2] + e1[3])) + ((e1[4] + e1[5]) + (e1[6] + e1[7]));
  fill(IC<13>{});
  const f32x2 s2 = ((e2[0] + e2[1]) + (e2[2] + e2[3])) + ((e2[4] + e2[5]) + (e2[6] + e2[7]));
  fill(IC<14>{});
  const float r1 = rcp(s1.x + s1.y), r2 = rcp(s2.x + s2.y);
  fill(IC<15>{});
#pragma unroll
  for (int i = 0; i < 8; ++i) { const f32x2 u = e1[i] * r1, v = e2[i] * r2; ph[i] = pk(u.x, u.y); ph[8 + i] = pk(v.x, v.y); }
}

#define TSCLK() __builtin_amdgcn_s_memrealtime()
#define TS(i) do {} while (0)
#define TS_DUMP() do {} while (0)
#define TS_INIT() do {} while (0)

__global__ void __launch_bounds__(512, 2) mega(Params p) {
  extern __shared__ __attribute__((aligned(16))) unsigned char smem[];
  LAS unsigned char* L = (LAS unsigned char*)smem;
  TS_INIT();
  TS(0);
  u64 pc0;
  asm volatile("s_getpc_b64 %0" : "=s"(pc0));
  const int tid = threadIdx.x, lane = tid & 63;
  int w = __builtin_amdgcn_readfirstlane(tid >> 6);
  const int b = blockIdx.x, xcd = b & 7, rank = b >> 3, panel = (xcd >> 1) * 4 + (rank >> 3), mem = (xcd & 1) * 8 + (rank & 7);
  const int hh = lane >> 5, l31 = lane & 31;
  unsigned char* ws = p.ws;
  const int rowh = mem & 1, colg = mem >> 1;
  const int pair0 = min(3 * mem, 2 * mem + 11);
  const bool three = mem < 11;
  const rsrc_t rs = __builtin_amdgcn_make_buffer_rsrc(ws, 0, WS_BYTES, 0x00020000);
  const int pb = OFF_PAN + panel * PSTR, fw = panel * 24;
  const int mcg = xcd * 8 + (rank >> 2), mkp = rank & 3;
  const rsrc_t rqkv = __builtin_amdgcn_make_buffer_rsrc((void*)p.wqkv, 0, D * 3 * D * 2, 0x00020000);
  const rsrc_t rwo = __builtin_amdgcn_make_buffer_rsrc((void*)p.wo, 0, D * D * 2, 0x00020000);
  const rsrc_t rgu = __builtin_amdgcn_make_buffer_rsrc((void*)((w & 1) ? p.wu : p.wg), 0, D * F * 2, 0x00020000);
  const rsrc_t rwd = __builtin_amdgcn_make_buffer_rsrc((void*)p.wd, 0, F * D * 2, 0x00020000);

  v4u r0[4];
  u32 cv0[4], epoch;
  u32 vq = (lane >> 2) * (3 * D * 2) + (lane & 3) * 16;
  asm volatile("" : "+v"(vq));
  {
    const u32 ld = (u32)(size_t)L + w * 1024;
    if (w <= 4) {
      const rsrc_t r1 = __builtin_amdgcn_make_buffer_rsrc((void*)(w == 4 ? p.wmod : p.x), 0, w == 4 ? D * 6 * D * 2 : 2048 * D * 2, 0x00020000);
      const int vo = w == 4 ? (128 * mkp + 4 * (lane >> 1)) * (6 * D * 2) + mcg * 32 + (lane & 1) * 16
                            : (panel * 128 + mem * 8 + (lane >> 3)) * (D * 2) + (lane & 7) * 16 + 256 * w;
#pragma unroll
      for (int i = 0; i < 4; ++i) r0[i] = __builtin_amdgcn_raw_buffer_load_b128(r1, vo, w == 4 ? i * (6 * D * 2) : i < 2 ? i * 128 : 1 << 30, 0);
      SB0();
    }
    {
      const rsrc_t r2 = __builtin_amdgcn_make_buffer_rsrc((void*)p.c, 0, w == 4 ? D * 2 : 0, 0x00020000);
      const v2u c2 = __builtin_amdgcn_raw_buffer_load_b64(r2, (128 * mkp + 4 * (lane >> 1)) * 2, 0, 0);
      cv0[0] = c2.x; cv0[1] = c2.y;
      epoch = __builtin_amdgcn_raw_buffer_load_b32(rs, 0, OFF_EPOCH, AUX_SC1);
      __builtin_amdgcn_raw_buffer_store_b32(0u, rs, (w == 4 && lane < 2) ? lane * 128 : WS_BYTES, OFF_ARR + b * 256, AUX_SC1);
      SB0();
    }
    if (w >= 5) {
      const rsrc_t rc = __builtin_amdgcn_make_buffer_rsrc((void*)pc0, 0, CODE_BYTES, 0x00020000);
      gldsi<slot(128), 0>(rc, lane < CODE_KB / 6 ? (u32)lane * 128u : 1u << 30, (u32)(((rank & 15) * 3 + w - 5) * (CODE_KB / 6) * 128), ld);
      const u32 tq = (rank * 16 + (lane >> 2)) * (3 * D * 2) + (xcd & 1) * 512 + (lane & 3) * 128;
      if (EQ || w != 5) gldsi<slot(128), 0>(rqkv, tq, (w - 5) * (D * 2), ld);
      if (w == 5) {
        gldsi<slot(128), 0>(rs, lane < 2 ? (u32)(pb + lane * (848 << 10)) : 1u << 30, 0u, ld);
        const rsrc_t rp = __builtin_amdgcn_make_buffer_rsrc((void*)p.wphi, 0, 32 * 256 * 2, 0x00020000);
        gldsi<L_CF + 2048, 0>(rp, lane * 16, mem * 1024, (u32)(size_t)L);
      }
    }
    if (tid < 64) *((LAS u32*)(L + L_HINT) + tid) = 0u;
    TS(27);
    bar();
    asm volatile("" : "+s"(w));
    if (w >= 6) {
      u32 lo = (u32)(size_t)L + (w & 1) * 1024, so = (D + (w & 1) * D + mem * 32) * 2;
#pragma nounroll
      for (int i = 0; i < KV1 / 2; ++i) {
        gldsi<0, 0>(rqkv, vq, so, lo);
        gldsi<2048, 16 * (3 * D * 2)>(rqkv, vq, so, lo);
        lo += 4096; so += 32 * (3 * D * 2);
      }
    }
    if (w == 4) {
      __builtin_amdgcn_s_setprio(3);
      TS(10);
      float sl[4], acc[8];
#pragma unroll
      for (int i = 0; i < 4; ++i) { const float c = (i & 1) ? hi16(cv0[i >> 1]) : lo16(cv0[i >> 1]); sl[i] = rb(c * rcp(1.f + __expf(-c))); }
      u32 tag = epoch;
      asm volatile("" : "+v"(tag));
      tag += 1u;
      TS(11);
#pragma unroll
      for (int j = 0; j < 8; ++j) acc[j] = 0.f;
#pragma unroll
      for (int i = 0; i < 4; ++i)
#pragma unroll
        for (int j = 0; j < 4; ++j) { acc[2 * j] += sl[i] * lo16(r0[i][j]); acc[2 * j + 1] += sl[i] * hi16(r0[i][j]); }
      float c0, c1;
      TS(12);
      red8q(acc, c0, c1);
      c0 = dpp_add<DPP_X2>(c0); c1 = dpp_add<DPP_X2>(c1);
      const int go = ((lane >> 1) & 7) * 32768 + (8 * (lane & 1) + 2 * (lane >> 4)) * 8;
      const v4u gv = {__float_as_uint(c0), tag, __float_as_uint(c1), tag};
      drain();
      st128(gv, rs, go, OFF_MODR + mkp * 8192 + mcg * 128);
      st128(gv, rs, go, OFF_MODR + 8 * 32768 + mkp * 8192 + mcg * 128);
      sig(L, 0, 0);
    }
    asm volatile("" : "+s"(w));
  }

  if (w == 4 || w == 5) {
    const int a = w & 1;
    u32 tag;
    tag = epoch;
    asm volatile("" : "+v"(tag));
    tag += 1u;
    drain();
    TS(1);
    bar();
    __builtin_amdgcn_s_setprio(APRIO);
    await<4>(L, 2);
    const int kp = xcd >> 1, cpair = (xcd & 1) * 24 + rank;
    const bool con = rank < ((xcd & 1) ? 19 : 24);
    const int col0 = a * F + cpair * 32;
    v4u wm[8];
    if (a == 1) {
      v4u wn[8]; u32 cv[8];
      const int row0 = 128 * mkp + (lane >> 2);
#pragma unroll
      for (int i = 0; i < 8; ++i) cv[i] = p.c[row0 + 16 * i];
#pragma unroll
      for (int i = 0; i < 8; ++i) wn[i] = *(const GAS v4u*)(p.wmod + (size_t)(row0 + 16 * i) * (6 * D) + 1024 + mcg * 32 + (lane & 3) * 8);
      SB0();
      float acc[8];
#pragma unroll
      for (int j = 0; j < 8; ++j) acc[j] = 0.f;
#pragma unroll
      for (int i = 0; i < 8; ++i) {
        const float c = lo16(cv[i]), sl = rb(c * rcp(1.f + __expf(-c)));
#pragma unroll
        for (int j = 0; j < 4; ++j) { acc[2 * j] += sl * lo16(wn[i][j]); acc[2 * j + 1] += sl * hi16(wn[i][j]); }
      }
      float c0, c1;
      red8q(acc, c0, c1);
      st128(v4u{__float_as_uint(c0), tag, __float_as_uint(c1), tag}, rs, (lane & 12) == 0 ? (8 * (lane & 3) + 2 * (lane >> 4)) * 8 : WS_BYTES,
            OFF_MODG + mkp * 16384 + mcg * 256);
    }
    reach<1>(L, H_C0, 2u, 2u, 2u, 2u);
    TS(2);
    {
      const int idx0 = (mem * 4 + 2 * a) * 16;
      v4u e[32], zz[2];
      {
        u32 todo = 0xFFu;
        for (u32 spins = 0; spins < SPIN_CAP && todo; ++spins) {
          const u32 bad = ~__builtin_amdgcn_readfirstlane(ask(rs, FL_KV + mem * 2 + a, lane));
          __builtin_amdgcn_fence(__ATOMIC_ACQUIRE, "wavefront");
          RELOAD();
          static_for<0, 8>([&](auto G) {
            constexpr int g = G.value;
            if (((todo >> g) & 1u) && !(bad & (0x00030003u << (2 * g)))) {
              todo &= ~(1u << g);
              e[2 * g] = ld128(rs, lane * 16, OFF_KVE + (idx0 + 2 * g) * 1024);
              e[2 * g + 1] = ld128(rs, lane * 16, OFF_KVE + (idx0 + 2 * g + 1) * 1024);
              e[16 + 2 * g] = ld128(rs, lane * 16, OFF_KVE + (idx0 + 16 + 2 * g) * 1024);
              e[17 + 2 * g] = ld128(rs, lane * 16, OFF_KVE + (idx0 + 17 + 2 * g) * 1024);
            }
          });
        }
      }
      TS(3);
      zz[0] = ldfrag<0>(rs, lane * 16, OFF_KVZ + idx0 * 64);
      zz[1] = ldfrag<1>(rs, lane * 16, OFF_KVZ + idx0 * 64);
      SB0();
#pragma unroll
      for (int q = 0; q < 2; ++q) {
        f32x4 s0 = __builtin_bit_cast(f32x4, e[16 * q]), s1 = __builtin_bit_cast(f32x4, e[16 * q + 1]);
#pragma unroll
        for (int i = 2; i < 16; i += 2) { s0 += __builtin_bit_cast(f32x4, e[16 * q + i]); s1 += __builtin_bit_cast(f32x4, e[16 * q + i + 1]); }
        s0 += s1;
        *(LAS v2u*)(L + L_KVF + (2 * a + q) * 512 + l31 * 16 + hh * 8) = v2u{pk(s0[0], s0[1]), pk(s0[2], s0[3])};
      }
      f32x4 z;
#pragma unroll
      for (int j = 0; j < 4; ++j) {
        float t = fold32(__uint_as_float(zz[0][j]), __uint_as_float(zz[1][j]));
        t = fold16(t, t);
        t = dpp_add<DPP_ROR4>(t);
        z[j] = dpp_add<DPP_ROR8>(t);
      }
      if (l31 < 4) *(LAS v2u*)(L + L_ZF + (2 * a + hh) * 32 + ((lane & 1) ^ 1) * 16 + ((lane >> 1) & 1) * 8) = v2u{pk(z[0], z[1]), pk(z[2], z[3])};
    }
    MARK(H_P0, 2 + a, 1u);
    __builtin_amdgcn_s_setprio(1);
    TS(4);
#pragma unroll
    for (int i = 0; i < 8; ++i)
      wm[i] = __builtin_amdgcn_raw_buffer_load_b128(rgu, con ? (128 * kp + 2 * (lane >> 2)) * (F * 2) + cpair * 64 + (lane & 3) * 16 : 1 << 30, (32 * (i >> 1) + (i & 1)) * (F * 2), 0);
    SB0();
    u32 gsb[4], gsc[4];
    {
      const int vo = ((hh ? 2 : 3 * a) * D + colg * 64 + 2 * l31) * 8, ro = (D + 128 * kp + 2 * lane) * 8;
      v4u g[12];
      for (u32 spins = 0; spins < SPIN_CAP; ++spins) {
        RELOAD();
#pragma unroll
        for (int i = 0; i < 12; ++i) g[i] = ld128(rs, i < 4 ? vo : ro + (i >> 3) * (D * 8), OFF_MODG + (i & 3) * 16384);
        bool ok = true;
#pragma unroll
        for (int i = 0; i < 12; ++i) ok &= (g[i].y == tag) & (g[i].w == tag);
        if (__all(ok)) break;
      }
      u32 pr[3];
#pragma unroll
      for (int t = 0; t < 3; ++t)
        pr[t] = pk((__uint_as_float(g[4 * t].x) + __uint_as_float(g[4 * t + 1].x)) + (__uint_as_float(g[4 * t + 2].x) + __uint_as_float(g[4 * t + 3].x)),
                   (__uint_as_float(g[4 * t].z) + __uint_as_float(g[4 * t + 1].z)) + (__uint_as_float(g[4 * t + 2].z) + __uint_as_float(g[4 * t + 3].z)));
      const u32 v1 = pk(1.f + lo16(pr[0]), 1.f + hi16(pr[0])), vv = hh ? v1 : pr[0];
      *(LAS f32x2*)(L + L_VEC + (hh ? 1 : 2 * a) * 256 + l31 * 8) = f32x2{lo16(vv), hi16(vv)};
      const u32 sc = pk(1.f + lo16(pr[2]), 1.f + hi16(pr[2]));
#pragma unroll
      for (int i = 0; i < 4; ++i) {
        gsb[i] = __builtin_amdgcn_ds_bpermute((16 * i + (lane >> 2)) * 4, pr[1]);
        gsc[i] = __builtin_amdgcn_ds_bpermute((16 * i + (lane >> 2)) * 4, sc);
      }
    }
    MARK(H_P0, 2 + a, 2u);
    if (con) {
      float a1[8], a2[8];
#pragma unroll
      for (int j = 0; j < 8; ++j) { a1[j] = 0.f; a2[j] = 0.f; }
#pragma unroll
      for (int i = 0; i < 8; ++i) {
        const float sb = (i & 1) ? hi16(gsb[i >> 1]) : lo16(gsb[i >> 1]), sc = (i & 1) ? hi16(gsc[i >> 1]) : lo16(gsc[i >> 1]);
#pragma unroll
        for (int j = 0; j < 4; ++j) {
          a1[2 * j] += sc * lo16(wm[i][j]); a1[2 * j + 1] += sc * hi16(wm[i][j]);
          a2[2 * j] += sb * lo16(wm[i][j]); a2[2 * j + 1] += sb * hi16(wm[i][j]);
        }
      }
      float c10, c11, c20, c21;
      red8q(a1, c10, c11); red8q(a2, c20, c21);
      const int k = (lane >> 2) & 3;
      const float val = k == 0 ? c10 : k == 1 ? c20 : k == 2 ? c11 : c21;
      st64(v2u{__float_as_uint(val), tag}, rs, (col0 + 8 * (lane & 3) + 2 * (lane >> 4)) * 16 + k * 8, OFF_CG + kp * CG_PART);
    }
    TS(5);
    reach<2>(L, H_C0, 4u, 4u, 4u, 4u);
    {
      const int ntile = three ? 6 : 4;
      v4u cg[8];
      int off[2];
#pragma unroll
      for (int i = 0; i < 2; ++i) {
        int tile = 4 * i + 2 * a + hh; tile = tile < ntile ? tile : 0;
        off[i] = (32 * pair0 + 16 * tile + (lane & 15) + ((lane >> 4) & 1) * F) * 16;
      }
      for (u32 spins = 0; spins < SPIN_CAP; ++spins) {
        RELOAD();
        bool ok = true;
#pragma unroll
        for (int i = 0; i < 8; ++i) cg[i] = ld128(rs, off[i >> 2], OFF_CG + (i & 3) * CG_PART);
#pragma unroll
        for (int i = 0; i < 8; ++i) ok &= (cg[i].y == tag) & (cg[i].w == tag);
        if (__all(ok)) break;
      }
#pragma unroll
      for (int i = 0; i < 2; ++i) {
        const float c1 = (__uint_as_float(cg[4 * i].x) + __uint_as_float(cg[4 * i + 1].x)) + (__uint_as_float(cg[4 * i + 2].x) + __uint_as_float(cg[4 * i + 3].x));
        const float c2 = (__uint_as_float(cg[4 * i].z) + __uint_as_float(cg[4 * i + 1].z)) + (__uint_as_float(cg[4 * i + 2].z) + __uint_as_float(cg[4 * i + 3].z));
        const float h1 = rb(c1), m1 = rb(c1 - h1), l1 = c1 - h1 - m1, h2 = rb(c2), m2 = rb(c2 - h2), l2 = c2 - h2 - m2;
        const int tile = 4 * i + 2 * a + hh;
        if (tile < 6) *(LAS v4u*)(L + L_CF + tile * 512 + l31 * 16) = v4u{pk(h1, m1), pk(l1, h2), pk(m2, l2), 0u};
      }
    }
    reach<1>(L, 5, a < 1, a < 1, a, a);
    {
      v4u st[16];
#pragma unroll
      for (int t = 0; t < 16; ++t) st[t] = ld128(rs, l31 * 16 + hh * 8192 + t * 512, pb + PN_ST + 2 * a * 8192);
      for (u32 spins = 0; spins < SPIN_CAP; ++spins) {
        bool ok = true;
#pragma unroll
        for (int t = 0; t < 16; ++t) ok &= (st[t].y == tag) & (st[t].w == tag);
        if (__all(ok)) break;
        RELOAD();
#pragma unroll
        for (int t = 0; t < 16; ++t) st[t] = ld128(rs, l31 * 16 + hh * 8192 + t * 512, pb + PN_ST + 2 * a * 8192);
      }
      float ms = 0.f;
#pragma unroll
      for (int t = 0; t < 16; ++t) ms += __uint_as_float(st[t].x);
      const float mean = ms * (1.f / 16.f);
      float q = 0.f;
#pragma unroll
      for (int t = 0; t < 16; ++t) { const float d = __uint_as_float(st[t].x) - mean; q += __uint_as_float(st[t].z) + 32.f * d * d; }
      const float den = rb(__builtin_amdgcn_sqrtf(rb(rb(q * (1.f / D)) + 1e-5f)));
      *(LAS v4u*)(L + L_KVF + a * 1024 + lane * 16) = v4u{pk(-mean, -mean), pk(-mean, den), pk(den, den), __float_as_uint(rcp(den * den))};
    }
    RELOAD();
    mark(L, H_PP, 2 + a, 5u);
    TS(6);
    await_cp(L, 2);
    {
      v4u wt[58];
      const u32 vd = (lane >> 2) * (D * 2) + (lane & 3) * 16, sd = a * 16 * (D * 2) + colg * 128;
      const Base3 wdst(L + a * 2048 + lane * 16);
      static_for<28, 86>([&](auto J) {
        wt[J.value - 28] = __builtin_amdgcn_raw_buffer_load_b128(rwd, NBV(vd + (J.value & 1) * 64), sd + pair_of(J.value >> 1) * 32 * (D * 2), 0); SB0(); });
      TS(7);
#define ST_WT(T0, T1, k) do { static_for<2 * (T0), 2 * (T1)>([&](auto J) { \
      *(LAS v4u*)wdst.template at<slot(P_WD + 4 * (J.value >> 1)) + (J.value & 1) * 1024>() = wt[J.value - 28]; WPACE(); }); \
      RELOAD(); mark(L, H_PP, 2 + a, k); RELOAD(); } while (0)
      await_cp(L, 4);
      ST_WT(14, 18, 6u);
      await_cp(L, 5);
      ST_WT(18, 26, 7u);
      await_cp(L, 6);
      ST_WT(26, 34, 8u);
      TS(8);
      await_cp(L, 7);
      ST_WT(34, 43, 9u);
      TS(9);
    }
    TS_DUMP();
    return;
  }

  if (w >= 6) {
    const int v = w & 1;
    __builtin_amdgcn_s_setprio(LPRIO);
    const u32 ldsb = (u32)(size_t)L;
    const u32 l1 = ldsb + v * 1024, l2 = ldsb + v * 2048;
    const u32 skv = (D + v * D + mem * 32) * 2, sq = v * 16 * (3 * D * 2) + mem * 64;
    const u32 so = 16 * v * 16 * (D * 2) + colg * 128, sd = v * 16 * (D * 2) + colg * 128;
    const u32 sg = pair0 * 64, sg2 = three ? sg + 128 : 1u << 30;
#define LD_KV(S0, S1, PC) static_for<S0, S1>([&](auto S) { gldsi<slot(P_KV + 2 * S.value), S.value * 16 * (3 * D * 2)>(rqkv, vq, skv, l1); PACE(PC); })
#define PACE(n) do { if ((n) > 0) __builtin_amdgcn_s_sleep(n); } while (0)
#define LD_Q(S0, S1, PC) static_for<S0, S1>([&](auto S) { gldsi<slot(P_Q + 2 * S.value), 2 * S.value * 16 * (3 * D * 2)>(rqkv, vq, sq, l1); PACE(PC); })
#define LD_DN(RS, SB, P0, T0, T1, PC)                                                                    \
  static_for<T0, T1>([&](auto T) {                                                                   \
    gldsi<slot((P0) + 4 * T.value), T.value * 16 * (D * 2)>(RS, vd, SB, l2);                         \
    gldsi<slot((P0) + 4 * T.value) + 1024, T.value * 16 * (D * 2) + 64>(RS, vd, SB, l2); PACE(PC);   \
  })
#define LD_GU(S0, S1, PC)                                                                                \
  static_for<S0, S1>([&](auto S) {                                                                   \
    gldsi<slot(P_GU + 6 * S.value), S.value * 16 * (F * 2)>(rgu, vg, sg, l1);                        \
    gldsi<slot(P_GU + 6 * S.value + 2), S.value * 16 * (F * 2) + 64>(rgu, vg, sg, l1);               \
    gldsi<slot(P_GU + 6 * S.value + 4), S.value * 16 * (F * 2)>(rgu, vg, sg2, l1); PACE(PC);         \
  })
    TS(1);
    VMCNT(0);
    SB0();
    int ln;
    asm volatile("v_mbcnt_lo_u32_b32 %0, -1, 0\n\tv_mbcnt_hi_u32_b32 %0, -1, %0" : "=v"(ln));
    const u32 vd = (ln >> 2) * (D * 2) + (ln & 3) * 16;
    const u32 vg = (ln >> 2) * (F * 2) + (((ln & 3) ^ (v << 1)) * 16);
    TS(2);
    LD_Q(0, 16, PQE);
    TS(11);
    bar_raw();
    v4u wt[36];
    const Base3 wdst1(L + v * 1024 + ln * 16);
#define GUN(s_, P_) (P_GU + ((s_) < 12 ? 6 * (s_) + 2 * (P_) : 72 + 40 * (P_) + 2 * ((s_) - 12)))
#define GUR(s_, P_) __builtin_amdgcn_raw_buffer_load_b128(rgu, NBV(vg), ((P_) == 2 ? sg2 : sg + (P_) * 64) + (s_) * 16 * (F * 2), 0)
#define GUW(s_, P_, r_) *(LAS v4u*)wdst1.template at<slot(GUN(s_, P_))>() = wt[r_]
#define LD_GU2(S0, S1, PC) static_for<S0, S1>([&](auto S) { constexpr int s_ = S.value; \
      gldsi<slot(GUN(s_, 0)), s_ * 16 * (F * 2)>(rgu, vg, sg, l1); SB0(); \
      if constexpr (s_ < 24) gldsi<slot(GUN(s_, 1)), s_ * 16 * (F * 2) + 64>(rgu, vg, sg, l1); \
      else wt[20 + s_ - 24] = GUR(s_, 1); \
      SB0(); wt[s_ - 12] = GUR(s_, 2); PACE(PC); SB0(); })
#define LD_WD(T0, T1) static_for<T0, T1>([&](auto T) { \
      gldsi<slot(P_WD + 4 * T.value), pair_of(T.value) * 32 * (D * 2)>(rwd, vd, sd, l2); \
      gldsi<slot(P_WD + 4 * T.value) + 1024, pair_of(T.value) * 32 * (D * 2) + 64>(rwd, vd, sd, l2); })
#define DONE(k) do { RELOAD(); mark(L, H_PP, v, k); RELOAD(); } while (0)
    await<4>(L, 2);
    VMCNT(0);
    MARK(H_P0, v, 1u);
    gldsi<L_CF, 0>(rwo, ln < 32 ? ((2 * rank + v) * 8 + (ln >> 2)) * (D * 2) + (xcd & 1) * 512 + (ln & 3) * 128 : 1u << 30, 0u, l1);
    gldsi<L_CF, 0>(rgu, (ln & 15) < 13 ? ((xcd >> 1) * 128 + 4 * rank + (ln >> 4)) * (F * 2) + (xcd & 1) * 1536 + (ln & 15) * 128 : 1u << 30, 0u, l1);
    LD_DN(rwo, so, P_WO, 0, W1N, 0);
    TS(3);
    VMCNT(0);
    MARK(H_P0, v, 1u);
    reach<1>(L, H_C0, 2u, 2u, 2u, 2u);
    LD_DN(rwo, so, P_WO, W1N, 16, 1);
    LD_GU(0, W2N, PGU);
    asm volatile("s_waitcnt vmcnt(%0)" ::"i"(3 * W2N) : "memory");
    MARK(H_P0, v, 2u);
    TS(4);
    reach<2>(L, H_C0, 3u, 3u, 3u, 3u);
    reach<2>(L, H_P0, 0u, 0u, 1u, 1u);
    LD_GU(W2N, 12, PG3);
    await<4>(L, 4);
    static_for<12, 20>([&](auto S) { constexpr int s_ = S.value;
      wt[20 + s_ - 12] = GUR(s_, 0); wt[28 + s_ - 12] = GUR(s_, 1); wt[s_ - 12] = GUR(s_, 2); SB0(); });
    VMCNT(24);
    MARK(H_P0, v, 3u);
    TS(5);
    reach<1>(L, H_C0, 4u, 4u, 4u, 4u);
    static_for<12, 20>([&](auto S) { GUW(S.value, 0, 20 + S.value - 12); GUW(S.value, 1, 28 + S.value - 12); });
    LD_GU2(20, 32, PG2);
    VMCNT(0);
    DONE(3);
    TS(6);
    await_cp(L, 1);
    {
      const u32 tdn = ((2 * rank + v) * 16 + (ln >> 2)) * (D * 2) + (xcd & 1) * 512 + (ln & 3) * 128;
      __builtin_amdgcn_s_waitcnt(0x0F70);
      gldsi<L_T, 0>(rwd, tdn, 0u, l1);
      gldsi<L_T, 1024 * (D * 2)>(rwd, tdn, 0u, l1);
    }
    static_for<24, 32>([&](auto S) { GUW(S.value, 1, 20 + S.value - 24); });
    DONE(4);
    await_cp(L, 2);
    static_for<12, 32>([&](auto S) { GUW(S.value, 2, S.value - 12); });
    DONE(5);
    TS(7);
    await_cp(L, 3);
    LD_WD(0, 10);
    await_cp(L, 4);
    LD_WD(10, 14);
    VMCNT(0);
    DONE(100);
    TS(8);
    TS_DUMP();
    return;
  }

  __builtin_amdgcn_s_setprio(2);
  const int s2 = w & 1, kh = w >> 1;
  const int strip2 = rowh * 2 + s2, ncol = colg * 64 + kh * 32;
  v4u aphi0, aphi1;
  u32 tag;
  {
    float xf[16];
#pragma unroll
    for (int i = 0; i < 8; ++i) { xf[2 * i] = lo16(r0[i >> 2][i & 3]); xf[2 * i + 1] = hi16(r0[i >> 2][i & 3]); }
    float sm = (((xf[0] + xf[1]) + (xf[2] + xf[3])) + ((xf[4] + xf[5]) + (xf[6] + xf[7]))) + (((xf[8] + xf[9]) + (xf[10] + xf[11])) + ((xf[12] + xf[13]) + (xf[14] + xf[15])));
    sm = dpp_add<DPP_X1>(sm); sm = dpp_add<DPP_X2>(sm); sm = dpp_add<DPP_HM>(sm);
    const float mw = sm * (1.f / 128.f);
    float q[4];
#pragma unroll
    for (int i = 0; i < 4; ++i) {
      const float d0 = xf[4 * i] - mw, d1 = xf[4 * i + 1] - mw, d2 = xf[4 * i + 2] - mw, d3 = xf[4 * i + 3] - mw;
      q[i] = (d0 * d0 + d1 * d1) + (d2 * d2 + d3 * d3);
    }
    float qq = (q[0] + q[1]) + (q[2] + q[3]);
    qq = dpp_add<DPP_X1>(qq); qq = dpp_add<DPP_X2>(qq); qq = dpp_add<DPP_HM>(qq);
    *(volatile LAS f32x2*)(L + L_KVF + (lane >> 3) * 32 + w * 8) = f32x2{mw, qq};
    sig(L, 1, w);
    await<4>(L, 1);
    const f32x4 pa = *(LAS f32x4*)(L + L_KVF + (lane >> 3) * 32), pc = *(LAS f32x4*)(L + L_KVF + (lane >> 3) * 32 + 16);
    const float mean = ((pa[0] + pa[2]) + (pc[0] + pc[2])) * 0.25f;
    const float f0 = pa[0] - mean, f1 = pa[2] - mean, f2 = pc[0] - mean, f3 = pc[2] - mean;
    const float m2 = ((pa[1] + pa[3]) + (pc[1] + pc[3])) + 128.f * ((f0 * f0 + f1 * f1) + (f2 * f2 + f3 * f3));
    const float m1 = rb(mean), r1 = rcp(rb(__builtin_amdgcn_sqrtf(rb(rb(m2 * (1.f / D)) + 1e-5f))));
    float xn[16];
#pragma unroll
    for (int i = 0; i < 8; ++i) {
      const u32 d = pk(xf[2 * i] - m1, xf[2 * i + 1] - m1);
      const u32 t = pk(lo16(d) * r1, hi16(d) * r1);
      xn[2 * i] = lo16(t); xn[2 * i + 1] = hi16(t);
    }
#pragma unroll
    for (int i = 0; i < 16; ++i) asm volatile("" : "+v"(xn[i]));
    tag = __builtin_amdgcn_readfirstlane(epoch) + 1u;
    TS(1);
    await<1>(L, 0);
    __builtin_amdgcn_s_sleep(PRE_MOD);
    TS(21);
    v4u g[8];
    {
      u32 todo = 0xFFu;
      for (u32 spins = 0; spins < SPIN_CAP && todo; ++spins) {
        RELOAD();
        static_for<0, 8>([&](auto I) {
          constexpr int i = I.value;
          if ((todo >> i) & 1u) g[i] = ld128(rs, lane * 16, OFF_MODR + panel * 32768 + (i & 3) * 8192 + (i >> 2) * 4096 + w * 1024);
        });
        todo = 0;
        static_for<0, 8>([&](auto I) { if (__builtin_amdgcn_ballot_w64((g[I.value].y != tag) | (g[I.value].w != tag))) todo |= 1u << I.value; });
      }
    }
    {
      u32 pr[2];
#pragma unroll
      for (int t = 0; t < 2; ++t)
        pr[t] = pk((__uint_as_float(g[4 * t].x) + __uint_as_float(g[4 * t + 1].x)) + (__uint_as_float(g[4 * t + 2].x) + __uint_as_float(g[4 * t + 3].x)),
                   (__uint_as_float(g[4 * t].z) + __uint_as_float(g[4 * t + 1].z)) + (__uint_as_float(g[4 * t + 2].z) + __uint_as_float(g[4 * t + 3].z)));
      *(LAS u32*)(L + L_T + w * TW + lane * 4) = pr[0];
      *(LAS u32*)(L + L_T + w * TW + 256 + lane * 4) = pk(1.f + lo16(pr[1]), 1.f + hi16(pr[1]));
    }
    v4u sav[2], cav[2];
#pragma unroll
    for (int i = 0; i < 2; ++i) {
      sav[i] = *(LAS v4u*)(L + L_T + w * TW + i * 128 + (lane & 7) * 16);
      cav[i] = *(LAS v4u*)(L + L_T + w * TW + 256 + i * 128 + (lane & 7) * 16);
    }
    sig(L, 3, w);
    TS(2);
    const int strip = mem >> 2, r = 8 * (mem & 3) + (lane >> 3);
#pragma unroll
    for (int i = 0; i < 2; ++i) {
      v4u o;
#pragma unroll
      for (int j = 0; j < 4; ++j) {
        const u32 sa = sav[i][j], sc = cav[i][j];
        const u32 t = pk(xn[8 * i + 2 * j] * lo16(sc), xn[8 * i + 2 * j + 1] * hi16(sc));
        o[j] = pk(lo16(t) + lo16(sa), hi16(t) + hi16(sa));
      }
      st128(o, rs, (4 * i + ((lane & 7) >> 1)) * 1024 + (r + 32 * (lane & 1)) * 16, pb + PN_H + strip * 32768 + w * 8192);
    }
    await<4>(L, 3);
    publish(rs, fw + FL_H + strip, (mem & 3) * 4 + w, lane);
  }
  TS(3);

  const int trq = (lane & 15) >> 2, trp = lane & 3, trb = (lane >> 4) & 1;
  const f32x16 zero16 = {0.f, 0.f, 0.f, 0.f, 0.f, 0.f, 0.f, 0.f, 0.f, 0.f, 0.f, 0.f, 0.f, 0.f, 0.f, 0.f};
  u32 phq[16];
  v4u wb[PD];
  {
    const Base3 wnat(L + (8 * hh + trq) * 64 + trb * 32 + trp * 8);
    waitn(rs, fw + FL_H + w, 0xFFFFu, lane);
    TS(4);
    v4u a[32];
    ldfrags<W0>(a, rs, lane * 16, pb + PN_H + w * 32768);
    bar_raw();
    {
      const int hs = (l31 >> 2) & 1;
      const LAS u16* wp = (const LAS u16*)(L + L_CF + 2048) + hs * 256 + 4 * hh * 16 + (l31 & 3) + 4 * (l31 >> 3);
      u32 e[8];
#pragma unroll
      for (int j = 0; j < 8; ++j) e[j] = wp[(8 * (j >> 2) + (j & 3)) * 16];
      const v4u av = {e[0] | (e[1] << 16), e[2] | (e[3] << 16), e[4] | (e[5] << 16), e[6] | (e[7] << 16)};
      const v4u z = {0, 0, 0, 0};
      aphi0 = hs == 0 ? av : z; aphi1 = hs == 1 ? av : z;
      asm volatile("" : "+v"(aphi0), "+v"(aphi1));
    }
    static_for<0, PD>([&](auto U) { wb[U.value] = ldw<256>(wnat.at<slot(P_KV + kv_piece(U.value))>()); });
    SB0();
    f32x16 kacc = zero16, vacc = zero16;
    auto unit = [&](auto U) {
      constexpr int u = decltype(U)::value, pc = kv_piece(u), st = pc >> 1;
      SB0();
      const v4u cur = wb[u % PD];
      if constexpr (u + PD < 64) wb[u % PD] = ldw<256>(wnat.at<slot(P_KV + kv_piece(u + PD))>());
      if constexpr (pc & 1) mfma_pin(a[st], cur, vacc); else mfma_pin(cur, a[st], kacc);
      if constexpr (!(pc & 1) && st + W0 < 32) a[st + W0] = ldfrag<st + W0>(rs, lane * 16, pb + PN_H + w * 32768);
      if constexpr (!(pc & 1) && st + W0 == 32) sig(L, 2, w);
      SB0();
    };
    static_for<0, 47>(unit);
    TS(5);
    const v4u kb0 = pack8<0>(kacc), kb1 = pack8<8>(kacc);
    f32x16 P = zero16;
    mfma_pin(aphi0, kb0, P);
    mfma_pin(aphi1, kb1, P);
    unit(IC<47>{});
    u32 phk[16];
    feat(P, phk, [&](auto N) { unit(IC<48 + N.value>{}); });
    const v4u vb0 = pack8<0>(vacc), vb1 = pack8<8>(vacc);
    LAS unsigned char* T = L + L_T + w * TW;
#pragma unroll
    for (int c = 0; c < 4; ++c)
      *(LAS v4u*)(T + lane * 64 + ((c ^ ((lane >> 1) & 3)) * 16)) = v4u{phk[4 * c], phk[4 * c + 1], phk[4 * c + 2], phk[4 * c + 3]};
    const v4u ones = {0x3F803F80u, 0x3F803F80u, 0x3F803F80u, 0x3F803F80u};
    LAS unsigned char* tr = T + (4 * hh + trq) * 64 + (((2 * trb + (trp >> 1)) ^ ((2 * hh + (trq >> 1)) & 3)) * 16) + 8 * (trp & 1);
    const v4u t0 = ldw<512>(tr), t1 = ldw<512>(tr + 1024), t2 = ldw<512>(tr + 2048), t3 = ldw<512>(tr + 3072);
    f32x16 D0 = mfma(t0, trb ? ones : vb0, zero16);
    f32x16 D1 = mfma(t2, trb ? vb0 : ones, zero16);
    D0 = mfma(t1, trb ? ones : vb1, D0);
    D1 = mfma(t3, trb ? vb1 : ones, D1);
    {
      LAS unsigned char* pe = T + lane * 16;
      LAS unsigned char* pz = T + 1024 + (lane >> 4) * 16;
      LAS unsigned char* p0 = trb ? pz : pe;
      LAS unsigned char* p1 = trb ? pe : pz;
#pragma unroll
      for (int g = 0; g < 4; ++g) *(LAS f32x4*)(p0 + g * 1088) = f32x4{D0[4 * g], D0[4 * g + 1], D0[4 * g + 2], D0[4 * g + 3]};
#pragma unroll
      for (int g = 0; g < 4; ++g) *(LAS f32x4*)(p1 + g * 1088) = f32x4{D1[4 * g], D1[4 * g + 1], D1[4 * g + 2], D1[4 * g + 3]};
    }
    TS(6);
    MARK(H_C0, w, 1u);
    reach<0>(L, H_C0, 1u, 1u, 1u, 1u);
    {
      f32x4 e[4], z[4];
#pragma unroll
      for (int v = 0; v < 4; ++v) {
        e[v] = *(LAS f32x4*)(L + L_T + v * TW + w * 1088 + lane * 16);
        z[v] = *(LAS f32x4*)(L + L_T + v * TW + w * 1088 + 1024 + (lane & 3) * 16);
      }
      SB0();
      const int idx = (mem * 4 + w) * 16 + panel;
      st128(__builtin_bit_cast(v4u, (e[0] + e[1]) + (e[2] + e[3])), rs, lane * 16, OFF_KVE + idx * 1024);
      st128(__builtin_bit_cast(v4u, (z[0] + z[1]) + (z[2] + z[3])), rs, lane < 4 ? lane * 16 : WS_BYTES, OFF_KVZ + idx * 64);
      publish(rs, FL_KV + mem * 2 + (w >> 1), (w & 1) * 16 + panel, lane);
      MARK(H_C0, w, 2u);
      reach<1>(L, H_P0, 1u, 1u, 0u, 0u);
      static_for<0, PD>([&](auto U) { wb[U.value] = ldw<256>(wnat.at<slot(P_Q + U.value)>()); });
    }
    TS(7);
    f32x16 qacc = zero16;
    static_for<0, 32>([&](auto U) {
      constexpr int u = U.value;
      const v4u cur = wb[u % PD];
      if constexpr (u + PD < 32) wb[u % PD] = ldw<256>(wnat.at<slot(P_Q + u + PD)>());
      mfma_pin(cur, a[u], qacc);
      SB0();
    });
    MARK(H_C0, w, 3u);
    P = mfma(aphi0, pack8<0>(qacc), zero16);
    P = mfma(aphi1, pack8<8>(qacc), P);
    feat(P, phq, [](auto) {});
#pragma unroll
    for (int i = 0; i < 16; ++i) asm volatile("" : "+v"(phq[i]));
  }
  v2u xres[4];
  {
    const u16* xr = p.x + (size_t)(panel * 128 + strip2 * 32 + l31) * D + ncol + 4 * hh;
#pragma unroll
    for (int i = 0; i < 4; ++i) xres[i] = *(const GAS v2u*)(xr + 8 * i);
  }
  TS(8);
  reach<1>(L, H_P0, 0u, 0u, 1u, 1u);
  TS(9);
  {
    const bool on = (l31 >> 4) == hh;
    const v4u zv = {0, 0, 0, 0};
    f32x16 num = zero16, den = zero16;
    v4u ak[4], az[4];
#pragma unroll
    for (int s4 = 0; s4 < 4; ++s4) { az[s4] = *(LAS v4u*)(L + L_ZF + s4 * 32 + hh * 16); ak[s4] = *(LAS v4u*)(L + L_KVF + s4 * 512 + l31 * 16); }
    SB0();
#pragma unroll
    for (int s4 = 0; s4 < 4; ++s4) den = mfma(on ? az[s4] : zv, v4u{phq[4 * s4], phq[4 * s4 + 1], phq[4 * s4 + 2], phq[4 * s4 + 3]}, den);
#pragma unroll
    for (int s4 = 0; s4 < 4; ++s4) num = mfma(on ? ak[s4] : zv, v4u{phq[4 * s4], phq[4 * s4 + 1], phq[4 * s4 + 2], phq[4 * s4 + 3]}, num);
    const float d0 = rcp(rb(rb(den[0]) + 1e-6f)), d1 = rcp(rb(rb(den[8]) + 1e-6f));
    const v4u n0 = pack8<0>(num), n1 = pack8<8>(num);
    v4u o0, o1;
#pragma unroll
    for (int j = 0; j < 4; ++j) { o0[j] = pk(lo16(n0[j]) * d0, hi16(n0[j]) * d0); o1[j] = pk(lo16(n1[j]) * d1, hi16(n1[j]) * d1); }
    st128(o0, rs, lane * 16, pb + PN_O + w * 32768 + (2 * mem) * 1024);
    st128(o1, rs, lane * 16 + 1024, pb + PN_O + w * 32768 + (2 * mem) * 1024);
  }
#define WDN_BASES() const Base3 wown(L + (4 * hh + trq) * 64 + trb * 32 + trp * 8 + 3 * kh * 1024), woth(L + (4 * hh + trq) * 64 + trb * 32 + trp * 8 + (kh + 1) * 1024)
#define WDN(base, u) ldw<512>((((u) & 1) ? wown : woth).template at<slot((base) + 4 * ((u) >> 1))>())
  u32 x1p[8];
  {
    WDN_BASES();
    publish(rs, fw + FL_O + w, mem, lane);
    reach<0>(L, H_P0, 2u, 2u, 0u, 0u);
    static_for<0, PD>([&](auto U) { wb[U.value] = WDN(P_WO, U.value); });
    TS(10);
    waitn(rs, fw + FL_O + strip2, 0xFFu << (kh * 8), lane);
    TS(11);
    v4u a[16];
    ldfrags<16>(a, rs, lane * 16, pb + PN_O + strip2 * 32768 + kh * 16384);
    sig(L, 4, w);
    reach<0>(L, H_P0, 0u, 0u, 2u, 2u);
    f32x4 ga[4], sc[4];
#pragma unroll
    for (int g = 0; g < 4; ++g) {
      ga[g] = *(LAS f32x4*)(L + L_VEC + (kh * 32 + 8 * g + 4 * hh) * 4);
      sc[g] = *(LAS f32x4*)(L + L_VEC + 256 + (kh * 32 + 8 * g + 4 * hh) * 4);
    }
    SB0();
    f32x16 c0 = zero16, c1 = zero16;
    static_for<0, 32>([&](auto U) {
      constexpr int u = U.value;
      const v4u cur = wb[u % PD];
      if constexpr (u + PD < 32) wb[u % PD] = WDN(P_WO, u + PD);
      if constexpr (u & 1) mfma_pin(cur, a[u >> 1], c0); else mfma_pin(cur, a[u >> 1], c1);
      SB0();
    });
#pragma unroll
    for (int g = 0; g < 4; ++g) *(LAS f32x4*)(L + L_T + w * TW + g * 1024 + lane * 16) = f32x4{c1[4 * g], c1[4 * g + 1], c1[4 * g + 2], c1[4 * g + 3]};
    TS(12);
    MARK(H_C0, w, 4u);
    reach1(L, H_C0, w ^ 2, 4u);
    f32x4 o[4];
#pragma unroll
    for (int g = 0; g < 4; ++g) o[g] = *(LAS f32x4*)(L + L_T + (w ^ 2) * TW + g * 1024 + lane * 16);
    SB0();
    float x1[16], sp[4];
    u32 yp[8];
#pragma unroll
    for (int g = 0; g < 4; ++g) {
#pragma unroll
      for (int j = 0; j < 2; ++j) {
        const u32 r = pk(lo16(xres[g][j]) + ga[g][2 * j] * (c0[4 * g + 2 * j] + o[g][2 * j]),
                         hi16(xres[g][j]) + ga[g][2 * j + 1] * (c0[4 * g + 2 * j + 1] + o[g][2 * j + 1]));
        x1p[2 * g + j] = r;
        x1[4 * g + 2 * j] = lo16(r); x1[4 * g + 2 * j + 1] = hi16(r);
        yp[2 * g + j] = pk(lo16(r) * sc[g][2 * j], hi16(r) * sc[g][2 * j + 1]);
      }
    }
    const int tile = 2 * colg + kh;
    st128(v4u{yp[0], yp[1], yp[2], yp[3]}, rs, lane * 16, pb + PN_X1 + strip2 * 32768 + tile * 2048);
    st128(v4u{yp[4], yp[5], yp[6], yp[7]}, rs, lane * 16 + 1024, pb + PN_X1 + strip2 * 32768 + tile * 2048);
    SB0();
#pragma unroll
    for (int g = 0; g < 4; ++g) sp[g] = (x1[4 * g] + x1[4 * g + 1]) + (x1[4 * g + 2] + x1[4 * g + 3]);
    const float ss = (sp[0] + sp[1]) + (sp[2] + sp[3]);
    const float mt = fold32(ss, ss) * (1.f / 32.f);
    float q[4];
#pragma unroll
    for (int g = 0; g < 4; ++g) {
      const float d0 = x1[4 * g] - mt, d1 = x1[4 * g + 1] - mt, d2 = x1[4 * g + 2] - mt, d3 = x1[4 * g + 3] - mt;
      q[g] = (d0 * d0 + d1 * d1) + (d2 * d2 + d3 * d3);
    }
    const float qs = (q[0] + q[1]) + (q[2] + q[3]);
    const float qt = fold32(qs, qs);
    v4u sv = {__float_as_uint(mt), tag, __float_as_uint(qt), tag};
    asm volatile("" : "+v"(sv));
    publish(rs, fw + FL_X + strip2, tile, lane);
    st128(sv, rs, lane < 32 ? lane * 16 : WS_BYTES, pb + PN_ST + strip2 * 8192 + tile * 512);
  }
  asm volatile("" : "+v"(x1p[0]), "+v"(x1p[1]), "+v"(x1p[2]), "+v"(x1p[3]), "+v"(x1p[4]), "+v"(x1p[5]), "+v"(x1p[6]), "+v"(x1p[7]));
#define GU_S(u) ((u) < 72 ? (u) / 6 : 12 + (((u) - 72) % 40) / 2)
#define GU_I(u) ((u) < 72 ? (u) % 6 : 2 * (((u) - 72) / 40) + ((u) & 1))
#define WGU(u) ldw<512>((((u) & 1) ? wy : wx).template at<slot(P_GU + ((u) & ~1))>())
  {
    const Base3 wx(L + (4 * hh + trq) * 64 + trp * 8 + trb * (1024 + 32)), wy(L + (4 * hh + trq) * 64 + trp * 8 + (trb ? 1024 : 32));
    reach<0>(L, H_P0, 3u, 3u, 0u, 0u);
    static_for<0, PD>([&](auto U) { wb[U.value] = WGU(U.value); });
    TS(13);
    waitn(rs, fw + FL_X + w, 0xFFFFu, lane);
    TS(14);
    constexpr int AW = 20;
    v4u a[AW];
    ldfrags<8>(a, rs, lane * 16, pb + PN_X1 + w * 32768);
    sig(L, 5, w);
    f32x16 acc[6];
#pragma unroll
    for (int i = 0; i < 6; ++i) acc[i] = zero16;
    float rr = 0.f;
    v4u bcor = {0, 0, 0, 0};
    v4u ho, cf0, cf1, pp;
    float t0 = 0.f, t1 = 0.f;
    const int go = pb + PN_G + (w * 96 + mem) * 1024, fi = fw + FL_G + strip2 * 3, gi = pb + PN_G + (strip2 * 96 + kh * 48) * 1024;
    u32 fl = 0;
    int gv = 0;
    bool early = false;
    v4u g[43];
    auto post = [&](int P) { (void)__builtin_amdgcn_raw_ptr_buffer_atomic_add_i32(1 << mem, rs, lane == 0 ? 0 : WS_BYTES, OFF_ARR + (fw + FL_G + w * 3 + P) * 128, 0); };
    auto epi = [&](auto Ic, auto Kc) {
      constexpr int I = decltype(Ic)::value, k = decltype(Kc)::value, j = k >> 1;
      const f32x16& A = acc[I];
      if constexpr ((k & 1) == 0) { t0 = A[2 * j] * rr; t1 = A[2 * j + 1] * rr; asm volatile("" : "+v"(t0)); t0 *= A[8 + 2 * j]; }
      else {
        t1 *= A[9 + 2 * j];
        ho[j] = pk(t0, t1);
        if constexpr (j == 3) st128(ho, rs, (I < 4 || three) ? lane * 16 : WS_BYTES, go + ((I & 1) * 48 + (I >> 1) * 16) * 1024);
      }
    };
    static_for<0, 192>([&](auto Uc) {
      constexpr int u = decltype(Uc)::value, s = GU_S(u), i = GU_I(u), q = (u + 8) % 40;
      if constexpr (u == 6) TS(22);
      if constexpr (u == 60) TS(23);
      if constexpr (u == 96) TS(24);
      if constexpr (u == 128) TS(25);
      if constexpr (u == 160) TS(26);
      if constexpr (u == 56) mark(L, H_CP, w, 2u);
      if constexpr (u == 96 || u == 128 || u == 160) mark(L, H_CP, w, u / 32);
      if constexpr (u == 60 || u == 96 || u == 140) pp = peek(L, H_PP);
      if constexpr (u == 64) need_pp<false>(L, pp, 3u);
      if constexpr (u == 100) need_pp<true>(L, pp, 4u);
      if constexpr (u == 144) need_pp<false>(L, pp, 5u);
      const v4u cur = wb[u % PD];
      if constexpr (u + PD < 192) wb[u % PD] = WGU(u + PD);
      if constexpr (u >= 72 && q == 32) cf0 = *(LAS v4u*)(L + L_CF + i * 512 + l31 * 16);
      if constexpr (u >= 72 && q == 33) cf1 = *(LAS v4u*)(L + L_CF + i * 512 + l31 * 16);
      mfma_pin(cur, a[s % AW], acc[i]);
      if constexpr (u < 72 && i == 5) a[s % AW] = ldfrag<s + AW>(rs, lane * 16, pb + PN_X1 + w * 32768);
      if constexpr (u < 2 * (AW - 8) && !(u & 1)) a[8 + (u >> 1)] = ldfrag<8 + (u >> 1)>(rs, lane * 16, pb + PN_X1 + w * 32768);
      if constexpr (u == 24) mark(L, H_CP, w, 1u);
      if constexpr (u == 101) bcor = *(LAS v4u*)(L + L_KVF + w * 512 + l31 * 16);
      if constexpr (u == 107) { rr = __uint_as_float(bcor.w); bcor = v4u{hh ? 0u : bcor.x, hh ? 0u : bcor.y, hh ? 0u : bcor.z, 0u}; }
      if constexpr (u >= 72 && q == 38) mfma_pin(cf0, bcor, acc[i]);
      if constexpr (u >= 72 && q == 39) mfma_pin(cf1, bcor, acc[i]);
      if constexpr (u >= 112 && q >= 1 && q < 9) {
        epi(IC<(i & 6) - 2 + (q - 1) / 4>{}, IC<2 * ((q - 1) % 4)>{});
        epi(IC<(i & 6) - 2 + (q - 1) / 4>{}, IC<2 * ((q - 1) % 4) + 1>{});
      }
      if constexpr (u == 112 + GPUB) publish(rs, fw + FL_G + w * 3, mem, lane);
      if constexpr (u == GASK) fl = ask(rs, fi, lane);
      if constexpr (u == 176) {
        early = (__builtin_amdgcn_readfirstlane(fl) & 0xFFFFu) == 0xFFFFu;
        __builtin_amdgcn_fence(__ATOMIC_ACQUIRE, "wavefront");
        RELOAD();
        gv = early ? lane * 16 : WS_BYTES;
      }
      if constexpr (u == 180) { VMCNT(4); post(1); }
      if constexpr (u >= 176) g[u - 176] = ldfrag<u - 176>(rs, gv, gi);
      if constexpr (u == 184) pp = peek(L, H_PP);
      SB0();
    });
    mark(L, H_CP, w, 6u);
    TS(15);
    need_pp<true>(L, pp, 6u);
    WDN_BASES();
    static_for<0, PD>([&](auto U) { wb[U.value] = WDN(P_WD, U.value); });
    if (!early) {
      spin(rs, fi, 0xFFFFu, lane);
      __builtin_amdgcn_fence(__ATOMIC_ACQUIRE, "wavefront");
      RELOAD();
      static_for<0, 16>([&](auto T) { g[T.value] = ld128(rs, lane * 16, gi + T.value * 1024); });
    }
    SB0();
    fl = ask(rs, fi + 1, lane);
    SB0();
    TS(18);
    f32x16 c0 = zero16, c1 = zero16;
    static_for<0, 86>([&](auto U) {
      constexpr int u = U.value;
      if constexpr (u == 18) mark(L, H_CP, w, 7u);
#define GL(t) ((t) < 32 ? GU0 + (t) - 16 : GU2 + (t) - 32)
      if constexpr (u == PUB2) {
        asm volatile("s_waitcnt vmcnt(%0)" ::"i"(PUB2 - GU0 < 16 ? PUB2 - GU0 : 16) : "memory");
        post(2);
      }
      if constexpr (u == 24 || u == 40 || u == 56) pp = peek(L, H_PP);
      if constexpr (u == 28 || u == 44 || u == 60) need_pp<true>(L, pp, 7u + (u - 28) / 16);
      if constexpr (u == GL(16)) poll(fl, rs, fi + 1, 0xFFFFu, lane);
      if constexpr (u == GL(32) - GA2) fl = ask(rs, fi + 2, lane);
      if constexpr (u == GL(32)) poll(fl, rs, fi + 2, 0x7FFu, lane);
      const v4u cur = wb[u % PD];
      if constexpr (u + PD < 86) wb[u % PD] = WDN(P_WD, u + PD);
      if constexpr (u & 1) mfma_pin(cur, g[u >> 1], c0); else mfma_pin(cur, g[u >> 1], c1);
      if constexpr (u >= 1 && u < 9) {
        epi(IC<4 + (u - 1) / 4>{}, IC<2 * ((u - 1) % 4)>{});
        epi(IC<4 + (u - 1) / 4>{}, IC<2 * ((u - 1) % 4) + 1>{});
      }
      static_for<16, 43>([&](auto T) { if constexpr (GL(T.value) == u) g[T.value] = ldfrag<T.value>(rs, lane * 16, gi); });
      SB0();
    });
#pragma unroll
    for (int g = 0; g < 4; ++g) *(LAS f32x4*)(L + L_T + w * TW + g * 1024 + lane * 16) = f32x4{c1[4 * g], c1[4 * g + 1], c1[4 * g + 2], c1[4 * g + 3]};
    f32x4 gb[4];
#pragma unroll
    for (int g = 0; g < 4; ++g) gb[g] = *(LAS f32x4*)(L + L_VEC + 512 + (kh * 32 + 8 * g + 4 * hh) * 4);
    TS(19);
    MARK(H_C0, w, 5u);
    reach1(L, H_C0, w ^ 2, 5u);
    f32x4 o[4];
#pragma unroll
    for (int g = 0; g < 4; ++g) o[g] = *(LAS f32x4*)(L + L_T + (w ^ 2) * TW + g * 1024 + lane * 16);
    SB0();
    const rsrc_t ro = __builtin_amdgcn_make_buffer_rsrc((void*)p.out, 0, 2048 * D * 2, 0x00020000);
    v2u r[4];
#pragma unroll
    for (int g = 0; g < 4; ++g)
#pragma unroll
      for (int j = 0; j < 2; ++j)
        r[g][j] = pk(lo16(x1p[2 * g + j]) + gb[g][2 * j] * (c0[4 * g + 2 * j] + o[g][2 * j]),
                     hi16(x1p[2 * g + j]) + gb[g][2 * j + 1] * (c0[4 * g + 2 * j + 1] + o[g][2 * j + 1]));
    const int oo = ((panel * 128 + strip2 * 32 + l31) * D + ncol + 8 * hh) * 2;
#pragma unroll
    for (int g = 0; g < 4; g += 2) {
      const auto x = __builtin_amdgcn_permlane32_swap(r[g][0], r[g + 1][0], false, false);
      const auto y = __builtin_amdgcn_permlane32_swap(r[g][1], r[g + 1][1], false, false);
      __builtin_amdgcn_raw_buffer_store_b128(v4u{x[0], y[0], x[1], y[1]}, ro, oo + g * 16, 0, OAUX);
    }
  }
  if (b == 0 && tid == 0) __hip_atomic_store((GAS u32*)(ws + OFF_EPOCH), tag, RLX_AGENT);
}
}

extern "C" void kernel_launch(void* const* d_in, const int* in_sizes, int n_in, void* d_out, int out_size, void* d_ws,
                              size_t ws_size, hipStream_t stream) {
  using namespace mk;
  Params p;
  p.x = (const u16*)d_in[0]; p.wqkv = (const u16*)d_in[1]; p.wphi = (const u16*)d_in[2]; p.wo = (const u16*)d_in[3];
  p.wg = (const u16*)d_in[5]; p.wu = (const u16*)d_in[6]; p.wd = (const u16*)d_in[7]; p.c = (const u16*)d_in[8];
  p.wmod = (const u16*)d_in[9]; p.out = (u16*)d_out; p.ws = (unsigned char*)d_ws;
  (void)hipFuncSetAttribute((const void*)mega, hipFuncAttributeMaxDynamicSharedMemorySize, LDS_BYTES);
  mega<<<256, 512, LDS_BYTES, stream>>>(p);
}
```

```cpp
#define PRE_MOD 10
#define OAUX 2
#include <hip/hip_runtime.h>

namespace mk {
typedef unsigned short u16;
typedef unsigned int u32;
typedef unsigned long long u64;
typedef u32 v4u __attribute__((ext_vector_type(4)));
typedef u32 v2u __attribute__((ext_vector_type(2)));
typedef float f32x2 __attribute__((ext_vector_type(2)));
typedef float f32x4 __attribute__((ext_vector_type(4)));
typedef float f32x16 __attribute__((ext_vector_type(16)));
typedef __bf16 bf16x2 __attribute__((ext_vector_type(2)));
typedef __bf16 bf16x8 __attribute__((ext_vector_type(8)));
typedef short s16x4 __attribute__((ext_vector_type(4)));
#define LAS __attribute__((address_space(3)))
#define GAS __attribute__((address_space(1)))
#define DI __device__ __forceinline__
#define RLX_AGENT __ATOMIC_RELAXED, __HIP_MEMORY_SCOPE_AGENT
#define SB0() __builtin_amdgcn_sched_barrier(0)
typedef __amdgpu_buffer_rsrc_t rsrc_t;

constexpr int D = 512, F = 1376;
constexpr int OFF_EPOCH = 0;
constexpr int OFF_MODG = 64 << 10;
constexpr int OFF_KVZ = 128 << 10;
constexpr int OFF_CG = 192 << 10;
constexpr int CG_PART = 2 * F * 16;
constexpr int OFF_ARR = 384 << 10;
constexpr int OFF_MODR = 512 << 10;
constexpr int OFF_KVE = 1 << 20;
constexpr int OFF_PAN = 2 << 20;
constexpr int PN_H = 0;
constexpr int PN_O = 128 << 10;
constexpr int PN_X1 = 256 << 10;
constexpr int PN_G = 384 << 10;
constexpr int PN_ST = 768 << 10;
constexpr int PN_FL = 800 << 10;
constexpr int FL_H = 0, FL_O = 4, FL_X = 8, FL_G = 12, FL_KV = 384;
constexpr int PSTR = (848 << 10) + 4224;
constexpr int WS_BYTES = 16 << 20;
constexpr int OFF_TS = 32 << 20;
constexpr int RING = 136;
constexpr int L_T = RING * 1024, TW = 4352;
constexpr int L_KVF = L_T + 4 * TW;
constexpr int L_ZF = L_KVF + 2048;
constexpr int L_VEC = L_ZF + 256;
constexpr int L_CF = L_VEC + 768;
constexpr int L_HINT = L_CF + 3072;
constexpr int LDS_BYTES = L_HINT + 256;
static_assert(LDS_BYTES <= 163840, "LDS");
constexpr int P_KV = 0, P_Q = 64, P_WO = 96, P_GU = 160, P_WD = 352;
constexpr int H_CP = 6, H_PP = 7, H_C0 = 8, H_P0 = 9;
constexpr int pair_of(int t) { return ((t & 15) < 11 ? 3 * (t & 15) : 2 * (t & 15) + 11) + (t >> 4); }
constexpr int slot(int n) { return (n % RING) * 1024; }
constexpr int kv_piece(int u) { return u < 45 ? (u % 3 < 2 ? 2 * (2 + 2 * (u / 3) + u % 3 - 2) : 2 * (u / 3) + 1) : u == 45 ? 60 : u == 46 ? 62 : 2 * (u - 32) + 1; }
constexpr int AUX_SC1 = 16;
constexpr u32 SPIN_CAP = 1u << 16;
#define HI_DELAY 8
#define POLL_GAP 6
#define PRE_FL 4
#define KV1 32
#define PGU 4
#define PKV 0
#define GPUB 26
#define LPRIO 0
#define APRIO 3
#define TMOD 0
#define CW 3
#define DW 0
#define CSLP 3
#define PG2 2
#define TLBT 0
#define LITE 1
#define EQ 1
#define PQE 2
#define GASK 158
#define PUB2 24
#define WG0 16
#define GU0 12
#define GLA 16
#define GA2 18
#define GU2 50
#define W1N 8
#define W2N 6
#define PG3 4
#define QT 1
#define TQKV 1
#define TWO 1
#define TWD 1
#define TCODE 1
#define W0 12
#define PDV 5
#define WPC 0
#define WPACE() do { if (WPC) { SB0(); __builtin_amdgcn_s_sleep(WPC); SB0(); } } while (0)
constexpr int PD = PDV;
constexpr int CODE_BYTES = 47000, CODE_KB = 48;

#define NBV(x) (x)
struct Params {
  const u16 *x, *wqkv, *wphi, *wo, *wg, *wu, *wd, *c, *wmod;
  u16* out;
  unsigned char* ws;
};

template <int V> struct IC { static constexpr int value = V; };
template <int I, int N, class Fn> DI void static_for(Fn&& f) {
  if constexpr (I < N) { f(IC<I>{}); static_for<I + 1, N>(f); }
}
DI float lo16(u32 v) { return __uint_as_float(v << 16); }
DI float hi16(u32 v) { return __uint_as_float(v & 0xffff0000u); }
DI u32 pk(float a, float b) { f32x2 v = {a, b}; return __builtin_bit_cast(u32, __builtin_convertvector(v, bf16x2)); }
DI float rb(float a) { return lo16(pk(a, 0.f)); }
DI float rcp(float a) { return __builtin_amdgcn_rcpf(a); }
DI f32x16 mfma(v4u a, v4u b, f32x16 c) {
  return __builtin_amdgcn_mfma_f32_32x32x16_bf16(__builtin_bit_cast(bf16x8, a), __builtin_bit_cast(bf16x8, b), c, 0, 0, 0);
}
DI void mfma_pin(v4u a, v4u b, f32x16& c) {
  c = mfma(a, b, c);
  asm volatile("" : "+v"(c));
}
template <int O> DI v4u pack8(const f32x16& x) {
  v4u r; r.x = pk(x[O], x[O + 1]); r.y = pk(x[O + 2], x[O + 3]); r.z = pk(x[O + 4], x[O + 5]); r.w = pk(x[O + 6], x[O + 7]); return r;
}
template <int T> DI v4u ldw(LAS unsigned char* p) {
  s16x4 a = __builtin_amdgcn_ds_read_tr16_b64_v4i16((LAS s16x4*)p);
  s16x4 b = __builtin_amdgcn_ds_read_tr16_b64_v4i16((LAS s16x4*)(p + T));
  return __builtin_bit_cast(v4u, __builtin_shufflevector(a, b, 0, 1, 2, 3, 4, 5, 6, 7));
}
struct Base3 {
  u32 b[3];
  DI explicit Base3(LAS unsigned char* p) {
    b[0] = (u32)(size_t)p; b[1] = b[0] + 65536u; b[2] = b[0] + 131072u;
    asm volatile("" : "+v"(b[0]), "+v"(b[1]), "+v"(b[2]));
  }
  template <int OFF> DI LAS unsigned char* at() const { return (LAS unsigned char*)(size_t)b[OFF >> 16] + (OFF & 65535); }
};
template <int LOFF, int SOFF> DI void gldsi(rsrc_t rs, u32 voff, u32 sbase, u32 lbase) {
  u32 t;
  asm volatile("s_add_u32 m0, %1, %2\n\ts_add_u32 %0, %3, %4\n\tbuffer_load_dwordx4 %5, %6, %0 offen lds"
               : "=&s"(t) : "s"(lbase), "i"(LOFF), "s"(sbase), "i"(SOFF), "v"(voff), "s"(rs) : "m0", "scc", "memory");
}
DI void bar() { asm volatile("s_waitcnt lgkmcnt(0)\n\ts_barrier" ::: "memory"); }
DI void bar_raw() { asm volatile("s_barrier" ::: "memory"); }
DI void drain() { asm volatile("s_waitcnt vmcnt(0)" ::: "memory"); }
#define VMCNT(n) asm volatile("s_waitcnt vmcnt(" #n ")" ::: "memory")
#define RELOAD() asm volatile("" ::: "memory")
DI v4u ld128(rsrc_t rs, int voff, int soff) { return __builtin_amdgcn_raw_buffer_load_b128(rs, voff, soff, AUX_SC1); }
DI v2u ld64(rsrc_t rs, int voff, int soff) { return __builtin_amdgcn_raw_buffer_load_b64(rs, voff, soff, AUX_SC1); }
DI void st128(v4u v, rsrc_t rs, int voff, int soff) { __builtin_amdgcn_raw_buffer_store_b128(v, rs, voff + soff, 0, AUX_SC1); }
DI void st64(v2u v, rsrc_t rs, int voff, int soff) { __builtin_amdgcn_raw_buffer_store_b64(v, rs, voff, soff, AUX_SC1); }
template <int S> DI v4u ldfrag(rsrc_t rs, int voff, int soff) { return ld128(rs, voff + (S & 3) * 1024, soff + (S >> 2) * 4096); }
template <int W, int N> DI void ldfrags(v4u (&a)[N], rsrc_t rs, int voff, int soff) {
  static_for<0, W>([&](auto S) { a[S.value] = ldfrag<S.value>(rs, voff, soff); SB0(); });
}
template <int CTRL> DI float dpp_add(float v) {
  return v + __uint_as_float(__builtin_amdgcn_update_dpp(0u, __float_as_uint(v), CTRL, 0xf, 0xf, false));
}
constexpr int DPP_X1 = 0xB1, DPP_X2 = 0x4E, DPP_HM = 0x141, DPP_ROR4 = 0x124, DPP_ROR8 = 0x128;
DI float fold32(float x, float y) {
  auto r = __builtin_amdgcn_permlane32_swap(__float_as_uint(x), __float_as_uint(y), false, false);
  return __uint_as_float(r[0]) + __uint_as_float(r[1]);
}
DI float fold16(float x, float y) {
  auto r = __builtin_amdgcn_permlane16_swap(__float_as_uint(x), __float_as_uint(y), false, false);
  return __uint_as_float(r[0]) + __uint_as_float(r[1]);
}
DI void red8q(const float (&a)[8], float& c0, float& c1) {
  const float b0 = fold32(a[0], a[4]), b1 = fold32(a[1], a[5]), b2 = fold32(a[2], a[6]), b3 = fold32(a[3], a[7]);
  c0 = fold16(b0, b2); c1 = fold16(b1, b3);
  c0 = dpp_add<DPP_ROR4>(c0); c1 = dpp_add<DPP_ROR4>(c1);
  c0 = dpp_add<DPP_ROR8>(c0); c1 = dpp_add<DPP_ROR8>(c1);
}
DI void red8(const float (&a)[8], float& c0, float& c1) {
  red8q(a, c0, c1);
  c0 = dpp_add<DPP_X1>(c0); c1 = dpp_add<DPP_X1>(c1);
  c0 = dpp_add<DPP_X2>(c0); c1 = dpp_add<DPP_X2>(c1);
}
DI void publish(rsrc_t rs, int word, int bit, int lane) {
  drain();
  (void)__builtin_amdgcn_raw_ptr_buffer_atomic_add_i32(1 << bit, rs, lane == 0 ? 0 : WS_BYTES, OFF_ARR + word * 128, 0);
}
DI u32 ask(rsrc_t rs, int word, int lane) { return __builtin_amdgcn_raw_ptr_buffer_atomic_add_i32(0, rs, lane == 0 ? 0 : WS_BYTES, OFF_ARR + word * 128, 0); }
DI void spin(rsrc_t rs, int word, u32 m, int lane) {
#pragma nounroll
  for (u32 spins = 0; spins < SPIN_CAP; ++spins)
    if ((__builtin_amdgcn_readfirstlane(ask(rs, word, lane)) & m) == m) break;
}
DI void poll(u32 f, rsrc_t rs, int word, u32 m, int lane) {
  if (__builtin_expect((__builtin_amdgcn_readfirstlane(f) & m) != m, 0)) spin(rs, word, m, lane);
  __builtin_amdgcn_fence(__ATOMIC_ACQUIRE, "wavefront");
  RELOAD();
}
DI void waitn(rsrc_t rs, int word, u32 m, int lane) {
  if (PRE_FL) __builtin_amdgcn_s_sleep(PRE_FL);
  spin(rs, word, m, lane);
  __builtin_amdgcn_fence(__ATOMIC_ACQUIRE, "wavefront");
  RELOAD();
}
DI void sig(LAS unsigned char* L, int i, int k) { *(volatile LAS u32*)(L + L_HINT + 16 * i + 4 * k) = 1u; }
template <int N> DI void await(LAS unsigned char* L, int i) {
#pragma nounroll
  for (u32 spins = 0; spins < (1u << 20); ++spins) {
    const v4u v = *(volatile LAS v4u*)(L + L_HINT + 16 * i);
    if (__builtin_amdgcn_readfirstlane(N == 1 ? v.x : (v.x & v.y & v.z & v.w))) break;
    __builtin_amdgcn_s_sleep(1);
  }
  RELOAD();
}
DI void mark(LAS unsigned char* L, int i, int k, u32 val) { *(volatile LAS u32*)(L + L_HINT + 16 * i + 4 * k) = val; }
DI v4u peek(LAS unsigned char* L, int i) { return *(volatile LAS v4u*)(L + L_HINT + 16 * i); }
DI u32 umin(u32 a, u32 b) { return a < b ? a : b; }
DI void await_cp(LAS unsigned char* L, u32 k) {
#pragma nounroll
  for (u32 spins = 0; spins < (1u << 20); ++spins) {
    const v4u v = peek(L, H_CP);
    if (__builtin_amdgcn_readfirstlane(umin(umin(v.x, v.y), umin(v.z, v.w))) >= k) break;
    __builtin_amdgcn_s_sleep(CSLP);
  }
  RELOAD();
}
template <int SLP> DI void reach(LAS unsigned char* L, int i, u32 k0, u32 k1, u32 k2, u32 k3) {
#pragma nounroll
  for (u32 spins = 0; spins < (1u << 20); ++spins) {
    const v4u v = peek(L, i);
    if (__builtin_amdgcn_readfirstlane((v.x >= k0) & (v.y >= k1) & (v.z >= k2) & (v.w >= k3))) break;
    if (SLP) __builtin_amdgcn_s_sleep(SLP);
  }
  RELOAD();
}
DI void reach1(LAS unsigned char* L, int i, int k, u32 val) {
#pragma nounroll
  for (u32 spins = 0; spins < (1u << 20); ++spins)
    if (__builtin_amdgcn_readfirstlane(*(volatile LAS u32*)(L + L_HINT + 16 * i + 4 * k)) >= val) break;
  RELOAD();
}
#define MARK(i, k, val) do { RELOAD(); mark(L, i, k, val); RELOAD(); } while (0)
template <bool AUX> DI u32 pp_min(v4u v) {
  u32 m = umin(v.x, v.y);
  if (AUX) m = umin(m, umin(v.z, v.w));
  return __builtin_amdgcn_readfirstlane(m);
}
template <bool AUX> DI void need_pp(LAS unsigned char* L, v4u v, u32 k) {
  if (__builtin_expect(pp_min<AUX>(v) < k, 0)) {
#pragma nounroll
    for (u32 spins = 0; spins < (1u << 20); ++spins)
      if (pp_min<AUX>(peek(L, H_PP)) >= k) break;
  }
  RELOAD();
}
template <class Fn> DI void feat(const f32x16& P, u32 (&ph)[16], Fn&& fill) {
#pragma clang fp contract(off)
  constexpr float L2E = 1.4426950408889634f;
  f32x2 p[8];
  float mx[4], mn[4];
  static_for<0, 4>([&](auto I) {
    constexpr int i = I.value;
    const u32 t0 = pk(P[4 * i], P[4 * i + 1]), t1 = pk(P[4 * i + 2], P[4 * i + 3]);
    p[2 * i] = f32x2{lo16(t0), hi16(t0)} * L2E; p[2 * i + 1] = f32x2{lo16(t1), hi16(t1)} * L2E;
    mx[i] = fmaxf(fmaxf(p[2 * i].x, p[2 * i].y), fmaxf(p[2 * i + 1].x, p[2 * i + 1].y));
    mn[i] = fminf(fminf(p[2 * i].x, p[2 * i].y), fminf(p[2 * i + 1].x, p[2 * i + 1].y));
    fill(IC<i>{});
  });
  const float mxl = fmaxf(fmaxf(mx[0], mx[1]), fmaxf(mx[2], mx[3])), mnl = fminf(fminf(mn[0], mn[1]), fminf(mn[2], mn[3]));
  f32x2 e1[8], e2[8];
#pragma unroll
  for (int i = 0; i < 8; ++i) { e1[i] = p[i] - mxl; e2[i] = mnl - p[i]; }
  SB0();
  static_for<0, 8>([&](auto I) {
    constexpr int i = I.value;
    fill(IC<4 + i>{});
    e1[i] = f32x2{__builtin_amdgcn_exp2f(e1[i].x), __builtin_amdgcn_exp2f(e1[i].y)};
    e2[i] = f32x2{__builtin_amdgcn_exp2f(e2[i].x), __builtin_amdgcn_exp2f(e2[i].y)};
  });
  fill(IC<12>{});
  const f32x2 s1 = ((e1[0] + e1[1]) + (e1[2] + e1[3])) + ((e1[4] + e1[5]) + (e1[6] + e1[7]));
  fill(IC<13>{});
  const f32x2 s2 = ((e2[0] + e2[1]) + (e2[2] + e2[3])) + ((e2[4] + e2[5]) + (e2[6] + e2[7]));
  fill(IC<14>{});
  const float r1 = rcp(s1.x + s1.y), r2 = rcp(s2.x + s2.y);
  fill(IC<15>{});
#pragma unroll
  for (int i = 0; i < 8; ++i) { const f32x2 u = e1[i] * r1, v = e2[i] * r2; ph[i] = pk(u.x, u.y); ph[8 + i] = pk(v.x, v.y); }
}

#define TSCLK() __builtin_amdgcn_s_memrealtime()
#define TS(i) do {} while (0)
#define TS_DUMP() do {} while (0)
#define TS_INIT() do {} while (0)

__global__ void __launch_bounds__(512, 2) mega(Params p) {
  extern __shared__ __attribute__((aligned(16))) unsigned char smem[];
  LAS unsigned char* L = (LAS unsigned char*)smem;
  TS_INIT();
  TS(0);
  u64 pc0;
  asm volatile("s_getpc_b64 %0" : "=s"(pc0));
  const int tid = threadIdx.x, lane = tid & 63;
  int w = __builtin_amdgcn_readfirstlane(tid >> 6);
  const int b = blockIdx.x, xcd = b & 7, rank = b >> 3, panel = (xcd >> 1) * 4 + (rank >> 3), mem = (xcd & 1) * 8 + (rank & 7);
  const int hh = lane >> 5, l31 = lane & 31;
  unsigned char* ws = p.ws;
  const int rowh = mem & 1, colg = mem >> 1;
  const int pair0 = min(3 * mem, 2 * mem + 11);
  const bool three = mem < 11;
  const rsrc_t rs = __builtin_amdgcn_make_buffer_rsrc(ws, 0, WS_BYTES, 0x00020000);
  const int pb = OFF_PAN + panel * PSTR, fw = panel * 24;
  const int mcg = xcd * 8 + (rank >> 2), mkp = rank & 3;
  const rsrc_t rqkv = __builtin_amdgcn_make_buffer_rsrc((void*)p.wqkv, 0, D * 3 * D * 2, 0x00020000);
  const rsrc_t rwo = __builtin_amdgcn_make_buffer_rsrc((void*)p.wo, 0, D * D * 2, 0x00020000);
  const rsrc_t rgu = __builtin_amdgcn_make_buffer_rsrc((void*)((w & 1) ? p.wu : p.wg), 0, D * F * 2, 0x00020000);
  const rsrc_t rwd = __builtin_amdgcn_make_buffer_rsrc((void*)p.wd, 0, F * D * 2, 0x00020000);

  v4u r0[4];
  u32 cv0[4], epoch;
  u32 vq = (lane >> 2) * (3 * D * 2) + (lane & 3) * 16;
  asm volatile("" : "+v"(vq));
  {
    const u32 ld = (u32)(size_t)L + w * 1024;
    if (w <= 4) {
      const rsrc_t r1 = __builtin_amdgcn_make_buffer_rsrc((void*)(w == 4 ? p.wmod : p.x), 0, w == 4 ? D * 6 * D * 2 : 2048 * D * 2, 0x00020000);
      const int vo = w == 4 ? (128 * mkp + 4 * (lane >> 1)) * (6 * D * 2) + mcg * 32 + (lane & 1) * 16
                            : (panel * 128 + mem * 8 + (lane >> 3)) * (D * 2) + (lane & 7) * 16 + 256 * w;
#pragma unroll
      for (int i = 0; i < 4; ++i) r0[i] = __builtin_amdgcn_raw_buffer_load_b128(r1, vo, w == 4 ? i * (6 * D * 2) : i < 2 ? i * 128 : 1 << 30, 0);
      SB0();
    }
    {
      const rsrc_t r2 = __builtin_amdgcn_make_buffer_rsrc((void*)p.c, 0, w == 4 ? D * 2 : 0, 0x00020000);
      const v2u c2 = __builtin_amdgcn_raw_buffer_load_b64(r2, (128 * mkp + 4 * (lane >> 1)) * 2, 0, 0);
      cv0[0] = c2.x; cv0[1] = c2.y;
      epoch = __builtin_amdgcn_raw_buffer_load_b32(rs, 0, OFF_EPOCH, AUX_SC1);
      __builtin_amdgcn_raw_buffer_store_b32(0u, rs, (w == 4 && lane < 2) ? lane * 128 : WS_BYTES, OFF_ARR + b * 256, AUX_SC1);
      SB0();
    }
    if (w >= 5) {
      const rsrc_t rc = __builtin_amdgcn_make_buffer_rsrc((void*)pc0, 0, CODE_BYTES, 0x00020000);
      if (TCODE) gldsi<slot(128), 0>(rc, lane < CODE_KB / 6 ? (u32)lane * 128u : 1u << 30, (u32)(((rank & 15) * 3 + w - 5) * (CODE_KB / 6) * 128), ld);
      const u32 tq = (rank * 16 + (lane >> 2)) * (3 * D * 2) + (xcd & 1) * 512 + (lane & 3) * 128;
      if (TQKV && (EQ || w != 5)) gldsi<slot(128), 0>(rqkv, tq, (w - 5) * (D * 2), ld);
      if (w == 5) {
        gldsi<slot(128), 0>(rs, lane < 2 ? (u32)(pb + lane * (848 << 10)) : 1u << 30, 0u, ld);
        const rsrc_t rp = __builtin_amdgcn_make_buffer_rsrc((void*)p.wphi, 0, 32 * 256 * 2, 0x00020000);
        gldsi<L_CF + 2048, 0>(rp, lane * 16, mem * 1024, (u32)(size_t)L);
      }
    }
    if (tid < 64) *((LAS u32*)(L + L_HINT) + tid) = 0u;
    TS(27);
    bar();
    asm volatile("" : "+s"(w));
    if (w >= 6) {
      u32 lo = (u32)(size_t)L + (w & 1) * 1024, so = (D + (w & 1) * D + mem * 32) * 2;
#pragma nounroll
      for (int i = 0; i < KV1 / 2; ++i) {
        gldsi<0, 0>(rqkv, vq, so, lo);
        gldsi<2048, 16 * (3 * D * 2)>(rqkv, vq, so, lo);
        lo += 4096; so += 32 * (3 * D * 2);
      }
    }
    if (w == 4) {
      __builtin_amdgcn_s_setprio(3);
      TS(10);
      float sl[4], acc[8];
#pragma unroll
      for (int i = 0; i < 4; ++i) { const float c = (i & 1) ? hi16(cv0[i >> 1]) : lo16(cv0[i >> 1]); sl[i] = rb(c * rcp(1.f + __expf(-c))); }
      u32 tag = epoch;
      asm volatile("" : "+v"(tag));
      tag += 1u;
      TS(11);
#pragma unroll
      for (int j = 0; j < 8; ++j) acc[j] = 0.f;
#pragma unroll
      for (int i = 0; i < 4; ++i)
#pragma unroll
        for (int j = 0; j < 4; ++j) { acc[2 * j] += sl[i] * lo16(r0[i][j]); acc[2 * j + 1] += sl[i] * hi16(r0[i][j]); }
      float c0, c1;
      TS(12);
      red8q(acc, c0, c1);
      c0 = dpp_add<DPP_X2>(c0); c1 = dpp_add<DPP_X2>(c1);
      const int go = ((lane >> 1) & 7) * 32768 + (8 * (lane & 1) + 2 * (lane >> 4)) * 8;
      const v4u gv = {__float_as_uint(c0), tag, __float_as_uint(c1), tag};
      drain();
      st128(gv, rs, go, OFF_MODR + mkp * 8192 + mcg * 128);
      st128(gv, rs, go, OFF_MODR + 8 * 32768 + mkp * 8192 + mcg * 128);
      sig(L, 0, 0);
    }
    asm volatile("" : "+s"(w));
  }

  if (w == 4 || w == 5) {
    const int a = w & 1;
    u32 tag;
    tag = epoch;
    asm volatile("" : "+v"(tag));
    tag += 1u;
    drain();
    TS(1);
    bar();
    __builtin_amdgcn_s_setprio(APRIO);
    await<4>(L, 2);
    const int kp = xcd >> 1, cpair = (xcd & 1) * 24 + rank;
    const bool con = rank < ((xcd & 1) ? 19 : 24);
    const int col0 = a * F + cpair * 32;
    v4u wm[8];
    if (a == 1) {
      v4u wn[8]; u32 cv[8];
      const int row0 = 128 * mkp + (lane >> 2);
#pragma unroll
      for (int i = 0; i < 8; ++i) cv[i] = p.c[row0 + 16 * i];
#pragma unroll
      for (int i = 0; i < 8; ++i) wn[i] = *(const GAS v4u*)(p.wmod + (size_t)(row0 + 16 * i) * (6 * D) + 1024 + mcg * 32 + (lane & 3) * 8);
      SB0();
      float acc[8];
#pragma unroll
      for (int j = 0; j < 8; ++j) acc[j] = 0.f;
#pragma unroll
      for (int i = 0; i < 8; ++i) {
        const float c = lo16(cv[i]), sl = rb(c * rcp(1.f + __expf(-c)));
#pragma unroll
        for (int j = 0; j < 4; ++j) { acc[2 * j] += sl * lo16(wn[i][j]); acc[2 * j + 1] += sl * hi16(wn[i][j]); }
      }
      float c0, c1;
      red8q(acc, c0, c1);
      st128(v4u{__float_as_uint(c0), tag, __float_as_uint(c1), tag}, rs, (lane & 12) == 0 ? (8 * (lane & 3) + 2 * (lane >> 4)) * 8 : WS_BYTES,
            OFF_MODG + mkp * 16384 + mcg * 256);
    }
    reach<1>(L, H_C0, 2u, 2u, 2u, 2u);
    TS(2);
    {
      const int idx0 = (mem * 4 + 2 * a) * 16;
      v4u e[32], zz[2];
      {
        u32 todo = 0xFFu;
        for (u32 spins = 0; spins < SPIN_CAP && todo; ++spins) {
          const u32 bad = ~__builtin_amdgcn_readfirstlane(ask(rs, FL_KV + mem * 2 + a, lane));
          __builtin_amdgcn_fence(__ATOMIC_ACQUIRE, "wavefront");
          RELOAD();
          static_for<0, 8>([&](auto G) {
            constexpr int g = G.value;
            if (((todo >> g) & 1u) && !(bad & (0x00030003u << (2 * g)))) {
              todo &= ~(1u << g);
              e[2 * g] = ld128(rs, lane * 16, OFF_KVE + (idx0 + 2 * g) * 1024);
              e[2 * g + 1] = ld128(rs, lane * 16, OFF_KVE + (idx0 + 2 * g + 1) * 1024);
              e[16 + 2 * g] = ld128(rs, lane * 16, OFF_KVE + (idx0 + 16 + 2 * g) * 1024);
              e[17 + 2 * g] = ld128(rs, lane * 16, OFF_KVE + (idx0 + 17 + 2 * g) * 1024);
            }
          });
        }
      }
      TS(3);
      zz[0] = ldfrag<0>(rs, lane * 16, OFF_KVZ + idx0 * 64);
      zz[1] = ldfrag<1>(rs, lane * 16, OFF_KVZ + idx0 * 64);
      SB0();
#pragma unroll
      for (int q = 0; q < 2; ++q) {
        f32x4 s0 = __builtin_bit_cast(f32x4, e[16 * q]), s1 = __builtin_bit_cast(f32x4, e[16 * q + 1]);
#pragma unroll
        for (int i = 2; i < 16; i += 2) { s0 += __builtin_bit_cast(f32x4, e[16 * q + i]); s1 += __builtin_bit_cast(f32x4, e[16 * q + i + 1]); }
        s0 += s1;
        *(LAS v2u*)(L + L_KVF + (2 * a + q) * 512 + l31 * 16 + hh * 8) = v2u{pk(s0[0], s0[1]), pk(s0[2], s0[3])};
      }
      f32x4 z;
#pragma unroll
      for (int j = 0; j < 4; ++j) {
        float t = fold32(__uint_as_float(zz[0][j]), __uint_as_float(zz[1][j]));
        t = fold16(t, t);
        t = dpp_add<DPP_ROR4>(t);
        z[j] = dpp_add<DPP_ROR8>(t);
      }
      if (l31 < 4) *(LAS v2u*)(L + L_ZF + (2 * a + hh) * 32 + ((lane & 1) ^ 1) * 16 + ((lane >> 1) & 1) * 8) = v2u{pk(z[0], z[1]), pk(z[2], z[3])};
    }
    MARK(H_P0, 2 + a, 1u);
    __builtin_amdgcn_s_setprio(1);
    TS(4);
#pragma unroll
    for (int i = 0; i < 8; ++i)
      wm[i] = __builtin_amdgcn_raw_buffer_load_b128(rgu, con ? (128 * kp + 2 * (lane >> 2)) * (F * 2) + cpair * 64 + (lane & 3) * 16 : 1 << 30, (32 * (i >> 1) + (i & 1)) * (F * 2), 0);
    SB0();
    u32 gsb[4], gsc[4];
    {
      const int vo = ((hh ? 2 : 3 * a) * D + colg * 64 + 2 * l31) * 8, ro = (D + 128 * kp + 2 * lane) * 8;
      v4u g[12];
      for (u32 spins = 0; spins < SPIN_CAP; ++spins) {
        RELOAD();
#pragma unroll
        for (int i = 0; i < 12; ++i) g[i] = ld128(rs, i < 4 ? vo : ro + (i >> 3) * (D * 8), OFF_MODG + (i & 3) * 16384);
        bool ok = true;
#pragma unroll
        for (int i = 0; i < 12; ++i) ok &= (g[i].y == tag) & (g[i].w == tag);
        if (__all(ok)) break;
      }
      u32 pr[3];
#pragma unroll
      for (int t = 0; t < 3; ++t)
        pr[t] = pk((__uint_as_float(g[4 * t].x) + __uint_as_float(g[4 * t + 1].x)) + (__uint_as_float(g[4 * t + 2].x) + __uint_as_float(g[4 * t + 3].x)),
                   (__uint_as_float(g[4 * t].z) + __uint_as_float(g[4 * t + 1].z)) + (__uint_as_float(g[4 * t + 2].z) + __uint_as_float(g[4 * t + 3].z)));
      const u32 v1 = pk(1.f + lo16(pr[0]), 1.f + hi16(pr[0])), vv = hh ? v1 : pr[0];
      *(LAS f32x2*)(L + L_VEC + (hh ? 1 : 2 * a) * 256 + l31 * 8) = f32x2{lo16(vv), hi16(vv)};
      const u32 sc = pk(1.f + lo16(pr[2]), 1.f + hi16(pr[2]));
#pragma unroll
      for (int i = 0; i < 4; ++i) {
        gsb[i] = __builtin_amdgcn_ds_bpermute((16 * i + (lane >> 2)) * 4, pr[1]);
        gsc[i] = __builtin_amdgcn_ds_bpermute((16 * i + (lane >> 2)) * 4, sc);
      }
    }
    MARK(H_P0, 2 + a, 2u);
    if (con) {
      float a1[8], a2[8];
#pragma unroll
      for (int j = 0; j < 8; ++j) { a1[j] = 0.f; a2[j] = 0.f; }
#pragma unroll
      for (int i = 0; i < 8; ++i) {
        const float sb = (i & 1) ? hi16(gsb[i >> 1]) : lo16(gsb[i >> 1]), sc = (i & 1) ? hi16(gsc[i >> 1]) : lo16(gsc[i >> 1]);
#pragma unroll
        for (int j = 0; j < 4; ++j) {
          a1[2 * j] += sc * lo16(wm[i][j]); a1[2 * j + 1] += sc * hi16(wm[i][j]);
          a2[2 * j] += sb * lo16(wm[i][j]); a2[2 * j + 1] += sb * hi16(wm[i][j]);
        }
      }
      float c10, c11, c20, c21;
      red8q(a1, c10, c11); red8q(a2, c20, c21);
      const int k = (lane >> 2) & 3;
      const float val = k == 0 ? c10 : k == 1 ? c20 : k == 2 ? c11 : c21;
      st64(v2u{__float_as_uint(val), tag}, rs, (col0 + 8 * (lane & 3) + 2 * (lane >> 4)) * 16 + k * 8, OFF_CG + kp * CG_PART);
    }
    TS(5);
    reach<2>(L, H_C0, 4u, 4u, 4u, 4u);
    {
      const int ntile = three ? 6 : 4;
      v4u cg[8];
      int off[2];
#pragma unroll
      for (int i = 0; i < 2; ++i) {
        int tile = 4 * i + 2 * a + hh; tile = tile < ntile ? tile : 0;
        off[i] = (32 * pair0 + 16 * tile + (lane & 15) + ((lane >> 4) & 1) * F) * 16;
      }
      for (u32 spins = 0; spins < SPIN_CAP; ++spins) {
        RELOAD();
        bool ok = true;
#pragma unroll
        for (int i = 0; i < 8; ++i) cg[i] = ld128(rs, off[i >> 2], OFF_CG + (i & 3) * CG_PART);
#pragma unroll
        for (int i = 0; i < 8; ++i) ok &= (cg[i].y == tag) & (cg[i].w == tag);
        if (__all(ok)) break;
      }
#pragma unroll
      for (int i = 0; i < 2; ++i) {
        const float c1 = (__uint_as_float(cg[4 * i].x) + __uint_as_float(cg[4 * i + 1].x)) + (__uint_as_float(cg[4 * i + 2].x) + __uint_as_float(cg[4 * i + 3].x));
        const float c2 = (__uint_as_float(cg[4 * i].z) + __uint_as_float(cg[4 * i + 1].z)) + (__uint_as_float(cg[4 * i + 2].z) + __uint_as_float(cg[4 * i + 3].z));
        const float h1 = rb(c1), m1 = rb(c1 - h1), l1 = c1 - h1 - m1, h2 = rb(c2), m2 = rb(c2 - h2), l2 = c2 - h2 - m2;
        const int tile = 4 * i + 2 * a + hh;
        if (tile < 6) *(LAS v4u*)(L + L_CF + tile * 512 + l31 * 16) = v4u{pk(h1, m1), pk(l1, h2), pk(m2, l2), 0u};
      }
    }
    reach<1>(L, 5, a < 1, a < 1, a, a);
    {
      v4u st[16];
#pragma unroll
      for (int t = 0; t < 16; ++t) st[t] = ld128(rs, l31 * 16 + hh * 8192 + t * 512, pb + PN_ST + 2 * a * 8192);
      for (u32 spins = 0; spins < SPIN_CAP; ++spins) {
        bool ok = true;
#pragma unroll
        for (int t = 0; t < 16; ++t) ok &= (st[t].y == tag) & (st[t].w == tag);
        if (__all(ok)) break;
        RELOAD();
#pragma unroll
        for (int t = 0; t < 16; ++t) st[t] = ld128(rs, l31 * 16 + hh * 8192 + t * 512, pb + PN_ST + 2 * a * 8192);
      }
      float ms = 0.f;
#pragma unroll
      for (int t = 0; t < 16; ++t) ms += __uint_as_float(st[t].x);
      const float mean = ms * (1.f / 16.f);
      float q = 0.f;
#pragma unroll
      for (int t = 0; t < 16; ++t) { const float d = __uint_as_float(st[t].x) - mean; q += __uint_as_float(st[t].z) + 32.f * d * d; }
      const float den = rb(__builtin_amdgcn_sqrtf(rb(rb(q * (1.f / D)) + 1e-5f)));
      *(LAS v4u*)(L + L_KVF + a * 1024 + lane * 16) = v4u{pk(-mean, -mean), pk(-mean, den), pk(den, den), __float_as_uint(rcp(den * den))};
    }
    RELOAD();
    mark(L, H_PP, 2 + a, 5u);
    TS(6);
    await_cp(L, 2);
    {
      v4u wt[58];
      const u32 vd = (lane >> 2) * (D * 2) + (lane & 3) * 16, sd = a * 16 * (D * 2) + colg * 128;
      const Base3 wdst(L + a * 2048 + lane * 16);
      static_for<28, 86>([&](auto J) {
        wt[J.value - 28] = __builtin_amdgcn_raw_buffer_load_b128(rwd, NBV(vd + (J.value & 1) * 64), sd + pair_of(J.value >> 1) * 32 * (D * 2), 0); SB0(); });
      TS(7);
#define ST_WT(T0, T1, k) do { static_for<2 * (T0), 2 * (T1)>([&](auto J) { \
      *(LAS v4u*)wdst.template at<slot(P_WD + 4 * (J.value >> 1)) + (J.value & 1) * 1024>() = wt[J.value - 28]; WPACE(); }); \
      RELOAD(); mark(L, H_PP, 2 + a, k); RELOAD(); } while (0)
      await_cp(L, 4);
      ST_WT(14, 18, 6u);
      await_cp(L, 5);
      ST_WT(18, 26, 7u);
      await_cp(L, 6);
      ST_WT(26, 34, 8u);
      TS(8);
      await_cp(L, 7);
      ST_WT(34, 43, 9u);
      TS(9);
    }
    TS_DUMP();
    return;
  }

  if (w >= 6) {
    const int v = w & 1;
    __builtin_amdgcn_s_setprio(LPRIO);
    const u32 ldsb = (u32)(size_t)L;
    const u32 l1 = ldsb + v * 1024, l2 = ldsb + v * 2048;
    const u32 skv = (D + v * D + mem * 32) * 2, sq = v * 16 * (3 * D * 2) + mem * 64;
    const u32 so = 16 * v * 16 * (D * 2) + colg * 128, sd = v * 16 * (D * 2) + colg * 128;
    const u32 sg = pair0 * 64, sg2 = three ? sg + 128 : 1u << 30;
#define LD_KV(S0, S1, PC) static_for<S0, S1>([&](auto S) { gldsi<slot(P_KV + 2 * S.value), S.value * 16 * (3 * D * 2)>(rqkv, vq, skv, l1); PACE(PC); })
#define PACE(n) do { if ((n) > 0) __builtin_amdgcn_s_sleep(n); } while (0)
#define LD_Q(S0, S1, PC) static_for<S0, S1>([&](auto S) { gldsi<slot(P_Q + 2 * S.value), 2 * S.value * 16 * (3 * D * 2)>(rqkv, vq, sq, l1); PACE(PC); })
#define LD_DN(RS, SB, P0, T0, T1, PC)                                                                    \
  static_for<T0, T1>([&](auto T) {                                                                   \
    gldsi<slot((P0) + 4 * T.value), T.value * 16 * (D * 2)>(RS, vd, SB, l2);                         \
    gldsi<slot((P0) + 4 * T.value) + 1024, T.value * 16 * (D * 2) + 64>(RS, vd, SB, l2); PACE(PC);   \
  })
#define LD_GU(S0, S1, PC)                                                                                \
  static_for<S0, S1>([&](auto S) {                                                                   \
    gldsi<slot(P_GU + 6 * S.value), S.value * 16 * (F * 2)>(rgu, vg, sg, l1);                        \
    gldsi<slot(P_GU + 6 * S.value + 2), S.value * 16 * (F * 2) + 64>(rgu, vg, sg, l1);               \
    gldsi<slot(P_GU + 6 * S.value + 4), S.value * 16 * (F * 2)>(rgu, vg, sg2, l1); PACE(PC);         \
  })
    TS(1);
    VMCNT(0);
    SB0();
    int ln;
    asm volatile("v_mbcnt_lo_u32_b32 %0, -1, 0\n\tv_mbcnt_hi_u32_b32 %0, -1, %0" : "=v"(ln));
    const u32 vd = (ln >> 2) * (D * 2) + (ln & 3) * 16;
    const u32 vg = (ln >> 2) * (F * 2) + (((ln & 3) ^ (v << 1)) * 16);
    TS(2);
    LD_Q(0, 16, PQE);
    TS(11);
    bar_raw();
    v4u wt[36];
    const Base3 wdst1(L + v * 1024 + ln * 16);
#define GUN(s_, P_) (P_GU + ((s_) < 12 ? 6 * (s_) + 2 * (P_) : 72 + 40 * (P_) + 2 * ((s_) - 12)))
#define GUR(s_, P_) __builtin_amdgcn_raw_buffer_load_b128(rgu, NBV(vg), ((P_) == 2 ? sg2 : sg + (P_) * 64) + (s_) * 16 * (F * 2), 0)
#define GUW(s_, P_, r_) *(LAS v4u*)wdst1.template at<slot(GUN(s_, P_))>() = wt[r_]
#define LD_GU2(S0, S1, PC) static_for<S0, S1>([&](auto S) { constexpr int s_ = S.value; \
      gldsi<slot(GUN(s_, 0)), s_ * 16 * (F * 2)>(rgu, vg, sg, l1); SB0(); \
      if constexpr (s_ < 24) gldsi<slot(GUN(s_, 1)), s_ * 16 * (F * 2) + 64>(rgu, vg, sg, l1); \
      else wt[20 + s_ - 24] = GUR(s_, 1); \
      SB0(); wt[s_ - 12] = GUR(s_, 2); PACE(PC); SB0(); })
#define LD_WD(T0, T1) static_for<T0, T1>([&](auto T) { \
      gldsi<slot(P_WD + 4 * T.value), pair_of(T.value) * 32 * (D * 2)>(rwd, vd, sd, l2); \
      gldsi<slot(P_WD + 4 * T.value) + 1024, pair_of(T.value) * 32 * (D * 2) + 64>(rwd, vd, sd, l2); })
#define DONE(k) do { RELOAD(); mark(L, H_PP, v, k); RELOAD(); } while (0)
    await<4>(L, 2);
    VMCNT(0);
    MARK(H_P0, v, 1u);
    if (TWO) gldsi<L_CF, 0>(rwo, ln < 32 ? ((2 * rank + v) * 8 + (ln >> 2)) * (D * 2) + (xcd & 1) * 512 + (ln & 3) * 128 : 1u << 30, 0u, l1);
    gldsi<L_CF, 0>(rgu, (ln & 15) < 13 ? ((xcd >> 1) * 128 + 4 * rank + (ln >> 4)) * (F * 2) + (xcd & 1) * 1536 + (ln & 15) * 128 : 1u << 30, 0u, l1);
    LD_DN(rwo, so, P_WO, 0, W1N, 0);
    TS(3);
    VMCNT(0);
    MARK(H_P0, v, 1u);
    reach<1>(L, H_C0, 2u, 2u, 2u, 2u);
    LD_DN(rwo, so, P_WO, W1N, 16, 1);
    LD_GU(0, W2N, PGU);
    asm volatile("s_waitcnt vmcnt(%0)" ::"i"(3 * W2N) : "memory");
    MARK(H_P0, v, 2u);
    TS(4);
    reach<2>(L, H_C0, 3u, 3u, 3u, 3u);
    reach<2>(L, H_P0, 0u, 0u, 1u, 1u);
    LD_GU(W2N, 12, PG3);
    await<4>(L, 4);
    static_for<12, 20>([&](auto S) { constexpr int s_ = S.value;
      wt[20 + s_ - 12] = GUR(s_, 0); wt[28 + s_ - 12] = GUR(s_, 1); wt[s_ - 12] = GUR(s_, 2); SB0(); });
    VMCNT(24);
    MARK(H_P0, v, 3u);
    TS(5);
    reach<1>(L, H_C0, 4u, 4u, 4u, 4u);
    static_for<12, 20>([&](auto S) { GUW(S.value, 0, 20 + S.value - 12); GUW(S.value, 1, 28 + S.value - 12); });
    LD_GU2(20, 32, PG2);
    VMCNT(0);
    DONE(3);
    TS(6);
    await_cp(L, 1);
    {
      const u32 tdn = ((2 * rank + v) * 16 + (ln >> 2)) * (D * 2) + (xcd & 1) * 512 + (ln & 3) * 128;
      __builtin_amdgcn_s_waitcnt(0x0F70);
      if (TWD) {
      gldsi<L_T, 0>(rwd, tdn, 0u, l1);
      gldsi<L_T, 1024 * (D * 2)>(rwd, tdn, 0u, l1);
      }
    }
    static_for<24, 32>([&](auto S) { GUW(S.value, 1, 20 + S.value - 24); });
    DONE(4);
    await_cp(L, 2);
    static_for<12, 32>([&](auto S) { GUW(S.value, 2, S.value - 12); });
    DONE(5);
    TS(7);
    await_cp(L, 3);
    LD_WD(0, 10);
    await_cp(L, 4);
    LD_WD(10, 14);
    VMCNT(0);
    DONE(100);
    TS(8);
    TS_DUMP();
    return;
  }

  __builtin_amdgcn_s_setprio(2);
  const int s2 = w & 1, kh = w >> 1;
  const int strip2 = rowh * 2 + s2, ncol = colg * 64 + kh * 32;
  v4u aphi0, aphi1;
  u32 tag;
  {
    float xf[16];
#pragma unroll
    for (int i = 0; i < 8; ++i) { xf[2 * i] = lo16(r0[i >> 2][i & 3]); xf[2 * i + 1] = hi16(r0[i >> 2][i & 3]); }
    float sm = (((xf[0] + xf[1]) + (xf[2] + xf[3])) + ((xf[4] + xf[5]) + (xf[6] + xf[7]))) + (((xf[8] + xf[9]) + (xf[10] + xf[11])) + ((xf[12] + xf[13]) + (xf[14] + xf[15])));
    sm = dpp_add<DPP_X1>(sm); sm = dpp_add<DPP_X2>(sm); sm = dpp_add<DPP_HM>(sm);
    const float mw = sm * (1.f / 128.f);
    float q[4];
#pragma unroll
    for (int i = 0; i < 4; ++i) {
      const float d0 = xf[4 * i] - mw, d1 = xf[4 * i + 1] - mw, d2 = xf[4 * i + 2] - mw, d3 = xf[4 * i + 3] - mw;
      q[i] = (d0 * d0 + d1 * d1) + (d2 * d2 + d3 * d3);
    }
    float qq = (q[0] + q[1]) + (q[2] + q[3]);
    qq = dpp_add<DPP_X1>(qq); qq = dpp_add<DPP_X2>(qq); qq = dpp_add<DPP_HM>(qq);
    *(volatile LAS f32x2*)(L + L_KVF + (lane >> 3) * 32 + w * 8) = f32x2{mw, qq};
    sig(L, 1, w);
    await<4>(L, 1);
    const f32x4 pa = *(LAS f32x4*)(L + L_KVF + (lane >> 3) * 32), pc = *(LAS f32x4*)(L + L_KVF + (lane >> 3) * 32 + 16);
    const float mean = ((pa[0] + pa[2]) + (pc[0] + pc[2])) * 0.25f;
    const float f0 = pa[0] - mean, f1 = pa[2] - mean, f2 = pc[0] - mean, f3 = pc[2] - mean;
    const float m2 = ((pa[1] + pa[3]) + (pc[1] + pc[3])) + 128.f * ((f0 * f0 + f1 * f1) + (f2 * f2 + f3 * f3));
    const float m1 = rb(mean), r1 = rcp(rb(__builtin_amdgcn_sqrtf(rb(rb(m2 * (1.f / D)) + 1e-5f))));
    float xn[16];
#pragma unroll
    for (int i = 0; i < 8; ++i) {
      const u32 d = pk(xf[2 * i] - m1, xf[2 * i + 1] - m1);
      const u32 t = pk(lo16(d) * r1, hi16(d) * r1);
      xn[2 * i] = lo16(t); xn[2 * i + 1] = hi16(t);
    }
#pragma unroll
    for (int i = 0; i < 16; ++i) asm volatile("" : "+v"(xn[i]));
    tag = __builtin_amdgcn_readfirstlane(epoch) + 1u;
    TS(1);
    await<1>(L, 0);
    __builtin_amdgcn_s_sleep(PRE_MOD);
    TS(21);
    v4u g[8];
    {
      u32 todo = 0xFFu;
      for (u32 spins = 0; spins < SPIN_CAP && todo; ++spins) {
        RELOAD();
        static_for<0, 8>([&](auto I) {
          constexpr int i = I.value;
          if ((todo >> i) & 1u) g[i] = ld128(rs, lane * 16, OFF_MODR + panel * 32768 + (i & 3) * 8192 + (i >> 2) * 4096 + w * 1024);
        });
        todo = 0;
        static_for<0, 8>([&](auto I) { if (__builtin_amdgcn_ballot_w64((g[I.value].y != tag) | (g[I.value].w != tag))) todo |= 1u << I.value; });
      }
    }
    {
      u32 pr[2];
#pragma unroll
      for (int t = 0; t < 2; ++t)
        pr[t] = pk((__uint_as_float(g[4 * t].x) + __uint_as_float(g[4 * t + 1].x)) + (__uint_as_float(g[4 * t + 2].x) + __uint_as_float(g[4 * t + 3].x)),
                   (__uint_as_float(g[4 * t].z) + __uint_as_float(g[4 * t + 1].z)) + (__uint_as_float(g[4 * t + 2].z) + __uint_as_float(g[4 * t + 3].z)));
      *(LAS u32*)(L + L_T + w * TW + lane * 4) = pr[0];
      *(LAS u32*)(L + L_T + w * TW + 256 + lane * 4) = pk(1.f + lo16(pr[1]), 1.f + hi16(pr[1]));
    }
    v4u sav[2], cav[2];
#pragma unroll
    for (int i = 0; i < 2; ++i) {
      sav[i] = *(LAS v4u*)(L + L_T + w * TW + i * 128 + (lane & 7) * 16);
      cav[i] = *(LAS v4u*)(L + L_T + w * TW + 256 + i * 128 + (lane & 7) * 16);
    }
    sig(L, 3, w);
    TS(2);
    const int strip = mem >> 2, r = 8 * (mem & 3) + (lane >> 3);
#pragma unroll
    for (int i = 0; i < 2; ++i) {
      v4u o;
#pragma unroll
      for (int j = 0; j < 4; ++j) {
        const u32 sa = sav[i][j], sc = cav[i][j];
        const u32 t = pk(xn[8 * i + 2 * j] * lo16(sc), xn[8 * i + 2 * j + 1] * hi16(sc));
        o[j] = pk(lo16(t) + lo16(sa), hi16(t) + hi16(sa));
      }
      st128(o, rs, (4 * i + ((lane & 7) >> 1)) * 1024 + (r + 32 * (lane & 1)) * 16, pb + PN_H + strip * 32768 + w * 8192);
    }
    await<4>(L, 3);
    publish(rs, fw + FL_H + strip, (mem & 3) * 4 + w, lane);
  }
  TS(3);

  const int trq = (lane & 15) >> 2, trp = lane & 3, trb = (lane >> 4) & 1;
  const f32x16 zero16 = {0.f, 0.f, 0.f, 0.f, 0.f, 0.f, 0.f, 0.f, 0.f, 0.f, 0.f, 0.f, 0.f, 0.f, 0.f, 0.f};
  u32 phq[16];
  v4u wb[PD];
  {
    const Base3 wnat(L + (8 * hh + trq) * 64 + trb * 32 + trp * 8);
    waitn(rs, fw + FL_H + w, 0xFFFFu, lane);
    TS(4);
    v4u a[32];
    ldfrags<W0>(a, rs, lane * 16, pb + PN_H + w * 32768);
    bar_raw();
    {
      const int hs = (l31 >> 2) & 1;
      const LAS u16* wp = (const LAS u16*)(L + L_CF + 2048) + hs * 256 + 4 * hh * 16 + (l31 & 3) + 4 * (l31 >> 3);
      u32 e[8];
#pragma unroll
      for (int j = 0; j < 8; ++j) e[j] = wp[(8 * (j >> 2) + (j & 3)) * 16];
      const v4u av = {e[0] | (e[1] << 16), e[2] | (e[3] << 16), e[4] | (e[5] << 16), e[6] | (e[7] << 16)};
      const v4u z = {0, 0, 0, 0};
      aphi0 = hs == 0 ? av : z; aphi1 = hs == 1 ? av : z;
      asm volatile("" : "+v"(aphi0), "+v"(aphi1));
    }
    static_for<0, PD>([&](auto U) { wb[U.value] = ldw<256>(wnat.at<slot(P_KV + kv_piece(U.value))>()); });
    SB0();
    f32x16 kacc = zero16, vacc = zero16;
    auto unit = [&](auto U) {
      constexpr int u = decltype(U)::value, pc = kv_piece(u), st = pc >> 1;
      SB0();
      const v4u cur = wb[u % PD];
      if constexpr (u + PD < 64) wb[u % PD] = ldw<256>(wnat.at<slot(P_KV + kv_piece(u + PD))>());
      if constexpr (pc & 1) mfma_pin(a[st], cur, vacc); else mfma_pin(cur, a[st], kacc);
      if constexpr (!(pc & 1) && st + W0 < 32) a[st + W0] = ldfrag<st + W0>(rs, lane * 16, pb + PN_H + w * 32768);
      if constexpr (!(pc & 1) && st + W0 == 32) sig(L, 2, w);
      SB0();
    };
    static_for<0, 47>(unit);
    TS(5);
    const v4u kb0 = pack8<0>(kacc), kb1 = pack8<8>(kacc);
    f32x16 P = zero16;
    mfma_pin(aphi0, kb0, P);
    mfma_pin(aphi1, kb1, P);
    unit(IC<47>{});
    u32 phk[16];
    feat(P, phk, [&](auto N) { unit(IC<48 + N.value>{}); });
    const v4u vb0 = pack8<0>(vacc), vb1 = pack8<8>(vacc);
    LAS unsigned char* T = L + L_T + w * TW;
#pragma unroll
    for (int c = 0; c < 4; ++c)
      *(LAS v4u*)(T + lane * 64 + ((c ^ ((lane >> 1) & 3)) * 16)) = v4u{phk[4 * c], phk[4 * c + 1], phk[4 * c + 2], phk[4 * c + 3]};
    const v4u ones = {0x3F803F80u, 0x3F803F80u, 0x3F803F80u, 0x3F803F80u};
    LAS unsigned char* tr = T + (4 * hh + trq) * 64 + (((2 * trb + (trp >> 1)) ^ ((2 * hh + (trq >> 1)) & 3)) * 16) + 8 * (trp & 1);
    const v4u t0 = ldw<512>(tr), t1 = ldw<512>(tr + 1024), t2 = ldw<512>(tr + 2048), t3 = ldw<512>(tr + 3072);
    f32x16 D0 = mfma(t0, trb ? ones : vb0, zero16);
    f32x16 D1 = mfma(t2, trb ? vb0 : ones, zero16);
    D0 = mfma(t1, trb ? ones : vb1, D0);
    D1 = mfma(t3, trb ? vb1 : ones, D1);
    {
      LAS unsigned char* pe = T + lane * 16;
      LAS unsigned char* pz = T + 1024 + (lane >> 4) * 16;
      LAS unsigned char* p0 = trb ? pz : pe;
      LAS unsigned char* p1 = trb ? pe : pz;
#pragma unroll
      for (int g = 0; g < 4; ++g) *(LAS f32x4*)(p0 + g * 1088) = f32x4{D0[4 * g], D0[4 * g + 1], D0[4 * g + 2], D0[4 * g + 3]};
#pragma unroll
      for (int g = 0; g < 4; ++g) *(LAS f32x4*)(p1 + g * 1088) = f32x4{D1[4 * g], D1[4 * g + 1], D1[4 * g + 2], D1[4 * g + 3]};
    }
    TS(6);
    MARK(H_C0, w, 1u);
    reach<0>(L, H_C0, 1u, 1u, 1u, 1u);
    {
      f32x4 e[4], z[4];
#pragma unroll
      for (int v = 0; v < 4; ++v) {
        e[v] = *(LAS f32x4*)(L + L_T + v * TW + w * 1088 + lane * 16);
        z[v] = *(LAS f32x4*)(L + L_T + v * TW + w * 1088 + 1024 + (lane & 3) * 16);
      }
      SB0();
      const int idx = (mem * 4 + w) * 16 + panel;
      st128(__builtin_bit_cast(v4u, (e[0] + e[1]) + (e[2] + e[3])), rs, lane * 16, OFF_KVE + idx * 1024);
      st128(__builtin_bit_cast(v4u, (z[0] + z[1]) + (z[2] + z[3])), rs, lane < 4 ? lane * 16 : WS_BYTES, OFF_KVZ + idx * 64);
      publish(rs, FL_KV + mem * 2 + (w >> 1), (w & 1) * 16 + panel, lane);
      MARK(H_C0, w, 2u);
      reach<1>(L, H_P0, 1u, 1u, 0u, 0u);
      static_for<0, PD>([&](auto U) { wb[U.value] = ldw<256>(wnat.at<slot(P_Q + U.value)>()); });
    }
    TS(7);
    f32x16 qacc = zero16;
    static_for<0, 32>([&](auto U) {
      constexpr int u = U.value;
      const v4u cur = wb[u % PD];
      if constexpr (u + PD < 32) wb[u % PD] = ldw<256>(wnat.at<slot(P_Q + u + PD)>());
      mfma_pin(cur, a[u], qacc);
      SB0();
    });
    MARK(H_C0, w, 3u);
    P = mfma(aphi0, pack8<0>(qacc), zero16);
    P = mfma(aphi1, pack8<8>(qacc), P);
    feat(P, phq, [](auto) {});
#pragma unroll
    for (int i = 0; i < 16; ++i) asm volatile("" : "+v"(phq[i]));
  }
  v2u xres[4];
  {
    const u16* xr = p.x + (size_t)(panel * 128 + strip2 * 32 + l31) * D + ncol + 4 * hh;
#pragma unroll
    for (int i = 0; i < 4; ++i) xres[i] = *(const GAS v2u*)(xr + 8 * i);
  }
  TS(8);
  reach<1>(L, H_P0, 0u, 0u, 1u, 1u);
  TS(9);
  {
    const bool on = (l31 >> 4) == hh;
    const v4u zv = {0, 0, 0, 0};
    f32x16 num = zero16, den = zero16;
    v4u ak[4], az[4];
#pragma unroll
    for (int s4 = 0; s4 < 4; ++s4) { az[s4] = *(LAS v4u*)(L + L_ZF + s4 * 32 + hh * 16); ak[s4] = *(LAS v4u*)(L + L_KVF + s4 * 512 + l31 * 16); }
    SB0();
#pragma unroll
    for (int s4 = 0; s4 < 4; ++s4) den = mfma(on ? az[s4] : zv, v4u{phq[4 * s4], phq[4 * s4 + 1], phq[4 * s4 + 2], phq[4 * s4 + 3]}, den);
#pragma unroll
    for (int s4 = 0; s4 < 4; ++s4) num = mfma(on ? ak[s4] : zv, v4u{phq[4 * s4], phq[4 * s4 + 1], phq[4 * s4 + 2], phq[4 * s4 + 3]}, num);
    const float d0 = rcp(rb(rb(den[0]) + 1e-6f)), d1 = rcp(rb(rb(den[8]) + 1e-6f));
    const v4u n0 = pack8<0>(num), n1 = pack8<8>(num);
    v4u o0, o1;
#pragma unroll
    for (int j = 0; j < 4; ++j) { o0[j] = pk(lo16(n0[j]) * d0, hi16(n0[j]) * d0); o1[j] = pk(lo16(n1[j]) * d1, hi16(n1[j]) * d1); }
    st128(o0, rs, lane * 16, pb + PN_O + w * 32768 + (2 * mem) * 1024);
    st128(o1, rs, lane * 16 + 1024, pb + PN_O + w * 32768 + (2 * mem) * 1024);
  }
#define WDN_BASES() const Base3 wown(L + (4 * hh + trq) * 64 + trb * 32 + trp * 8 + 3 * kh * 1024), woth(L + (4 * hh + trq) * 64 + trb * 32 + trp * 8 + (kh + 1) * 1024)
#define WDN(base, u) ldw<512>((((u) & 1) ? wown : woth).template at<slot((base) + 4 * ((u) >> 1))>())
  u32 x1p[8];
  {
    WDN_BASES();
    publish(rs, fw + FL_O + w, mem, lane);
    reach<0>(L, H_P0, 2u, 2u, 0u, 0u);
    static_for<0, PD>([&](auto U) { wb[U.value] = WDN(P_WO, U.value); });
    TS(10);
    waitn(rs, fw + FL_O + strip2, 0xFFu << (kh * 8), lane);
    TS(11);
    v4u a[16];
    ldfrags<16>(a, rs, lane * 16, pb + PN_O + strip2 * 32768 + kh * 16384);
    sig(L, 4, w);
    reach<0>(L, H_P0, 0u, 0u, 2u, 2u);
    f32x4 ga[4], sc[4];
#pragma unroll
    for (int g = 0; g < 4; ++g) {
      ga[g] = *(LAS f32x4*)(L + L_VEC + (kh * 32 + 8 * g + 4 * hh) * 4);
      sc[g] = *(LAS f32x4*)(L + L_VEC + 256 + (kh * 32 + 8 * g + 4 * hh) * 4);
    }
    SB0();
    f32x16 c0 = zero16, c1 = zero16;
    static_for<0, 32>([&](auto U) {
      constexpr int u = U.value;
      const v4u cur = wb[u % PD];
      if constexpr (u + PD < 32) wb[u % PD] = WDN(P_WO, u + PD);
      if constexpr (u & 1) mfma_pin(cur, a[u >> 1], c0); else mfma_pin(cur, a[u >> 1], c1);
      SB0();
    });
#pragma unroll
    for (int g = 0; g < 4; ++g) *(LAS f32x4*)(L + L_T + w * TW + g * 1024 + lane * 16) = f32x4{c1[4 * g], c1[4 * g + 1], c1[4 * g + 2], c1[4 * g + 3]};
    TS(12);
    MARK(H_C0, w, 4u);
    reach1(L, H_C0, w ^ 2, 4u);
    f32x4 o[4];
#pragma unroll
    for (int g = 0; g < 4; ++g) o[g] = *(LAS f32x4*)(L + L_T + (w ^ 2) * TW + g * 1024 + lane * 16);
    SB0();
    float x1[16], sp[4];
    u32 yp[8];
#pragma unroll
    for (int g = 0; g < 4; ++g) {
#pragma unroll
      for (int j = 0; j < 2; ++j) {
        const u32 r = pk(lo16(xres[g][j]) + ga[g][2 * j] * (c0[4 * g + 2 * j] + o[g][2 * j]),
                         hi16(xres[g][j]) + ga[g][2 * j + 1] * (c0[4 * g + 2 * j + 1] + o[g][2 * j + 1]));
        x1p[2 * g + j] = r;
        x1[4 * g + 2 * j] = lo16(r); x1[4 * g + 2 * j + 1] = hi16(r);
        yp[2 * g + j] = pk(lo16(r) * sc[g][2 * j], hi16(r) * sc[g][2 * j + 1]);
      }
    }
    const int tile = 2 * colg + kh;
    st128(v4u{yp[0], yp[1], yp[2], yp[3]}, rs, lane * 16, pb + PN_X1 + strip2 * 32768 + tile * 2048);
    st128(v4u{yp[4], yp[5], yp[6], yp[7]}, rs, lane * 16 + 1024, pb + PN_X1 + strip2 * 32768 + tile * 2048);
    SB0();
#pragma unroll
    for (int g = 0; g < 4; ++g) sp[g] = (x1[4 * g] + x1[4 * g + 1]) + (x1[4 * g + 2] + x1[4 * g + 3]);
    const float ss = (sp[0] + sp[1]) + (sp[2] + sp[3]);
    const float mt = fold32(ss, ss) * (1.f / 32.f);
    float q[4];
#pragma unroll
    for (int g = 0; g < 4; ++g) {
      const float d0 = x1[4 * g] - mt, d1 = x1[4 * g + 1] - mt, d2 = x1[4 * g + 2] - mt, d3 = x1[4 * g + 3] - mt;
      q[g] = (d0 * d0 + d1 * d1) + (d2 * d2 + d3 * d3);
    }
    const float qs = (q[0] + q[1]) + (q[2] + q[3]);
    const float qt = fold32(qs, qs);
    v4u sv = {__float_as_uint(mt), tag, __float_as_uint(qt), tag};
    asm volatile("" : "+v"(sv));
    publish(rs, fw + FL_X + strip2, tile, lane);
    st128(sv, rs, lane < 32 ? lane * 16 : WS_BYTES, pb + PN_ST + strip2 * 8192 + tile * 512);
  }
  asm volatile("" : "+v"(x1p[0]), "+v"(x1p[1]), "+v"(x1p[2]), "+v"(x1p[3]), "+v"(x1p[4]), "+v"(x1p[5]), "+v"(x1p[6]), "+v"(x1p[7]));
#define GU_S(u) ((u) < 72 ? (u) / 6 : 12 + (((u) - 72) % 40) / 2)
#define GU_I(u) ((u) < 72 ? (u) % 6 : 2 * (((u) - 72) / 40) + ((u) & 1))
#define WGU(u) ldw<512>((((u) & 1) ? wy : wx).template at<slot(P_GU + ((u) & ~1))>())
  {
    const Base3 wx(L + (4 * hh + trq) * 64 + trp * 8 + trb * (1024 + 32)), wy(L + (4 * hh + trq) * 64 + trp * 8 + (trb ? 1024 : 32));
    reach<0>(L, H_P0, 3u, 3u, 0u, 0u);
    static_for<0, PD>([&](auto U) { wb[U.value] = WGU(U.value); });
    TS(13);
    waitn(rs, fw + FL_X + w, 0xFFFFu, lane);
    TS(14);
    constexpr int AW = 20;
    v4u a[AW];
    ldfrags<8>(a, rs, lane * 16, pb + PN_X1 + w * 32768);
    sig(L, 5, w);
    f32x16 acc[6];
#pragma unroll
    for (int i = 0; i < 6; ++i) acc[i] = zero16;
    float rr = 0.f;
    v4u bcor = {0, 0, 0, 0};
    v4u ho, cf0, cf1, pp;
    float t0 = 0.f, t1 = 0.f;
    const int go = pb + PN_G + (w * 96 + mem) * 1024, fi = fw + FL_G + strip2 * 3, gi = pb + PN_G + (strip2 * 96 + kh * 48) * 1024;
    u32 fl = 0;
    int gv = 0;
    bool early = false;
    v4u g[43];
    auto post = [&](int P) { (void)__builtin_amdgcn_raw_ptr_buffer_atomic_add_i32(1 << mem, rs, lane == 0 ? 0 : WS_BYTES, OFF_ARR + (fw + FL_G + w * 3 + P) * 128, 0); };
    auto epi = [&](auto Ic, auto Kc) {
      constexpr int I = decltype(Ic)::value, k = decltype(Kc)::value, j = k >> 1;
      const f32x16& A = acc[I];
      if constexpr ((k & 1) == 0) { t0 = A[2 * j] * rr; t1 = A[2 * j + 1] * rr; asm volatile("" : "+v"(t0)); t0 *= A[8 + 2 * j]; }
      else {
        t1 *= A[9 + 2 * j];
        ho[j] = pk(t0, t1);
        if constexpr (j == 3) st128(ho, rs, (I < 4 || three) ? lane * 16 : WS_BYTES, go + ((I & 1) * 48 + (I >> 1) * 16) * 1024);
      }
    };
    static_for<0, 192>([&](auto Uc) {
      constexpr int u = decltype(Uc)::value, s = GU_S(u), i = GU_I(u), q = (u + 8) % 40;
      if constexpr (u == 6) TS(22);
      if constexpr (u == 60) TS(23);
      if constexpr (u == 96) TS(24);
      if constexpr (u == 128) TS(25);
      if constexpr (u == 160) TS(26);
      if constexpr (u == 56) mark(L, H_CP, w, 2u);
      if constexpr (u == 96 || u == 128 || u == 160) mark(L, H_CP, w, u / 32);
      if constexpr (u == 60 || u == 96 || u == 140) pp = peek(L, H_PP);
      if constexpr (u == 64) need_pp<false>(L, pp, 3u);
      if constexpr (u == 100) need_pp<true>(L, pp, 4u);
      if constexpr (u == 144) need_pp<false>(L, pp, 5u);
      const v4u cur = wb[u % PD];
      if constexpr (u + PD < 192) wb[u % PD] = WGU(u + PD);
      if constexpr (u >= 72 && q == 32) cf0 = *(LAS v4u*)(L + L_CF + i * 512 + l31 * 16);
      if constexpr (u >= 72 && q == 33) cf1 = *(LAS v4u*)(L + L_CF + i * 512 + l31 * 16);
      mfma_pin(cur, a[s % AW], acc[i]);
      if constexpr (u < 72 && i == 5) a[s % AW] = ldfrag<s + AW>(rs, lane * 16, pb + PN_X1 + w * 32768);
      if constexpr (u < 2 * (AW - 8) && !(u & 1)) a[8 + (u >> 1)] = ldfrag<8 + (u >> 1)>(rs, lane * 16, pb + PN_X1 + w * 32768);
      if constexpr (u == 24) mark(L, H_CP, w, 1u);
      if constexpr (u == 101) bcor = *(LAS v4u*)(L + L_KVF + w * 512 + l31 * 16);
      if constexpr (u == 107) { rr = __uint_as_float(bcor.w); bcor = v4u{hh ? 0u : bcor.x, hh ? 0u : bcor.y, hh ? 0u : bcor.z, 0u}; }
      if constexpr (u >= 72 && q == 38) mfma_pin(cf0, bcor, acc[i]);
      if constexpr (u >= 72 && q == 39) mfma_pin(cf1, bcor, acc[i]);
      if constexpr (u >= 112 && q >= 1 && q < 9) {
        epi(IC<(i & 6) - 2 + (q - 1) / 4>{}, IC<2 * ((q - 1) % 4)>{});
        epi(IC<(i & 6) - 2 + (q - 1) / 4>{}, IC<2 * ((q - 1) % 4) + 1>{});
      }
      if constexpr (u == 112 + GPUB) publish(rs, fw + FL_G + w * 3, mem, lane);
      if constexpr (u == GASK) fl = ask(rs, fi, lane);
      if constexpr (u == 176) {
        early = (__builtin_amdgcn_readfirstlane(fl) & 0xFFFFu) == 0xFFFFu;
        __builtin_amdgcn_fence(__ATOMIC_ACQUIRE, "wavefront");
        RELOAD();
        gv = early ? lane * 16 : WS_BYTES;
      }
      if constexpr (u == 180) { VMCNT(4); post(1); }
      if constexpr (u >= 176) g[u - 176] = ldfrag<u - 176>(rs, gv, gi);
      if constexpr (u == 184) pp = peek(L, H_PP);
      SB0();
    });
    mark(L, H_CP, w, 6u);
    TS(15);
    need_pp<true>(L, pp, 6u);
    WDN_BASES();
    static_for<0, PD>([&](auto U) { wb[U.value] = WDN(P_WD, U.value); });
    if (!early) {
      spin(rs, fi, 0xFFFFu, lane);
      __builtin_amdgcn_fence(__ATOMIC_ACQUIRE, "wavefront");
      RELOAD();
      static_for<0, 16>([&](auto T) { g[T.value] = ld128(rs, lane * 16, gi + T.value * 1024); });
    }
    SB0();
    fl = ask(rs, fi + 1, lane);
    SB0();
    TS(18);
    f32x16 c0 = zero16, c1 = zero16;
    static_for<0, 86>([&](auto U) {
      constexpr int u = U.value;
      if constexpr (u == 18) mark(L, H_CP, w, 7u);
#define GL(t) ((t) < 32 ? GU0 + (t) - 16 : GU2 + (t) - 32)
      if constexpr (u == PUB2) {
        asm volatile("s_waitcnt vmcnt(%0)" ::"i"(PUB2 - GU0 < 16 ? PUB2 - GU0 : 16) : "memory");
        post(2);
      }
      if constexpr (u == 24 || u == 40 || u == 56) pp = peek(L, H_PP);
      if constexpr (u == 28 || u == 44 || u == 60) need_pp<true>(L, pp, 7u + (u - 28) / 16);
      if constexpr (u == GL(16)) poll(fl, rs, fi + 1, 0xFFFFu, lane);
      if constexpr (u == GL(32) - GA2) fl = ask(rs, fi + 2, lane);
      if constexpr (u == GL(32)) poll(fl, rs, fi + 2, 0x7FFu, lane);
      const v4u cur = wb[u % PD];
      if constexpr (u + PD < 86) wb[u % PD] = WDN(P_WD, u + PD);
      if constexpr (u & 1) mfma_pin(cur, g[u >> 1], c0); else mfma_pin(cur, g[u >> 1], c1);
      if constexpr (u >= 1 && u < 9) {
        epi(IC<4 + (u - 1) / 4>{}, IC<2 * ((u - 1) % 4)>{});
        epi(IC<4 + (u - 1) / 4>{}, IC<2 * ((u - 1) % 4) + 1>{});
      }
      static_for<16, 43>([&](auto T) { if constexpr (GL(T.value) == u) g[T.value] = ldfrag<T.value>(rs, lane * 16, gi); });
      SB0();
    });
#pragma unroll
    for (int g = 0; g < 4; ++g) *(LAS f32x4*)(L + L_T + w * TW + g * 1024 + lane * 16) = f32x4{c1[4 * g], c1[4 * g + 1], c1[4 * g + 2], c1[4 * g + 3]};
    f32x4 gb[4];
#pragma unroll
    for (int g = 0; g < 4; ++g) gb[g] = *(LAS f32x4*)(L + L_VEC + 512 + (kh * 32 + 8 * g + 4 * hh) * 4);
    TS(19);
    MARK(H_C0, w, 5u);
    reach1(L, H_C0, w ^ 2, 5u);
    f32x4 o[4];
#pragma unroll
    for (int g = 0; g < 4; ++g) o[g] = *(LAS f32x4*)(L + L_T + (w ^ 2) * TW + g * 1024 + lane * 16);
    SB0();
    const rsrc_t ro = __builtin_amdgcn_make_buffer_rsrc((void*)p.out, 0, 2048 * D * 2, 0x00020000);
    v2u r[4];
#pragma unroll
    for (int g = 0; g < 4; ++g)
#pragma unroll
      for (int j = 0; j < 2; ++j)
        r[g][j] = pk(lo16(x1p[2 * g + j]) + gb[g][2 * j] * (c0[4 * g + 2 * j] + o[g][2 * j]),
                     hi16(x1p[2 * g + j]) + gb[g][2 * j + 1] * (c0[4 * g + 2 * j + 1] + o[g][2 * j + 1]));
    const int oo = ((panel * 128 + strip2 * 32 + l31) * D + ncol + 8 * hh) * 2;
#pragma unroll
    for (int g = 0; g < 4; g += 2) {
      const auto x = __builtin_amdgcn_permlane32_swap(r[g][0], r[g + 1][0], false, false);
      const auto y = __builtin_amdgcn_permlane32_swap(r[g][1], r[g + 1][1], false, false);
      __builtin_amdgcn_raw_buffer_store_b128(v4u{x[0], y[0], x[1], y[1]}, ro, oo + g * 16, 0, OAUX);
    }
  }
  if (b == 0 && tid == 0) __hip_atomic_store((GAS u32*)(ws + OFF_EPOCH), tag, RLX_AGENT);
}
}

extern "C" void kernel_launch(void* const* d_in, const int* in_sizes, int n_in, void* d_out, int out_size, void* d_ws,
                              size_t ws_size, hipStream_t stream) {
  using namespace mk;
  Params p;
  p.x = (const u16*)d_in[0]; p.wqkv = (const u16*)d_in[1]; p.wphi = (const u16*)d_in[2]; p.wo = (const u16*)d_in[3];
  p.wg = (const u16*)d_in[5]; p.wu = (const u16*)d_in[6]; p.wd = (const u16*)d_in[7]; p.c = (const u16*)d_in[8];
  p.wmod = (const u16*)d_in[9]; p.out = (u16*)d_out; p.ws = (unsigned char*)d_ws;
  (void)hipFuncSetAttribute((const void*)mega, hipFuncAttributeMaxDynamicSharedMemorySize, LDS_BYTES);
  mega<<<256, 512, LDS_BYTES, stream>>>(p);
}
```

```cpp
#include <hip/hip_runtime.h>

namespace mk {
typedef unsigned short u16;
typedef unsigned int u32;
typedef unsigned long long u64;
typedef u32 v4u __attribute__((ext_vector_type(4)));
typedef u32 v2u __attribute__((ext_vector_type(2)));
typedef float f32x2 __attribute__((ext_vector_type(2)));
typedef float f32x4 __attribute__((ext_vector_type(4)));
typedef float f32x16 __attribute__((ext_vector_type(16)));
typedef __bf16 bf16x2 __attribute__((ext_vector_type(2)));
typedef __bf16 bf16x8 __attribute__((ext_vector_type(8)));
typedef short s16x4 __attribute__((ext_vector_type(4)));
#define LAS __attribute__((address_space(3)))
#define GAS __attribute__((address_space(1)))
#define DI __device__ __forceinline__
#define RLX_AGENT __ATOMIC_RELAXED, __HIP_MEMORY_SCOPE_AGENT
#define SB0() __builtin_amdgcn_sched_barrier(0)
typedef __amdgpu_buffer_rsrc_t rsrc_t;

constexpr int D = 512, F = 1376;
constexpr int OFF_EPOCH = 0;
constexpr int OFF_MODG = 64 << 10;
constexpr int OFF_KVZ = 128 << 10;
constexpr int OFF_CG = 192 << 10;
constexpr int CG_PART = 2 * F * 16;
constexpr int OFF_ARR = 384 << 10;
constexpr int OFF_MODR = 512 << 10;
constexpr int OFF_KVE = 1 << 20;
constexpr int OFF_PAN = (2 << 20) + 4096;
constexpr int PN_H = 0;
constexpr int PN_O = 128 << 10;
constexpr int PN_X1 = 256 << 10;
constexpr int PN_G = 384 << 10;
constexpr int PN_ST = 768 << 10;
constexpr int FL_H = 0, FL_O = 4, FL_X = 8, FL_G = 12, FL_KV = 384;
constexpr int PSTR = (848 << 10) + 4224;
constexpr int WS_BYTES = 16 << 20;
constexpr int RING = 136;
constexpr int L_T = RING * 1024, TW = 4352;
constexpr int L_KVF = L_T + 4 * TW;
constexpr int L_ZF = L_KVF + 2048;
constexpr int L_VEC = L_ZF + 256;
constexpr int L_CF = L_VEC + 768;
constexpr int L_HINT = L_CF + 3072;
constexpr int LDS_BYTES = L_HINT + 256;
static_assert(LDS_BYTES <= 163840, "LDS");
constexpr int P_KV = 0, P_Q = 64, P_WO = 96, P_GU = 160, P_WD = 352;
constexpr int H_CP = 6, H_PP = 7, H_C0 = 8, H_P0 = 9;
constexpr int pair_of(int t) { return ((t & 15) < 11 ? 3 * (t & 15) : 2 * (t & 15) + 11) + (t >> 4); }
constexpr int slot(int n) { return (n % RING) * 1024; }
constexpr int kv_piece(int u) { return u < 45 ? (u % 3 < 2 ? 2 * (2 + 2 * (u / 3) + u % 3 - 2) : 2 * (u / 3) + 1) : u == 45 ? 60 : u == 46 ? 62 : 2 * (u - 32) + 1; }
constexpr int AUX_SC1 = 16;
constexpr u32 SPIN_CAP = 1u << 19, LDS_CAP = 1u << 23;
constexpr int PRE_MOD = 20;
constexpr int PRE_FL = 4;
constexpr int KV1 = 32;
constexpr int PGU = 4;
constexpr int GPUB = 26;
constexpr int LPRIO = 0;
constexpr int APRIO = 3;
constexpr int CSLP = 3;
constexpr int PG2 = 2;
constexpr int PQE = 2;
constexpr int GASK = 158;
constexpr int PUB2 = 24;
constexpr int GU0 = 12;
constexpr int GA2 = 18;
constexpr int GU2 = 50;
constexpr int W1N = 8;
constexpr int W2N = 6;
constexpr int PG3 = 4;
constexpr int CT = 3;
constexpr int CM = 15;
constexpr int W0 = 12;
constexpr int PDV = 5;
constexpr int OAUX = 2;
constexpr int PD = PDV;
constexpr int CODE_BYTES = 46000;

struct Params {
  const u16 *x, *wqkv, *wphi, *wo, *wg, *wu, *wd, *c, *wmod;
  u16* out;
  unsigned char* ws;
};

template <int V> struct IC { static constexpr int value = V; };
template <int I, int N, class Fn> DI void static_for(Fn&& f) {
  if constexpr (I < N) { f(IC<I>{}); static_for<I + 1, N>(f); }
}
DI float lo16(u32 v) { return __uint_as_float(v << 16); }
DI float hi16(u32 v) { return __uint_as_float(v & 0xffff0000u); }
DI u32 pk(float a, float b) { f32x2 v = {a, b}; return __builtin_bit_cast(u32, __builtin_convertvector(v, bf16x2)); }
DI float rb(float a) { return lo16(pk(a, 0.f)); }
DI float rcp(float a) { return __builtin_amdgcn_rcpf(a); }
DI float silu_bf(float c) {
  const float d = 1.f + __expf(-c), r = rcp(d), q = c * r, q2 = __builtin_fmaf(__builtin_fmaf(-q, d, c), r, q);
  return rb(q2 == q2 ? q2 : q);
}
DI f32x16 mfma(v4u a, v4u b, f32x16 c) {
  return __builtin_amdgcn_mfma_f32_32x32x16_bf16(__builtin_bit_cast(bf16x8, a), __builtin_bit_cast(bf16x8, b), c, 0, 0, 0);
}
DI void mfma_pin(v4u a, v4u b, f32x16& c) {
  c = mfma(a, b, c);
  asm volatile("" : "+v"(c));
}
template <int O> DI v4u pack8(const f32x16& x) {
  v4u r; r.x = pk(x[O], x[O + 1]); r.y = pk(x[O + 2], x[O + 3]); r.z = pk(x[O + 4], x[O + 5]); r.w = pk(x[O + 6], x[O + 7]); return r;
}
template <int T> DI v4u ldw(LAS unsigned char* p) {
  s16x4 a = __builtin_amdgcn_ds_read_tr16_b64_v4i16((LAS s16x4*)p);
  s16x4 b = __builtin_amdgcn_ds_read_tr16_b64_v4i16((LAS s16x4*)(p + T));
  return __builtin_bit_cast(v4u, __builtin_shufflevector(a, b, 0, 1, 2, 3, 4, 5, 6, 7));
}
struct Base3 {
  u32 b[3];
  DI explicit Base3(LAS unsigned char* p) {
    b[0] = (u32)(size_t)p; b[1] = b[0] + 65536u; b[2] = b[0] + 131072u;
    asm volatile("" : "+v"(b[0]), "+v"(b[1]), "+v"(b[2]));
  }
  template <int OFF> DI LAS unsigned char* at() const { return (LAS unsigned char*)(size_t)b[OFF >> 16] + (OFF & 65535); }
};
template <int LOFF, int SOFF> DI void gldsi(rsrc_t rs, u32 voff, u32 sbase, u32 lbase) {
  u32 t;
  asm volatile("s_add_u32 m0, %1, %2\n\ts_add_u32 %0, %3, %4\n\tbuffer_load_dwordx4 %5, %6, %0 offen lds"
               : "=&s"(t) : "s"(lbase), "i"(LOFF), "s"(sbase), "i"(SOFF), "v"(voff), "s"(rs) : "m0", "scc", "memory");
}
DI void bar() { asm volatile("s_waitcnt lgkmcnt(0)\n\ts_barrier" ::: "memory"); }
DI void bar_raw() { asm volatile("s_barrier" ::: "memory"); }
DI void drain() { asm volatile("s_waitcnt vmcnt(0)" ::: "memory"); }
#define VMCNT(n) asm volatile("s_waitcnt vmcnt(" #n ")" ::: "memory")
#define RELOAD() asm volatile("" ::: "memory")
DI v4u ld128(rsrc_t rs, int voff, int soff) { return __builtin_amdgcn_raw_buffer_load_b128(rs, voff, soff, AUX_SC1); }
DI void st128(v4u v, rsrc_t rs, int voff, int soff) { __builtin_amdgcn_raw_buffer_store_b128(v, rs, voff + soff, 0, AUX_SC1); }
DI void st64(v2u v, rsrc_t rs, int voff, int soff) { __builtin_amdgcn_raw_buffer_store_b64(v, rs, voff, soff, AUX_SC1); }
template <int S> DI v4u ldfrag(rsrc_t rs, int voff, int soff) { return ld128(rs, voff + (S & 3) * 1024, soff + (S >> 2) * 4096); }
template <int W, int N> DI void ldfrags(v4u (&a)[N], rsrc_t rs, int voff, int soff) {
  static_for<0, W>([&](auto S) { a[S.value] = ldfrag<S.value>(rs, voff, soff); SB0(); });
}
template <int CTRL> DI float dpp_add(float v) {
  return v + __uint_as_float(__builtin_amdgcn_update_dpp(0u, __float_as_uint(v), CTRL, 0xf, 0xf, false));
}
constexpr int DPP_X1 = 0xB1, DPP_X2 = 0x4E, DPP_HM = 0x141, DPP_ROR4 = 0x124, DPP_ROR8 = 0x128;
DI float fold32(float x, float y) {
  auto r = __builtin_amdgcn_permlane32_swap(__float_as_uint(x), __float_as_uint(y), false, false);
  return __uint_as_float(r[0]) + __uint_as_float(r[1]);
}
DI float fold16(float x, float y) {
  auto r = __builtin_amdgcn_permlane16_swap(__float_as_uint(x), __float_as_uint(y), false, false);
  return __uint_as_float(r[0]) + __uint_as_float(r[1]);
}
DI void red8q(const float (&a)[8], float& c0, float& c1) {
  const float b0 = fold32(a[0], a[4]), b1 = fold32(a[1], a[5]), b2 = fold32(a[2], a[6]), b3 = fold32(a[3], a[7]);
  c0 = fold16(b0, b2); c1 = fold16(b1, b3);
  c0 = dpp_add<DPP_ROR4>(c0); c1 = dpp_add<DPP_ROR4>(c1);
  c0 = dpp_add<DPP_ROR8>(c0); c1 = dpp_add<DPP_ROR8>(c1);
}
DI void publish(rsrc_t rs, int word, int bit, int lane) {
  drain();
  (void)__builtin_amdgcn_raw_ptr_buffer_atomic_add_i32(1 << bit, rs, lane == 0 ? 0 : WS_BYTES, OFF_ARR + word * 128, 0);
}
DI u32 ask(rsrc_t rs, int word, int lane) { return __builtin_amdgcn_raw_ptr_buffer_atomic_add_i32(0, rs, lane == 0 ? 0 : WS_BYTES, OFF_ARR + word * 128, 0); }
DI void spin(rsrc_t rs, int word, u32 m, int lane) {
#pragma nounroll
  for (u32 spins = 0; spins < SPIN_CAP; ++spins)
    if ((__builtin_amdgcn_readfirstlane(ask(rs, word, lane)) & m) == m) break;
}
DI void poll(u32 f, rsrc_t rs, int word, u32 m, int lane) {
  if (__builtin_expect((__builtin_amdgcn_readfirstlane(f) & m) != m, 0)) spin(rs, word, m, lane);
  __builtin_amdgcn_fence(__ATOMIC_ACQUIRE, "wavefront");
  RELOAD();
}
DI void waitn(rsrc_t rs, int word, u32 m, int lane) {
  if (PRE_FL) __builtin_amdgcn_s_sleep(PRE_FL);
  spin(rs, word, m, lane);
  __builtin_amdgcn_fence(__ATOMIC_ACQUIRE, "wavefront");
  RELOAD();
}
DI void sig(LAS unsigned char* L, int i, int k) { *(volatile LAS u32*)(L + L_HINT + 16 * i + 4 * k) = 1u; }
template <int N> DI void await(LAS unsigned char* L, int i) {
#pragma nounroll
  for (u32 spins = 0; spins < LDS_CAP; ++spins) {
    const v4u v = *(volatile LAS v4u*)(L + L_HINT + 16 * i);
    if (__builtin_amdgcn_readfirstlane(N == 1 ? v.x : (v.x & v.y & v.z & v.w))) break;
    __builtin_amdgcn_s_sleep(1);
  }
  RELOAD();
}
DI void mark(LAS unsigned char* L, int i, int k, u32 val) { *(volatile LAS u32*)(L + L_HINT + 16 * i + 4 * k) = val; }
DI v4u peek(LAS unsigned char* L, int i) { return *(volatile LAS v4u*)(L + L_HINT + 16 * i); }
DI u32 umin(u32 a, u32 b) { return a < b ? a : b; }
DI void await_cp(LAS unsigned char* L, u32 k) {
#pragma nounroll
  for (u32 spins = 0; spins < LDS_CAP; ++spins) {
    const v4u v = peek(L, H_CP);
    if (__builtin_amdgcn_readfirstlane(umin(umin(v.x, v.y), umin(v.z, v.w))) >= k) break;
    __builtin_amdgcn_s_sleep(CSLP);
  }
  RELOAD();
}
template <int SLP> DI void reach(LAS unsigned char* L, int i, u32 k0, u32 k1, u32 k2, u32 k3) {
#pragma nounroll
  for (u32 spins = 0; spins < LDS_CAP; ++spins) {
    const v4u v = peek(L, i);
    if (__builtin_amdgcn_readfirstlane((v.x >= k0) & (v.y >= k1) & (v.z >= k2) & (v.w >= k3))) break;
    if (SLP) __builtin_amdgcn_s_sleep(SLP);
  }
  RELOAD();
}
DI void reach1(LAS unsigned char* L, int i, int k, u32 val) {
#pragma nounroll
  for (u32 spins = 0; spins < LDS_CAP; ++spins)
    if (__builtin_amdgcn_readfirstlane(*(volatile LAS u32*)(L + L_HINT + 16 * i + 4 * k)) >= val) break;
  RELOAD();
}
#define MARK(i, k, val) do { RELOAD(); mark(L, i, k, val); RELOAD(); } while (0)
template <bool AUX> DI u32 pp_min(v4u v) {
  u32 m = umin(v.x, v.y);
  if (AUX) m = umin(m, umin(v.z, v.w));
  return __builtin_amdgcn_readfirstlane(m);
}
template <bool AUX> DI void need_pp(LAS unsigned char* L, v4u v, u32 k) {
  if (__builtin_expect(pp_min<AUX>(v) < k, 0)) {
#pragma nounroll
    for (u32 spins = 0; spins < LDS_CAP; ++spins)
      if (pp_min<AUX>(peek(L, H_PP)) >= k) break;
  }
  RELOAD();
}
template <class Fn> DI void feat(const f32x16& P, u32 (&ph)[16], Fn&& fill) {
#pragma clang fp contract(off)
  constexpr float L2E = 1.4426950408889634f;
  f32x2 p[8];
  float mx[4], mn[4];
  static_for<0, 4>([&](auto I) {
    constexpr int i = I.value;
    const u32 t0 = pk(P[4 * i], P[4 * i + 1]), t1 = pk(P[4 * i + 2], P[4 * i + 3]);
    p[2 * i] = f32x2{lo16(t0), hi16(t0)} * L2E; p[2 * i + 1] = f32x2{lo16(t1), hi16(t1)} * L2E;
    mx[i] = fmaxf(fmaxf(p[2 * i].x, p[2 * i].y), fmaxf(p[2 * i + 1].x, p[2 * i + 1].y));
    mn[i] = fminf(fminf(p[2 * i].x, p[2 * i].y), fminf(p[2 * i + 1].x, p[2 * i + 1].y));
    fill(IC<i>{});
  });
  const float mxl = fmaxf(fmaxf(mx[0], mx[1]), fmaxf(mx[2], mx[3])), mnl = fminf(fminf(mn[0], mn[1]), fminf(mn[2], mn[3]));
  f32x2 e1[8], e2[8];
#pragma unroll
  for (int i = 0; i < 8; ++i) { e1[i] = p[i] - mxl; e2[i] = mnl - p[i]; }
  SB0();
  static_for<0, 8>([&](auto I) {
    constexpr int i = I.value;
    fill(IC<4 + i>{});
    e1[i] = f32x2{__builtin_amdgcn_exp2f(e1[i].x), __builtin_amdgcn_exp2f(e1[i].y)};
    e2[i] = f32x2{__builtin_amdgcn_exp2f(e2[i].x), __builtin_amdgcn_exp2f(e2[i].y)};
  });
  fill(IC<12>{});
  const f32x2 s1 = ((e1[0] + e1[1]) + (e1[2] + e1[3])) + ((e1[4] + e1[5]) + (e1[6] + e1[7]));
  fill(IC<13>{});
  const f32x2 s2 = ((e2[0] + e2[1]) + (e2[2] + e2[3])) + ((e2[4] + e2[5]) + (e2[6] + e2[7]));
  fill(IC<14>{});
  const float r1 = rcp(s1.x + s1.y), r2 = rcp(s2.x + s2.y);
  fill(IC<15>{});
#pragma unroll
  for (int i = 0; i < 8; ++i) { const f32x2 u = e1[i] * r1, v = e2[i] * r2; ph[i] = pk(u.x, u.y); ph[8 + i] = pk(v.x, v.y); }
}


__global__ void __launch_bounds__(512, 2) mega(Params p) {
  extern __shared__ __attribute__((aligned(16))) unsigned char smem[];
  LAS unsigned char* L = (LAS unsigned char*)smem;
  u64 pc0;
  asm volatile("s_getpc_b64 %0" : "=s"(pc0));
  const int tid = threadIdx.x, lane = tid & 63;
  int w = __builtin_amdgcn_readfirstlane(tid >> 6);
  {
    const rsrc_t rc = __builtin_amdgcn_make_buffer_rsrc((void*)pc0, 0, CODE_BYTES, 0x00020000);
    gldsi<slot(128), 0>(rc, lane < CT ? (u32)lane * 128u : 1u << 30, (u32)((((blockIdx.x >> 3) & CM) * 8 + w) * CT * 128), (u32)(size_t)L + w * 1024);
    SB0();
  }
  const int b = blockIdx.x, xcd = b & 7, rank = b >> 3, panel = (xcd >> 1) * 4 + (rank >> 3), mem = (xcd & 1) * 8 + (rank & 7);
  const int hh = lane >> 5, l31 = lane & 31;
  unsigned char* ws = p.ws;
  const int rowh = mem & 1, colg = mem >> 1;
  const int pair0 = min(3 * mem, 2 * mem + 11);
  const bool three = mem < 11;
  const rsrc_t rs = __builtin_amdgcn_make_buffer_rsrc(ws, 0, WS_BYTES, 0x00020000);
  const int pb = OFF_PAN + panel * PSTR, fw = panel * 24;
  const int mcg = xcd * 8 + (rank >> 2), mkp = rank & 3;
  const rsrc_t rqkv = __builtin_amdgcn_make_buffer_rsrc((void*)p.wqkv, 0, D * 3 * D * 2, 0x00020000);
  const rsrc_t rwo = __builtin_amdgcn_make_buffer_rsrc((void*)p.wo, 0, D * D * 2, 0x00020000);
  const rsrc_t rgu = __builtin_amdgcn_make_buffer_rsrc((void*)((w & 1) ? p.wu : p.wg), 0, D * F * 2, 0x00020000);
  const rsrc_t rwd = __builtin_amdgcn_make_buffer_rsrc((void*)p.wd, 0, F * D * 2, 0x00020000);

  v4u r0[4];
  u32 cv0[4], epoch;
  u32 vq = (lane >> 2) * (3 * D * 2) + (lane & 3) * 16;
  asm volatile("" : "+v"(vq));
  {
    const u32 ld = (u32)(size_t)L + w * 1024;
    {
      const rsrc_t r2 = __builtin_amdgcn_make_buffer_rsrc((void*)p.c, 0, w == 4 ? D * 2 : 0, 0x00020000);
      const v2u c2 = __builtin_amdgcn_raw_buffer_load_b64(r2, (128 * mkp + 4 * (lane >> 1)) * 2, 0, 0);
      cv0[0] = c2.x; cv0[1] = c2.y;
      SB0();
    }
    if (w <= 4) {
      const rsrc_t r1 = __builtin_amdgcn_make_buffer_rsrc((void*)(w == 4 ? p.wmod : p.x), 0, w == 4 ? D * 6 * D * 2 : 2048 * D * 2, 0x00020000);
      const int vo = w == 4 ? (128 * mkp + 4 * (lane >> 1)) * (6 * D * 2) + mcg * 32 + (lane & 1) * 16
                            : (panel * 128 + mem * 8 + (lane >> 3)) * (D * 2) + (lane & 7) * 16 + 256 * w;
#pragma unroll
      for (int i = 0; i < 4; ++i) r0[i] = __builtin_amdgcn_raw_buffer_load_b128(r1, vo, w == 4 ? i * (6 * D * 2) : i < 2 ? i * 128 : 1 << 30, 0);
      SB0();
    }
    {
      epoch = __builtin_amdgcn_raw_buffer_load_b32(rs, 0, OFF_EPOCH, AUX_SC1);
      __builtin_amdgcn_raw_buffer_store_b32(0u, rs, (w == 4 && lane < 2) ? lane * 128 : WS_BYTES, OFF_ARR + b * 256, AUX_SC1);
      SB0();
    }
    if (w >= 5) {
      const u32 tq = (rank * 16 + (lane >> 2)) * (3 * D * 2) + (xcd & 1) * 512 + (lane & 3) * 128;
      gldsi<slot(128), 0>(rqkv, tq, (w - 5) * (D * 2), ld);
      if (w == 5) {
        gldsi<slot(128), 0>(rs, lane < 2 ? (u32)(pb - 2048 + lane * PSTR) : 1u << 30, 0u, ld);
        const rsrc_t rp = __builtin_amdgcn_make_buffer_rsrc((void*)p.wphi, 0, 32 * 256 * 2, 0x00020000);
        gldsi<L_CF + 2048, 0>(rp, lane * 16, mem * 1024, (u32)(size_t)L);
      }
    }
    if (tid < 64) *((LAS u32*)(L + L_HINT) + tid) = 0u;
    bar();
    asm volatile("" : "+s"(w));
    if (w >= 6) {
      u32 lo = (u32)(size_t)L + (w & 1) * 1024, so = (D + (w & 1) * D + mem * 32) * 2;
#pragma nounroll
      for (int i = 0; i < KV1 / 2; ++i) {
        gldsi<0, 0>(rqkv, vq, so, lo);
        gldsi<2048, 16 * (3 * D * 2)>(rqkv, vq, so, lo);
        lo += 4096; so += 32 * (3 * D * 2);
      }
    }
    if (w == 4) {
      __builtin_amdgcn_s_setprio(3);
      float sl[4], acc[8];
#pragma unroll
      for (int i = 0; i < 4; ++i) { const float c = (i & 1) ? hi16(cv0[i >> 1]) : lo16(cv0[i >> 1]); sl[i] = silu_bf(c); }
      u32 tag = epoch;
      asm volatile("" : "+v"(tag));
      tag += 1u;
#pragma unroll
      for (int j = 0; j < 8; ++j) acc[j] = 0.f;
#pragma unroll
      for (int i = 0; i < 4; ++i)
#pragma unroll
        for (int j = 0; j < 4; ++j) { acc[2 * j] += sl[i] * lo16(r0[i][j]); acc[2 * j + 1] += sl[i] * hi16(r0[i][j]); }
      float c0, c1;
      red8q(acc, c0, c1);
      c0 = dpp_add<DPP_X2>(c0); c1 = dpp_add<DPP_X2>(c1);
      const int go = ((lane >> 1) & 7) * 32768 + (8 * (lane & 1) + 2 * (lane >> 4)) * 8;
      const v4u gv = {__float_as_uint(c0), tag, __float_as_uint(c1), tag};
      drain();
      st128(gv, rs, go, OFF_MODR + mkp * 8192 + mcg * 128);
      st128(gv, rs, go, OFF_MODR + 8 * 32768 + mkp * 8192 + mcg * 128);
      sig(L, 0, 0);
    }
    asm volatile("" : "+s"(w));
  }

  if (w == 4 || w == 5) {
    const int a = w & 1;
    u32 tag;
    tag = epoch;
    asm volatile("" : "+v"(tag));
    tag += 1u;
    drain();
    bar();
    __builtin_amdgcn_s_setprio(APRIO);
    await<4>(L, 2);
    const int kp = xcd >> 1, cpair = (xcd & 1) * 24 + rank;
    const bool con = rank < ((xcd & 1) ? 19 : 24);
    const int col0 = a * F + cpair * 32;
    v4u wm[8];
    if (a == 1) {
      v4u wn[8]; u32 cv[8];
      const int row0 = 128 * mkp + (lane >> 2);
#pragma unroll
      for (int i = 0; i < 8; ++i) cv[i] = p.c[row0 + 16 * i];
#pragma unroll
      for (int i = 0; i < 8; ++i) wn[i] = *(const GAS v4u*)(p.wmod + (size_t)(row0 + 16 * i) * (6 * D) + 1024 + mcg * 32 + (lane & 3) * 8);
      SB0();
      float acc[8];
#pragma unroll
      for (int j = 0; j < 8; ++j) acc[j] = 0.f;
#pragma unroll
      for (int i = 0; i < 8; ++i) {
        const float c = lo16(cv[i]), sl = silu_bf(c);
#pragma unroll
        for (int j = 0; j < 4; ++j) { acc[2 * j] += sl * lo16(wn[i][j]); acc[2 * j + 1] += sl * hi16(wn[i][j]); }
      }
      float c0, c1;
      red8q(acc, c0, c1);
      st128(v4u{__float_as_uint(c0), tag, __float_as_uint(c1), tag}, rs, (lane & 12) == 0 ? (8 * (lane & 3) + 2 * (lane >> 4)) * 8 : WS_BYTES,
            OFF_MODG + mkp * 16384 + mcg * 256);
    }
    reach<1>(L, H_C0, 2u, 2u, 2u, 2u);
    {
      const int idx0 = (mem * 4 + 2 * a) * 16;
      v4u e[32], zz[2];
      {
        u32 todo = 0xFFu;
        for (u32 spins = 0; spins < SPIN_CAP && todo; ++spins) {
          const u32 bad = ~__builtin_amdgcn_readfirstlane(ask(rs, FL_KV + mem * 2 + a, lane));
          __builtin_amdgcn_fence(__ATOMIC_ACQUIRE, "wavefront");
          RELOAD();
          static_for<0, 8>([&](auto G) {
            constexpr int g = G.value;
            if (((todo >> g) & 1u) && !(bad & (0x00030003u << (2 * g)))) {
              todo &= ~(1u << g);
              e[2 * g] = ld128(rs, lane * 16, OFF_KVE + (idx0 + 2 * g) * 1024);
              e[2 * g + 1] = ld128(rs, lane * 16, OFF_KVE + (idx0 + 2 * g + 1) * 1024);
              e[16 + 2 * g] = ld128(rs, lane * 16, OFF_KVE + (idx0 + 16 + 2 * g) * 1024);
              e[17 + 2 * g] = ld128(rs, lane * 16, OFF_KVE + (idx0 + 17 + 2 * g) * 1024);
            }
          });
        }
      }
      zz[0] = ldfrag<0>(rs, lane * 16, OFF_KVZ + idx0 * 64);
      zz[1] = ldfrag<1>(rs, lane * 16, OFF_KVZ + idx0 * 64);
      SB0();
#pragma unroll
      for (int q = 0; q < 2; ++q) {
        f32x4 s0 = __builtin_bit_cast(f32x4, e[16 * q]), s1 = __builtin_bit_cast(f32x4, e[16 * q + 1]);
#pragma unroll
        for (int i = 2; i < 16; i += 2) { s0 += __builtin_bit_cast(f32x4, e[16 * q + i]); s1 += __builtin_bit_cast(f32x4, e[16 * q + i + 1]); }
        s0 += s1;
        *(LAS v2u*)(L + L_KVF + (2 * a + q) * 512 + l31 * 16 + hh * 8) = v2u{pk(s0[0], s0[1]), pk(s0[2], s0[3])};
      }
      f32x4 z;
#pragma unroll
      for (int j = 0; j < 4; ++j) {
        float t = fold32(__uint_as_float(zz[0][j]), __uint_as_float(zz[1][j]));
        t = fold16(t, t);
        t = dpp_add<DPP_ROR4>(t);
        z[j] = dpp_add<DPP_ROR8>(t);
      }
      if (l31 < 4) *(LAS v2u*)(L + L_ZF + (2 * a + hh) * 32 + ((lane & 1) ^ 1) * 16 + ((lane >> 1) & 1) * 8) = v2u{pk(z[0], z[1]), pk(z[2], z[3])};
    }
    MARK(H_P0, 2 + a, 1u);
    __builtin_amdgcn_s_setprio(1);
#pragma unroll
    for (int i = 0; i < 8; ++i)
      wm[i] = __builtin_amdgcn_raw_buffer_load_b128(rgu, con ? (128 * kp + 2 * (lane >> 2)) * (F * 2) + cpair * 64 + (lane & 3) * 16 : 1 << 30, (32 * (i >> 1) + (i & 1)) * (F * 2), 0);
    SB0();
    u32 gsb[4], gsc[4];
    {
      const int vo = ((hh ? 2 : 3 * a) * D + colg * 64 + 2 * l31) * 8, ro = (D + 128 * kp + 2 * lane) * 8;
      v4u g[12];
      for (u32 spins = 0; spins < SPIN_CAP; ++spins) {
        RELOAD();
#pragma unroll
        for (int i = 0; i < 12; ++i) g[i] = ld128(rs, i < 4 ? vo : ro + (i >> 3) * (D * 8), OFF_MODG + (i & 3) * 16384);
        bool ok = true;
#pragma unroll
        for (int i = 0; i < 12; ++i) ok &= (g[i].y == tag) & (g[i].w == tag);
        if (__all(ok)) break;
      }
      u32 pr[3];
#pragma unroll
      for (int t = 0; t < 3; ++t)
        pr[t] = pk((__uint_as_float(g[4 * t].x) + __uint_as_float(g[4 * t + 1].x)) + (__uint_as_float(g[4 * t + 2].x) + __uint_as_float(g[4 * t + 3].x)),
                   (__uint_as_float(g[4 * t].z) + __uint_as_float(g[4 * t + 1].z)) + (__uint_as_float(g[4 * t + 2].z) + __uint_as_float(g[4 * t + 3].z)));
      const u32 v1 = pk(1.f + lo16(pr[0]), 1.f + hi16(pr[0])), vv = hh ? v1 : pr[0];
      *(LAS f32x2*)(L + L_VEC + (hh ? 1 : 2 * a) * 256 + l31 * 8) = f32x2{lo16(vv), hi16(vv)};
      const u32 sc = pk(1.f + lo16(pr[2]), 1.f + hi16(pr[2]));
#pragma unroll
      for (int i = 0; i < 4; ++i) {
        gsb[i] = __builtin_amdgcn_ds_bpermute((16 * i + (lane >> 2)) * 4, pr[1]);
        gsc[i] = __builtin_amdgcn_ds_bpermute((16 * i + (lane >> 2)) * 4, sc);
      }
    }
    MARK(H_P0, 2 + a, 2u);
    if (con) {
      float a1[8], a2[8];
#pragma unroll
      for (int j = 0; j < 8; ++j) { a1[j] = 0.f; a2[j] = 0.f; }
#pragma unroll
      for (int i = 0; i < 8; ++i) {
        const float sb = (i & 1) ? hi16(gsb[i >> 1]) : lo16(gsb[i >> 1]), sc = (i & 1) ? hi16(gsc[i >> 1]) : lo16(gsc[i >> 1]);
#pragma unroll
        for (int j = 0; j < 4; ++j) {
          a1[2 * j] += sc * lo16(wm[i][j]); a1[2 * j + 1] += sc * hi16(wm[i][j]);
          a2[2 * j] += sb * lo16(wm[i][j]); a2[2 * j + 1] += sb * hi16(wm[i][j]);
        }
      }
      float c10, c11, c20, c21;
      red8q(a1, c10, c11); red8q(a2, c20, c21);
      const int k = (lane >> 2) & 3;
      const float val = k == 0 ? c10 : k == 1 ? c20 : k == 2 ? c11 : c21;
      st64(v2u{__float_as_uint(val), tag}, rs, (col0 + 8 * (lane & 3) + 2 * (lane >> 4)) * 16 + k * 8, OFF_CG + kp * CG_PART);
    }
    reach<2>(L, H_C0, 4u, 4u, 4u, 4u);
    {
      const int ntile = three ? 6 : 4;
      v4u cg[8];
      int off[2];
#pragma unroll
      for (int i = 0; i < 2; ++i) {
        int tile = 4 * i + 2 * a + hh; tile = tile < ntile ? tile : 0;
        off[i] = (32 * pair0 + 16 * tile + (lane & 15) + ((lane >> 4) & 1) * F) * 16;
      }
      for (u32 spins = 0; spins < SPIN_CAP; ++spins) {
        RELOAD();
        bool ok = true;
#pragma unroll
        for (int i = 0; i < 8; ++i) cg[i] = ld128(rs, off[i >> 2], OFF_CG + (i & 3) * CG_PART);
#pragma unroll
        for (int i = 0; i < 8; ++i) ok &= (cg[i].y == tag) & (cg[i].w == tag);
        if (__all(ok)) break;
      }
#pragma unroll
      for (int i = 0; i < 2; ++i) {
        const float c1 = (__uint_as_float(cg[4 * i].x) + __uint_as_float(cg[4 * i + 1].x)) + (__uint_as_float(cg[4 * i + 2].x) + __uint_as_float(cg[4 * i + 3].x));
        const float c2 = (__uint_as_float(cg[4 * i].z) + __uint_as_float(cg[4 * i + 1].z)) + (__uint_as_float(cg[4 * i + 2].z) + __uint_as_float(cg[4 * i + 3].z));
        const float h1 = rb(c1), m1 = rb(c1 - h1), l1 = c1 - h1 - m1, h2 = rb(c2), m2 = rb(c2 - h2), l2 = c2 - h2 - m2;
        const int tile = 4 * i + 2 * a + hh;
        if (tile < 6) *(LAS v4u*)(L + L_CF + tile * 512 + l31 * 16) = v4u{pk(h1, m1), pk(l1, h2), pk(m2, l2), 0u};
      }
    }
    reach<1>(L, 5, a < 1, a < 1, a, a);
    {
      v4u st[16];
#pragma unroll
      for (int t = 0; t < 16; ++t) st[t] = ld128(rs, l31 * 16 + hh * 8192 + t * 512, pb + PN_ST + 2 * a * 8192);
      for (u32 spins = 0; spins < SPIN_CAP; ++spins) {
        bool ok = true;
#pragma unroll
        for (int t = 0; t < 16; ++t) ok &= (st[t].y == tag) & (st[t].w == tag);
        if (__all(ok)) break;
        RELOAD();
#pragma unroll
        for (int t = 0; t < 16; ++t) st[t] = ld128(rs, l31 * 16 + hh * 8192 + t * 512, pb + PN_ST + 2 * a * 8192);
      }
      float ms = 0.f;
#pragma unroll
      for (int t = 0; t < 16; ++t) ms += __uint_as_float(st[t].x);
      const float mean = ms * (1.f / 16.f);
      float q = 0.f;
#pragma unroll
      for (int t = 0; t < 16; ++t) { const float d = __uint_as_float(st[t].x) - mean; q += __uint_as_float(st[t].z) + 32.f * d * d; }
      const float den = rb(__builtin_amdgcn_sqrtf(rb(rb(q * (1.f / D)) + 1e-5f)));
      *(LAS v4u*)(L + L_KVF + a * 1024 + lane * 16) = v4u{pk(-mean, -mean), pk(-mean, den), pk(den, den), __float_as_uint(rcp(den * den))};
    }
    RELOAD();
    mark(L, H_PP, 2 + a, 5u);
    await_cp(L, 2);
    {
      v4u wt[58];
      const u32 vd = (lane >> 2) * (D * 2) + (lane & 3) * 16, sd = a * 16 * (D * 2) + colg * 128;
      const Base3 wdst(L + a * 2048 + lane * 16);
      static_for<28, 86>([&](auto J) {
        wt[J.value - 28] = __builtin_amdgcn_raw_buffer_load_b128(rwd, vd + (J.value & 1) * 64, sd + pair_of(J.value >> 1) * 32 * (D * 2), 0); SB0(); });
#define ST_WT(T0, T1, k) do { static_for<2 * (T0), 2 * (T1)>([&](auto J) { \
      *(LAS v4u*)wdst.template at<slot(P_WD + 4 * (J.value >> 1)) + (J.value & 1) * 1024>() = wt[J.value - 28]; }); \
      RELOAD(); mark(L, H_PP, 2 + a, k); RELOAD(); } while (0)
      await_cp(L, 4);
      ST_WT(14, 18, 6u);
      await_cp(L, 5);
      ST_WT(18, 26, 7u);
      await_cp(L, 6);
      ST_WT(26, 34, 8u);
      await_cp(L, 7);
      ST_WT(34, 43, 9u);
    }
    return;
  }

  if (w >= 6) {
    const int v = w & 1;
    __builtin_amdgcn_s_setprio(LPRIO);
    const u32 ldsb = (u32)(size_t)L;
    const u32 l1 = ldsb + v * 1024, l2 = ldsb + v * 2048;
    const u32 skv = (D + v * D + mem * 32) * 2, sq = v * 16 * (3 * D * 2) + mem * 64;
    const u32 so = 16 * v * 16 * (D * 2) + colg * 128, sd = v * 16 * (D * 2) + colg * 128;
    const u32 sg = pair0 * 64, sg2 = three ? sg + 128 : 1u << 30;
#define LD_KV(S0, S1, PC) static_for<S0, S1>([&](auto S) { gldsi<slot(P_KV + 2 * S.value), S.value * 16 * (3 * D * 2)>(rqkv, vq, skv, l1); PACE(PC); })
#define PACE(n) do { if ((n) > 0) __builtin_amdgcn_s_sleep(n); } while (0)
#define LD_Q(S0, S1, PC) static_for<S0, S1>([&](auto S) { gldsi<slot(P_Q + 2 * S.value), 2 * S.value * 16 * (3 * D * 2)>(rqkv, vq, sq, l1); PACE(PC); })
#define LD_DN(RS, SB, P0, T0, T1, PC)                                                                    \
  static_for<T0, T1>([&](auto T) {                                                                   \
    gldsi<slot((P0) + 4 * T.value), T.value * 16 * (D * 2)>(RS, vd, SB, l2);                         \
    gldsi<slot((P0) + 4 * T.value) + 1024, T.value * 16 * (D * 2) + 64>(RS, vd, SB, l2); PACE(PC);   \
  })
#define LD_GU(S0, S1, PC)                                                                                \
  static_for<S0, S1>([&](auto S) {                                                                   \
    gldsi<slot(P_GU + 6 * S.value), S.value * 16 * (F * 2)>(rgu, vg, sg, l1);                        \
    gldsi<slot(P_GU + 6 * S.value + 2), S.value * 16 * (F * 2) + 64>(rgu, vg, sg, l1);               \
    gldsi<slot(P_GU + 6 * S.value + 4), S.value * 16 * (F * 2)>(rgu, vg, sg2, l1); PACE(PC);         \
  })
    VMCNT(0);
    SB0();
    int ln;
    asm volatile("v_mbcnt_lo_u32_b32 %0, -1, 0\n\tv_mbcnt_hi_u32_b32 %0, -1, %0" : "=v"(ln));
    const u32 vd = (ln >> 2) * (D * 2) + (ln & 3) * 16;
    const u32 vg = (ln >> 2) * (F * 2) + (((ln & 3) ^ (v << 1)) * 16);
    LD_Q(0, 16, PQE);
    bar_raw();
    v4u wt[36];
    const Base3 wdst1(L + v * 1024 + ln * 16);
#define GUN(s_, P_) (P_GU + ((s_) < 12 ? 6 * (s_) + 2 * (P_) : 72 + 40 * (P_) + 2 * ((s_) - 12)))
#define GUR(s_, P_) __builtin_amdgcn_raw_buffer_load_b128(rgu, vg, ((P_) == 2 ? sg2 : sg + (P_) * 64) + (s_) * 16 * (F * 2), 0)
#define GUW(s_, P_, r_) *(LAS v4u*)wdst1.template at<slot(GUN(s_, P_))>() = wt[r_]
#define LD_GU2(S0, S1, PC) static_for<S0, S1>([&](auto S) { constexpr int s_ = S.value; \
      gldsi<slot(GUN(s_, 0)), s_ * 16 * (F * 2)>(rgu, vg, sg, l1); SB0(); \
      if constexpr (s_ < 24) gldsi<slot(GUN(s_, 1)), s_ * 16 * (F * 2) + 64>(rgu, vg, sg, l1); \
      else wt[20 + s_ - 24] = GUR(s_, 1); \
      SB0(); wt[s_ - 12] = GUR(s_, 2); PACE(PC); SB0(); })
#define LD_WD(T0, T1) static_for<T0, T1>([&](auto T) { \
      gldsi<slot(P_WD + 4 * T.value), pair_of(T.value) * 32 * (D * 2)>(rwd, vd, sd, l2); \
      gldsi<slot(P_WD + 4 * T.value) + 1024, pair_of(T.value) * 32 * (D * 2) + 64>(rwd, vd, sd, l2); })
#define DONE(k) do { RELOAD(); mark(L, H_PP, v, k); RELOAD(); } while (0)
    await<4>(L, 2);
    VMCNT(0);
    MARK(H_P0, v, 1u);
    gldsi<L_CF, 0>(rwo, ln < 32 ? ((2 * rank + v) * 8 + (ln >> 2)) * (D * 2) + (xcd & 1) * 512 + (ln & 3) * 128 : 1u << 30, 0u, l1);
    gldsi<L_CF, 0>(rgu, (ln & 15) < 13 ? ((xcd >> 1) * 128 + 4 * rank + (ln >> 4)) * (F * 2) + (xcd & 1) * 1536 + (ln & 15) * 128 : 1u << 30, 0u, l1);
    LD_DN(rwo, so, P_WO, 0, W1N, 0);
    VMCNT(0);
    MARK(H_P0, v, 1u);
    reach<1>(L, H_C0, 2u, 2u, 2u, 2u);
    LD_DN(rwo, so, P_WO, W1N, 16, 1);
    LD_GU(0, W2N, PGU);
    asm volatile("s_waitcnt vmcnt(%0)" ::"i"(3 * W2N) : "memory");
    MARK(H_P0, v, 2u);
    reach<2>(L, H_C0, 3u, 3u, 3u, 3u);
    reach<2>(L, H_P0, 0u, 0u, 1u, 1u);
    LD_GU(W2N, 12, PG3);
    await<4>(L, 4);
    static_for<12, 20>([&](auto S) { constexpr int s_ = S.value;
      wt[20 + s_ - 12] = GUR(s_, 0); wt[28 + s_ - 12] = GUR(s_, 1); wt[s_ - 12] = GUR(s_, 2); SB0(); });
    VMCNT(24);
    MARK(H_P0, v, 3u);
    reach<1>(L, H_C0, 4u, 4u, 4u, 4u);
    static_for<12, 20>([&](auto S) { GUW(S.value, 0, 20 + S.value - 12); GUW(S.value, 1, 28 + S.value - 12); });
    LD_GU2(20, 32, PG2);
    VMCNT(0);
    DONE(3);
    await_cp(L, 1);
    {
      const u32 tdn = ((2 * rank + v) * 16 + (ln >> 2)) * (D * 2) + (xcd & 1) * 512 + (ln & 3) * 128;
      __builtin_amdgcn_s_waitcnt(0x0F70);
      gldsi<L_T, 0>(rwd, tdn, 0u, l1);
      gldsi<L_T, 1024 * (D * 2)>(rwd, tdn, 0u, l1);
    }
    static_for<24, 32>([&](auto S) { GUW(S.value, 1, 20 + S.value - 24); });
    DONE(4);
    await_cp(L, 2);
    static_for<12, 32>([&](auto S) { GUW(S.value, 2, S.value - 12); });
    DONE(5);
    await_cp(L, 3);
    LD_WD(0, 10);
    await_cp(L, 4);
    LD_WD(10, 14);
    VMCNT(0);
    DONE(100);
    return;
  }

  __builtin_amdgcn_s_setprio(2);
  const int s2 = w & 1, kh = w >> 1;
  const int strip2 = rowh * 2 + s2, ncol = colg * 64 + kh * 32;
  v4u aphi0, aphi1;
  u32 tag;
  {
    float xf[16];
#pragma unroll
    for (int i = 0; i < 8; ++i) { xf[2 * i] = lo16(r0[i >> 2][i & 3]); xf[2 * i + 1] = hi16(r0[i >> 2][i & 3]); }
    float sm = (((xf[0] + xf[1]) + (xf[2] + xf[3])) + ((xf[4] + xf[5]) + (xf[6] + xf[7]))) + (((xf[8] + xf[9]) + (xf[10] + xf[11])) + ((xf[12] + xf[13]) + (xf[14] + xf[15])));
    sm = dpp_add<DPP_X1>(sm); sm = dpp_add<DPP_X2>(sm); sm = dpp_add<DPP_HM>(sm);
    const float mw = sm * (1.f / 128.f);
    float q[4];
#pragma unroll
    for (int i = 0; i < 4; ++i) {
      const float d0 = xf[4 * i] - mw, d1 = xf[4 * i + 1] - mw, d2 = xf[4 * i + 2] - mw, d3 = xf[4 * i + 3] - mw;
      q[i] = (d0 * d0 + d1 * d1) + (d2 * d2 + d3 * d3);
    }
    float qq = (q[0] + q[1]) + (q[2] + q[3]);
    qq = dpp_add<DPP_X1>(qq); qq = dpp_add<DPP_X2>(qq); qq = dpp_add<DPP_HM>(qq);
    *(volatile LAS f32x2*)(L + L_KVF + (lane >> 3) * 32 + w * 8) = f32x2{mw, qq};
    sig(L, 1, w);
    await<4>(L, 1);
    const f32x4 pa = *(LAS f32x4*)(L + L_KVF + (lane >> 3) * 32), pc = *(LAS f32x4*)(L + L_KVF + (lane >> 3) * 32 + 16);
    const float mean = ((pa[0] + pa[2]) + (pc[0] + pc[2])) * 0.25f;
    const float f0 = pa[0] - mean, f1 = pa[2] - mean, f2 = pc[0] - mean, f3 = pc[2] - mean;
    const float m2 = ((pa[1] + pa[3]) + (pc[1] + pc[3])) + 128.f * ((f0 * f0 + f1 * f1) + (f2 * f2 + f3 * f3));
    const float m1 = rb(mean), r1 = rcp(rb(__builtin_amdgcn_sqrtf(rb(rb(m2 * (1.f / D)) + 1e-5f))));
    float xn[16];
#pragma unroll
    for (int i = 0; i < 8; ++i) {
      const u32 d = pk(xf[2 * i] - m1, xf[2 * i + 1] - m1);
      const u32 t = pk(lo16(d) * r1, hi16(d) * r1);
      xn[2 * i] = lo16(t); xn[2 * i + 1] = hi16(t);
    }
#pragma unroll
    for (int i = 0; i < 16; ++i) asm volatile("" : "+v"(xn[i]));
    tag = __builtin_amdgcn_readfirstlane(epoch) + 1u;
    await<1>(L, 0);
    __builtin_amdgcn_s_sleep(PRE_MOD);
    v4u g[8];
    {
      u32 todo = 0xFFu;
      for (u32 spins = 0; spins < SPIN_CAP && todo; ++spins) {
        RELOAD();
        static_for<0, 8>([&](auto I) {
          constexpr int i = I.value;
          if ((todo >> i) & 1u) g[i] = ld128(rs, lane * 16, OFF_MODR + panel * 32768 + (i & 3) * 8192 + (i >> 2) * 4096 + w * 1024);
        });
        todo = 0;
        static_for<0, 8>([&](auto I) { if (__builtin_amdgcn_ballot_w64((g[I.value].y != tag) | (g[I.value].w != tag))) todo |= 1u << I.value; });
      }
    }
    {
      u32 pr[2];
#pragma unroll
      for (int t = 0; t < 2; ++t)
        pr[t] = pk((__uint_as_float(g[4 * t].x) + __uint_as_float(g[4 * t + 1].x)) + (__uint_as_float(g[4 * t + 2].x) + __uint_as_float(g[4 * t + 3].x)),
                   (__uint_as_float(g[4 * t].z) + __uint_as_float(g[4 * t + 1].z)) + (__uint_as_float(g[4 * t + 2].z) + __uint_as_float(g[4 * t + 3].z)));
      *(LAS u32*)(L + L_T + w * TW + lane * 4) = pr[0];
      *(LAS u32*)(L + L_T + w * TW + 256 + lane * 4) = pk(1.f + lo16(pr[1]), 1.f + hi16(pr[1]));
    }
    v4u sav[2], cav[2];
#pragma unroll
    for (int i = 0; i < 2; ++i) {
      sav[i] = *(LAS v4u*)(L + L_T + w * TW + i * 128 + (lane & 7) * 16);
      cav[i] = *(LAS v4u*)(L + L_T + w * TW + 256 + i * 128 + (lane & 7) * 16);
    }
    sig(L, 3, w);
    const int strip = mem >> 2, r = 8 * (mem & 3) + (lane >> 3);
#pragma unroll
    for (int i = 0; i < 2; ++i) {
      v4u o;
#pragma unroll
      for (int j = 0; j < 4; ++j) {
        const u32 sa = sav[i][j], sc = cav[i][j];
        const u32 t = pk(xn[8 * i + 2 * j] * lo16(sc), xn[8 * i + 2 * j + 1] * hi16(sc));
        o[j] = pk(lo16(t) + lo16(sa), hi16(t) + hi16(sa));
      }
      st128(o, rs, (4 * i + ((lane & 7) >> 1)) * 1024 + (r + 32 * (lane & 1)) * 16, pb + PN_H + strip * 32768 + w * 8192);
    }
    await<4>(L, 3);
    publish(rs, fw + FL_H + strip, (mem & 3) * 4 + w, lane);
  }

  const int trq = (lane & 15) >> 2, trp = lane & 3, trb = (lane >> 4) & 1;
  const f32x16 zero16 = {0.f, 0.f, 0.f, 0.f, 0.f, 0.f, 0.f, 0.f, 0.f, 0.f, 0.f, 0.f, 0.f, 0.f, 0.f, 0.f};
  u32 phq[16];
  v4u wb[PD];
  {
    const Base3 wnat(L + (8 * hh + trq) * 64 + trb * 32 + trp * 8);
    waitn(rs, fw + FL_H + w, 0xFFFFu, lane);
    v4u a[32];
    ldfrags<W0>(a, rs, lane * 16, pb + PN_H + w * 32768);
    bar_raw();
    {
      const int hs = (l31 >> 2) & 1;
      const LAS u16* wp = (const LAS u16*)(L + L_CF + 2048) + hs * 256 + 4 * hh * 16 + (l31 & 3) + 4 * (l31 >> 3);
      u32 e[8];
#pragma unroll
      for (int j = 0; j < 8; ++j) e[j] = wp[(8 * (j >> 2) + (j & 3)) * 16];
      const v4u av = {e[0] | (e[1] << 16), e[2] | (e[3] << 16), e[4] | (e[5] << 16), e[6] | (e[7] << 16)};
      const v4u z = {0, 0, 0, 0};
      aphi0 = hs == 0 ? av : z; aphi1 = hs == 1 ? av : z;
      asm volatile("" : "+v"(aphi0), "+v"(aphi1));
    }
    static_for<0, PD>([&](auto U) { wb[U.value] = ldw<256>(wnat.at<slot(P_KV + kv_piece(U.value))>()); });
    SB0();
    f32x16 kacc = zero16, vacc = zero16;
    auto unit = [&](auto U) {
      constexpr int u = decltype(U)::value, pc = kv_piece(u), st = pc >> 1;
      SB0();
      const v4u cur = wb[u % PD];
      if constexpr (u + PD < 64) wb[u % PD] = ldw<256>(wnat.at<slot(P_KV + kv_piece(u + PD))>());
      if constexpr (pc & 1) mfma_pin(a[st], cur, vacc); else mfma_pin(cur, a[st], kacc);
      if constexpr (!(pc & 1) && st + W0 < 32) a[st + W0] = ldfrag<st + W0>(rs, lane * 16, pb + PN_H + w * 32768);
      if constexpr (!(pc & 1) && st + W0 == 32) sig(L, 2, w);
      SB0();
    };
    static_for<0, 47>(unit);
    const v4u kb0 = pack8<0>(kacc), kb1 = pack8<8>(kacc);
    f32x16 P = zero16;
    mfma_pin(aphi0, kb0, P);
    mfma_pin(aphi1, kb1, P);
    unit(IC<47>{});
    u32 phk[16];
    feat(P, phk, [&](auto N) { unit(IC<48 + N.value>{}); });
    const v4u vb0 = pack8<0>(vacc), vb1 = pack8<8>(vacc);
    LAS unsigned char* T = L + L_T + w * TW;
#pragma unroll
    for (int c = 0; c < 4; ++c)
      *(LAS v4u*)(T + lane * 64 + ((c ^ ((lane >> 1) & 3)) * 16)) = v4u{phk[4 * c], phk[4 * c + 1], phk[4 * c + 2], phk[4 * c + 3]};
    const v4u ones = {0x3F803F80u, 0x3F803F80u, 0x3F803F80u, 0x3F803F80u};
    LAS unsigned char* tr = T + (4 * hh + trq) * 64 + (((2 * trb + (trp >> 1)) ^ ((2 * hh + (trq >> 1)) & 3)) * 16) + 8 * (trp & 1);
    const v4u t0 = ldw<512>(tr), t1 = ldw<512>(tr + 1024), t2 = ldw<512>(tr + 2048), t3 = ldw<512>(tr + 3072);
    f32x16 D0 = mfma(t0, trb ? ones : vb0, zero16);
    f32x16 D1 = mfma(t2, trb ? vb0 : ones, zero16);
    D0 = mfma(t1, trb ? ones : vb1, D0);
    D1 = mfma(t3, trb ? vb1 : ones, D1);
    {
      LAS unsigned char* pe = T + lane * 16;
      LAS unsigned char* pz = T + 1024 + (lane >> 4) * 16;
      LAS unsigned char* p0 = trb ? pz : pe;
      LAS unsigned char* p1 = trb ? pe : pz;
#pragma unroll
      for (int g = 0; g < 4; ++g) *(LAS f32x4*)(p0 + g * 1088) = f32x4{D0[4 * g], D0[4 * g + 1], D0[4 * g + 2], D0[4 * g + 3]};
#pragma unroll
      for (int g = 0; g < 4; ++g) *(LAS f32x4*)(p1 + g * 1088) = f32x4{D1[4 * g], D1[4 * g + 1], D1[4 * g + 2], D1[4 * g + 3]};
    }
    MARK(H_C0, w, 1u);
    reach<0>(L, H_C0, 1u, 1u, 1u, 1u);
    {
      f32x4 e[4], z[4];
#pragma unroll
      for (int v = 0; v < 4; ++v) {
        e[v] = *(LAS f32x4*)(L + L_T + v * TW + w * 1088 + lane * 16);
        z[v] = *(LAS f32x4*)(L + L_T + v * TW + w * 1088 + 1024 + (lane & 3) * 16);
      }
      SB0();
      const int idx = (mem * 4 + w) * 16 + panel;
      st128(__builtin_bit_cast(v4u, (e[0] + e[1]) + (e[2] + e[3])), rs, lane * 16, OFF_KVE + idx * 1024);
      st128(__builtin_bit_cast(v4u, (z[0] + z[1]) + (z[2] + z[3])), rs, lane < 4 ? lane * 16 : WS_BYTES, OFF_KVZ + idx * 64);
      publish(rs, FL_KV + mem * 2 + (w >> 1), (w & 1) * 16 + panel, lane);
      MARK(H_C0, w, 2u);
      reach<1>(L, H_P0, 1u, 1u, 0u, 0u);
      static_for<0, PD>([&](auto U) { wb[U.value] = ldw<256>(wnat.at<slot(P_Q + U.value)>()); });
    }
    f32x16 qacc = zero16;
    static_for<0, 32>([&](auto U) {
      constexpr int u = U.value;
      const v4u cur = wb[u % PD];
      if constexpr (u + PD < 32) wb[u % PD] = ldw<256>(wnat.at<slot(P_Q + u + PD)>());
      mfma_pin(cur, a[u], qacc);
      SB0();
    });
    MARK(H_C0, w, 3u);
    P = mfma(aphi0, pack8<0>(qacc), zero16);
    P = mfma(aphi1, pack8<8>(qacc), P);
    feat(P, phq, [](auto) {});
#pragma unroll
    for (int i = 0; i < 16; ++i) asm volatile("" : "+v"(phq[i]));
  }
  v2u xres[4];
  {
    const u16* xr = p.x + (size_t)(panel * 128 + strip2 * 32 + l31) * D + ncol + 4 * hh;
#pragma unroll
    for (int i = 0; i < 4; ++i) xres[i] = *(const GAS v2u*)(xr + 8 * i);
  }
  reach<1>(L, H_P0, 0u, 0u, 1u, 1u);
  {
    const bool on = (l31 >> 4) == hh;
    const v4u zv = {0, 0, 0, 0};
    f32x16 num = zero16, den = zero16;
    v4u ak[4], az[4];
#pragma unroll
    for (int s4 = 0; s4 < 4; ++s4) { az[s4] = *(LAS v4u*)(L + L_ZF + s4 * 32 + hh * 16); ak[s4] = *(LAS v4u*)(L + L_KVF + s4 * 512 + l31 * 16); }
    SB0();
#pragma unroll
    for (int s4 = 0; s4 < 4; ++s4) den = mfma(on ? az[s4] : zv, v4u{phq[4 * s4], phq[4 * s4 + 1], phq[4 * s4 + 2], phq[4 * s4 + 3]}, den);
#pragma unroll
    for (int s4 = 0; s4 < 4; ++s4) num = mfma(on ? ak[s4] : zv, v4u{phq[4 * s4], phq[4 * s4 + 1], phq[4 * s4 + 2], phq[4 * s4 + 3]}, num);
    const float d0 = rcp(rb(rb(den[0]) + 1e-6f)), d1 = rcp(rb(rb(den[8]) + 1e-6f));
    const v4u n0 = pack8<0>(num), n1 = pack8<8>(num);
    v4u o0, o1;
#pragma unroll
    for (int j = 0; j < 4; ++j) { o0[j] = pk(lo16(n0[j]) * d0, hi16(n0[j]) * d0); o1[j] = pk(lo16(n1[j]) * d1, hi16(n1[j]) * d1); }
    st128(o0, rs, lane * 16, pb + PN_O + w * 32768 + (2 * mem) * 1024);
    st128(o1, rs, lane * 16 + 1024, pb + PN_O + w * 32768 + (2 * mem) * 1024);
  }
#define WDN_BASES() const Base3 wown(L + (4 * hh + trq) * 64 + trb * 32 + trp * 8 + 3 * kh * 1024), woth(L + (4 * hh + trq) * 64 + trb * 32 + trp * 8 + (kh + 1) * 1024)
#define WDN(base, u) ldw<512>((((u) & 1) ? wown : woth).template at<slot((base) + 4 * ((u) >> 1))>())
  u32 x1p[8];
  {
    WDN_BASES();
    publish(rs, fw + FL_O + w, mem, lane);
    reach<0>(L, H_P0, 2u, 2u, 0u, 0u);
    static_for<0, PD>([&](auto U) { wb[U.value] = WDN(P_WO, U.value); });
    waitn(rs, fw + FL_O + strip2, 0xFFu << (kh * 8), lane);
    v4u a[16];
    ldfrags<16>(a, rs, lane * 16, pb + PN_O + strip2 * 32768 + kh * 16384);
    sig(L, 4, w);
    reach<0>(L, H_P0, 0u, 0u, 2u, 2u);
    f32x4 ga[4], sc[4];
#pragma unroll
    for (int g = 0; g < 4; ++g) {
      ga[g] = *(LAS f32x4*)(L + L_VEC + (kh * 32 + 8 * g + 4 * hh) * 4);
      sc[g] = *(LAS f32x4*)(L + L_VEC + 256 + (kh * 32 + 8 * g + 4 * hh) * 4);
    }
    SB0();
    f32x16 c0 = zero16, c1 = zero16;
    static_for<0, 32>([&](auto U) {
      constexpr int u = U.value;
      const v4u cur = wb[u % PD];
      if constexpr (u + PD < 32) wb[u % PD] = WDN(P_WO, u + PD);
      if constexpr (u & 1) mfma_pin(cur, a[u >> 1], c0); else mfma_pin(cur, a[u >> 1], c1);
      SB0();
    });
#pragma unroll
    for (int g = 0; g < 4; ++g) *(LAS f32x4*)(L + L_T + w * TW + g * 1024 + lane * 16) = f32x4{c1[4 * g], c1[4 * g + 1], c1[4 * g + 2], c1[4 * g + 3]};
    MARK(H_C0, w, 4u);
    reach1(L, H_C0, w ^ 2, 4u);
    f32x4 o[4];
#pragma unroll
    for (int g = 0; g < 4; ++g) o[g] = *(LAS f32x4*)(L + L_T + (w ^ 2) * TW + g * 1024 + lane * 16);
    SB0();
    float x1[16], sp[4];
    u32 yp[8];
#pragma unroll
    for (int g = 0; g < 4; ++g) {
#pragma unroll
      for (int j = 0; j < 2; ++j) {
        const u32 r = pk(lo16(xres[g][j]) + ga[g][2 * j] * (c0[4 * g + 2 * j] + o[g][2 * j]),
                         hi16(xres[g][j]) + ga[g][2 * j + 1] * (c0[4 * g + 2 * j + 1] + o[g][2 * j + 1]));
        x1p[2 * g + j] = r;
        x1[4 * g + 2 * j] = lo16(r); x1[4 * g + 2 * j + 1] = hi16(r);
        yp[2 * g + j] = pk(lo16(r) * sc[g][2 * j], hi16(r) * sc[g][2 * j + 1]);
      }
    }
    const int tile = 2 * colg + kh;
    st128(v4u{yp[0], yp[1], yp[2], yp[3]}, rs, lane * 16, pb + PN_X1 + strip2 * 32768 + tile * 2048);
    st128(v4u{yp[4], yp[5], yp[6], yp[7]}, rs, lane * 16 + 1024, pb + PN_X1 + strip2 * 32768 + tile * 2048);
    SB0();
#pragma unroll
    for (int g = 0; g < 4; ++g) sp[g] = (x1[4 * g] + x1[4 * g + 1]) + (x1[4 * g + 2] + x1[4 * g + 3]);
    const float ss = (sp[0] + sp[1]) + (sp[2] + sp[3]);
    const float mt = fold32(ss, ss) * (1.f / 32.f);
    float q[4];
#pragma unroll
    for (int g = 0; g < 4; ++g) {
      const float d0 = x1[4 * g] - mt, d1 = x1[4 * g + 1] - mt, d2 = x1[4 * g + 2] - mt, d3 = x1[4 * g + 3] - mt;
      q[g] = (d0 * d0 + d1 * d1) + (d2 * d2 + d3 * d3);
    }
    const float qs = (q[0] + q[1]) + (q[2] + q[3]);
    const float qt = fold32(qs, qs);
    v4u sv = {__float_as_uint(mt), tag, __float_as_uint(qt), tag};
    asm volatile("" : "+v"(sv));
    publish(rs, fw + FL_X + strip2, tile, lane);
    st128(sv, rs, lane < 32 ? lane * 16 : WS_BYTES, pb + PN_ST + strip2 * 8192 + tile * 512);
  }
  asm volatile("" : "+v"(x1p[0]), "+v"(x1p[1]), "+v"(x1p[2]), "+v"(x1p[3]), "+v"(x1p[4]), "+v"(x1p[5]), "+v"(x1p[6]), "+v"(x1p[7]));
#define GU_S(u) ((u) < 72 ? (u) / 6 : 12 + (((u) - 72) % 40) / 2)
#define GU_I(u) ((u) < 72 ? (u) % 6 : 2 * (((u) - 72) / 40) + ((u) & 1))
#define WGU(u) ldw<512>((((u) & 1) ? wy : wx).template at<slot(P_GU + ((u) & ~1))>())
  {
    const Base3 wx(L + (4 * hh + trq) * 64 + trp * 8 + trb * (1024 + 32)), wy(L + (4 * hh + trq) * 64 + trp * 8 + (trb ? 1024 : 32));
    reach<0>(L, H_P0, 3u, 3u, 0u, 0u);
    static_for<0, PD>([&](auto U) { wb[U.value] = WGU(U.value); });
    waitn(rs, fw + FL_X + w, 0xFFFFu, lane);
    constexpr int AW = 20;
    v4u a[AW];
    ldfrags<8>(a, rs, lane * 16, pb + PN_X1 + w * 32768);
    sig(L, 5, w);
    f32x16 acc[6];
#pragma unroll
    for (int i = 0; i < 6; ++i) acc[i] = zero16;
    float rr = 0.f;
    v4u bcor = {0, 0, 0, 0};
    v4u ho, cf0, cf1, pp;
    float t0 = 0.f, t1 = 0.f;
    const int go = pb + PN_G + (w * 96 + mem) * 1024, fi = fw + FL_G + strip2 * 3, gi = pb + PN_G + (strip2 * 96 + kh * 48) * 1024;
    u32 fl = 0;
    int gv = 0;
    bool early = false;
    v4u g[43];
    auto post = [&](int P) { (void)__builtin_amdgcn_raw_ptr_buffer_atomic_add_i32(1 << mem, rs, lane == 0 ? 0 : WS_BYTES, OFF_ARR + (fw + FL_G + w * 3 + P) * 128, 0); };
    auto epi = [&](auto Ic, auto Kc) {
      constexpr int I = decltype(Ic)::value, k = decltype(Kc)::value, j = k >> 1;
      const f32x16& A = acc[I];
      if constexpr ((k & 1) == 0) { t0 = A[2 * j] * rr; t1 = A[2 * j + 1] * rr; asm volatile("" : "+v"(t0)); t0 *= A[8 + 2 * j]; }
      else {
        t1 *= A[9 + 2 * j];
        ho[j] = pk(t0, t1);
        if constexpr (j == 3) st128(ho, rs, (I < 4 || three) ? lane * 16 : WS_BYTES, go + ((I & 1) * 48 + (I >> 1) * 16) * 1024);
      }
    };
    static_for<0, 192>([&](auto Uc) {
      constexpr int u = decltype(Uc)::value, s = GU_S(u), i = GU_I(u), q = (u + 8) % 40;
      if constexpr (u == 56) mark(L, H_CP, w, 2u);
      if constexpr (u == 96 || u == 128 || u == 160) mark(L, H_CP, w, u / 32);
      if constexpr (u == 60 || u == 96 || u == 140) pp = peek(L, H_PP);
      if constexpr (u == 64) need_pp<false>(L, pp, 3u);
      if constexpr (u == 100) need_pp<true>(L, pp, 4u);
      if constexpr (u == 144) need_pp<false>(L, pp, 5u);
      const v4u cur = wb[u % PD];
      if constexpr (u + PD < 192) wb[u % PD] = WGU(u + PD);
      if constexpr (u >= 72 && q == 32) cf0 = *(LAS v4u*)(L + L_CF + i * 512 + l31 * 16);
      if constexpr (u >= 72 && q == 33) cf1 = *(LAS v4u*)(L + L_CF + i * 512 + l31 * 16);
      mfma_pin(cur, a[s % AW], acc[i]);
      if constexpr (u < 72 && i == 5) a[s % AW] = ldfrag<s + AW>(rs, lane * 16, pb + PN_X1 + w * 32768);
      if constexpr (u < 2 * (AW - 8) && !(u & 1)) a[8 + (u >> 1)] = ldfrag<8 + (u >> 1)>(rs, lane * 16, pb + PN_X1 + w * 32768);
      if constexpr (u == 24) mark(L, H_CP, w, 1u);
      if constexpr (u == 101) bcor = *(LAS v4u*)(L + L_KVF + w * 512 + l31 * 16);
      if constexpr (u == 107) { rr = __uint_as_float(bcor.w); bcor = v4u{hh ? 0u : bcor.x, hh ? 0u : bcor.y, hh ? 0u : bcor.z, 0u}; }
      if constexpr (u >= 72 && q == 38) mfma_pin(cf0, bcor, acc[i]);
      if constexpr (u >= 72 && q == 39) mfma_pin(cf1, bcor, acc[i]);
      if constexpr (u >= 112 && q >= 1 && q < 9) {
        epi(IC<(i & 6) - 2 + (q - 1) / 4>{}, IC<2 * ((q - 1) % 4)>{});
        epi(IC<(i & 6) - 2 + (q - 1) / 4>{}, IC<2 * ((q - 1) % 4) + 1>{});
      }
      if constexpr (u == 112 + GPUB) publish(rs, fw + FL_G + w * 3, mem, lane);
      if constexpr (u == GASK) fl = ask(rs, fi, lane);
      if constexpr (u == 176) {
        early = (__builtin_amdgcn_readfirstlane(fl) & 0xFFFFu) == 0xFFFFu;
        __builtin_amdgcn_fence(__ATOMIC_ACQUIRE, "wavefront");
        RELOAD();
        gv = early ? lane * 16 : WS_BYTES;
      }
      if constexpr (u == 180) { VMCNT(4); post(1); }
      if constexpr (u >= 176) g[u - 176] = ldfrag<u - 176>(rs, gv, gi);
      if constexpr (u == 184) pp = peek(L, H_PP);
      SB0();
    });
    mark(L, H_CP, w, 6u);
    need_pp<true>(L, pp, 6u);
    WDN_BASES();
    static_for<0, PD>([&](auto U) { wb[U.value] = WDN(P_WD, U.value); });
    if (!early) {
      spin(rs, fi, 0xFFFFu, lane);
      __builtin_amdgcn_fence(__ATOMIC_ACQUIRE, "wavefront");
      RELOAD();
      static_for<0, 16>([&](auto T) { g[T.value] = ld128(rs, lane * 16, gi + T.value * 1024); });
    }
    SB0();
    fl = ask(rs, fi + 1, lane);
    SB0();
    f32x16 c0 = zero16, c1 = zero16;
    static_for<0, 86>([&](auto U) {
      constexpr int u = U.value;
      if constexpr (u == 18) mark(L, H_CP, w, 7u);
#define GL(t) ((t) < 32 ? GU0 + (t) - 16 : GU2 + (t) - 32)
      if constexpr (u == PUB2) {
        asm volatile("s_waitcnt vmcnt(%0)" ::"i"(PUB2 - GU0 < 16 ? PUB2 - GU0 : 16) : "memory");
        post(2);
      }
      if constexpr (u == 24 || u == 40 || u == 56) pp = peek(L, H_PP);
      if constexpr (u == 28 || u == 44 || u == 60) need_pp<true>(L, pp, 7u + (u - 28) / 16);
      if constexpr (u == GL(16)) poll(fl, rs, fi + 1, 0xFFFFu, lane);
      if constexpr (u == GL(32) - GA2) fl = ask(rs, fi + 2, lane);
      if constexpr (u == GL(32)) poll(fl, rs, fi + 2, 0x7FFu, lane);
      const v4u cur = wb[u % PD];
      if constexpr (u + PD < 86) wb[u % PD] = WDN(P_WD, u + PD);
      if constexpr (u & 1) mfma_pin(cur, g[u >> 1], c0); else mfma_pin(cur, g[u >> 1], c1);
      if constexpr (u >= 1 && u < 9) {
        epi(IC<4 + (u - 1) / 4>{}, IC<2 * ((u - 1) % 4)>{});
        epi(IC<4 + (u - 1) / 4>{}, IC<2 * ((u - 1) % 4) + 1>{});
      }
      static_for<16, 43>([&](auto T) { if constexpr (GL(T.value) == u) g[T.value] = ldfrag<T.value>(rs, lane * 16, gi); });
      SB0();
    });
#pragma unroll
    for (int g = 0; g < 4; ++g) *(LAS f32x4*)(L + L_T + w * TW + g * 1024 + lane * 16) = f32x4{c1[4 * g], c1[4 * g + 1], c1[4 * g + 2], c1[4 * g + 3]};
    f32x4 gb[4];
#pragma unroll
    for (int g = 0; g < 4; ++g) gb[g] = *(LAS f32x4*)(L + L_VEC + 512 + (kh * 32 + 8 * g + 4 * hh) * 4);
    MARK(H_C0, w, 5u);
    reach1(L, H_C0, w ^ 2, 5u);
    f32x4 o[4];
#pragma unroll
    for (int g = 0; g < 4; ++g) o[g] = *(LAS f32x4*)(L + L_T + (w ^ 2) * TW + g * 1024 + lane * 16);
    SB0();
    const rsrc_t ro = __builtin_amdgcn_make_buffer_rsrc((void*)p.out, 0, 2048 * D * 2, 0x00020000);
    v2u r[4];
#pragma unroll
    for (int g = 0; g < 4; ++g)
#pragma unroll
      for (int j = 0; j < 2; ++j)
        r[g][j] = pk(lo16(x1p[2 * g + j]) + gb[g][2 * j] * (c0[4 * g + 2 * j] + o[g][2 * j]),
                     hi16(x1p[2 * g + j]) + gb[g][2 * j + 1] * (c0[4 * g + 2 * j + 1] + o[g][2 * j + 1]));
    const int oo = ((panel * 128 + strip2 * 32 + l31) * D + ncol + 8 * hh) * 2;
#pragma unroll
    for (int g = 0; g < 4; g += 2) {
      const auto x = __builtin_amdgcn_permlane32_swap(r[g][0], r[g + 1][0], false, false);
      const auto y = __builtin_amdgcn_permlane32_swap(r[g][1], r[g + 1][1], false, false);
      __builtin_amdgcn_raw_buffer_store_b128(v4u{x[0], y[0], x[1], y[1]}, ro, oo + g * 16, 0, OAUX);
    }
  }
  if (b == 0 && tid == 0) __hip_atomic_store((GAS u32*)(ws + OFF_EPOCH), tag, RLX_AGENT);
}
}

extern "C" void kernel_launch(void* const* d_in, const int* in_sizes, int n_in, void* d_out, int out_size, void* d_ws,
                              size_t ws_size, hipStream_t stream) {
  using namespace mk;
  Params p;
  p.x = (const u16*)d_in[0]; p.wqkv = (const u16*)d_in[1]; p.wphi = (const u16*)d_in[2]; p.wo = (const u16*)d_in[3];
  p.wg = (const u16*)d_in[5]; p.wu = (const u16*)d_in[6]; p.wd = (const u16*)d_in[7]; p.c = (const u16*)d_in[8];
  p.wmod = (const u16*)d_in[9]; p.out = (u16*)d_out; p.ws = (unsigned char*)d_ws;
  (void)hipFuncSetAttribute((const void*)mega, hipFuncAttributeMaxDynamicSharedMemorySize, LDS_BYTES);
  mega<<<256, 512, LDS_BYTES, stream>>>(p);
}
```
